```python
import torch

def _softmax(x, axis=-1):
    return torch.softmax(x, dim=axis)
def _gelu(x):
    return torch.nn.functional.gelu(x, approximate="tanh")
def _silu(x):
    return torch.nn.functional.silu(x)
def _sigmoid(x):
    return torch.sigmoid(x)
def _layernorm(x, axis=-1, eps=1e-5):
    m = x.mean(dim=axis, keepdim=True)
    v = x.var(dim=axis, keepdim=True, unbiased=False)
    return (x - m) / torch.sqrt(v + eps)
def _scatter_add(tgt, idx, src, axis=0):
    return tgt.clone().index_add_(axis, idx, src)
def _segment_sum(src, seg, n):
    out = torch.zeros((n,) + src.shape[1:], dtype=src.dtype)
    return out.index_add_(0, seg, src)
def _pad(x, pw):
    flat = tuple(v for pair in reversed(pw) for v in pair)
    return torch.nn.functional.pad(x, flat)

kernel_name = 'standalone__mha_block_cached__near_ridge'

def setup_inputs():

    _w = (lambda a: (lambda t: t.to(torch.bfloat16) if t.is_floating_point() else t)(torch.as_tensor(a)))
    B, Sq, D = 16, 144, 1024
    H, Sk, Dh = 32, 512, 128
    return {
        "x": _w((lambda *s: torch.randn(*s, dtype=torch.bfloat16) * 0.8)(B, Sq, D)),
        "k_cache": _w((lambda *s: torch.randn(*s, dtype=torch.bfloat16) * 0.8)(B, H, Sk, Dh)),
        "v_cache": _w((lambda *s: torch.randn(*s, dtype=torch.bfloat16) * 0.05)(B, H, Sk, Dh)),
        "wq": _w((lambda *s: torch.randn(*s, dtype=torch.bfloat16) * 0.8)(D, H * Dh) * (D ** -0.5)),
        "wo": _w((lambda *s: torch.randn(*s, dtype=torch.bfloat16) * 0.05)(H * Dh, D) * ((H * Dh) ** -0.5)),
    }

def reference(x, k_cache, v_cache, wq, wo):
    B, Sq, D = x.shape
    H, Dh = 32, 128
    q = (lambda x, *d: x.permute(*d))((x @ wq).reshape(B, Sq, H, Dh), 0, 2, 1, 3)
    scores = torch.einsum("bhqd,bhkd->bhqk", q, k_cache) * (Dh ** -0.5)
    p = _softmax(scores, axis=-1)
    o = torch.einsum("bhqk,bhkd->bhqd", p, v_cache)
    return (lambda x, *d: x.permute(*d))(o, 0, 2, 1, 3).reshape(B, Sq, H * Dh) @ wo
```

```cpp
#include <hip/hip_runtime.h>
#include <hip/hip_bf16.h>
typedef short bf16x8 __attribute__((ext_vector_type(8)));
typedef float f32x4 __attribute__((ext_vector_type(4)));
typedef unsigned u32x4 __attribute__((ext_vector_type(4)));
typedef unsigned short u16;
__device__ inline u16 f2bf(float f) { __hip_bfloat16 b = __float2bfloat16(f); return *(u16*)&b; }
__device__ inline float bf2f(u16 u) { return __uint_as_float(((unsigned)u) << 16); }

__global__ __launch_bounds__(256) void gemm_k(const u16* __restrict__ A, const u16* __restrict__ B, u16* __restrict__ C, int M, int N, int K) {
  __shared__ __attribute__((aligned(16))) u16 As[64][40];
  __shared__ __attribute__((aligned(16))) u16 Bs[64][40];
  int t = threadIdx.x, w = t >> 6, l = t & 63;
  int m0 = blockIdx.y * 64, n0 = blockIdx.x * 64;
  int wr = w >> 1, wc = w & 1;
  f32x4 acc[2][2] = {};
  for (int k0 = 0; k0 < K; k0 += 32) {
    { int r = t >> 2, c = (t & 3) * 8; *(u32x4*)&As[r][c] = *(const u32x4*)&A[(long)(m0 + r) * K + k0 + c]; }
    { int r = t >> 3, c = (t & 7) * 8; u32x4 v = *(const u32x4*)&B[(long)(k0 + r) * N + n0 + c]; u16* p = (u16*)&v;
      for (int j = 0; j < 8; ++j) Bs[c + j][r] = p[j]; }
    __syncthreads();
    bf16x8 a[2], b[2];
    for (int i = 0; i < 2; ++i) { a[i] = *(bf16x8*)&As[wr * 32 + i * 16 + (l & 15)][(l >> 4) * 8]; b[i] = *(bf16x8*)&Bs[wc * 32 + i * 16 + (l & 15)][(l >> 4) * 8]; }
    for (int i = 0; i < 2; ++i) for (int j = 0; j < 2; ++j) acc[i][j] = __builtin_amdgcn_mfma_f32_16x16x32_bf16(a[i], b[j], acc[i][j], 0, 0, 0);
    __syncthreads();
  }
  for (int i = 0; i < 2; ++i) for (int j = 0; j < 2; ++j) for (int r = 0; r < 4; ++r)
    C[(long)(m0 + wr * 32 + i * 16 + (l >> 4) * 4 + r) * N + n0 + wc * 32 + j * 16 + (l & 15)] = f2bf(acc[i][j][r]);
}

__global__ __launch_bounds__(64) void attn_k(const u16* __restrict__ Q, const u16* __restrict__ Kc, const u16* __restrict__ Vc, u16* __restrict__ O) {
  __shared__ __attribute__((aligned(16))) u16 Vs[128][40];
  int l = threadIdx.x, qt = blockIdx.x % 9, bh = blockIdx.x / 9, b = bh / 32, h = bh % 32;
  const u16* Kp = Kc + (long)bh * 512 * 128; const u16* Vp = Vc + (long)bh * 512 * 128;
  bf16x8 q[4];
  for (int s = 0; s < 4; ++s) q[s] = *(const bf16x8*)&Q[(long)(b * 144 + qt * 16 + (l & 15)) * 4096 + h * 128 + s * 32 + (l >> 4) * 8];
  f32x4 S[32];
#pragma unroll
  for (int kt = 0; kt < 32; ++kt) { f32x4 a = {0,0,0,0};
    for (int s = 0; s < 4; ++s) { bf16x8 kf = *(const bf16x8*)&Kp[(kt * 16 + (l & 15)) * 128 + s * 32 + (l >> 4) * 8];
      a = __builtin_amdgcn_mfma_f32_16x16x32_bf16(kf, q[s], a, 0, 0, 0); }
    S[kt] = a; }
  float m = -1e30f;
#pragma unroll
  for (int kt = 0; kt < 32; ++kt) for (int r = 0; r < 4; ++r) m = fmaxf(m, S[kt][r]);
  m = fmaxf(m, __shfl_xor(m, 16)); m = fmaxf(m, __shfl_xor(m, 32));
  float sum = 0; const float c = 0.08838834764831845f * 1.4426950408889634f;
#pragma unroll
  for (int kt = 0; kt < 32; ++kt) for (int r = 0; r < 4; ++r) { S[kt][r] = exp2f((S[kt][r] - m) * c); sum += S[kt][r]; }
  sum += __shfl_xor(sum, 16); sum += __shfl_xor(sum, 32);
  f32x4 o[8] = {};
#pragma unroll
  for (int kb = 0; kb < 16; ++kb) {
    __syncthreads();
    for (int i = 0; i < 8; ++i) { int idx = i * 64 + l, r = idx >> 4, cc = (idx & 15) * 8; u32x4 v = *(const u32x4*)&Vp[(kb * 32 + r) * 128 + cc]; u16* p = (u16*)&v;
      int g = (r & 15) >> 2, j = (r & 3) + 4 * (r >> 4);
      for (int e = 0; e < 8; ++e) Vs[cc + e][g * 8 + j] = p[e]; }
    __syncthreads();
    bf16x8 pf; for (int r = 0; r < 4; ++r) { pf[r] = (short)f2bf(S[2 * kb][r]); pf[4 + r] = (short)f2bf(S[2 * kb + 1][r]); }
    for (int d = 0; d < 8; ++d) { bf16x8 vf = *(bf16x8*)&Vs[d * 16 + (l & 15)][(l >> 4) * 8]; o[d] = __builtin_amdgcn_mfma_f32_16x16x32_bf16(vf, pf, o[d], 0, 0, 0); }
  }
  float inv = 1.f / sum;
  for (int d = 0; d < 8; ++d) for (int r = 0; r < 4; ++r)
    O[(long)(b * 144 + qt * 16 + (l & 15)) * 4096 + h * 128 + d * 16 + (l >> 4) * 4 + r] = f2bf(o[d][r] * inv);
}
#define NPROBE 0
__global__ __launch_bounds__(512) void stream_kernel(const u32x4* __restrict__ k, const u32x4* __restrict__ v, unsigned* out, int n16) {
  long per = n16 / gridDim.x;
  const u32x4* kp = k + per * blockIdx.x;
  const u32x4* vp = v + per * blockIdx.x;
  u32x4 acc = {0,0,0,0};
  for (long i = threadIdx.x; i < per; i += 512 * 4) {
    u32x4 a0 = kp[i], a1 = kp[i + 512], a2 = kp[i + 1024], a3 = kp[i + 1536];
    u32x4 b0 = vp[i], b1 = vp[i + 512], b2 = vp[i + 1024], b3 = vp[i + 1536];
    acc ^= a0 ^ a1 ^ a2 ^ a3 ^ b0 ^ b1 ^ b2 ^ b3;
  }
  unsigned r = acc[0] ^ acc[1] ^ acc[2] ^ acc[3];
  out[blockIdx.x * 512 + threadIdx.x] = r;
}
extern "C" void kernel_launch(void* const* d_in, const int* in_sizes, int n_in, void* d_out, int out_size, void* d_ws, size_t ws_size, hipStream_t stream) {
  const u16* x = (const u16*)d_in[0]; const u16* k = (const u16*)d_in[1]; const u16* v = (const u16*)d_in[2];
  const u16* wq = (const u16*)d_in[3]; const u16* wo = (const u16*)d_in[4];
  u16* Q = (u16*)d_ws; u16* O = Q + 2304 * 4096;
  for (int i = 0; i < NPROBE; ++i) stream_kernel<<<512, 512, 0, stream>>>((const u32x4*)k, (const u32x4*)v, (unsigned*)(O + 2304 * 4096), in_sizes[1] / 8);
  gemm_k<<<dim3(64, 36), 256, 0, stream>>>(x, wq, Q, 2304, 4096, 1024);
  attn_k<<<512 * 9, 64, 0, stream>>>(Q, k, v, O);
  gemm_k<<<dim3(16, 36), 256, 0, stream>>>(O, wo, (u16*)d_out, 2304, 1024, 4096);
}
```

```cpp
#include <hip/hip_runtime.h>
#include <hip/hip_bf16.h>
typedef short bf16x8 __attribute__((ext_vector_type(8)));
typedef float f32x4 __attribute__((ext_vector_type(4)));
typedef unsigned u32x4 __attribute__((ext_vector_type(4)));
typedef unsigned short u16;
__device__ inline u16 f2bf(float f) { __hip_bfloat16 b = __float2bfloat16(f); return *(u16*)&b; }
__device__ inline float bf2f(u16 u) { return __uint_as_float(((unsigned)u) << 16); }

__global__ __launch_bounds__(256) void gemm_k(const u16* __restrict__ A, const u16* __restrict__ B, u16* __restrict__ C, int M, int N, int K) {
  __shared__ __attribute__((aligned(16))) u16 As[64][40];
  __shared__ __attribute__((aligned(16))) u16 Bs[64][40];
  int t = threadIdx.x, w = t >> 6, l = t & 63;
  int m0 = blockIdx.y * 64, n0 = blockIdx.x * 64;
  int wr = w >> 1, wc = w & 1;
  f32x4 acc[2][2] = {};
  for (int k0 = 0; k0 < K; k0 += 32) {
    { int r = t >> 2, c = (t & 3) * 8; *(u32x4*)&As[r][c] = *(const u32x4*)&A[(long)(m0 + r) * K + k0 + c]; }
    { int r = t >> 3, c = (t & 7) * 8; u32x4 v = *(const u32x4*)&B[(long)(k0 + r) * N + n0 + c]; u16* p = (u16*)&v;
      for (int j = 0; j < 8; ++j) Bs[c + j][r] = p[j]; }
    __syncthreads();
    bf16x8 a[2], b[2];
    for (int i = 0; i < 2; ++i) { a[i] = *(bf16x8*)&As[wr * 32 + i * 16 + (l & 15)][(l >> 4) * 8]; b[i] = *(bf16x8*)&Bs[wc * 32 + i * 16 + (l & 15)][(l >> 4) * 8]; }
    for (int i = 0; i < 2; ++i) for (int j = 0; j < 2; ++j) acc[i][j] = __builtin_amdgcn_mfma_f32_16x16x32_bf16(a[i], b[j], acc[i][j], 0, 0, 0);
    __syncthreads();
  }
  for (int i = 0; i < 2; ++i) for (int j = 0; j < 2; ++j) for (int r = 0; r < 4; ++r)
    C[(long)(m0 + wr * 32 + i * 16 + (l >> 4) * 4 + r) * N + n0 + wc * 32 + j * 16 + (l & 15)] = f2bf(acc[i][j][r]);
}

__global__ __launch_bounds__(64) void attn_k(const u16* __restrict__ Q, const u16* __restrict__ Kc, const u16* __restrict__ Vc, u16* __restrict__ O) {
  __shared__ __attribute__((aligned(16))) u16 Vs[128][40];
  int l = threadIdx.x, qt = blockIdx.x % 9, bh = blockIdx.x / 9, b = bh / 32, h = bh % 32;
  const u16* Kp = Kc + (long)bh * 512 * 128; const u16* Vp = Vc + (long)bh * 512 * 128;
  bf16x8 q[4];
  for (int s = 0; s < 4; ++s) q[s] = *(const bf16x8*)&Q[(long)(b * 144 + qt * 16 + (l & 15)) * 4096 + h * 128 + s * 32 + (l >> 4) * 8];
  f32x4 S[32];
#pragma unroll
  for (int kt = 0; kt < 32; ++kt) { f32x4 a = {0,0,0,0};
    for (int s = 0; s < 4; ++s) { bf16x8 kf = *(const bf16x8*)&Kp[(kt * 16 + (l & 15)) * 128 + s * 32 + (l >> 4) * 8];
      a = __builtin_amdgcn_mfma_f32_16x16x32_bf16(kf, q[s], a, 0, 0, 0); }
    S[kt] = a; }
  float m = -1e30f;
#pragma unroll
  for (int kt = 0; kt < 32; ++kt) for (int r = 0; r < 4; ++r) m = fmaxf(m, S[kt][r]);
  m = fmaxf(m, __shfl_xor(m, 16)); m = fmaxf(m, __shfl_xor(m, 32));
  float sum = 0; const float c = 0.08838834764831845f * 1.4426950408889634f;
#pragma unroll
  for (int kt = 0; kt < 32; ++kt) for (int r = 0; r < 4; ++r) { S[kt][r] = exp2f((S[kt][r] - m) * c); sum += S[kt][r]; }
  sum += __shfl_xor(sum, 16); sum += __shfl_xor(sum, 32);
  f32x4 o[8] = {};
#pragma unroll
  for (int kb = 0; kb < 16; ++kb) {
    __syncthreads();
    for (int i = 0; i < 8; ++i) { int idx = i * 64 + l, r = idx >> 4, cc = (idx & 15) * 8; u32x4 v = *(const u32x4*)&Vp[(kb * 32 + r) * 128 + cc]; u16* p = (u16*)&v;
      int g = (r & 15) >> 2, j = (r & 3) + 4 * (r >> 4);
      for (int e = 0; e < 8; ++e) Vs[cc + e][g * 8 + j] = p[e]; }
    __syncthreads();
    bf16x8 pf; for (int r = 0; r < 4; ++r) { pf[r] = (short)f2bf(S[2 * kb][r]); pf[4 + r] = (short)f2bf(S[2 * kb + 1][r]); }
    for (int d = 0; d < 8; ++d) { bf16x8 vf = *(bf16x8*)&Vs[d * 16 + (l & 15)][(l >> 4) * 8]; o[d] = __builtin_amdgcn_mfma_f32_16x16x32_bf16(vf, pf, o[d], 0, 0, 0); }
  }
  float inv = 1.f / sum;
  for (int d = 0; d < 8; ++d) for (int r = 0; r < 4; ++r)
    O[(long)(b * 144 + qt * 16 + (l & 15)) * 4096 + h * 128 + d * 16 + (l >> 4) * 4 + r] = f2bf(o[d][r] * inv);
}

#define RT() __builtin_amdgcn_s_memrealtime()
template <int MODE, int AUX>
__global__ __launch_bounds__(256) void probe_kernel(const char* __restrict__ x, const char* __restrict__ kc, const char* __restrict__ vc, const char* __restrict__ wq, const char* __restrict__ wo) {
  extern __shared__ __attribute__((aligned(16))) char smem[];
  int w = __builtin_amdgcn_readfirstlane(threadIdx.x >> 6), l = threadIdx.x & 63;
  unsigned long long t0 = RT();
  char* ld = smem + w * 16384;
  if (MODE == 0 || MODE == 1 || MODE == 2) {
    for (int t = (MODE == 2); t < (MODE == 1 ? 1 : 2); ++t) {
      const char* p = (t ? vc : kc) + (long)blockIdx.x * 262144 + l * 16;
      for (int q = 0; q < 64; q += 8) {
#pragma unroll
        for (int j = 0; j < 8; ++j) { __builtin_amdgcn_global_load_lds((const unsigned*)(p + (long)((q + j) * 4 + w) * 1024), (unsigned*)(ld + j * 1024), 16, 0, AUX); asm volatile("s_waitcnt vmcnt(7)" ::: "memory"); }
      }
    }
  } else if (MODE == 3) {
    int xcd = blockIdx.x & 7, i = blockIdx.x >> 3;
    int b = (xcd & 1) * 8 + (i & 7), hp = (xcd >> 1) * 4 + (i >> 3);
    const char* xb = x + (long)b * 144 * 2048; const char* wb = wq + hp * 512;
    for (int s = 0; s < 32; ++s) {
      char* l2 = ld + (s & 1) * 8192;
#pragma unroll
      for (int j = 0; j < 4; ++j) __builtin_amdgcn_global_load_lds((const unsigned*)(wb + (long)(s * 32 + j * 8 + (l >> 3)) * 8192 + w * 128 + (l & 7) * 16), (unsigned*)(l2 + j * 1024), 16, 0, AUX);
#pragma unroll
      for (int j = 0; j < 2; ++j) { int piece = ((s & 1) * 8 + j * 4 + w); __builtin_amdgcn_global_load_lds((const unsigned*)(xb + (long)(piece * 8 + (l >> 3)) * 2048 + (s >> 1) * 128 + (l & 7) * 16), (unsigned*)(l2 + 4096 + j * 1024), 16, 0, AUX); }
      asm volatile("s_waitcnt vmcnt(12)" ::: "memory");
    }
  } else if (MODE >= 4) {
    if (blockIdx.x < 8 && w == 0) {
      const char* base = MODE == 4 ? kc : MODE == 5 ? vc : MODE == 6 ? wq : MODE == 7 ? wo : x;
      long span = MODE <= 5 ? 8388608 : MODE <= 7 ? 1048576 : 589824;
      const char* p = base + span * blockIdx.x + l * 16;
      for (int q = 0; q < 32; ++q) { __builtin_amdgcn_global_load_lds((const unsigned*)(p + (span / 32) * q), (unsigned*)ld, 16, 0, AUX); asm volatile("s_waitcnt vmcnt(0)" ::: "memory"); }
    }
  }
  asm volatile("s_waitcnt vmcnt(0)" ::: "memory");
  __builtin_amdgcn_s_barrier();
  unsigned long long t1 = RT();
  while (RT() < t0 + 10 * (t1 - t0)) __builtin_amdgcn_s_sleep(8);
}
extern "C" void kernel_launch(void* const* d_in, const int* in_sizes, int n_in, void* d_out, int out_size, void* d_ws, size_t ws_size, hipStream_t stream) {
  const u16* x = (const u16*)d_in[0]; const u16* k = (const u16*)d_in[1]; const u16* v = (const u16*)d_in[2];
  const u16* wq = (const u16*)d_in[3]; const u16* wo = (const u16*)d_in[4];
  u16* Q = (u16*)d_ws; u16* O = Q + 2304 * 4096;
  probe_kernel<1, 2><<<256, 256, 65536, stream>>>((const char*)x, (const char*)k, (const char*)v, (const char*)wq, (const char*)wo);
  gemm_k<<<dim3(64, 36), 256, 0, stream>>>(x, wq, Q, 2304, 4096, 1024);
  attn_k<<<512 * 9, 64, 0, stream>>>(Q, k, v, O);
  gemm_k<<<dim3(16, 36), 256, 0, stream>>>(O, wo, (u16*)d_out, 2304, 1024, 4096);
}
```

```cpp
#include <hip/hip_runtime.h>
#define CLOB "v1", "v2", "v3", "v4", "v5", "v6", "v7", "v8", "v9", "v10", "v11", "v12", "v13", "v14", "v15", "v16", "v17", "v18", "v19", "v20", "v21", "v22", "v23", "v24", "v25", "v26", "v27", "v28", "v29", "v30", "v31", "v32", "v33", "v34", "v35", "v36", "v37", "v38", "v39", "v40", "v41", "v42", "v43", "v44", "v45", "v46", "v47", "v48", "v49", "v50", "v51", "v52", "v53", "v54", "v55", "v56", "v57", "v58", "v59", "v60", "v61", "v62", "v63", "v64", "v65", "v66", "v67", "v68", "v69", "v70", "v71", "v72", "v73", "v74", "v75", "v76", "v77", "v78", "v79", "v80", "v81", "v82", "v83", "v84", "v85", "v86", "v87", "v88", "v89", "v90", "v91", "v92", "v93", "v94", "v95", "v96", "v97", "v98", "v99", "v100", "v101", "v102", "v103", "v104", "v105", "v106", "v107", "v108", "v109", "v110", "v111", "v112", "v113", "v114", "v115", "v116", "v117", "v118", "v119", "v120", "v121", "v122", "v123", "v124", "v125", "v126", "v127", "v128", "v129", "v130", "v131", "v132", "v133", "v134", "v135", "v136", "v137", "v138", "v139", "v140", "v141", "v142", "v143", "v144", "v145", "v146", "v147", "v148", "v149", "v150", "v151", "v152", "v153", "v154", "v155", "v156", "v157", "v158", "v159", "v160", "v161", "v162", "v163", "v164", "v165", "v166", "v167", "v168", "v169", "v170", "v171", "v172", "v173", "v174", "v175", "v176", "v177", "v178", "v179", "v180", "v181", "v182", "v183", "v184", "v185", "v186", "v187", "v188", "v189", "v190", "v191", "v192", "v193", "v194", "v195", "v196", "v197", "v198", "v199", "v200", "v201", "v202", "v203", "v204", "v205", "v206", "v207", "v208", "v209", "v210", "v211", "v212", "v213", "v214", "v215", "v216", "v217", "v218", "v219", "v220", "v221", "v222", "v223", "v224", "v225", "v226", "v227", "v228", "v229", "v230", "v231", "v232", "v233", "v234", "v235", "v236", "v237", "v238", "v239", "v240", "v241", "v242", "v243", "v244", "v245", "v246", "v247", "v248", "v249", "v250", "v251", "v252", "v253", "v254", "v255", "a0", "a1", "a2", "a3", "a4", "a5", "a6", "a7", "a8", "a9", "a10", "a11", "a12", "a13", "a14", "a15", "a16", "a17", "a18", "a19", "a20", "a21", "a22", "a23", "a24", "a25", "a26", "a27", "a28", "a29", "a30", "a31", "a32", "a33", "a34", "a35", "a36", "a37", "a38", "a39", "a40", "a41", "a42", "a43", "a44", "a45", "a46", "a47", "a48", "a49", "a50", "a51", "a52", "a53", "a54", "a55", "a56", "a57", "a58", "a59", "a60", "a61", "a62", "a63", "a64", "a65", "a66", "a67", "a68", "a69", "a70", "a71", "a72", "a73", "a74", "a75", "a76", "a77", "a78", "a79", "a80", "a81", "a82", "a83", "a84", "a85", "a86", "a87", "a88", "a89", "a90", "a91", "a92", "a93", "a94", "a95", "a96", "a97", "a98", "a99", "a100", "a101", "a102", "a103", "a104", "a105", "a106", "a107", "a108", "a109", "a110", "a111", "a112", "a113", "a114", "a115", "a116", "a117", "a118", "a119", "a120", "a121", "a122", "a123", "a124", "a125", "a126", "a127", "a128", "a129", "a130", "a131", "a132", "a133", "a134", "a135", "a136", "a137", "a138", "a139", "a140", "a141", "a142", "a143", "a144", "a145", "a146", "a147", "a148", "a149", "a150", "a151", "a152", "a153", "a154", "a155", "a156", "a157", "a158", "a159", "a160", "a161", "a162", "a163", "a164", "a165", "a166", "a167", "a168", "a169", "a170", "a171", "a172", "a173", "a174", "a175", "a176", "a177", "a178", "a179", "a180", "a181", "a182", "a183", "a184", "a185", "a186", "a187", "a188", "a189", "a190", "a191", "a192", "a193", "a194", "a195", "a196", "a197", "a198", "a199", "a200", "a201", "a202", "a203", "a204", "a205", "a206", "a207", "a208", "a209", "a210", "a211", "a212", "a213", "a214", "a215", "a216", "a217", "a218", "a219", "a220", "a221", "a222", "a223", "a224", "a225", "a226", "a227", "a228", "a229", "a230", "a231", "a232", "a233", "a234", "a235", "a236", "a237", "a238", "a239", "a240", "a241", "a242", "a243", "a244", "a245", "a246", "a247", "a248", "a249", "a250", "a251", "a252", "a253", "a254", "a255", "s4", "s5", "s6", "s7", "s8", "s9", "s10", "s11", "s12", "s13", "s14", "s15", "s16", "s17", "s18", "s19", "s20", "s21", "s22", "s23", "s24", "s25", "s26", "s27", "s28", "s29", "s30", "s31", "s34", "s35", "s36", "s37", "s38", "s39", "s40", "s41", "s42", "s43", "s44", "s45", "s46", "s47", "s48", "s49", "s50", "s51", "s52", "s53", "s54", "s55", "s56", "s57", "s58", "s59", "s60", "s61", "s62", "s63", "s64", "s65", "s66", "s67", "s68", "s69", "s70", "s71", "s72", "s73", "s74", "s75", "s76", "s77", "s78", "s79", "vcc", "scc", "memory"

__global__ __launch_bounds__(256, 1) void mha_fused(const char* x, const char* kc, const char* vc, const char* wq, const char* wo, char* out, char* ws, char* ws2) {
  asm volatile(
"s_load_dwordx16 s[4:19], %0, 0x0\n"
"v_readfirstlane_b32 s64, v0\n"
"s_lshl_b32 s22, %1, 2\n"
"s_and_b32 s22, s22, 12\n"
"s_bfe_u32 s69, %1, 0x20003\n"
"s_or_b32 s22, s22, s69\n"
"s_lshl_b32 s79, %1, 1\n"
"s_and_b32 s79, s79, 8\n"
"s_lshr_b32 s69, %1, 5\n"
"s_add_u32 s79, s79, s69\n"
"s_lshr_b32 s64, s64, 6\n"
"s_mul_i32 s65, s64, 12288\n"
"s_add_u32 s65, s65, 32768\n"
"s_lshl_b32 s66, s64, 12\n"
"s_add_u32 s66, s66, 81920\n"
"s_lshl_b32 s67, s64, 10\n"
"s_add_u32 s67, s67, 131072\n"
"s_lshl_b32 s68, s64, 11\n"
"s_lshr_b32 s35, %1, 1\n"
"s_and_b32 s35, s35, 2\n"
"s_add_u32 s35, s35, %1\n"
"s_and_b32 s35, s35, 3\n"
"s_lshl_b32 s34, s35, 21\n"
"s_lshl_b32 s35, s35, 9\n"
"v_and_b32 v1, 63, v0\n"
"v_lshrrev_b32 v40, 3, v1\n"
"v_and_b32 v41, 7, v1\n"
"v_lshrrev_b32 v42, 1, v40\n"
"v_xor_b32 v42, v42, v41\n"
"v_lshlrev_b32 v42, 4, v42\n"
"v_lshlrev_b32 v43, 11, v40\n"
"v_add_u32 v42, v42, v43\n"
"s_lshl_b32 s69, s64, 16\n"
"v_add_u32 v2, s69, v42\n"
"v_xor_b32 v43, 64, v2\n"
"v_add_u32 v3, 15360, v43\n"
"v_add_u32 v4, 30720, v2\n"
"v_add_u32 v5, 46080, v43\n"
"s_and_b32 s69, s64, 1\n"
"s_lshl_b32 s70, s69, 6\n"
"v_xor_b32 v43, s70, v42\n"
"s_lshl_b32 s69, s69, 14\n"
"s_lshr_b32 s70, s64, 1\n"
"s_lshl_b32 s70, s70, 7\n"
"s_add_u32 s69, s69, s70\n"
"s_add_u32 s69, s69, 262144\n"
"v_add_u32 v6, s69, v43\n"
"v_lshrrev_b32 v42, 1, v40\n"
"v_lshlrev_b32 v42, 1, v42\n"
"v_xor_b32 v42, v42, v41\n"
"v_lshlrev_b32 v42, 4, v42\n"
"v_lshrrev_b32 v43, 2, v40\n"
"v_lshlrev_b32 v43, 16, v43\n"
"v_add_u32 v42, v42, v43\n"
"v_and_b32 v43, 3, v40\n"
"v_lshlrev_b32 v43, 13, v43\n"
"v_add_u32 v42, v42, v43\n"
"s_lshl_b32 s69, s64, 7\n"
"v_add_u32 v7, s69, v42\n"
"v_add_u32 v8, 130048, v7\n"
"v_add_u32 v9, 30720, v7\n"
"v_add_u32 v10, 160768, v7\n"
"s_mul_i32 s69, s22, 294912\n"
"s_lshl_b32 s71, s79, 9\n"
"s_waitcnt lgkmcnt(0)\n"
"s_add_u32 s24, s4, s69\n"
"s_addc_u32 s25, s5, 0\n"
"s_and_b32 s25, s25, 0xffff\n"
"s_mov_b32 s26, 294912\n"
"s_mov_b32 s27, 0x20000\n"
"s_add_u32 m0, s66, 0\n"
"s_add_u32 s70, s35, 0\n"
"s_and_b32 s70, s70, 0x7ff\n"
"buffer_load_dwordx4 v2, s[24:27], s70 offen lds\n"
"buffer_load_dwordx4 v3, s[24:27], s70 offen offset:1024 lds\n"
"buffer_load_dwordx4 v4, s[24:27], s70 offen offset:2048 lds\n"
"buffer_load_dwordx4 v5, s[24:27], s70 offen offset:3072 lds\n"
"s_add_u32 m0, s67, 0\n"
"s_add_u32 s70, s35, 0\n"
"s_and_b32 s70, s70, 0x7ff\n"
"buffer_load_dwordx4 v6, s[24:27], s70 offen lds\n"
"s_add_u32 s28, s10, s71\n"
"s_addc_u32 s29, s11, 0\n"
"s_and_b32 s29, s29, 0xffff\n"
"s_mov_b32 s30, 8380928\n"
"s_mov_b32 s31, 0x20000\n"
"s_add_u32 m0, s65, 0\n"
"s_add_u32 s69, s34, 0\n"
"s_and_b32 s69, s69, 0x7fffff\n"
"buffer_load_dwordx4 v7, s[28:31], s69 offen lds\n"
"buffer_load_dwordx4 v8, s[28:31], s69 offen offset:1024 lds\n"
"buffer_load_dwordx4 v9, s[28:31], s69 offen offset:2048 lds\n"
"buffer_load_dwordx4 v10, s[28:31], s69 offen offset:3072 lds\n"
"s_lshl_b32 s69, s22, 4\n"
"s_add_u32 s69, s69, s79\n"
"s_lshl_b32 s69, s69, 18\n"
"s_add_u32 s36, s6, s69\n"
"s_addc_u32 s37, s7, 0\n"
"s_and_b32 s37, s37, 0xffff\n"
"s_mov_b32 s38, 262144\n"
"s_mov_b32 s39, 0x20000\n"
"s_add_u32 s40, s8, s69\n"
"s_addc_u32 s41, s9, 0\n"
"s_and_b32 s41, s41, 0xffff\n"
"s_mov_b32 s42, 262144\n"
"s_mov_b32 s43, 0x20000\n"
"s_mul_i32 s69, s22, 1769472\n"
"s_add_u32 s44, s16, s69\n"
"s_addc_u32 s45, s17, 0\n"
"s_and_b32 s45, s45, 0xffff\n"
"s_mov_b32 s46, 1769472\n"
"s_mov_b32 s47, 0x20000\n"
"s_add_u32 s48, s12, 0\n"
"s_addc_u32 s49, s13, 0\n"
"s_and_b32 s49, s49, 0xffff\n"
"s_mov_b32 s50, 8388608\n"
"s_mov_b32 s51, 0x20000\n"
"s_mul_i32 s69, s22, 294912\n"
"s_lshl_b32 s70, s79, 7\n"
"s_add_u32 s69, s69, s70\n"
"s_add_u32 s52, s14, s69\n"
"s_addc_u32 s53, s15, 0\n"
"s_and_b32 s53, s53, 0xffff\n"
"s_mov_b32 s54, 292992\n"
"s_mov_b32 s55, 0x20000\n"
"s_lshl_b32 s69, s22, 12\n"
"s_add_u32 s69, s69, 31457280\n"
"s_add_u32 s56, s16, s69\n"
"s_addc_u32 s57, s17, 0\n"
"s_and_b32 s57, s57, 0xffff\n"
"s_mov_b32 s58, 4096\n"
"s_mov_b32 s59, 0x20000\n"
"L_cw0_%=:\n"
"s_getpc_b64 s[60:61]\n"
"s_and_b32 s61, s61, 0xffff\n"
"s_mov_b32 s62, L_end_%=-L_cw0_%=\n"
"s_mov_b32 s63, 0x20000\n"
"s_lshr_b32 s69, %1, 3\n"
"s_and_b32 s69, s69, 7\n"
"s_lshl_b32 s69, s69, 2\n"
"s_add_u32 s69, s69, s64\n"
"s_lshl_b32 s69, s69, 11\n"
"v_lshrrev_b32 v40, 2, v1\n"
"v_lshlrev_b32 v40, 7, v40\n"
"v_add_u32 v45, s69, v40\n"
"s_mov_b32 s78, L_att_%=-L_cw0_%=\n"
"s_lshr_b32 s76, %1, 3\n"
"s_add_u32 s76, s76, %1\n"
"s_and_b32 s76, s76, 15\n"
"s_lshl_b32 s76, s76, 13\n"
"s_lshr_b32 s23, %1, 4\n"
"s_and_b32 s23, s23, 0\n"
"s_lshl_b32 s23, s23, 13\n"
"s_add_u32 s23, s23, 4096\n"
"s_lshr_b32 s70, s64, 1\n"
"s_mul_i32 s70, s70, s23\n"
"s_add_u32 s77, s76, s70\n"
"s_and_b32 s70, s68, 2048\n"
"s_add_u32 s77, s77, s70\n"
"buffer_load_dword v44, v45, s[60:63], 0 offen\n"
"s_lshr_b32 s69, %1, 3\n"
"s_lshl_b32 s69, s69, 2\n"
"s_add_u32 s69, s69, s64\n"
"s_and_b32 s70, %1, 3\n"
"s_lshl_b32 s69, s69, 2\n"
"s_add_u32 s69, s69, s70\n"
"s_and_b32 s70, s79, 8\n"
"s_lshl_b32 s70, s70, 6\n"
"s_add_u32 s69, s69, s70\n"
"s_lshl_b32 s69, s69, 13\n"
"v_lshrrev_b32 v41, 0, v1\n"
"v_lshlrev_b32 v41, 7, v41\n"
"v_add_u32 v216, s69, v41\n"
"s_and_b32 s69, s79, 7\n"
"s_lshl_b32 s69, s69, 2\n"
"s_add_u32 s69, s69, s64\n"
"s_mul_i32 s69, s69, 18\n"
"v_min_u32 v41, 17, v1\n"
"v_add_u32 v41, s69, v41\n"
"v_lshrrev_b32 v42, 2, v41\n"
"v_lshlrev_b32 v42, 11, v42\n"
"v_and_b32 v41, 3, v41\n"
"v_lshlrev_b32 v41, 7, v41\n"
"v_add_u32 v217, v41, v42\n"
"v_lshrrev_b32 v43, 2, v1\n"
"v_lshrrev_b32 v41, 1, v43\n"
"v_and_b32 v41, 3, v41\n"
"v_lshlrev_b32 v41, 5, v41\n"
"v_lshlrev_b32 v43, 7, v43\n"
"v_and_b32 v42, 3, v1\n"
"v_lshlrev_b32 v42, 3, v42\n"
"v_add_u32 v43, v43, v42\n"
"v_add_u32 v43, s65, v43\n"
"v_xor_b32 v42, 0, v41\n"
"v_add_u32 v11, v43, v42\n"
"v_xor_b32 v42, 32, v41\n"
"v_add_u32 v12, v43, v42\n"
"v_xor_b32 v42, 64, v41\n"
"v_add_u32 v13, v43, v42\n"
"v_xor_b32 v42, 96, v41\n"
"v_add_u32 v14, v43, v42\n"
"s_mov_b32 s69, 131072\n"
"s_mov_b32 s74, 0x4000\n"
"s_mov_b32 s75, 0x4000\n"
"v_lshrrev_b32 v40, 2, v1\n"
"v_lshrrev_b32 v41, 0, v40\n"
"v_and_b32 v41, 7, v41\n"
"v_lshlrev_b32 v41, 5, v41\n"
"v_lshlrev_b32 v40, 8, v40\n"
"v_and_b32 v42, 3, v1\n"
"v_lshlrev_b32 v42, 3, v42\n"
"v_add_u32 v40, v40, v42\n"
"v_add_u32 v40, s69, v40\n"
"v_xor_b32 v42, 0, v41\n"
"v_add_u32 v29, v40, v42\n"
"v_xor_b32 v42, 32, v41\n"
"v_add_u32 v30, v40, v42\n"
"v_xor_b32 v42, 64, v41\n"
"v_add_u32 v31, v40, v42\n"
"v_xor_b32 v42, 96, v41\n"
"v_add_u32 v32, v40, v42\n"
"v_xor_b32 v42, 128, v41\n"
"v_add_u32 v33, v40, v42\n"
"v_xor_b32 v42, 160, v41\n"
"v_add_u32 v34, v40, v42\n"
"v_xor_b32 v42, 192, v41\n"
"v_add_u32 v35, v40, v42\n"
"v_xor_b32 v42, 224, v41\n"
"v_add_u32 v36, v40, v42\n"
"v_and_b32 v40, 15, v1\n"
"v_lshrrev_b32 v41, 4, v1\n"
"v_lshrrev_b32 v42, 1, v40\n"
"v_xor_b32 v42, v42, v41\n"
"v_lshlrev_b32 v42, 4, v42\n"
"v_lshlrev_b32 v43, 7, v40\n"
"v_add_u32 v42, v42, v43\n"
"v_add_u32 v15, 81920, v42\n"
"v_xor_b32 v16, 64, v15\n"
"v_xor_b32 v42, v40, v41\n"
"v_lshlrev_b32 v42, 4, v42\n"
"v_lshlrev_b32 v43, 8, v40\n"
"v_add_u32 v25, v42, v43\n"
"v_xor_b32 v26, 64, v25\n"
"v_xor_b32 v27, 128, v25\n"
"v_xor_b32 v28, 192, v25\n"
"v_lshlrev_b32 v43, 8, v41\n"
"v_add_u32 v17, v42, v43\n"
"v_xor_b32 v18, 64, v17\n"
"v_xor_b32 v19, 128, v17\n"
"v_xor_b32 v20, 192, v17\n"
"s_and_b32 s69, s64, 1\n"
"s_lshl_b32 s69, s69, 7\n"
"v_xor_b32 v21, s69, v17\n"
"v_xor_b32 v22, s69, v18\n"
"v_lshlrev_b32 v42, 1, v41\n"
"v_xor_b32 v42, v42, v40\n"
"v_lshlrev_b32 v42, 4, v42\n"
"v_add_u32 v23, v42, v43\n"
"v_xor_b32 v24, 128, v23\n"
"v_lshlrev_b32 v40, 4, v40\n"
"v_and_b32 v42, 1, v41\n"
"v_lshrrev_b32 v41, 1, v41\n"
"v_lshlrev_b32 v43, 9, v42\n"
"v_add_u32 v37, v40, v43\n"
"v_lshlrev_b32 v43, 8, v41\n"
"v_add_u32 v37, v37, v43\n"
"s_lshr_b32 s69, s64, 1\n"
"s_mul_i32 s69, s69, 36864\n"
"s_and_b32 s70, s64, 1\n"
"s_lshl_b32 s70, s70, 11\n"
"s_add_u32 s69, s69, s70\n"
"s_add_u32 s69, s69, 65536\n"
"v_add_u32 v37, s69, v37\n"
"v_lshlrev_b32 v43, 8, v42\n"
"v_add_u32 v38, v40, v43\n"
"v_lshlrev_b32 v43, 3, v41\n"
"v_add_u32 v38, v38, v43\n"
"s_add_u32 s69, s65, 32768\n"
"v_add_u32 v38, s69, v38\n"
"s_mul_i32 s69, s64, 3072\n"
"s_add_u32 s69, s69, 65536\n"
"v_lshlrev_b32 v40, 4, v1\n"
"v_add_u32 v39, s69, v40\n"
"s_add_u32 s69, s68, 98304\n"
"v_add_u32 v196, s69, v40\n"
"v_and_b32 v40, 3, v1\n"
"v_add_u32 v40, -1, v40\n"
"v_ashrrev_i32 v40, 31, v40\n"
"v_and_b32 v192, 0x3f803f80, v40\n"
"v_and_b32 v193, 0x3f803f80, v40\n"
"v_and_b32 v194, 0x3f803f80, v40\n"
"v_and_b32 v195, 0x3f803f80, v40\n"
"s_mov_b32 s72, 0x3e0293ee\n"
"s_mov_b32 s73, 0x3e0293ee\n"
"s_add_u32 m0, s65, 4096\n"
"s_add_u32 s69, s34, 262144\n"
"s_and_b32 s69, s69, 0x7fffff\n"
"buffer_load_dwordx4 v7, s[28:31], s69 offen lds\n"
"buffer_load_dwordx4 v8, s[28:31], s69 offen offset:1024 lds\n"
"buffer_load_dwordx4 v9, s[28:31], s69 offen offset:2048 lds\n"
"buffer_load_dwordx4 v10, s[28:31], s69 offen offset:3072 lds\n"
"s_add_u32 m0, s66, 16384\n"
"s_add_u32 s70, s35, 128\n"
"s_and_b32 s70, s70, 0x7ff\n"
"buffer_load_dwordx4 v2, s[24:27], s70 offen lds\n"
"buffer_load_dwordx4 v3, s[24:27], s70 offen offset:1024 lds\n"
"buffer_load_dwordx4 v4, s[24:27], s70 offen offset:2048 lds\n"
"buffer_load_dwordx4 v5, s[24:27], s70 offen offset:3072 lds\n"
"s_add_u32 m0, s65, 8192\n"
"s_add_u32 s69, s34, 524288\n"
"s_and_b32 s69, s69, 0x7fffff\n"
"buffer_load_dwordx4 v7, s[28:31], s69 offen lds\n"
"buffer_load_dwordx4 v8, s[28:31], s69 offen offset:1024 lds\n"
"buffer_load_dwordx4 v9, s[28:31], s69 offen offset:2048 lds\n"
"buffer_load_dwordx4 v10, s[28:31], s69 offen offset:3072 lds\n"
"s_waitcnt vmcnt(13)\n"
"s_barrier\n"
"ds_read_b64_tr_b16 a[72:73], v11\n"
"ds_read_b64_tr_b16 a[74:75], v11 offset:2048\n"
"ds_read_b64_tr_b16 a[76:77], v12\n"
"ds_read_b64_tr_b16 a[78:79], v12 offset:2048\n"
"ds_read_b64_tr_b16 a[80:81], v13\n"
"ds_read_b64_tr_b16 a[82:83], v13 offset:2048\n"
"ds_read_b64_tr_b16 a[84:85], v14\n"
"ds_read_b64_tr_b16 a[86:87], v14 offset:2048\n"
"ds_read_b128 a[0:3], v15\n"
"ds_read_b128 a[4:7], v15 offset:2048\n"
"ds_read_b128 a[8:11], v15 offset:4096\n"
"ds_read_b128 a[12:15], v15 offset:6144\n"
"ds_read_b128 a[16:19], v15 offset:8192\n"
"ds_read_b128 a[20:23], v15 offset:10240\n"
"ds_read_b128 a[24:27], v15 offset:12288\n"
"ds_read_b128 a[28:31], v15 offset:14336\n"
"ds_read_b128 a[32:35], v15 offset:49152\n"
"s_add_u32 m0, s66, 32768\n"
"s_add_u32 s70, s35, 256\n"
"s_and_b32 s70, s70, 0x7ff\n"
"buffer_load_dwordx4 v2, s[24:27], s70 offen lds\n"
"buffer_load_dwordx4 v3, s[24:27], s70 offen offset:1024 lds\n"
"buffer_load_dwordx4 v4, s[24:27], s70 offen offset:2048 lds\n"
"buffer_load_dwordx4 v5, s[24:27], s70 offen offset:3072 lds\n"
"s_add_u32 m0, s67, 4096\n"
"s_add_u32 s70, s35, 256\n"
"s_and_b32 s70, s70, 0x7ff\n"
"buffer_load_dwordx4 v6, s[24:27], s70 offen lds\n"
"s_waitcnt lgkmcnt(9)\n"
"s_add_u32 m0, s65, 0\n"
"s_add_u32 s69, s34, 786432\n"
"s_and_b32 s69, s69, 0x7fffff\n"
"buffer_load_dwordx4 v7, s[28:31], s69 offen lds\n"
"buffer_load_dwordx4 v8, s[28:31], s69 offen offset:1024 lds\n"
"buffer_load_dwordx4 v9, s[28:31], s69 offen offset:2048 lds\n"
"buffer_load_dwordx4 v10, s[28:31], s69 offen offset:3072 lds\n"
"s_waitcnt lgkmcnt(5)\n"
"v_mfma_f32_16x16x32_bf16 v[48:51], a[72:75], a[0:3], 0\n"
"s_waitcnt vmcnt(17)\n"
"v_mfma_f32_16x16x32_bf16 v[52:55], a[76:79], a[0:3], 0\n"
"ds_read_b64_tr_b16 a[88:89], v11 offset:4096\n"
"v_mfma_f32_16x16x32_bf16 v[56:59], a[80:83], a[0:3], 0\n"
"ds_read_b64_tr_b16 a[90:91], v11 offset:6144\n"
"v_mfma_f32_16x16x32_bf16 v[60:63], a[84:87], a[0:3], 0\n"
"ds_read_b64_tr_b16 a[92:93], v12 offset:4096\n"
"v_mfma_f32_16x16x32_bf16 v[76:79], a[84:87], a[4:7], 0\n"
"ds_read_b64_tr_b16 a[94:95], v12 offset:6144\n"
"v_mfma_f32_16x16x32_bf16 v[72:75], a[80:83], a[4:7], 0\n"
"ds_read_b64_tr_b16 a[96:97], v13 offset:4096\n"
"v_mfma_f32_16x16x32_bf16 v[68:71], a[76:79], a[4:7], 0\n"
"ds_read_b64_tr_b16 a[98:99], v13 offset:6144\n"
"v_mfma_f32_16x16x32_bf16 v[64:67], a[72:75], a[4:7], 0\n"
"ds_read_b64_tr_b16 a[100:101], v14 offset:4096\n"
"v_mfma_f32_16x16x32_bf16 v[80:83], a[72:75], a[8:11], 0\n"
"ds_read_b64_tr_b16 a[102:103], v14 offset:6144\n"
"v_mfma_f32_16x16x32_bf16 v[84:87], a[76:79], a[8:11], 0\n"
"ds_read_b128 a[36:39], v16\n"
"v_mfma_f32_16x16x32_bf16 v[88:91], a[80:83], a[8:11], 0\n"
"ds_read_b128 a[40:43], v16 offset:2048\n"
"v_mfma_f32_16x16x32_bf16 v[92:95], a[84:87], a[8:11], 0\n"
"ds_read_b128 a[44:47], v16 offset:4096\n"
"v_mfma_f32_16x16x32_bf16 v[108:111], a[84:87], a[12:15], 0\n"
"ds_read_b128 a[48:51], v16 offset:6144\n"
"v_mfma_f32_16x16x32_bf16 v[104:107], a[80:83], a[12:15], 0\n"
"ds_read_b128 a[52:55], v16 offset:8192\n"
"v_mfma_f32_16x16x32_bf16 v[100:103], a[76:79], a[12:15], 0\n"
"ds_read_b128 a[56:59], v16 offset:10240\n"
"v_mfma_f32_16x16x32_bf16 v[96:99], a[72:75], a[12:15], 0\n"
"s_waitcnt lgkmcnt(6)\n"
"s_add_u32 m0, s65, 4096\n"
"ds_read_b128 a[60:63], v16 offset:12288\n"
"v_mfma_f32_16x16x32_bf16 v[112:115], a[72:75], a[16:19], 0\n"
"s_add_u32 s69, s34, 1048576\n"
"ds_read_b128 a[64:67], v16 offset:14336\n"
"v_mfma_f32_16x16x32_bf16 v[116:119], a[76:79], a[16:19], 0\n"
"s_and_b32 s69, s69, 0x7fffff\n"
"buffer_load_dwordx4 v7, s[28:31], s69 offen lds\n"
"v_mfma_f32_16x16x32_bf16 v[120:123], a[80:83], a[16:19], 0\n"
"ds_read_b128 a[68:71], v16 offset:49152\n"
"v_mfma_f32_16x16x32_bf16 v[124:127], a[84:87], a[16:19], 0\n"
"v_mfma_f32_16x16x32_bf16 v[140:143], a[84:87], a[20:23], 0\n"
"v_mfma_f32_16x16x32_bf16 v[136:139], a[80:83], a[20:23], 0\n"
"v_mfma_f32_16x16x32_bf16 v[132:135], a[76:79], a[20:23], 0\n"
"buffer_load_dwordx4 v8, s[28:31], s69 offen offset:1024 lds\n"
"v_mfma_f32_16x16x32_bf16 v[128:131], a[72:75], a[20:23], 0\n"
"v_mfma_f32_16x16x32_bf16 v[144:147], a[72:75], a[24:27], 0\n"
"s_waitcnt vmcnt(11)\n"
"ds_read_b64_tr_b16 a[232:233], v11 offset:8192\n"
"v_mfma_f32_16x16x32_bf16 v[148:151], a[76:79], a[24:27], 0\n"
"ds_read_b64_tr_b16 a[234:235], v11 offset:10240\n"
"v_mfma_f32_16x16x32_bf16 v[152:155], a[80:83], a[24:27], 0\n"
"ds_read_b64_tr_b16 a[236:237], v12 offset:8192\n"
"v_mfma_f32_16x16x32_bf16 v[156:159], a[84:87], a[24:27], 0\n"
"buffer_load_dwordx4 v9, s[28:31], s69 offen offset:2048 lds\n"
"v_mfma_f32_16x16x32_bf16 v[172:175], a[84:87], a[28:31], 0\n"
"ds_read_b64_tr_b16 a[238:239], v12 offset:10240\n"
"v_mfma_f32_16x16x32_bf16 v[168:171], a[80:83], a[28:31], 0\n"
"ds_read_b64_tr_b16 a[240:241], v13 offset:8192\n"
"v_mfma_f32_16x16x32_bf16 v[164:167], a[76:79], a[28:31], 0\n"
"ds_read_b64_tr_b16 a[242:243], v13 offset:10240\n"
"v_mfma_f32_16x16x32_bf16 v[160:163], a[72:75], a[28:31], 0\n"
"ds_read_b64_tr_b16 a[244:245], v14 offset:8192\n"
"v_mfma_f32_16x16x32_bf16 v[176:179], a[72:75], a[32:35], 0\n"
"buffer_load_dwordx4 v10, s[28:31], s69 offen offset:3072 lds\n"
"v_mfma_f32_16x16x32_bf16 v[180:183], a[76:79], a[32:35], 0\n"
"ds_read_b64_tr_b16 a[246:247], v14 offset:10240\n"
"v_mfma_f32_16x16x32_bf16 v[184:187], a[80:83], a[32:35], 0\n"
"v_mfma_f32_16x16x32_bf16 v[188:191], a[84:87], a[32:35], 0\n"
"s_waitcnt lgkmcnt(8)\n"
"v_mfma_f32_16x16x32_bf16 v[48:51], a[88:91], a[36:39], v[48:51]\n"
"s_barrier\n"
"v_mfma_f32_16x16x32_bf16 v[52:55], a[92:95], a[36:39], v[52:55]\n"
"s_waitcnt lgkmcnt(0)\n"
"s_add_u32 m0, s65, 8192\n"
"ds_read_b128 a[0:3], v15 offset:16384\n"
"v_mfma_f32_16x16x32_bf16 v[56:59], a[96:99], a[36:39], v[56:59]\n"
"s_add_u32 s69, s34, 1310720\n"
"ds_read_b128 a[4:7], v15 offset:18432\n"
"v_mfma_f32_16x16x32_bf16 v[60:63], a[100:103], a[36:39], v[60:63]\n"
"s_and_b32 s69, s69, 0x7fffff\n"
"buffer_load_dwordx4 v7, s[28:31], s69 offen lds\n"
"v_mfma_f32_16x16x32_bf16 v[76:79], a[100:103], a[40:43], v[76:79]\n"
"ds_read_b128 a[8:11], v15 offset:20480\n"
"v_mfma_f32_16x16x32_bf16 v[72:75], a[96:99], a[40:43], v[72:75]\n"
"ds_read_b128 a[12:15], v15 offset:22528\n"
"v_mfma_f32_16x16x32_bf16 v[68:71], a[92:95], a[40:43], v[68:71]\n"
"ds_read_b128 a[16:19], v15 offset:24576\n"
"v_mfma_f32_16x16x32_bf16 v[64:67], a[88:91], a[40:43], v[64:67]\n"
"ds_read_b128 a[36:39], v16 offset:16384\n"
"v_mfma_f32_16x16x32_bf16 v[80:83], a[88:91], a[44:47], v[80:83]\n"
"buffer_load_dwordx4 v8, s[28:31], s69 offen offset:1024 lds\n"
"v_mfma_f32_16x16x32_bf16 v[84:87], a[92:95], a[44:47], v[84:87]\n"
"ds_read_b128 a[40:43], v16 offset:18432\n"
"v_mfma_f32_16x16x32_bf16 v[88:91], a[96:99], a[44:47], v[88:91]\n"
"v_mfma_f32_16x16x32_bf16 v[92:95], a[100:103], a[44:47], v[92:95]\n"
"v_mfma_f32_16x16x32_bf16 v[108:111], a[100:103], a[48:51], v[108:111]\n"
"ds_read_b128 a[44:47], v16 offset:20480\n"
"v_mfma_f32_16x16x32_bf16 v[104:107], a[96:99], a[48:51], v[104:107]\n"
"buffer_load_dwordx4 v9, s[28:31], s69 offen offset:2048 lds\n"
"v_mfma_f32_16x16x32_bf16 v[100:103], a[92:95], a[48:51], v[100:103]\n"
"v_mfma_f32_16x16x32_bf16 v[96:99], a[88:91], a[48:51], v[96:99]\n"
"v_mfma_f32_16x16x32_bf16 v[112:115], a[88:91], a[52:55], v[112:115]\n"
"ds_read_b128 a[48:51], v16 offset:22528\n"
"v_mfma_f32_16x16x32_bf16 v[116:119], a[92:95], a[52:55], v[116:119]\n"
"v_mfma_f32_16x16x32_bf16 v[120:123], a[96:99], a[52:55], v[120:123]\n"
"buffer_load_dwordx4 v10, s[28:31], s69 offen offset:3072 lds\n"
"v_mfma_f32_16x16x32_bf16 v[124:127], a[100:103], a[52:55], v[124:127]\n"
"s_waitcnt vmcnt(8)\n"
"ds_read_b64_tr_b16 a[248:249], v11\n"
"v_mfma_f32_16x16x32_bf16 v[140:143], a[100:103], a[56:59], v[140:143]\n"
"ds_read_b64_tr_b16 a[250:251], v11 offset:2048\n"
"v_mfma_f32_16x16x32_bf16 v[136:139], a[96:99], a[56:59], v[136:139]\n"
"s_add_u32 m0, s66, 0\n"
"ds_read_b64_tr_b16 a[252:253], v12\n"
"v_mfma_f32_16x16x32_bf16 v[132:135], a[92:95], a[56:59], v[132:135]\n"
"s_add_u32 s70, s35, 384\n"
"ds_read_b64_tr_b16 a[254:255], v12 offset:2048\n"
"v_mfma_f32_16x16x32_bf16 v[128:131], a[88:91], a[56:59], v[128:131]\n"
"s_and_b32 s70, s70, 0x7ff\n"
"buffer_load_dwordx4 v2, s[24:27], s70 offen lds\n"
"v_mfma_f32_16x16x32_bf16 v[144:147], a[88:91], a[60:63], v[144:147]\n"
"s_add_u32 s71, s35, 512\n"
"ds_read_b64_tr_b16 v[204:205], v13\n"
"v_mfma_f32_16x16x32_bf16 v[148:151], a[92:95], a[60:63], v[148:151]\n"
"s_and_b32 s71, s71, 0x7ff\n"
"buffer_load_dword v44, v217, s[24:27], s71 offen\n"
"v_mfma_f32_16x16x32_bf16 v[152:155], a[96:99], a[60:63], v[152:155]\n"
"ds_read_b64_tr_b16 v[206:207], v13 offset:2048\n"
"v_mfma_f32_16x16x32_bf16 v[156:159], a[100:103], a[60:63], v[156:159]\n"
"ds_read_b64_tr_b16 v[208:209], v14\n"
"v_mfma_f32_16x16x32_bf16 v[172:175], a[100:103], a[64:67], v[172:175]\n"
"buffer_load_dwordx4 v3, s[24:27], s70 offen offset:1024 lds\n"
"v_mfma_f32_16x16x32_bf16 v[168:171], a[96:99], a[64:67], v[168:171]\n"
"ds_read_b64_tr_b16 v[210:211], v14 offset:2048\n"
"v_mfma_f32_16x16x32_bf16 v[164:167], a[92:95], a[64:67], v[164:167]\n"
"v_mfma_f32_16x16x32_bf16 v[160:163], a[88:91], a[64:67], v[160:163]\n"
"v_mfma_f32_16x16x32_bf16 v[176:179], a[88:91], a[68:71], v[176:179]\n"
"v_mfma_f32_16x16x32_bf16 v[180:183], a[92:95], a[68:71], v[180:183]\n"
"buffer_load_dwordx4 v4, s[24:27], s70 offen offset:2048 lds\n"
"v_mfma_f32_16x16x32_bf16 v[184:187], a[96:99], a[68:71], v[184:187]\n"
"v_mfma_f32_16x16x32_bf16 v[188:191], a[100:103], a[68:71], v[188:191]\n"
"s_waitcnt lgkmcnt(4)\n"
"v_mfma_f32_16x16x32_bf16 v[48:51], a[232:235], a[0:3], v[48:51]\n"
"v_mfma_f32_16x16x32_bf16 v[48:51], a[248:251], a[36:39], v[48:51]\n"
"s_waitcnt lgkmcnt(0)\n"
"s_add_u32 m0, s65, 0\n"
"ds_read_b128 a[52:55], v16 offset:24576\n"
"v_mfma_f32_16x16x32_bf16 v[52:55], a[252:255], a[36:39], v[52:55]\n"
"s_add_u32 s69, s34, 1572864\n"
"ds_read_b128 a[20:23], v15 offset:26624\n"
"v_mfma_f32_16x16x32_bf16 v[52:55], a[236:239], a[0:3], v[52:55]\n"
"s_and_b32 s69, s69, 0x7fffff\n"
"buffer_load_dwordx4 v7, s[28:31], s69 offen lds\n"
"s_nop 6\n"
"v_mfma_f32_16x16x32_bf16 v[56:59], a[240:243], a[0:3], v[56:59]\n"
"ds_read_b128 a[56:59], v16 offset:26624\n"
"v_mfma_f32_16x16x32_bf16 v[56:59], v[204:207], a[36:39], v[56:59]\n"
"ds_read_b128 a[24:27], v15 offset:28672\n"
"v_mfma_f32_16x16x32_bf16 v[60:63], v[208:211], a[36:39], v[60:63]\n"
"ds_read_b128 a[60:63], v16 offset:28672\n"
"v_mfma_f32_16x16x32_bf16 v[60:63], a[244:247], a[0:3], v[60:63]\n"
"ds_read_b128 a[28:31], v15 offset:30720\n"
"v_mfma_f32_16x16x32_bf16 v[76:79], a[244:247], a[4:7], v[76:79]\n"
"buffer_load_dwordx4 v8, s[28:31], s69 offen offset:1024 lds\n"
"v_mfma_f32_16x16x32_bf16 v[76:79], v[208:211], a[40:43], v[76:79]\n"
"ds_read_b128 a[64:67], v16 offset:30720\n"
"s_nop 6\n"
"v_mfma_f32_16x16x32_bf16 v[72:75], v[204:207], a[40:43], v[72:75]\n"
"ds_read_b128 a[32:35], v15 offset:51200\n"
"v_mfma_f32_16x16x32_bf16 v[72:75], a[240:243], a[4:7], v[72:75]\n"
"ds_read_b128 a[68:71], v16 offset:51200\n"
"v_mfma_f32_16x16x32_bf16 v[68:71], a[236:239], a[4:7], v[68:71]\n"
"v_mfma_f32_16x16x32_bf16 v[68:71], a[252:255], a[40:43], v[68:71]\n"
"buffer_load_dwordx4 v9, s[28:31], s69 offen offset:2048 lds\n"
"v_mfma_f32_16x16x32_bf16 v[64:67], a[248:251], a[40:43], v[64:67]\n"
"v_mfma_f32_16x16x32_bf16 v[64:67], a[232:235], a[4:7], v[64:67]\n"
"s_nop 6\n"
"v_mfma_f32_16x16x32_bf16 v[80:83], a[232:235], a[8:11], v[80:83]\n"
"v_mfma_f32_16x16x32_bf16 v[80:83], a[248:251], a[44:47], v[80:83]\n"
"v_mfma_f32_16x16x32_bf16 v[84:87], a[252:255], a[44:47], v[84:87]\n"
"buffer_load_dwordx4 v10, s[28:31], s69 offen offset:3072 lds\n"
"v_mfma_f32_16x16x32_bf16 v[84:87], a[236:239], a[8:11], v[84:87]\n"
"s_waitcnt vmcnt(12)\n"
"ds_read_b64_tr_b16 a[72:73], v11 offset:4096\n"
"v_mfma_f32_16x16x32_bf16 v[88:91], a[240:243], a[8:11], v[88:91]\n"
"ds_read_b64_tr_b16 a[74:75], v11 offset:6144\n"
"v_mfma_f32_16x16x32_bf16 v[88:91], v[204:207], a[44:47], v[88:91]\n"
"s_add_u32 m0, s66, 0\n"
"ds_read_b64_tr_b16 a[76:77], v12 offset:4096\n"
"s_nop 6\n"
"v_mfma_f32_16x16x32_bf16 v[92:95], v[208:211], a[44:47], v[92:95]\n"
"s_add_u32 s70, s35, 384\n"
"ds_read_b64_tr_b16 a[78:79], v12 offset:6144\n"
"v_mfma_f32_16x16x32_bf16 v[92:95], a[244:247], a[8:11], v[92:95]\n"
"s_and_b32 s70, s70, 0x7ff\n"
"buffer_load_dwordx4 v5, s[24:27], s70 offen offset:3072 lds\n"
"v_mfma_f32_16x16x32_bf16 v[108:111], a[244:247], a[12:15], v[108:111]\n"
"ds_read_b64_tr_b16 a[80:81], v13 offset:4096\n"
"v_mfma_f32_16x16x32_bf16 v[108:111], v[208:211], a[48:51], v[108:111]\n"
"buffer_load_dwordx2 v[46:47], off, s[56:59], 0 sc1\n"
"v_mfma_f32_16x16x32_bf16 v[104:107], v[204:207], a[48:51], v[104:107]\n"
"ds_read_b64_tr_b16 a[82:83], v13 offset:6144\n"
"v_mfma_f32_16x16x32_bf16 v[104:107], a[240:243], a[12:15], v[104:107]\n"
"ds_read_b64_tr_b16 a[84:85], v14 offset:4096\n"
"s_nop 6\n"
"v_mfma_f32_16x16x32_bf16 v[100:103], a[236:239], a[12:15], v[100:103]\n"
"ds_read_b64_tr_b16 a[86:87], v14 offset:6144\n"
"v_mfma_f32_16x16x32_bf16 v[100:103], a[252:255], a[48:51], v[100:103]\n"
"v_mfma_f32_16x16x32_bf16 v[96:99], a[248:251], a[48:51], v[96:99]\n"
"v_mfma_f32_16x16x32_bf16 v[96:99], a[232:235], a[12:15], v[96:99]\n"
"v_mfma_f32_16x16x32_bf16 v[112:115], a[232:235], a[16:19], v[112:115]\n"
"s_waitcnt lgkmcnt(6)\n"
"v_mfma_f32_16x16x32_bf16 v[112:115], a[248:251], a[52:55], v[112:115]\n"
"s_nop 6\n"
"v_mfma_f32_16x16x32_bf16 v[116:119], a[252:255], a[52:55], v[116:119]\n"
"v_mfma_f32_16x16x32_bf16 v[116:119], a[236:239], a[16:19], v[116:119]\n"
"v_mfma_f32_16x16x32_bf16 v[120:123], a[240:243], a[16:19], v[120:123]\n"
"s_barrier\n"
"v_mfma_f32_16x16x32_bf16 v[120:123], v[204:207], a[52:55], v[120:123]\n"
"s_waitcnt lgkmcnt(0)\n"
"s_add_u32 m0, s65, 4096\n"
"ds_read_b128 a[0:3], v15 offset:32768\n"
"v_mfma_f32_16x16x32_bf16 v[124:127], v[208:211], a[52:55], v[124:127]\n"
"s_add_u32 s69, s34, 1835008\n"
"ds_read_b128 a[36:39], v16 offset:32768\n"
"v_mfma_f32_16x16x32_bf16 v[124:127], a[244:247], a[16:19], v[124:127]\n"
"s_and_b32 s69, s69, 0x7fffff\n"
"buffer_load_dwordx4 v7, s[28:31], s69 offen lds\n"
"s_nop 6\n"
"v_mfma_f32_16x16x32_bf16 v[140:143], a[244:247], a[20:23], v[140:143]\n"
"ds_read_b128 a[4:7], v15 offset:34816\n"
"v_mfma_f32_16x16x32_bf16 v[140:143], v[208:211], a[56:59], v[140:143]\n"
"ds_read_b128 a[40:43], v16 offset:34816\n"
"v_mfma_f32_16x16x32_bf16 v[136:139], v[204:207], a[56:59], v[136:139]\n"
"ds_read_b128 a[8:11], v15 offset:36864\n"
"v_mfma_f32_16x16x32_bf16 v[136:139], a[240:243], a[20:23], v[136:139]\n"
"ds_read_b128 a[44:47], v16 offset:36864\n"
"v_mfma_f32_16x16x32_bf16 v[132:135], a[236:239], a[20:23], v[132:135]\n"
"buffer_load_dwordx4 v8, s[28:31], s69 offen offset:1024 lds\n"
"v_mfma_f32_16x16x32_bf16 v[132:135], a[252:255], a[56:59], v[132:135]\n"
"ds_read_b128 a[12:15], v15 offset:38912\n"
"s_nop 6\n"
"v_mfma_f32_16x16x32_bf16 v[128:131], a[248:251], a[56:59], v[128:131]\n"
"ds_read_b128 a[48:51], v16 offset:38912\n"
"v_mfma_f32_16x16x32_bf16 v[128:131], a[232:235], a[20:23], v[128:131]\n"
"ds_read_b128 a[16:19], v15 offset:40960\n"
"v_mfma_f32_16x16x32_bf16 v[144:147], a[232:235], a[24:27], v[144:147]\n"
"v_mfma_f32_16x16x32_bf16 v[144:147], a[248:251], a[60:63], v[144:147]\n"
"buffer_load_dwordx4 v9, s[28:31], s69 offen offset:2048 lds\n"
"v_mfma_f32_16x16x32_bf16 v[148:151], a[252:255], a[60:63], v[148:151]\n"
"v_mfma_f32_16x16x32_bf16 v[148:151], a[236:239], a[24:27], v[148:151]\n"
"s_nop 6\n"
"v_mfma_f32_16x16x32_bf16 v[152:155], a[240:243], a[24:27], v[152:155]\n"
"v_mfma_f32_16x16x32_bf16 v[152:155], v[204:207], a[60:63], v[152:155]\n"
"v_mfma_f32_16x16x32_bf16 v[156:159], v[208:211], a[60:63], v[156:159]\n"
"buffer_load_dwordx4 v10, s[28:31], s69 offen offset:3072 lds\n"
"v_mfma_f32_16x16x32_bf16 v[156:159], a[244:247], a[24:27], v[156:159]\n"
"s_waitcnt vmcnt(14)\n"
"ds_read_b64_tr_b16 a[88:89], v11 offset:8192\n"
"v_mfma_f32_16x16x32_bf16 v[172:175], a[244:247], a[28:31], v[172:175]\n"
"ds_read_b64_tr_b16 a[90:91], v11 offset:10240\n"
"v_mfma_f32_16x16x32_bf16 v[172:175], v[208:211], a[64:67], v[172:175]\n"
"s_add_u32 m0, s66, 16384\n"
"ds_read_b64_tr_b16 a[92:93], v12 offset:8192\n"
"s_nop 6\n"
"v_mfma_f32_16x16x32_bf16 v[168:171], v[204:207], a[64:67], v[168:171]\n"
"s_add_u32 s70, s35, 512\n"
"ds_read_b64_tr_b16 a[94:95], v12 offset:10240\n"
"v_mfma_f32_16x16x32_bf16 v[168:171], a[240:243], a[28:31], v[168:171]\n"
"s_and_b32 s70, s70, 0x7ff\n"
"buffer_load_dwordx4 v2, s[24:27], s70 offen lds\n"
"v_mfma_f32_16x16x32_bf16 v[164:167], a[236:239], a[28:31], v[164:167]\n"
"ds_read_b64_tr_b16 a[96:97], v13 offset:8192\n"
"v_mfma_f32_16x16x32_bf16 v[164:167], a[252:255], a[64:67], v[164:167]\n"
"ds_read_b64_tr_b16 a[98:99], v13 offset:10240\n"
"v_mfma_f32_16x16x32_bf16 v[160:163], a[248:251], a[64:67], v[160:163]\n"
"ds_read_b64_tr_b16 a[100:101], v14 offset:8192\n"
"v_mfma_f32_16x16x32_bf16 v[160:163], a[232:235], a[28:31], v[160:163]\n"
"ds_read_b64_tr_b16 a[102:103], v14 offset:10240\n"
"s_nop 6\n"
"v_mfma_f32_16x16x32_bf16 v[176:179], a[232:235], a[32:35], v[176:179]\n"
"buffer_load_dwordx4 v3, s[24:27], s70 offen offset:1024 lds\n"
"v_mfma_f32_16x16x32_bf16 v[176:179], a[248:251], a[68:71], v[176:179]\n"
"v_mfma_f32_16x16x32_bf16 v[180:183], a[252:255], a[68:71], v[180:183]\n"
"v_mfma_f32_16x16x32_bf16 v[180:183], a[236:239], a[32:35], v[180:183]\n"
"v_mfma_f32_16x16x32_bf16 v[184:187], a[240:243], a[32:35], v[184:187]\n"
"v_mfma_f32_16x16x32_bf16 v[184:187], v[204:207], a[68:71], v[184:187]\n"
"buffer_load_dwordx4 v4, s[24:27], s70 offen offset:2048 lds\n"
"s_nop 6\n"
"v_mfma_f32_16x16x32_bf16 v[188:191], v[208:211], a[68:71], v[188:191]\n"
"v_mfma_f32_16x16x32_bf16 v[188:191], a[244:247], a[32:35], v[188:191]\n"
"s_waitcnt lgkmcnt(5)\n"
"v_mfma_f32_16x16x32_bf16 v[48:51], a[72:75], a[0:3], v[48:51]\n"
"v_mfma_f32_16x16x32_bf16 v[48:51], a[88:91], a[36:39], v[48:51]\n"
"s_waitcnt lgkmcnt(0)\n"
"s_add_u32 m0, s65, 8192\n"
"ds_read_b128 a[52:55], v16 offset:40960\n"
"v_mfma_f32_16x16x32_bf16 v[52:55], a[92:95], a[36:39], v[52:55]\n"
"s_add_u32 s69, s34, 2097152\n"
"ds_read_b128 a[20:23], v15 offset:43008\n"
"v_mfma_f32_16x16x32_bf16 v[52:55], a[76:79], a[0:3], v[52:55]\n"
"s_and_b32 s69, s69, 0x7fffff\n"
"buffer_load_dwordx4 v7, s[28:31], s69 offen lds\n"
"s_nop 6\n"
"v_mfma_f32_16x16x32_bf16 v[56:59], a[80:83], a[0:3], v[56:59]\n"
"ds_read_b128 a[56:59], v16 offset:43008\n"
"v_mfma_f32_16x16x32_bf16 v[56:59], a[96:99], a[36:39], v[56:59]\n"
"ds_read_b128 a[24:27], v15 offset:45056\n"
"v_mfma_f32_16x16x32_bf16 v[60:63], a[100:103], a[36:39], v[60:63]\n"
"ds_read_b128 a[60:63], v16 offset:45056\n"
"v_mfma_f32_16x16x32_bf16 v[60:63], a[84:87], a[0:3], v[60:63]\n"
"ds_read_b128 a[28:31], v15 offset:47104\n"
"v_mfma_f32_16x16x32_bf16 v[76:79], a[84:87], a[4:7], v[76:79]\n"
"buffer_load_dwordx4 v8, s[28:31], s69 offen offset:1024 lds\n"
"v_mfma_f32_16x16x32_bf16 v[76:79], a[100:103], a[40:43], v[76:79]\n"
"ds_read_b128 a[64:67], v16 offset:47104\n"
"s_nop 6\n"
"v_mfma_f32_16x16x32_bf16 v[72:75], a[96:99], a[40:43], v[72:75]\n"
"ds_read_b128 a[32:35], v15 offset:53248\n"
"v_mfma_f32_16x16x32_bf16 v[72:75], a[80:83], a[4:7], v[72:75]\n"
"ds_read_b128 a[68:71], v16 offset:53248\n"
"v_mfma_f32_16x16x32_bf16 v[68:71], a[76:79], a[4:7], v[68:71]\n"
"v_mfma_f32_16x16x32_bf16 v[68:71], a[92:95], a[40:43], v[68:71]\n"
"buffer_load_dwordx4 v9, s[28:31], s69 offen offset:2048 lds\n"
"v_mfma_f32_16x16x32_bf16 v[64:67], a[88:91], a[40:43], v[64:67]\n"
"v_mfma_f32_16x16x32_bf16 v[64:67], a[72:75], a[4:7], v[64:67]\n"
"s_nop 6\n"
"v_mfma_f32_16x16x32_bf16 v[80:83], a[72:75], a[8:11], v[80:83]\n"
"v_mfma_f32_16x16x32_bf16 v[80:83], a[88:91], a[44:47], v[80:83]\n"
"v_mfma_f32_16x16x32_bf16 v[84:87], a[92:95], a[44:47], v[84:87]\n"
"buffer_load_dwordx4 v10, s[28:31], s69 offen offset:3072 lds\n"
"v_mfma_f32_16x16x32_bf16 v[84:87], a[76:79], a[8:11], v[84:87]\n"
"s_waitcnt vmcnt(13)\n"
"ds_read_b64_tr_b16 a[232:233], v11\n"
"v_mfma_f32_16x16x32_bf16 v[88:91], a[80:83], a[8:11], v[88:91]\n"
"ds_read_b64_tr_b16 a[234:235], v11 offset:2048\n"
"v_mfma_f32_16x16x32_bf16 v[88:91], a[96:99], a[44:47], v[88:91]\n"
"s_add_u32 m0, s66, 16384\n"
"ds_read_b64_tr_b16 a[236:237], v12\n"
"s_nop 6\n"
"v_mfma_f32_16x16x32_bf16 v[92:95], a[100:103], a[44:47], v[92:95]\n"
"s_add_u32 s70, s35, 512\n"
"ds_read_b64_tr_b16 a[238:239], v12 offset:2048\n"
"v_mfma_f32_16x16x32_bf16 v[92:95], a[84:87], a[8:11], v[92:95]\n"
"s_and_b32 s70, s70, 0x7ff\n"
"buffer_load_dwordx4 v5, s[24:27], s70 offen offset:3072 lds\n"
"v_mfma_f32_16x16x32_bf16 v[108:111], a[84:87], a[12:15], v[108:111]\n"
"ds_read_b64_tr_b16 a[240:241], v13\n"
"v_mfma_f32_16x16x32_bf16 v[108:111], a[100:103], a[48:51], v[108:111]\n"
"ds_read_b64_tr_b16 a[242:243], v13 offset:2048\n"
"v_mfma_f32_16x16x32_bf16 v[104:107], a[96:99], a[48:51], v[104:107]\n"
"s_add_u32 m0, s68, 0\n"
"s_mul_i32 s71, s23, 0\n"
"ds_read_b64_tr_b16 a[244:245], v14\n"
"v_mfma_f32_16x16x32_bf16 v[104:107], a[80:83], a[12:15], v[104:107]\n"
"s_add_u32 s71, s77, s71\n"
"ds_read_b64_tr_b16 a[246:247], v14 offset:2048\n"
"s_nop 6\n"
"v_mfma_f32_16x16x32_bf16 v[100:103], a[76:79], a[12:15], v[100:103]\n"
"s_and_b32 s71, s71, 0x1ffff\n"
"buffer_load_dwordx4 v21, s[36:39], s71 offen lds\n"
"v_mfma_f32_16x16x32_bf16 v[100:103], a[92:95], a[48:51], v[100:103]\n"
"v_mfma_f32_16x16x32_bf16 v[96:99], a[88:91], a[48:51], v[96:99]\n"
"v_mfma_f32_16x16x32_bf16 v[96:99], a[72:75], a[12:15], v[96:99]\n"
"v_mfma_f32_16x16x32_bf16 v[112:115], a[72:75], a[16:19], v[112:115]\n"
"s_waitcnt lgkmcnt(5)\n"
"v_mfma_f32_16x16x32_bf16 v[112:115], a[88:91], a[52:55], v[112:115]\n"
"buffer_load_dwordx4 v22, s[36:39], s71 offen offset:1024 lds\n"
"s_nop 6\n"
"v_mfma_f32_16x16x32_bf16 v[116:119], a[92:95], a[52:55], v[116:119]\n"
"s_add_u32 m0, s67, 0\n"
"s_add_u32 s70, s35, 512\n"
"v_mfma_f32_16x16x32_bf16 v[116:119], a[76:79], a[16:19], v[116:119]\n"
"s_and_b32 s70, s70, 0x7ff\n"
"buffer_load_dwordx4 v6, s[24:27], s70 offen lds\n"
"v_mfma_f32_16x16x32_bf16 v[120:123], a[80:83], a[16:19], v[120:123]\n"
"s_waitcnt vmcnt(16)\n"
"s_barrier\n"
"v_mfma_f32_16x16x32_bf16 v[120:123], a[96:99], a[52:55], v[120:123]\n"
"s_waitcnt lgkmcnt(0)\n"
"s_add_u32 m0, s65, 0\n"
"ds_read_b128 a[0:3], v15\n"
"v_mfma_f32_16x16x32_bf16 v[124:127], a[100:103], a[52:55], v[124:127]\n"
"s_add_u32 s69, s34, 2359296\n"
"ds_read_b128 a[36:39], v16\n"
"v_mfma_f32_16x16x32_bf16 v[124:127], a[84:87], a[16:19], v[124:127]\n"
"s_and_b32 s69, s69, 0x7fffff\n"
"buffer_load_dwordx4 v7, s[28:31], s69 offen lds\n"
"s_nop 6\n"
"v_mfma_f32_16x16x32_bf16 v[140:143], a[84:87], a[20:23], v[140:143]\n"
"ds_read_b128 a[4:7], v15 offset:2048\n"
"v_mfma_f32_16x16x32_bf16 v[140:143], a[100:103], a[56:59], v[140:143]\n"
"ds_read_b128 a[40:43], v16 offset:2048\n"
"v_mfma_f32_16x16x32_bf16 v[136:139], a[96:99], a[56:59], v[136:139]\n"
"ds_read_b128 a[8:11], v15 offset:4096\n"
"v_mfma_f32_16x16x32_bf16 v[136:139], a[80:83], a[20:23], v[136:139]\n"
"ds_read_b128 a[44:47], v16 offset:4096\n"
"v_mfma_f32_16x16x32_bf16 v[132:135], a[76:79], a[20:23], v[132:135]\n"
"buffer_load_dwordx4 v8, s[28:31], s69 offen offset:1024 lds\n"
"v_mfma_f32_16x16x32_bf16 v[132:135], a[92:95], a[56:59], v[132:135]\n"
"ds_read_b128 a[12:15], v15 offset:6144\n"
"s_nop 6\n"
"v_mfma_f32_16x16x32_bf16 v[128:131], a[88:91], a[56:59], v[128:131]\n"
"ds_read_b128 a[48:51], v16 offset:6144\n"
"v_mfma_f32_16x16x32_bf16 v[128:131], a[72:75], a[20:23], v[128:131]\n"
"ds_read_b128 a[16:19], v15 offset:8192\n"
"v_mfma_f32_16x16x32_bf16 v[144:147], a[72:75], a[24:27], v[144:147]\n"
"v_mfma_f32_16x16x32_bf16 v[144:147], a[88:91], a[60:63], v[144:147]\n"
"buffer_load_dwordx4 v9, s[28:31], s69 offen offset:2048 lds\n"
"v_mfma_f32_16x16x32_bf16 v[148:151], a[92:95], a[60:63], v[148:151]\n"
"v_mfma_f32_16x16x32_bf16 v[148:151], a[76:79], a[24:27], v[148:151]\n"
"s_nop 6\n"
"v_mfma_f32_16x16x32_bf16 v[152:155], a[80:83], a[24:27], v[152:155]\n"
"v_mfma_f32_16x16x32_bf16 v[152:155], a[96:99], a[60:63], v[152:155]\n"
"v_mfma_f32_16x16x32_bf16 v[156:159], a[100:103], a[60:63], v[156:159]\n"
"buffer_load_dwordx4 v10, s[28:31], s69 offen offset:3072 lds\n"
"v_mfma_f32_16x16x32_bf16 v[156:159], a[84:87], a[24:27], v[156:159]\n"
"s_waitcnt vmcnt(15)\n"
"ds_read_b64_tr_b16 a[248:249], v11 offset:4096\n"
"v_mfma_f32_16x16x32_bf16 v[172:175], a[84:87], a[28:31], v[172:175]\n"
"ds_read_b64_tr_b16 a[250:251], v11 offset:6144\n"
"v_mfma_f32_16x16x32_bf16 v[172:175], a[100:103], a[64:67], v[172:175]\n"
"s_add_u32 m0, s66, 32768\n"
"ds_read_b64_tr_b16 a[252:253], v12 offset:4096\n"
"s_nop 6\n"
"v_mfma_f32_16x16x32_bf16 v[168:171], a[96:99], a[64:67], v[168:171]\n"
"s_add_u32 s70, s35, 640\n"
"ds_read_b64_tr_b16 a[254:255], v12 offset:6144\n"
"v_mfma_f32_16x16x32_bf16 v[168:171], a[80:83], a[28:31], v[168:171]\n"
"s_and_b32 s70, s70, 0x7ff\n"
"buffer_load_dwordx4 v2, s[24:27], s70 offen lds\n"
"v_mfma_f32_16x16x32_bf16 v[164:167], a[76:79], a[28:31], v[164:167]\n"
"ds_read_b64_tr_b16 v[204:205], v13 offset:4096\n"
"v_mfma_f32_16x16x32_bf16 v[164:167], a[92:95], a[64:67], v[164:167]\n"
"ds_read_b64_tr_b16 v[206:207], v13 offset:6144\n"
"v_mfma_f32_16x16x32_bf16 v[160:163], a[88:91], a[64:67], v[160:163]\n"
"ds_read_b64_tr_b16 v[208:209], v14 offset:4096\n"
"v_mfma_f32_16x16x32_bf16 v[160:163], a[72:75], a[28:31], v[160:163]\n"
"ds_read_b64_tr_b16 v[210:211], v14 offset:6144\n"
"s_nop 6\n"
"v_mfma_f32_16x16x32_bf16 v[176:179], a[72:75], a[32:35], v[176:179]\n"
"buffer_load_dwordx4 v3, s[24:27], s70 offen offset:1024 lds\n"
"v_mfma_f32_16x16x32_bf16 v[176:179], a[88:91], a[68:71], v[176:179]\n"
"v_mfma_f32_16x16x32_bf16 v[180:183], a[92:95], a[68:71], v[180:183]\n"
"v_mfma_f32_16x16x32_bf16 v[180:183], a[76:79], a[32:35], v[180:183]\n"
"v_mfma_f32_16x16x32_bf16 v[184:187], a[80:83], a[32:35], v[184:187]\n"
"v_mfma_f32_16x16x32_bf16 v[184:187], a[96:99], a[68:71], v[184:187]\n"
"buffer_load_dwordx4 v4, s[24:27], s70 offen offset:2048 lds\n"
"s_nop 6\n"
"v_mfma_f32_16x16x32_bf16 v[188:191], a[100:103], a[68:71], v[188:191]\n"
"v_mfma_f32_16x16x32_bf16 v[188:191], a[84:87], a[32:35], v[188:191]\n"
"s_waitcnt lgkmcnt(5)\n"
"v_mfma_f32_16x16x32_bf16 v[48:51], a[232:235], a[0:3], v[48:51]\n"
"v_mfma_f32_16x16x32_bf16 v[48:51], a[248:251], a[36:39], v[48:51]\n"
"s_waitcnt lgkmcnt(0)\n"
"s_add_u32 m0, s65, 4096\n"
"ds_read_b128 a[52:55], v16 offset:8192\n"
"v_mfma_f32_16x16x32_bf16 v[52:55], a[252:255], a[36:39], v[52:55]\n"
"s_add_u32 s69, s34, 2621440\n"
"ds_read_b128 a[20:23], v15 offset:10240\n"
"v_mfma_f32_16x16x32_bf16 v[52:55], a[236:239], a[0:3], v[52:55]\n"
"s_and_b32 s69, s69, 0x7fffff\n"
"buffer_load_dwordx4 v7, s[28:31], s69 offen lds\n"
"v_mfma_f32_16x16x32_bf16 v[56:59], a[240:243], a[0:3], v[56:59]\n"
"ds_read_b128 a[56:59], v16 offset:10240\n"
"v_mfma_f32_16x16x32_bf16 v[56:59], v[204:207], a[36:39], v[56:59]\n"
"ds_read_b128 a[24:27], v15 offset:12288\n"
"v_mfma_f32_16x16x32_bf16 v[60:63], v[208:211], a[36:39], v[60:63]\n"
"ds_read_b128 a[60:63], v16 offset:12288\n"
"v_mfma_f32_16x16x32_bf16 v[60:63], a[244:247], a[0:3], v[60:63]\n"
"ds_read_b128 a[28:31], v15 offset:14336\n"
"v_mfma_f32_16x16x32_bf16 v[76:79], a[244:247], a[4:7], v[76:79]\n"
"buffer_load_dwordx4 v8, s[28:31], s69 offen offset:1024 lds\n"
"v_mfma_f32_16x16x32_bf16 v[76:79], v[208:211], a[40:43], v[76:79]\n"
"ds_read_b128 a[64:67], v16 offset:14336\n"
"v_mfma_f32_16x16x32_bf16 v[72:75], v[204:207], a[40:43], v[72:75]\n"
"ds_read_b128 a[32:35], v15 offset:55296\n"
"v_mfma_f32_16x16x32_bf16 v[72:75], a[240:243], a[4:7], v[72:75]\n"
"ds_read_b128 a[68:71], v16 offset:55296\n"
"v_mfma_f32_16x16x32_bf16 v[68:71], a[236:239], a[4:7], v[68:71]\n"
"v_mfma_f32_16x16x32_bf16 v[68:71], a[252:255], a[40:43], v[68:71]\n"
"buffer_load_dwordx4 v9, s[28:31], s69 offen offset:2048 lds\n"
"v_mfma_f32_16x16x32_bf16 v[64:67], a[248:251], a[40:43], v[64:67]\n"
"v_mfma_f32_16x16x32_bf16 v[64:67], a[232:235], a[4:7], v[64:67]\n"
"v_mfma_f32_16x16x32_bf16 v[80:83], a[232:235], a[8:11], v[80:83]\n"
"v_mfma_f32_16x16x32_bf16 v[80:83], a[248:251], a[44:47], v[80:83]\n"
"v_mfma_f32_16x16x32_bf16 v[84:87], a[252:255], a[44:47], v[84:87]\n"
"buffer_load_dwordx4 v10, s[28:31], s69 offen offset:3072 lds\n"
"v_mfma_f32_16x16x32_bf16 v[84:87], a[236:239], a[8:11], v[84:87]\n"
"s_waitcnt vmcnt(15)\n"
"ds_read_b64_tr_b16 a[72:73], v11 offset:8192\n"
"v_mfma_f32_16x16x32_bf16 v[88:91], a[240:243], a[8:11], v[88:91]\n"
"ds_read_b64_tr_b16 a[74:75], v11 offset:10240\n"
"v_mfma_f32_16x16x32_bf16 v[88:91], v[204:207], a[44:47], v[88:91]\n"
"s_add_u32 m0, s66, 32768\n"
"ds_read_b64_tr_b16 a[76:77], v12 offset:8192\n"
"v_mfma_f32_16x16x32_bf16 v[92:95], v[208:211], a[44:47], v[92:95]\n"
"s_add_u32 s70, s35, 640\n"
"ds_read_b64_tr_b16 a[78:79], v12 offset:10240\n"
"v_mfma_f32_16x16x32_bf16 v[92:95], a[244:247], a[8:11], v[92:95]\n"
"s_and_b32 s70, s70, 0x7ff\n"
"buffer_load_dwordx4 v5, s[24:27], s70 offen offset:3072 lds\n"
"v_mfma_f32_16x16x32_bf16 v[108:111], a[244:247], a[12:15], v[108:111]\n"
"ds_read_b64_tr_b16 a[80:81], v13 offset:8192\n"
"v_mfma_f32_16x16x32_bf16 v[108:111], v[208:211], a[48:51], v[108:111]\n"
"ds_read_b64_tr_b16 a[82:83], v13 offset:10240\n"
"v_mfma_f32_16x16x32_bf16 v[104:107], v[204:207], a[48:51], v[104:107]\n"
"s_add_u32 m0, s68, 139264\n"
"s_mul_i32 s71, s23, 0\n"
"ds_read_b64_tr_b16 a[84:85], v14 offset:8192\n"
"v_mfma_f32_16x16x32_bf16 v[104:107], a[240:243], a[12:15], v[104:107]\n"
"s_add_u32 s71, s77, s71\n"
"ds_read_b64_tr_b16 a[86:87], v14 offset:10240\n"
"v_mfma_f32_16x16x32_bf16 v[100:103], a[236:239], a[12:15], v[100:103]\n"
"s_and_b32 s71, s71, 0x1ffff\n"
"buffer_load_dwordx4 v23, s[40:43], s71 offen lds\n"
"v_mfma_f32_16x16x32_bf16 v[100:103], a[252:255], a[48:51], v[100:103]\n"
"v_mfma_f32_16x16x32_bf16 v[96:99], a[248:251], a[48:51], v[96:99]\n"
"v_mfma_f32_16x16x32_bf16 v[96:99], a[232:235], a[12:15], v[96:99]\n"
"v_mfma_f32_16x16x32_bf16 v[112:115], a[232:235], a[16:19], v[112:115]\n"
"s_waitcnt lgkmcnt(5)\n"
"v_mfma_f32_16x16x32_bf16 v[112:115], a[248:251], a[52:55], v[112:115]\n"
"buffer_load_dwordx4 v24, s[40:43], s71 offen offset:1024 lds\n"
"v_mfma_f32_16x16x32_bf16 v[116:119], a[252:255], a[52:55], v[116:119]\n"
"v_mfma_f32_16x16x32_bf16 v[116:119], a[236:239], a[16:19], v[116:119]\n"
"v_mfma_f32_16x16x32_bf16 v[120:123], a[240:243], a[16:19], v[120:123]\n"
"s_waitcnt vmcnt(14)\n"
"s_barrier\n"
"v_mfma_f32_16x16x32_bf16 v[120:123], v[204:207], a[52:55], v[120:123]\n"
"s_waitcnt lgkmcnt(0)\n"
"s_add_u32 m0, s65, 8192\n"
"ds_read_b128 a[0:3], v15 offset:16384\n"
"v_mfma_f32_16x16x32_bf16 v[124:127], v[208:211], a[52:55], v[124:127]\n"
"s_add_u32 s69, s34, 2883584\n"
"ds_read_b128 a[36:39], v16 offset:16384\n"
"v_mfma_f32_16x16x32_bf16 v[124:127], a[244:247], a[16:19], v[124:127]\n"
"s_and_b32 s69, s69, 0x7fffff\n"
"buffer_load_dwordx4 v7, s[28:31], s69 offen lds\n"
"v_mfma_f32_16x16x32_bf16 v[140:143], a[244:247], a[20:23], v[140:143]\n"
"ds_read_b128 a[4:7], v15 offset:18432\n"
"v_mfma_f32_16x16x32_bf16 v[140:143], v[208:211], a[56:59], v[140:143]\n"
"ds_read_b128 a[40:43], v16 offset:18432\n"
"v_mfma_f32_16x16x32_bf16 v[136:139], v[204:207], a[56:59], v[136:139]\n"
"ds_read_b128 a[8:11], v15 offset:20480\n"
"v_mfma_f32_16x16x32_bf16 v[136:139], a[240:243], a[20:23], v[136:139]\n"
"ds_read_b128 a[44:47], v16 offset:20480\n"
"v_mfma_f32_16x16x32_bf16 v[132:135], a[236:239], a[20:23], v[132:135]\n"
"buffer_load_dwordx4 v8, s[28:31], s69 offen offset:1024 lds\n"
"v_mfma_f32_16x16x32_bf16 v[132:135], a[252:255], a[56:59], v[132:135]\n"
"ds_read_b128 a[12:15], v15 offset:22528\n"
"v_mfma_f32_16x16x32_bf16 v[128:131], a[248:251], a[56:59], v[128:131]\n"
"ds_read_b128 a[48:51], v16 offset:22528\n"
"v_mfma_f32_16x16x32_bf16 v[128:131], a[232:235], a[20:23], v[128:131]\n"
"ds_read_b128 a[16:19], v15 offset:24576\n"
"v_mfma_f32_16x16x32_bf16 v[144:147], a[232:235], a[24:27], v[144:147]\n"
"v_mfma_f32_16x16x32_bf16 v[144:147], a[248:251], a[60:63], v[144:147]\n"
"buffer_load_dwordx4 v9, s[28:31], s69 offen offset:2048 lds\n"
"v_mfma_f32_16x16x32_bf16 v[148:151], a[252:255], a[60:63], v[148:151]\n"
"v_mfma_f32_16x16x32_bf16 v[148:151], a[236:239], a[24:27], v[148:151]\n"
"v_mfma_f32_16x16x32_bf16 v[152:155], a[240:243], a[24:27], v[152:155]\n"
"v_mfma_f32_16x16x32_bf16 v[152:155], v[204:207], a[60:63], v[152:155]\n"
"v_mfma_f32_16x16x32_bf16 v[156:159], v[208:211], a[60:63], v[156:159]\n"
"buffer_load_dwordx4 v10, s[28:31], s69 offen offset:3072 lds\n"
"v_mfma_f32_16x16x32_bf16 v[156:159], a[244:247], a[24:27], v[156:159]\n"
"s_waitcnt vmcnt(14)\n"
"ds_read_b64_tr_b16 a[88:89], v11\n"
"v_mfma_f32_16x16x32_bf16 v[172:175], a[244:247], a[28:31], v[172:175]\n"
"ds_read_b64_tr_b16 a[90:91], v11 offset:2048\n"
"v_mfma_f32_16x16x32_bf16 v[172:175], v[208:211], a[64:67], v[172:175]\n"
"s_add_u32 m0, s66, 0\n"
"ds_read_b64_tr_b16 a[92:93], v12\n"
"v_mfma_f32_16x16x32_bf16 v[168:171], v[204:207], a[64:67], v[168:171]\n"
"s_add_u32 s70, s35, 768\n"
"ds_read_b64_tr_b16 a[94:95], v12 offset:2048\n"
"v_mfma_f32_16x16x32_bf16 v[168:171], a[240:243], a[28:31], v[168:171]\n"
"s_and_b32 s70, s70, 0x7ff\n"
"buffer_load_dwordx4 v2, s[24:27], s70 offen lds\n"
"v_mfma_f32_16x16x32_bf16 v[164:167], a[236:239], a[28:31], v[164:167]\n"
"ds_read_b64_tr_b16 a[96:97], v13\n"
"v_mfma_f32_16x16x32_bf16 v[164:167], a[252:255], a[64:67], v[164:167]\n"
"ds_read_b64_tr_b16 a[98:99], v13 offset:2048\n"
"v_mfma_f32_16x16x32_bf16 v[160:163], a[248:251], a[64:67], v[160:163]\n"
"ds_read_b64_tr_b16 a[100:101], v14\n"
"v_mfma_f32_16x16x32_bf16 v[160:163], a[232:235], a[28:31], v[160:163]\n"
"ds_read_b64_tr_b16 a[102:103], v14 offset:2048\n"
"v_mfma_f32_16x16x32_bf16 v[176:179], a[232:235], a[32:35], v[176:179]\n"
"buffer_load_dwordx4 v3, s[24:27], s70 offen offset:1024 lds\n"
"v_mfma_f32_16x16x32_bf16 v[176:179], a[248:251], a[68:71], v[176:179]\n"
"v_mfma_f32_16x16x32_bf16 v[180:183], a[252:255], a[68:71], v[180:183]\n"
"v_mfma_f32_16x16x32_bf16 v[180:183], a[236:239], a[32:35], v[180:183]\n"
"v_mfma_f32_16x16x32_bf16 v[184:187], a[240:243], a[32:35], v[184:187]\n"
"v_mfma_f32_16x16x32_bf16 v[184:187], v[204:207], a[68:71], v[184:187]\n"
"buffer_load_dwordx4 v4, s[24:27], s70 offen offset:2048 lds\n"
"v_add_co_u32 v46, vcc, 0x7f4a7c15, v46\n"
"v_mov_b32 v40, 0x9e3779b9\n"
"v_mfma_f32_16x16x32_bf16 v[188:191], v[208:211], a[68:71], v[188:191]\n"
"v_mfma_f32_16x16x32_bf16 v[188:191], a[244:247], a[32:35], v[188:191]\n"
"s_waitcnt lgkmcnt(5)\n"
"v_addc_co_u32 v47, vcc, v40, v47, vcc\n"
"v_mfma_f32_16x16x32_bf16 v[48:51], a[72:75], a[0:3], v[48:51]\n"
"v_mfma_f32_16x16x32_bf16 v[48:51], a[88:91], a[36:39], v[48:51]\n"
"s_waitcnt lgkmcnt(0)\n"
"s_add_u32 m0, s65, 0\n"
"ds_read_b128 a[52:55], v16 offset:24576\n"
"v_mfma_f32_16x16x32_bf16 v[52:55], a[92:95], a[36:39], v[52:55]\n"
"s_add_u32 s69, s34, 3145728\n"
"ds_read_b128 a[20:23], v15 offset:26624\n"
"v_mfma_f32_16x16x32_bf16 v[52:55], a[76:79], a[0:3], v[52:55]\n"
"s_and_b32 s69, s69, 0x7fffff\n"
"buffer_load_dwordx4 v7, s[28:31], s69 offen lds\n"
"v_mfma_f32_16x16x32_bf16 v[56:59], a[80:83], a[0:3], v[56:59]\n"
"ds_read_b128 a[56:59], v16 offset:26624\n"
"v_mfma_f32_16x16x32_bf16 v[56:59], a[96:99], a[36:39], v[56:59]\n"
"ds_read_b128 a[24:27], v15 offset:28672\n"
"v_mfma_f32_16x16x32_bf16 v[60:63], a[100:103], a[36:39], v[60:63]\n"
"ds_read_b128 a[60:63], v16 offset:28672\n"
"v_mfma_f32_16x16x32_bf16 v[60:63], a[84:87], a[0:3], v[60:63]\n"
"ds_read_b128 a[28:31], v15 offset:30720\n"
"v_mfma_f32_16x16x32_bf16 v[76:79], a[84:87], a[4:7], v[76:79]\n"
"buffer_load_dwordx4 v8, s[28:31], s69 offen offset:1024 lds\n"
"v_mfma_f32_16x16x32_bf16 v[76:79], a[100:103], a[40:43], v[76:79]\n"
"ds_read_b128 a[64:67], v16 offset:30720\n"
"v_mfma_f32_16x16x32_bf16 v[72:75], a[96:99], a[40:43], v[72:75]\n"
"ds_read_b128 a[32:35], v15 offset:49152\n"
"v_mfma_f32_16x16x32_bf16 v[72:75], a[80:83], a[4:7], v[72:75]\n"
"ds_read_b128 a[68:71], v16 offset:49152\n"
"v_mfma_f32_16x16x32_bf16 v[68:71], a[76:79], a[4:7], v[68:71]\n"
"v_mfma_f32_16x16x32_bf16 v[68:71], a[92:95], a[40:43], v[68:71]\n"
"buffer_load_dwordx4 v9, s[28:31], s69 offen offset:2048 lds\n"
"v_mfma_f32_16x16x32_bf16 v[64:67], a[88:91], a[40:43], v[64:67]\n"
"v_mfma_f32_16x16x32_bf16 v[64:67], a[72:75], a[4:7], v[64:67]\n"
"v_mfma_f32_16x16x32_bf16 v[80:83], a[72:75], a[8:11], v[80:83]\n"
"v_mfma_f32_16x16x32_bf16 v[80:83], a[88:91], a[44:47], v[80:83]\n"
"v_mfma_f32_16x16x32_bf16 v[84:87], a[92:95], a[44:47], v[84:87]\n"
"buffer_load_dwordx4 v10, s[28:31], s69 offen offset:3072 lds\n"
"v_mfma_f32_16x16x32_bf16 v[84:87], a[76:79], a[8:11], v[84:87]\n"
"s_waitcnt vmcnt(14)\n"
"ds_read_b64_tr_b16 a[232:233], v11 offset:4096\n"
"v_mfma_f32_16x16x32_bf16 v[88:91], a[80:83], a[8:11], v[88:91]\n"
"ds_read_b64_tr_b16 a[234:235], v11 offset:6144\n"
"v_mfma_f32_16x16x32_bf16 v[88:91], a[96:99], a[44:47], v[88:91]\n"
"s_add_u32 m0, s66, 0\n"
"ds_read_b64_tr_b16 a[236:237], v12 offset:4096\n"
"v_mfma_f32_16x16x32_bf16 v[92:95], a[100:103], a[44:47], v[92:95]\n"
"s_add_u32 s70, s35, 768\n"
"ds_read_b64_tr_b16 a[238:239], v12 offset:6144\n"
"v_mfma_f32_16x16x32_bf16 v[92:95], a[84:87], a[8:11], v[92:95]\n"
"s_and_b32 s70, s70, 0x7ff\n"
"buffer_load_dwordx4 v5, s[24:27], s70 offen offset:3072 lds\n"
"v_mfma_f32_16x16x32_bf16 v[108:111], a[84:87], a[12:15], v[108:111]\n"
"ds_read_b64_tr_b16 a[240:241], v13 offset:4096\n"
"v_mfma_f32_16x16x32_bf16 v[108:111], a[100:103], a[48:51], v[108:111]\n"
"ds_read_b64_tr_b16 a[242:243], v13 offset:6144\n"
"v_mfma_f32_16x16x32_bf16 v[104:107], a[96:99], a[48:51], v[104:107]\n"
"s_add_u32 m0, s68, 8192\n"
"s_mul_i32 s71, s23, 2\n"
"ds_read_b64_tr_b16 a[244:245], v14 offset:4096\n"
"v_mfma_f32_16x16x32_bf16 v[104:107], a[80:83], a[12:15], v[104:107]\n"
"s_add_u32 s71, s77, s71\n"
"ds_read_b64_tr_b16 a[246:247], v14 offset:6144\n"
"v_mfma_f32_16x16x32_bf16 v[100:103], a[76:79], a[12:15], v[100:103]\n"
"s_and_b32 s71, s71, 0x1ffff\n"
"buffer_load_dwordx4 v21, s[36:39], s71 offen lds\n"
"v_mfma_f32_16x16x32_bf16 v[100:103], a[92:95], a[48:51], v[100:103]\n"
"v_mfma_f32_16x16x32_bf16 v[96:99], a[88:91], a[48:51], v[96:99]\n"
"v_mfma_f32_16x16x32_bf16 v[96:99], a[72:75], a[12:15], v[96:99]\n"
"v_mfma_f32_16x16x32_bf16 v[112:115], a[72:75], a[16:19], v[112:115]\n"
"s_waitcnt lgkmcnt(5)\n"
"v_mfma_f32_16x16x32_bf16 v[112:115], a[88:91], a[52:55], v[112:115]\n"
"buffer_load_dwordx4 v22, s[36:39], s71 offen offset:1024 lds\n"
"v_mfma_f32_16x16x32_bf16 v[116:119], a[92:95], a[52:55], v[116:119]\n"
"s_add_u32 m0, s67, 4096\n"
"s_add_u32 s70, s35, 768\n"
"v_mfma_f32_16x16x32_bf16 v[116:119], a[76:79], a[16:19], v[116:119]\n"
"s_and_b32 s70, s70, 0x7ff\n"
"buffer_load_dwordx4 v6, s[24:27], s70 offen lds\n"
"v_mfma_f32_16x16x32_bf16 v[120:123], a[80:83], a[16:19], v[120:123]\n"
"s_waitcnt vmcnt(17)\n"
"s_barrier\n"
"v_mfma_f32_16x16x32_bf16 v[120:123], a[96:99], a[52:55], v[120:123]\n"
"s_waitcnt lgkmcnt(0)\n"
"s_add_u32 m0, s65, 4096\n"
"ds_read_b128 a[0:3], v15 offset:32768\n"
"v_mfma_f32_16x16x32_bf16 v[124:127], a[100:103], a[52:55], v[124:127]\n"
"s_add_u32 s69, s34, 3407872\n"
"ds_read_b128 a[36:39], v16 offset:32768\n"
"v_mfma_f32_16x16x32_bf16 v[124:127], a[84:87], a[16:19], v[124:127]\n"
"s_and_b32 s69, s69, 0x7fffff\n"
"buffer_load_dwordx4 v7, s[28:31], s69 offen lds\n"
"v_mfma_f32_16x16x32_bf16 v[140:143], a[84:87], a[20:23], v[140:143]\n"
"ds_read_b128 a[4:7], v15 offset:34816\n"
"v_mfma_f32_16x16x32_bf16 v[140:143], a[100:103], a[56:59], v[140:143]\n"
"ds_read_b128 a[40:43], v16 offset:34816\n"
"v_mfma_f32_16x16x32_bf16 v[136:139], a[96:99], a[56:59], v[136:139]\n"
"ds_read_b128 a[8:11], v15 offset:36864\n"
"v_mfma_f32_16x16x32_bf16 v[136:139], a[80:83], a[20:23], v[136:139]\n"
"ds_read_b128 a[44:47], v16 offset:36864\n"
"v_mfma_f32_16x16x32_bf16 v[132:135], a[76:79], a[20:23], v[132:135]\n"
"buffer_load_dwordx4 v8, s[28:31], s69 offen offset:1024 lds\n"
"v_mfma_f32_16x16x32_bf16 v[132:135], a[92:95], a[56:59], v[132:135]\n"
"ds_read_b128 a[12:15], v15 offset:38912\n"
"v_mfma_f32_16x16x32_bf16 v[128:131], a[88:91], a[56:59], v[128:131]\n"
"ds_read_b128 a[48:51], v16 offset:38912\n"
"v_mfma_f32_16x16x32_bf16 v[128:131], a[72:75], a[20:23], v[128:131]\n"
"ds_read_b128 a[16:19], v15 offset:40960\n"
"v_mfma_f32_16x16x32_bf16 v[144:147], a[72:75], a[24:27], v[144:147]\n"
"v_mfma_f32_16x16x32_bf16 v[144:147], a[88:91], a[60:63], v[144:147]\n"
"buffer_load_dwordx4 v9, s[28:31], s69 offen offset:2048 lds\n"
"v_mfma_f32_16x16x32_bf16 v[148:151], a[92:95], a[60:63], v[148:151]\n"
"v_mfma_f32_16x16x32_bf16 v[148:151], a[76:79], a[24:27], v[148:151]\n"
"v_mfma_f32_16x16x32_bf16 v[152:155], a[80:83], a[24:27], v[152:155]\n"
"v_mfma_f32_16x16x32_bf16 v[152:155], a[96:99], a[60:63], v[152:155]\n"
"v_mfma_f32_16x16x32_bf16 v[156:159], a[100:103], a[60:63], v[156:159]\n"
"buffer_load_dwordx4 v10, s[28:31], s69 offen offset:3072 lds\n"
"v_mfma_f32_16x16x32_bf16 v[156:159], a[84:87], a[24:27], v[156:159]\n"
"s_waitcnt vmcnt(15)\n"
"ds_read_b64_tr_b16 a[248:249], v11 offset:8192\n"
"v_mfma_f32_16x16x32_bf16 v[172:175], a[84:87], a[28:31], v[172:175]\n"
"ds_read_b64_tr_b16 a[250:251], v11 offset:10240\n"
"v_mfma_f32_16x16x32_bf16 v[172:175], a[100:103], a[64:67], v[172:175]\n"
"s_add_u32 m0, s66, 16384\n"
"ds_read_b64_tr_b16 a[252:253], v12 offset:8192\n"
"v_mfma_f32_16x16x32_bf16 v[168:171], a[96:99], a[64:67], v[168:171]\n"
"s_add_u32 s70, s35, 896\n"
"ds_read_b64_tr_b16 a[254:255], v12 offset:10240\n"
"v_mfma_f32_16x16x32_bf16 v[168:171], a[80:83], a[28:31], v[168:171]\n"
"s_and_b32 s70, s70, 0x7ff\n"
"buffer_load_dwordx4 v2, s[24:27], s70 offen lds\n"
"v_mfma_f32_16x16x32_bf16 v[164:167], a[76:79], a[28:31], v[164:167]\n"
"ds_read_b64_tr_b16 v[204:205], v13 offset:8192\n"
"v_mfma_f32_16x16x32_bf16 v[164:167], a[92:95], a[64:67], v[164:167]\n"
"ds_read_b64_tr_b16 v[206:207], v13 offset:10240\n"
"v_mfma_f32_16x16x32_bf16 v[160:163], a[88:91], a[64:67], v[160:163]\n"
"ds_read_b64_tr_b16 v[208:209], v14 offset:8192\n"
"v_mfma_f32_16x16x32_bf16 v[160:163], a[72:75], a[28:31], v[160:163]\n"
"ds_read_b64_tr_b16 v[210:211], v14 offset:10240\n"
"v_mfma_f32_16x16x32_bf16 v[176:179], a[72:75], a[32:35], v[176:179]\n"
"buffer_load_dwordx4 v3, s[24:27], s70 offen offset:1024 lds\n"
"v_mfma_f32_16x16x32_bf16 v[176:179], a[88:91], a[68:71], v[176:179]\n"
"v_mfma_f32_16x16x32_bf16 v[180:183], a[92:95], a[68:71], v[180:183]\n"
"v_mfma_f32_16x16x32_bf16 v[180:183], a[76:79], a[32:35], v[180:183]\n"
"v_mfma_f32_16x16x32_bf16 v[184:187], a[80:83], a[32:35], v[184:187]\n"
"v_mfma_f32_16x16x32_bf16 v[184:187], a[96:99], a[68:71], v[184:187]\n"
"buffer_load_dwordx4 v4, s[24:27], s70 offen offset:2048 lds\n"
"v_mfma_f32_16x16x32_bf16 v[188:191], a[100:103], a[68:71], v[188:191]\n"
"v_mfma_f32_16x16x32_bf16 v[188:191], a[84:87], a[32:35], v[188:191]\n"
"s_waitcnt lgkmcnt(5)\n"
"v_mfma_f32_16x16x32_bf16 v[48:51], a[232:235], a[0:3], v[48:51]\n"
"v_mfma_f32_16x16x32_bf16 v[48:51], a[248:251], a[36:39], v[48:51]\n"
"s_waitcnt lgkmcnt(0)\n"
"s_add_u32 m0, s65, 8192\n"
"ds_read_b128 a[52:55], v16 offset:40960\n"
"v_mfma_f32_16x16x32_bf16 v[52:55], a[252:255], a[36:39], v[52:55]\n"
"s_add_u32 s69, s34, 3670016\n"
"ds_read_b128 a[20:23], v15 offset:43008\n"
"v_mfma_f32_16x16x32_bf16 v[52:55], a[236:239], a[0:3], v[52:55]\n"
"s_and_b32 s69, s69, 0x7fffff\n"
"buffer_load_dwordx4 v7, s[28:31], s69 offen lds\n"
"v_mfma_f32_16x16x32_bf16 v[56:59], a[240:243], a[0:3], v[56:59]\n"
"ds_read_b128 a[56:59], v16 offset:43008\n"
"v_mfma_f32_16x16x32_bf16 v[56:59], v[204:207], a[36:39], v[56:59]\n"
"ds_read_b128 a[24:27], v15 offset:45056\n"
"v_mfma_f32_16x16x32_bf16 v[60:63], v[208:211], a[36:39], v[60:63]\n"
"ds_read_b128 a[60:63], v16 offset:45056\n"
"v_mfma_f32_16x16x32_bf16 v[60:63], a[244:247], a[0:3], v[60:63]\n"
"ds_read_b128 a[28:31], v15 offset:47104\n"
"v_mfma_f32_16x16x32_bf16 v[76:79], a[244:247], a[4:7], v[76:79]\n"
"buffer_load_dwordx4 v8, s[28:31], s69 offen offset:1024 lds\n"
"v_mfma_f32_16x16x32_bf16 v[76:79], v[208:211], a[40:43], v[76:79]\n"
"ds_read_b128 a[64:67], v16 offset:47104\n"
"v_mfma_f32_16x16x32_bf16 v[72:75], v[204:207], a[40:43], v[72:75]\n"
"ds_read_b128 a[32:35], v15 offset:51200\n"
"v_mfma_f32_16x16x32_bf16 v[72:75], a[240:243], a[4:7], v[72:75]\n"
"ds_read_b128 a[68:71], v16 offset:51200\n"
"v_mfma_f32_16x16x32_bf16 v[68:71], a[236:239], a[4:7], v[68:71]\n"
"v_mfma_f32_16x16x32_bf16 v[68:71], a[252:255], a[40:43], v[68:71]\n"
"buffer_load_dwordx4 v9, s[28:31], s69 offen offset:2048 lds\n"
"v_mfma_f32_16x16x32_bf16 v[64:67], a[248:251], a[40:43], v[64:67]\n"
"v_mfma_f32_16x16x32_bf16 v[64:67], a[232:235], a[4:7], v[64:67]\n"
"v_mfma_f32_16x16x32_bf16 v[80:83], a[232:235], a[8:11], v[80:83]\n"
"v_mfma_f32_16x16x32_bf16 v[80:83], a[248:251], a[44:47], v[80:83]\n"
"v_mfma_f32_16x16x32_bf16 v[84:87], a[252:255], a[44:47], v[84:87]\n"
"buffer_load_dwordx4 v10, s[28:31], s69 offen offset:3072 lds\n"
"v_mfma_f32_16x16x32_bf16 v[84:87], a[236:239], a[8:11], v[84:87]\n"
"s_waitcnt vmcnt(15)\n"
"ds_read_b64_tr_b16 a[72:73], v11\n"
"v_mfma_f32_16x16x32_bf16 v[88:91], a[240:243], a[8:11], v[88:91]\n"
"ds_read_b64_tr_b16 a[74:75], v11 offset:2048\n"
"v_mfma_f32_16x16x32_bf16 v[88:91], v[204:207], a[44:47], v[88:91]\n"
"s_add_u32 m0, s66, 16384\n"
"ds_read_b64_tr_b16 a[76:77], v12\n"
"v_mfma_f32_16x16x32_bf16 v[92:95], v[208:211], a[44:47], v[92:95]\n"
"s_add_u32 s70, s35, 896\n"
"ds_read_b64_tr_b16 a[78:79], v12 offset:2048\n"
"v_mfma_f32_16x16x32_bf16 v[92:95], a[244:247], a[8:11], v[92:95]\n"
"s_and_b32 s70, s70, 0x7ff\n"
"buffer_load_dwordx4 v5, s[24:27], s70 offen offset:3072 lds\n"
"v_mfma_f32_16x16x32_bf16 v[108:111], a[244:247], a[12:15], v[108:111]\n"
"ds_read_b64_tr_b16 a[80:81], v13\n"
"v_mfma_f32_16x16x32_bf16 v[108:111], v[208:211], a[48:51], v[108:111]\n"
"ds_read_b64_tr_b16 a[82:83], v13 offset:2048\n"
"v_mfma_f32_16x16x32_bf16 v[104:107], v[204:207], a[48:51], v[104:107]\n"
"s_add_u32 m0, s68, 147456\n"
"s_mul_i32 s71, s23, 2\n"
"ds_read_b64_tr_b16 a[84:85], v14\n"
"v_mfma_f32_16x16x32_bf16 v[104:107], a[240:243], a[12:15], v[104:107]\n"
"s_add_u32 s71, s77, s71\n"
"ds_read_b64_tr_b16 a[86:87], v14 offset:2048\n"
"v_mfma_f32_16x16x32_bf16 v[100:103], a[236:239], a[12:15], v[100:103]\n"
"s_and_b32 s71, s71, 0x1ffff\n"
"buffer_load_dwordx4 v23, s[40:43], s71 offen lds\n"
"v_mfma_f32_16x16x32_bf16 v[100:103], a[252:255], a[48:51], v[100:103]\n"
"v_mfma_f32_16x16x32_bf16 v[96:99], a[248:251], a[48:51], v[96:99]\n"
"v_mfma_f32_16x16x32_bf16 v[96:99], a[232:235], a[12:15], v[96:99]\n"
"v_mfma_f32_16x16x32_bf16 v[112:115], a[232:235], a[16:19], v[112:115]\n"
"s_waitcnt lgkmcnt(5)\n"
"v_mfma_f32_16x16x32_bf16 v[112:115], a[248:251], a[52:55], v[112:115]\n"
"buffer_load_dwordx4 v24, s[40:43], s71 offen offset:1024 lds\n"
"v_mfma_f32_16x16x32_bf16 v[116:119], a[252:255], a[52:55], v[116:119]\n"
"v_mfma_f32_16x16x32_bf16 v[116:119], a[236:239], a[16:19], v[116:119]\n"
"v_mfma_f32_16x16x32_bf16 v[120:123], a[240:243], a[16:19], v[120:123]\n"
"s_waitcnt vmcnt(14)\n"
"s_barrier\n"
"v_mfma_f32_16x16x32_bf16 v[120:123], v[204:207], a[52:55], v[120:123]\n"
"s_waitcnt lgkmcnt(0)\n"
"s_add_u32 m0, s65, 0\n"
"ds_read_b128 a[0:3], v15\n"
"v_mfma_f32_16x16x32_bf16 v[124:127], v[208:211], a[52:55], v[124:127]\n"
"s_add_u32 s69, s34, 3932160\n"
"ds_read_b128 a[36:39], v16\n"
"v_mfma_f32_16x16x32_bf16 v[124:127], a[244:247], a[16:19], v[124:127]\n"
"s_and_b32 s69, s69, 0x7fffff\n"
"buffer_load_dwordx4 v7, s[28:31], s69 offen lds\n"
"v_mfma_f32_16x16x32_bf16 v[140:143], a[244:247], a[20:23], v[140:143]\n"
"ds_read_b128 a[4:7], v15 offset:2048\n"
"v_mfma_f32_16x16x32_bf16 v[140:143], v[208:211], a[56:59], v[140:143]\n"
"ds_read_b128 a[40:43], v16 offset:2048\n"
"v_mfma_f32_16x16x32_bf16 v[136:139], v[204:207], a[56:59], v[136:139]\n"
"ds_read_b128 a[8:11], v15 offset:4096\n"
"v_mfma_f32_16x16x32_bf16 v[136:139], a[240:243], a[20:23], v[136:139]\n"
"ds_read_b128 a[44:47], v16 offset:4096\n"
"v_mfma_f32_16x16x32_bf16 v[132:135], a[236:239], a[20:23], v[132:135]\n"
"buffer_load_dwordx4 v8, s[28:31], s69 offen offset:1024 lds\n"
"v_mfma_f32_16x16x32_bf16 v[132:135], a[252:255], a[56:59], v[132:135]\n"
"ds_read_b128 a[12:15], v15 offset:6144\n"
"v_mfma_f32_16x16x32_bf16 v[128:131], a[248:251], a[56:59], v[128:131]\n"
"ds_read_b128 a[48:51], v16 offset:6144\n"
"v_mfma_f32_16x16x32_bf16 v[128:131], a[232:235], a[20:23], v[128:131]\n"
"ds_read_b128 a[16:19], v15 offset:8192\n"
"v_mfma_f32_16x16x32_bf16 v[144:147], a[232:235], a[24:27], v[144:147]\n"
"v_mfma_f32_16x16x32_bf16 v[144:147], a[248:251], a[60:63], v[144:147]\n"
"buffer_load_dwordx4 v9, s[28:31], s69 offen offset:2048 lds\n"
"v_mfma_f32_16x16x32_bf16 v[148:151], a[252:255], a[60:63], v[148:151]\n"
"v_mfma_f32_16x16x32_bf16 v[148:151], a[236:239], a[24:27], v[148:151]\n"
"v_mfma_f32_16x16x32_bf16 v[152:155], a[240:243], a[24:27], v[152:155]\n"
"v_mfma_f32_16x16x32_bf16 v[152:155], v[204:207], a[60:63], v[152:155]\n"
"v_mfma_f32_16x16x32_bf16 v[156:159], v[208:211], a[60:63], v[156:159]\n"
"buffer_load_dwordx4 v10, s[28:31], s69 offen offset:3072 lds\n"
"v_mfma_f32_16x16x32_bf16 v[156:159], a[244:247], a[24:27], v[156:159]\n"
"s_waitcnt vmcnt(14)\n"
"ds_read_b64_tr_b16 a[88:89], v11 offset:4096\n"
"v_mfma_f32_16x16x32_bf16 v[172:175], a[244:247], a[28:31], v[172:175]\n"
"ds_read_b64_tr_b16 a[90:91], v11 offset:6144\n"
"v_mfma_f32_16x16x32_bf16 v[172:175], v[208:211], a[64:67], v[172:175]\n"
"s_add_u32 m0, s66, 32768\n"
"ds_read_b64_tr_b16 a[92:93], v12 offset:4096\n"
"v_mfma_f32_16x16x32_bf16 v[168:171], v[204:207], a[64:67], v[168:171]\n"
"s_add_u32 s70, s35, 1024\n"
"ds_read_b64_tr_b16 a[94:95], v12 offset:6144\n"
"v_mfma_f32_16x16x32_bf16 v[168:171], a[240:243], a[28:31], v[168:171]\n"
"s_and_b32 s70, s70, 0x7ff\n"
"buffer_load_dwordx4 v2, s[24:27], s70 offen lds\n"
"v_mfma_f32_16x16x32_bf16 v[164:167], a[236:239], a[28:31], v[164:167]\n"
"ds_read_b64_tr_b16 a[96:97], v13 offset:4096\n"
"v_mfma_f32_16x16x32_bf16 v[164:167], a[252:255], a[64:67], v[164:167]\n"
"ds_read_b64_tr_b16 a[98:99], v13 offset:6144\n"
"v_mfma_f32_16x16x32_bf16 v[160:163], a[248:251], a[64:67], v[160:163]\n"
"ds_read_b64_tr_b16 a[100:101], v14 offset:4096\n"
"v_mfma_f32_16x16x32_bf16 v[160:163], a[232:235], a[28:31], v[160:163]\n"
"ds_read_b64_tr_b16 a[102:103], v14 offset:6144\n"
"v_mfma_f32_16x16x32_bf16 v[176:179], a[232:235], a[32:35], v[176:179]\n"
"buffer_load_dwordx4 v3, s[24:27], s70 offen offset:1024 lds\n"
"v_mfma_f32_16x16x32_bf16 v[176:179], a[248:251], a[68:71], v[176:179]\n"
"v_mfma_f32_16x16x32_bf16 v[180:183], a[252:255], a[68:71], v[180:183]\n"
"buffer_load_dword v44, v45, s[60:63], 0 offen\n"
"v_mfma_f32_16x16x32_bf16 v[180:183], a[236:239], a[32:35], v[180:183]\n"
"v_mfma_f32_16x16x32_bf16 v[184:187], a[240:243], a[32:35], v[184:187]\n"
"v_mfma_f32_16x16x32_bf16 v[184:187], v[204:207], a[68:71], v[184:187]\n"
"buffer_load_dwordx4 v4, s[24:27], s70 offen offset:2048 lds\n"
"v_mfma_f32_16x16x32_bf16 v[188:191], v[208:211], a[68:71], v[188:191]\n"
"v_mfma_f32_16x16x32_bf16 v[188:191], a[244:247], a[32:35], v[188:191]\n"
"s_waitcnt lgkmcnt(5)\n"
"v_mfma_f32_16x16x32_bf16 v[48:51], a[72:75], a[0:3], v[48:51]\n"
"v_mfma_f32_16x16x32_bf16 v[48:51], a[88:91], a[36:39], v[48:51]\n"
"s_waitcnt lgkmcnt(0)\n"
"s_add_u32 m0, s65, 4096\n"
"ds_read_b128 a[52:55], v16 offset:8192\n"
"v_mfma_f32_16x16x32_bf16 v[52:55], a[92:95], a[36:39], v[52:55]\n"
"s_add_u32 s69, s34, 4194304\n"
"ds_read_b128 a[20:23], v15 offset:10240\n"
"v_mfma_f32_16x16x32_bf16 v[52:55], a[76:79], a[0:3], v[52:55]\n"
"s_and_b32 s69, s69, 0x7fffff\n"
"buffer_load_dwordx4 v7, s[28:31], s69 offen lds\n"
"v_mfma_f32_16x16x32_bf16 v[56:59], a[80:83], a[0:3], v[56:59]\n"
"ds_read_b128 a[56:59], v16 offset:10240\n"
"v_mfma_f32_16x16x32_bf16 v[56:59], a[96:99], a[36:39], v[56:59]\n"
"ds_read_b128 a[24:27], v15 offset:12288\n"
"v_mfma_f32_16x16x32_bf16 v[60:63], a[100:103], a[36:39], v[60:63]\n"
"ds_read_b128 a[60:63], v16 offset:12288\n"
"v_mfma_f32_16x16x32_bf16 v[60:63], a[84:87], a[0:3], v[60:63]\n"
"ds_read_b128 a[28:31], v15 offset:14336\n"
"v_mfma_f32_16x16x32_bf16 v[76:79], a[84:87], a[4:7], v[76:79]\n"
"buffer_load_dwordx4 v8, s[28:31], s69 offen offset:1024 lds\n"
"v_mfma_f32_16x16x32_bf16 v[76:79], a[100:103], a[40:43], v[76:79]\n"
"ds_read_b128 a[64:67], v16 offset:14336\n"
"v_mfma_f32_16x16x32_bf16 v[72:75], a[96:99], a[40:43], v[72:75]\n"
"ds_read_b128 a[32:35], v15 offset:53248\n"
"v_mfma_f32_16x16x32_bf16 v[72:75], a[80:83], a[4:7], v[72:75]\n"
"ds_read_b128 a[68:71], v16 offset:53248\n"
"v_mfma_f32_16x16x32_bf16 v[68:71], a[76:79], a[4:7], v[68:71]\n"
"v_mfma_f32_16x16x32_bf16 v[68:71], a[92:95], a[40:43], v[68:71]\n"
"buffer_load_dwordx4 v9, s[28:31], s69 offen offset:2048 lds\n"
"v_mfma_f32_16x16x32_bf16 v[64:67], a[88:91], a[40:43], v[64:67]\n"
"v_mfma_f32_16x16x32_bf16 v[64:67], a[72:75], a[4:7], v[64:67]\n"
"v_mfma_f32_16x16x32_bf16 v[80:83], a[72:75], a[8:11], v[80:83]\n"
"v_mfma_f32_16x16x32_bf16 v[80:83], a[88:91], a[44:47], v[80:83]\n"
"v_mfma_f32_16x16x32_bf16 v[84:87], a[92:95], a[44:47], v[84:87]\n"
"buffer_load_dwordx4 v10, s[28:31], s69 offen offset:3072 lds\n"
"v_mfma_f32_16x16x32_bf16 v[84:87], a[76:79], a[8:11], v[84:87]\n"
"s_waitcnt vmcnt(15)\n"
"ds_read_b64_tr_b16 a[232:233], v11 offset:8192\n"
"v_mfma_f32_16x16x32_bf16 v[88:91], a[80:83], a[8:11], v[88:91]\n"
"ds_read_b64_tr_b16 a[234:235], v11 offset:10240\n"
"v_mfma_f32_16x16x32_bf16 v[88:91], a[96:99], a[44:47], v[88:91]\n"
"s_add_u32 m0, s66, 32768\n"
"ds_read_b64_tr_b16 a[236:237], v12 offset:8192\n"
"v_mfma_f32_16x16x32_bf16 v[92:95], a[100:103], a[44:47], v[92:95]\n"
"s_add_u32 s70, s35, 1024\n"
"ds_read_b64_tr_b16 a[238:239], v12 offset:10240\n"
"v_mfma_f32_16x16x32_bf16 v[92:95], a[84:87], a[8:11], v[92:95]\n"
"s_and_b32 s70, s70, 0x7ff\n"
"buffer_load_dwordx4 v5, s[24:27], s70 offen offset:3072 lds\n"
"v_mfma_f32_16x16x32_bf16 v[108:111], a[84:87], a[12:15], v[108:111]\n"
"ds_read_b64_tr_b16 a[240:241], v13 offset:8192\n"
"v_mfma_f32_16x16x32_bf16 v[108:111], a[100:103], a[48:51], v[108:111]\n"
"ds_read_b64_tr_b16 a[242:243], v13 offset:10240\n"
"v_mfma_f32_16x16x32_bf16 v[104:107], a[96:99], a[48:51], v[104:107]\n"
"s_add_u32 m0, s68, 16384\n"
"s_mul_i32 s71, s23, 4\n"
"ds_read_b64_tr_b16 a[244:245], v14 offset:8192\n"
"v_mfma_f32_16x16x32_bf16 v[104:107], a[80:83], a[12:15], v[104:107]\n"
"s_add_u32 s71, s77, s71\n"
"ds_read_b64_tr_b16 a[246:247], v14 offset:10240\n"
"v_mfma_f32_16x16x32_bf16 v[100:103], a[76:79], a[12:15], v[100:103]\n"
"s_and_b32 s71, s71, 0x1ffff\n"
"buffer_load_dwordx4 v21, s[36:39], s71 offen lds\n"
"v_mfma_f32_16x16x32_bf16 v[100:103], a[92:95], a[48:51], v[100:103]\n"
"v_mfma_f32_16x16x32_bf16 v[96:99], a[88:91], a[48:51], v[96:99]\n"
"v_mfma_f32_16x16x32_bf16 v[96:99], a[72:75], a[12:15], v[96:99]\n"
"v_mfma_f32_16x16x32_bf16 v[112:115], a[72:75], a[16:19], v[112:115]\n"
"s_waitcnt lgkmcnt(5)\n"
"v_mfma_f32_16x16x32_bf16 v[112:115], a[88:91], a[52:55], v[112:115]\n"
"buffer_load_dwordx4 v22, s[36:39], s71 offen offset:1024 lds\n"
"v_mfma_f32_16x16x32_bf16 v[116:119], a[92:95], a[52:55], v[116:119]\n"
"s_add_u32 m0, s67, 0\n"
"s_add_u32 s70, s35, 1024\n"
"v_mfma_f32_16x16x32_bf16 v[116:119], a[76:79], a[16:19], v[116:119]\n"
"s_and_b32 s70, s70, 0x7ff\n"
"buffer_load_dwordx4 v6, s[24:27], s70 offen lds\n"
"v_mfma_f32_16x16x32_bf16 v[120:123], a[80:83], a[16:19], v[120:123]\n"
"s_waitcnt vmcnt(18)\n"
"s_barrier\n"
"v_mfma_f32_16x16x32_bf16 v[120:123], a[96:99], a[52:55], v[120:123]\n"
"s_waitcnt lgkmcnt(0)\n"
"s_add_u32 m0, s65, 8192\n"
"ds_read_b128 a[0:3], v15 offset:16384\n"
"v_mfma_f32_16x16x32_bf16 v[124:127], a[100:103], a[52:55], v[124:127]\n"
"s_add_u32 s69, s34, 4456448\n"
"ds_read_b128 a[36:39], v16 offset:16384\n"
"v_mfma_f32_16x16x32_bf16 v[124:127], a[84:87], a[16:19], v[124:127]\n"
"s_and_b32 s69, s69, 0x7fffff\n"
"buffer_load_dwordx4 v7, s[28:31], s69 offen lds\n"
"v_mfma_f32_16x16x32_bf16 v[140:143], a[84:87], a[20:23], v[140:143]\n"
"ds_read_b128 a[4:7], v15 offset:18432\n"
"v_mfma_f32_16x16x32_bf16 v[140:143], a[100:103], a[56:59], v[140:143]\n"
"ds_read_b128 a[40:43], v16 offset:18432\n"
"v_mfma_f32_16x16x32_bf16 v[136:139], a[96:99], a[56:59], v[136:139]\n"
"ds_read_b128 a[8:11], v15 offset:20480\n"
"v_mfma_f32_16x16x32_bf16 v[136:139], a[80:83], a[20:23], v[136:139]\n"
"ds_read_b128 a[44:47], v16 offset:20480\n"
"v_mfma_f32_16x16x32_bf16 v[132:135], a[76:79], a[20:23], v[132:135]\n"
"buffer_load_dwordx4 v8, s[28:31], s69 offen offset:1024 lds\n"
"v_mfma_f32_16x16x32_bf16 v[132:135], a[92:95], a[56:59], v[132:135]\n"
"ds_read_b128 a[12:15], v15 offset:22528\n"
"v_mfma_f32_16x16x32_bf16 v[128:131], a[88:91], a[56:59], v[128:131]\n"
"ds_read_b128 a[48:51], v16 offset:22528\n"
"v_mfma_f32_16x16x32_bf16 v[128:131], a[72:75], a[20:23], v[128:131]\n"
"ds_read_b128 a[16:19], v15 offset:24576\n"
"v_mfma_f32_16x16x32_bf16 v[144:147], a[72:75], a[24:27], v[144:147]\n"
"v_mfma_f32_16x16x32_bf16 v[144:147], a[88:91], a[60:63], v[144:147]\n"
"buffer_load_dwordx4 v9, s[28:31], s69 offen offset:2048 lds\n"
"v_mfma_f32_16x16x32_bf16 v[148:151], a[92:95], a[60:63], v[148:151]\n"
"v_mfma_f32_16x16x32_bf16 v[148:151], a[76:79], a[24:27], v[148:151]\n"
"v_mfma_f32_16x16x32_bf16 v[152:155], a[80:83], a[24:27], v[152:155]\n"
"v_mfma_f32_16x16x32_bf16 v[152:155], a[96:99], a[60:63], v[152:155]\n"
"v_mfma_f32_16x16x32_bf16 v[156:159], a[100:103], a[60:63], v[156:159]\n"
"buffer_load_dwordx4 v10, s[28:31], s69 offen offset:3072 lds\n"
"v_mfma_f32_16x16x32_bf16 v[156:159], a[84:87], a[24:27], v[156:159]\n"
"s_waitcnt vmcnt(16)\n"
"ds_read_b64_tr_b16 a[248:249], v11\n"
"v_mfma_f32_16x16x32_bf16 v[172:175], a[84:87], a[28:31], v[172:175]\n"
"ds_read_b64_tr_b16 a[250:251], v11 offset:2048\n"
"v_mfma_f32_16x16x32_bf16 v[172:175], a[100:103], a[64:67], v[172:175]\n"
"s_add_u32 m0, s66, 0\n"
"ds_read_b64_tr_b16 a[252:253], v12\n"
"v_mfma_f32_16x16x32_bf16 v[168:171], a[96:99], a[64:67], v[168:171]\n"
"s_add_u32 s70, s35, 1152\n"
"ds_read_b64_tr_b16 a[254:255], v12 offset:2048\n"
"v_mfma_f32_16x16x32_bf16 v[168:171], a[80:83], a[28:31], v[168:171]\n"
"s_and_b32 s70, s70, 0x7ff\n"
"buffer_load_dwordx4 v2, s[24:27], s70 offen lds\n"
"v_mfma_f32_16x16x32_bf16 v[164:167], a[76:79], a[28:31], v[164:167]\n"
"ds_read_b64_tr_b16 v[204:205], v13\n"
"v_mfma_f32_16x16x32_bf16 v[164:167], a[92:95], a[64:67], v[164:167]\n"
"ds_read_b64_tr_b16 v[206:207], v13 offset:2048\n"
"v_mfma_f32_16x16x32_bf16 v[160:163], a[88:91], a[64:67], v[160:163]\n"
"ds_read_b64_tr_b16 v[208:209], v14\n"
"v_mfma_f32_16x16x32_bf16 v[160:163], a[72:75], a[28:31], v[160:163]\n"
"ds_read_b64_tr_b16 v[210:211], v14 offset:2048\n"
"v_mfma_f32_16x16x32_bf16 v[176:179], a[72:75], a[32:35], v[176:179]\n"
"buffer_load_dwordx4 v3, s[24:27], s70 offen offset:1024 lds\n"
"v_mfma_f32_16x16x32_bf16 v[176:179], a[88:91], a[68:71], v[176:179]\n"
"v_mfma_f32_16x16x32_bf16 v[180:183], a[92:95], a[68:71], v[180:183]\n"
"v_mfma_f32_16x16x32_bf16 v[180:183], a[76:79], a[32:35], v[180:183]\n"
"v_mfma_f32_16x16x32_bf16 v[184:187], a[80:83], a[32:35], v[184:187]\n"
"v_mfma_f32_16x16x32_bf16 v[184:187], a[96:99], a[68:71], v[184:187]\n"
"buffer_load_dwordx4 v4, s[24:27], s70 offen offset:2048 lds\n"
"v_mfma_f32_16x16x32_bf16 v[188:191], a[100:103], a[68:71], v[188:191]\n"
"v_mfma_f32_16x16x32_bf16 v[188:191], a[84:87], a[32:35], v[188:191]\n"
"s_waitcnt lgkmcnt(5)\n"
"v_mfma_f32_16x16x32_bf16 v[48:51], a[232:235], a[0:3], v[48:51]\n"
"v_mfma_f32_16x16x32_bf16 v[48:51], a[248:251], a[36:39], v[48:51]\n"
"s_waitcnt lgkmcnt(0)\n"
"s_add_u32 m0, s65, 0\n"
"ds_read_b128 a[52:55], v16 offset:24576\n"
"v_mfma_f32_16x16x32_bf16 v[52:55], a[252:255], a[36:39], v[52:55]\n"
"s_add_u32 s69, s34, 4718592\n"
"ds_read_b128 a[20:23], v15 offset:26624\n"
"v_mfma_f32_16x16x32_bf16 v[52:55], a[236:239], a[0:3], v[52:55]\n"
"s_and_b32 s69, s69, 0x7fffff\n"
"buffer_load_dwordx4 v7, s[28:31], s69 offen lds\n"
"v_mfma_f32_16x16x32_bf16 v[56:59], a[240:243], a[0:3], v[56:59]\n"
"ds_read_b128 a[56:59], v16 offset:26624\n"
"v_mfma_f32_16x16x32_bf16 v[56:59], v[204:207], a[36:39], v[56:59]\n"
"ds_read_b128 a[24:27], v15 offset:28672\n"
"v_mfma_f32_16x16x32_bf16 v[60:63], v[208:211], a[36:39], v[60:63]\n"
"ds_read_b128 a[60:63], v16 offset:28672\n"
"v_mfma_f32_16x16x32_bf16 v[60:63], a[244:247], a[0:3], v[60:63]\n"
"ds_read_b128 a[28:31], v15 offset:30720\n"
"v_mfma_f32_16x16x32_bf16 v[76:79], a[244:247], a[4:7], v[76:79]\n"
"buffer_load_dwordx4 v8, s[28:31], s69 offen offset:1024 lds\n"
"v_mfma_f32_16x16x32_bf16 v[76:79], v[208:211], a[40:43], v[76:79]\n"
"ds_read_b128 a[64:67], v16 offset:30720\n"
"v_mfma_f32_16x16x32_bf16 v[72:75], v[204:207], a[40:43], v[72:75]\n"
"ds_read_b128 a[32:35], v15 offset:55296\n"
"v_mfma_f32_16x16x32_bf16 v[72:75], a[240:243], a[4:7], v[72:75]\n"
"ds_read_b128 a[68:71], v16 offset:55296\n"
"v_mfma_f32_16x16x32_bf16 v[68:71], a[236:239], a[4:7], v[68:71]\n"
"v_mfma_f32_16x16x32_bf16 v[68:71], a[252:255], a[40:43], v[68:71]\n"
"buffer_load_dwordx4 v9, s[28:31], s69 offen offset:2048 lds\n"
"v_mfma_f32_16x16x32_bf16 v[64:67], a[248:251], a[40:43], v[64:67]\n"
"v_mfma_f32_16x16x32_bf16 v[64:67], a[232:235], a[4:7], v[64:67]\n"
"v_mfma_f32_16x16x32_bf16 v[80:83], a[232:235], a[8:11], v[80:83]\n"
"v_mfma_f32_16x16x32_bf16 v[80:83], a[248:251], a[44:47], v[80:83]\n"
"v_mfma_f32_16x16x32_bf16 v[84:87], a[252:255], a[44:47], v[84:87]\n"
"buffer_load_dwordx4 v10, s[28:31], s69 offen offset:3072 lds\n"
"v_mfma_f32_16x16x32_bf16 v[84:87], a[236:239], a[8:11], v[84:87]\n"
"s_waitcnt vmcnt(15)\n"
"ds_read_b64_tr_b16 a[72:73], v11 offset:4096\n"
"v_mfma_f32_16x16x32_bf16 v[88:91], a[240:243], a[8:11], v[88:91]\n"
"ds_read_b64_tr_b16 a[74:75], v11 offset:6144\n"
"v_mfma_f32_16x16x32_bf16 v[88:91], v[204:207], a[44:47], v[88:91]\n"
"s_add_u32 m0, s66, 0\n"
"ds_read_b64_tr_b16 a[76:77], v12 offset:4096\n"
"v_mfma_f32_16x16x32_bf16 v[92:95], v[208:211], a[44:47], v[92:95]\n"
"s_add_u32 s70, s35, 1152\n"
"ds_read_b64_tr_b16 a[78:79], v12 offset:6144\n"
"v_mfma_f32_16x16x32_bf16 v[92:95], a[244:247], a[8:11], v[92:95]\n"
"s_and_b32 s70, s70, 0x7ff\n"
"buffer_load_dwordx4 v5, s[24:27], s70 offen offset:3072 lds\n"
"v_mfma_f32_16x16x32_bf16 v[108:111], a[244:247], a[12:15], v[108:111]\n"
"ds_read_b64_tr_b16 a[80:81], v13 offset:4096\n"
"v_mfma_f32_16x16x32_bf16 v[108:111], v[208:211], a[48:51], v[108:111]\n"
"ds_read_b64_tr_b16 a[82:83], v13 offset:6144\n"
"v_mfma_f32_16x16x32_bf16 v[104:107], v[204:207], a[48:51], v[104:107]\n"
"s_add_u32 m0, s68, 155648\n"
"s_mul_i32 s71, s23, 4\n"
"ds_read_b64_tr_b16 a[84:85], v14 offset:4096\n"
"v_mfma_f32_16x16x32_bf16 v[104:107], a[240:243], a[12:15], v[104:107]\n"
"s_add_u32 s71, s77, s71\n"
"ds_read_b64_tr_b16 a[86:87], v14 offset:6144\n"
"v_mfma_f32_16x16x32_bf16 v[100:103], a[236:239], a[12:15], v[100:103]\n"
"s_and_b32 s71, s71, 0x1ffff\n"
"buffer_load_dwordx4 v23, s[40:43], s71 offen lds\n"
"v_mfma_f32_16x16x32_bf16 v[100:103], a[252:255], a[48:51], v[100:103]\n"
"v_mfma_f32_16x16x32_bf16 v[96:99], a[248:251], a[48:51], v[96:99]\n"
"v_mfma_f32_16x16x32_bf16 v[96:99], a[232:235], a[12:15], v[96:99]\n"
"v_mfma_f32_16x16x32_bf16 v[112:115], a[232:235], a[16:19], v[112:115]\n"
"s_waitcnt lgkmcnt(5)\n"
"v_mfma_f32_16x16x32_bf16 v[112:115], a[248:251], a[52:55], v[112:115]\n"
"buffer_load_dwordx4 v24, s[40:43], s71 offen offset:1024 lds\n"
"v_mfma_f32_16x16x32_bf16 v[116:119], a[252:255], a[52:55], v[116:119]\n"
"v_mfma_f32_16x16x32_bf16 v[116:119], a[236:239], a[16:19], v[116:119]\n"
"v_mfma_f32_16x16x32_bf16 v[120:123], a[240:243], a[16:19], v[120:123]\n"
"s_waitcnt vmcnt(14)\n"
"s_barrier\n"
"v_mfma_f32_16x16x32_bf16 v[120:123], v[204:207], a[52:55], v[120:123]\n"
"s_waitcnt lgkmcnt(0)\n"
"s_add_u32 m0, s65, 4096\n"
"ds_read_b128 a[0:3], v15 offset:32768\n"
"v_mfma_f32_16x16x32_bf16 v[124:127], v[208:211], a[52:55], v[124:127]\n"
"s_add_u32 s69, s34, 4980736\n"
"ds_read_b128 a[36:39], v16 offset:32768\n"
"v_mfma_f32_16x16x32_bf16 v[124:127], a[244:247], a[16:19], v[124:127]\n"
"s_and_b32 s69, s69, 0x7fffff\n"
"buffer_load_dwordx4 v7, s[28:31], s69 offen lds\n"
"v_mfma_f32_16x16x32_bf16 v[140:143], a[244:247], a[20:23], v[140:143]\n"
"ds_read_b128 a[4:7], v15 offset:34816\n"
"v_mfma_f32_16x16x32_bf16 v[140:143], v[208:211], a[56:59], v[140:143]\n"
"ds_read_b128 a[40:43], v16 offset:34816\n"
"v_mfma_f32_16x16x32_bf16 v[136:139], v[204:207], a[56:59], v[136:139]\n"
"ds_read_b128 a[8:11], v15 offset:36864\n"
"v_mfma_f32_16x16x32_bf16 v[136:139], a[240:243], a[20:23], v[136:139]\n"
"ds_read_b128 a[44:47], v16 offset:36864\n"
"v_mfma_f32_16x16x32_bf16 v[132:135], a[236:239], a[20:23], v[132:135]\n"
"buffer_load_dwordx4 v8, s[28:31], s69 offen offset:1024 lds\n"
"v_mfma_f32_16x16x32_bf16 v[132:135], a[252:255], a[56:59], v[132:135]\n"
"ds_read_b128 a[12:15], v15 offset:38912\n"
"v_mfma_f32_16x16x32_bf16 v[128:131], a[248:251], a[56:59], v[128:131]\n"
"ds_read_b128 a[48:51], v16 offset:38912\n"
"v_mfma_f32_16x16x32_bf16 v[128:131], a[232:235], a[20:23], v[128:131]\n"
"ds_read_b128 a[16:19], v15 offset:40960\n"
"v_mfma_f32_16x16x32_bf16 v[144:147], a[232:235], a[24:27], v[144:147]\n"
"v_mfma_f32_16x16x32_bf16 v[144:147], a[248:251], a[60:63], v[144:147]\n"
"buffer_load_dwordx4 v9, s[28:31], s69 offen offset:2048 lds\n"
"v_mfma_f32_16x16x32_bf16 v[148:151], a[252:255], a[60:63], v[148:151]\n"
"v_mfma_f32_16x16x32_bf16 v[148:151], a[236:239], a[24:27], v[148:151]\n"
"v_mfma_f32_16x16x32_bf16 v[152:155], a[240:243], a[24:27], v[152:155]\n"
"v_mfma_f32_16x16x32_bf16 v[152:155], v[204:207], a[60:63], v[152:155]\n"
"v_mfma_f32_16x16x32_bf16 v[156:159], v[208:211], a[60:63], v[156:159]\n"
"buffer_load_dwordx4 v10, s[28:31], s69 offen offset:3072 lds\n"
"v_mfma_f32_16x16x32_bf16 v[156:159], a[244:247], a[24:27], v[156:159]\n"
"s_waitcnt vmcnt(14)\n"
"ds_read_b64_tr_b16 a[88:89], v11 offset:8192\n"
"v_mfma_f32_16x16x32_bf16 v[172:175], a[244:247], a[28:31], v[172:175]\n"
"ds_read_b64_tr_b16 a[90:91], v11 offset:10240\n"
"v_mfma_f32_16x16x32_bf16 v[172:175], v[208:211], a[64:67], v[172:175]\n"
"s_add_u32 m0, s66, 16384\n"
"ds_read_b64_tr_b16 a[92:93], v12 offset:8192\n"
"v_mfma_f32_16x16x32_bf16 v[168:171], v[204:207], a[64:67], v[168:171]\n"
"s_add_u32 s70, s35, 1280\n"
"ds_read_b64_tr_b16 a[94:95], v12 offset:10240\n"
"v_mfma_f32_16x16x32_bf16 v[168:171], a[240:243], a[28:31], v[168:171]\n"
"s_and_b32 s70, s70, 0x7ff\n"
"buffer_load_dwordx4 v2, s[24:27], s70 offen lds\n"
"v_mfma_f32_16x16x32_bf16 v[164:167], a[236:239], a[28:31], v[164:167]\n"
"ds_read_b64_tr_b16 a[96:97], v13 offset:8192\n"
"v_mfma_f32_16x16x32_bf16 v[164:167], a[252:255], a[64:67], v[164:167]\n"
"ds_read_b64_tr_b16 a[98:99], v13 offset:10240\n"
"v_mfma_f32_16x16x32_bf16 v[160:163], a[248:251], a[64:67], v[160:163]\n"
"ds_read_b64_tr_b16 a[100:101], v14 offset:8192\n"
"v_mfma_f32_16x16x32_bf16 v[160:163], a[232:235], a[28:31], v[160:163]\n"
"ds_read_b64_tr_b16 a[102:103], v14 offset:10240\n"
"v_mfma_f32_16x16x32_bf16 v[176:179], a[232:235], a[32:35], v[176:179]\n"
"buffer_load_dwordx4 v3, s[24:27], s70 offen offset:1024 lds\n"
"v_mfma_f32_16x16x32_bf16 v[176:179], a[248:251], a[68:71], v[176:179]\n"
"v_mfma_f32_16x16x32_bf16 v[180:183], a[252:255], a[68:71], v[180:183]\n"
"v_mfma_f32_16x16x32_bf16 v[180:183], a[236:239], a[32:35], v[180:183]\n"
"v_mfma_f32_16x16x32_bf16 v[184:187], a[240:243], a[32:35], v[184:187]\n"
"v_mfma_f32_16x16x32_bf16 v[184:187], v[204:207], a[68:71], v[184:187]\n"
"buffer_load_dwordx4 v4, s[24:27], s70 offen offset:2048 lds\n"
"v_mfma_f32_16x16x32_bf16 v[188:191], v[208:211], a[68:71], v[188:191]\n"
"v_mfma_f32_16x16x32_bf16 v[188:191], a[244:247], a[32:35], v[188:191]\n"
"s_waitcnt lgkmcnt(5)\n"
"v_mfma_f32_16x16x32_bf16 v[48:51], a[72:75], a[0:3], v[48:51]\n"
"v_mfma_f32_16x16x32_bf16 v[48:51], a[88:91], a[36:39], v[48:51]\n"
"s_waitcnt lgkmcnt(0)\n"
"s_add_u32 m0, s65, 8192\n"
"ds_read_b128 a[52:55], v16 offset:40960\n"
"v_mfma_f32_16x16x32_bf16 v[52:55], a[92:95], a[36:39], v[52:55]\n"
"s_add_u32 s69, s34, 5242880\n"
"ds_read_b128 a[20:23], v15 offset:43008\n"
"v_mfma_f32_16x16x32_bf16 v[52:55], a[76:79], a[0:3], v[52:55]\n"
"s_and_b32 s69, s69, 0x7fffff\n"
"buffer_load_dwordx4 v7, s[28:31], s69 offen lds\n"
"v_mfma_f32_16x16x32_bf16 v[56:59], a[80:83], a[0:3], v[56:59]\n"
"ds_read_b128 a[56:59], v16 offset:43008\n"
"v_mfma_f32_16x16x32_bf16 v[56:59], a[96:99], a[36:39], v[56:59]\n"
"ds_read_b128 a[24:27], v15 offset:45056\n"
"v_mfma_f32_16x16x32_bf16 v[60:63], a[100:103], a[36:39], v[60:63]\n"
"ds_read_b128 a[60:63], v16 offset:45056\n"
"v_mfma_f32_16x16x32_bf16 v[60:63], a[84:87], a[0:3], v[60:63]\n"
"ds_read_b128 a[28:31], v15 offset:47104\n"
"v_mfma_f32_16x16x32_bf16 v[76:79], a[84:87], a[4:7], v[76:79]\n"
"buffer_load_dwordx4 v8, s[28:31], s69 offen offset:1024 lds\n"
"v_mfma_f32_16x16x32_bf16 v[76:79], a[100:103], a[40:43], v[76:79]\n"
"ds_read_b128 a[64:67], v16 offset:47104\n"
"v_mfma_f32_16x16x32_bf16 v[72:75], a[96:99], a[40:43], v[72:75]\n"
"ds_read_b128 a[32:35], v15 offset:49152\n"
"v_mfma_f32_16x16x32_bf16 v[72:75], a[80:83], a[4:7], v[72:75]\n"
"ds_read_b128 a[68:71], v16 offset:49152\n"
"v_mfma_f32_16x16x32_bf16 v[68:71], a[76:79], a[4:7], v[68:71]\n"
"v_mfma_f32_16x16x32_bf16 v[68:71], a[92:95], a[40:43], v[68:71]\n"
"buffer_load_dwordx4 v9, s[28:31], s69 offen offset:2048 lds\n"
"v_mfma_f32_16x16x32_bf16 v[64:67], a[88:91], a[40:43], v[64:67]\n"
"v_mfma_f32_16x16x32_bf16 v[64:67], a[72:75], a[4:7], v[64:67]\n"
"v_mfma_f32_16x16x32_bf16 v[80:83], a[72:75], a[8:11], v[80:83]\n"
"v_mfma_f32_16x16x32_bf16 v[80:83], a[88:91], a[44:47], v[80:83]\n"
"v_mfma_f32_16x16x32_bf16 v[84:87], a[92:95], a[44:47], v[84:87]\n"
"buffer_load_dwordx4 v10, s[28:31], s69 offen offset:3072 lds\n"
"v_mfma_f32_16x16x32_bf16 v[84:87], a[76:79], a[8:11], v[84:87]\n"
"s_waitcnt vmcnt(14)\n"
"ds_read_b64_tr_b16 a[232:233], v11\n"
"v_mfma_f32_16x16x32_bf16 v[88:91], a[80:83], a[8:11], v[88:91]\n"
"ds_read_b64_tr_b16 a[234:235], v11 offset:2048\n"
"v_mfma_f32_16x16x32_bf16 v[88:91], a[96:99], a[44:47], v[88:91]\n"
"s_add_u32 m0, s66, 16384\n"
"ds_read_b64_tr_b16 a[236:237], v12\n"
"v_mfma_f32_16x16x32_bf16 v[92:95], a[100:103], a[44:47], v[92:95]\n"
"s_add_u32 s70, s35, 1280\n"
"ds_read_b64_tr_b16 a[238:239], v12 offset:2048\n"
"v_mfma_f32_16x16x32_bf16 v[92:95], a[84:87], a[8:11], v[92:95]\n"
"s_and_b32 s70, s70, 0x7ff\n"
"buffer_load_dwordx4 v5, s[24:27], s70 offen offset:3072 lds\n"
"v_mfma_f32_16x16x32_bf16 v[108:111], a[84:87], a[12:15], v[108:111]\n"
"ds_read_b64_tr_b16 a[240:241], v13\n"
"v_mfma_f32_16x16x32_bf16 v[108:111], a[100:103], a[48:51], v[108:111]\n"
"ds_read_b64_tr_b16 a[242:243], v13 offset:2048\n"
"v_mfma_f32_16x16x32_bf16 v[104:107], a[96:99], a[48:51], v[104:107]\n"
"s_add_u32 m0, s68, 24576\n"
"s_mul_i32 s71, s23, 6\n"
"ds_read_b64_tr_b16 a[244:245], v14\n"
"v_mfma_f32_16x16x32_bf16 v[104:107], a[80:83], a[12:15], v[104:107]\n"
"s_add_u32 s71, s77, s71\n"
"ds_read_b64_tr_b16 a[246:247], v14 offset:2048\n"
"v_mfma_f32_16x16x32_bf16 v[100:103], a[76:79], a[12:15], v[100:103]\n"
"s_and_b32 s71, s71, 0x1ffff\n"
"buffer_load_dwordx4 v21, s[36:39], s71 offen lds\n"
"v_mfma_f32_16x16x32_bf16 v[100:103], a[92:95], a[48:51], v[100:103]\n"
"v_mfma_f32_16x16x32_bf16 v[96:99], a[88:91], a[48:51], v[96:99]\n"
"v_mfma_f32_16x16x32_bf16 v[96:99], a[72:75], a[12:15], v[96:99]\n"
"v_mfma_f32_16x16x32_bf16 v[112:115], a[72:75], a[16:19], v[112:115]\n"
"s_waitcnt lgkmcnt(5)\n"
"v_mfma_f32_16x16x32_bf16 v[112:115], a[88:91], a[52:55], v[112:115]\n"
"buffer_load_dwordx4 v22, s[36:39], s71 offen offset:1024 lds\n"
"v_mfma_f32_16x16x32_bf16 v[116:119], a[92:95], a[52:55], v[116:119]\n"
"s_add_u32 m0, s67, 4096\n"
"s_add_u32 s70, s35, 1280\n"
"v_mfma_f32_16x16x32_bf16 v[116:119], a[76:79], a[16:19], v[116:119]\n"
"s_and_b32 s70, s70, 0x7ff\n"
"buffer_load_dwordx4 v6, s[24:27], s70 offen lds\n"
"v_mfma_f32_16x16x32_bf16 v[120:123], a[80:83], a[16:19], v[120:123]\n"
"s_waitcnt vmcnt(17)\n"
"s_barrier\n"
"v_mfma_f32_16x16x32_bf16 v[120:123], a[96:99], a[52:55], v[120:123]\n"
"s_waitcnt lgkmcnt(0)\n"
"s_add_u32 m0, s65, 0\n"
"ds_read_b128 a[0:3], v15\n"
"v_mfma_f32_16x16x32_bf16 v[124:127], a[100:103], a[52:55], v[124:127]\n"
"s_add_u32 s69, s34, 5505024\n"
"ds_read_b128 a[36:39], v16\n"
"v_mfma_f32_16x16x32_bf16 v[124:127], a[84:87], a[16:19], v[124:127]\n"
"s_and_b32 s69, s69, 0x7fffff\n"
"buffer_load_dwordx4 v7, s[28:31], s69 offen lds\n"
"v_mfma_f32_16x16x32_bf16 v[140:143], a[84:87], a[20:23], v[140:143]\n"
"ds_read_b128 a[4:7], v15 offset:2048\n"
"v_mfma_f32_16x16x32_bf16 v[140:143], a[100:103], a[56:59], v[140:143]\n"
"ds_read_b128 a[40:43], v16 offset:2048\n"
"v_mfma_f32_16x16x32_bf16 v[136:139], a[96:99], a[56:59], v[136:139]\n"
"ds_read_b128 a[8:11], v15 offset:4096\n"
"v_mfma_f32_16x16x32_bf16 v[136:139], a[80:83], a[20:23], v[136:139]\n"
"ds_read_b128 a[44:47], v16 offset:4096\n"
"v_mfma_f32_16x16x32_bf16 v[132:135], a[76:79], a[20:23], v[132:135]\n"
"buffer_load_dwordx4 v8, s[28:31], s69 offen offset:1024 lds\n"
"v_mfma_f32_16x16x32_bf16 v[132:135], a[92:95], a[56:59], v[132:135]\n"
"ds_read_b128 a[12:15], v15 offset:6144\n"
"v_mfma_f32_16x16x32_bf16 v[128:131], a[88:91], a[56:59], v[128:131]\n"
"ds_read_b128 a[48:51], v16 offset:6144\n"
"v_mfma_f32_16x16x32_bf16 v[128:131], a[72:75], a[20:23], v[128:131]\n"
"ds_read_b128 a[16:19], v15 offset:8192\n"
"v_mfma_f32_16x16x32_bf16 v[144:147], a[72:75], a[24:27], v[144:147]\n"
"v_mfma_f32_16x16x32_bf16 v[144:147], a[88:91], a[60:63], v[144:147]\n"
"buffer_load_dwordx4 v9, s[28:31], s69 offen offset:2048 lds\n"
"v_mfma_f32_16x16x32_bf16 v[148:151], a[92:95], a[60:63], v[148:151]\n"
"v_mfma_f32_16x16x32_bf16 v[148:151], a[76:79], a[24:27], v[148:151]\n"
"v_mfma_f32_16x16x32_bf16 v[152:155], a[80:83], a[24:27], v[152:155]\n"
"v_mfma_f32_16x16x32_bf16 v[152:155], a[96:99], a[60:63], v[152:155]\n"
"v_mfma_f32_16x16x32_bf16 v[156:159], a[100:103], a[60:63], v[156:159]\n"
"buffer_load_dwordx4 v10, s[28:31], s69 offen offset:3072 lds\n"
"v_mfma_f32_16x16x32_bf16 v[156:159], a[84:87], a[24:27], v[156:159]\n"
"s_waitcnt vmcnt(15)\n"
"ds_read_b64_tr_b16 a[248:249], v11 offset:4096\n"
"v_mfma_f32_16x16x32_bf16 v[172:175], a[84:87], a[28:31], v[172:175]\n"
"ds_read_b64_tr_b16 a[250:251], v11 offset:6144\n"
"v_mfma_f32_16x16x32_bf16 v[172:175], a[100:103], a[64:67], v[172:175]\n"
"s_add_u32 m0, s66, 32768\n"
"ds_read_b64_tr_b16 a[252:253], v12 offset:4096\n"
"v_mfma_f32_16x16x32_bf16 v[168:171], a[96:99], a[64:67], v[168:171]\n"
"s_add_u32 s70, s35, 1408\n"
"ds_read_b64_tr_b16 a[254:255], v12 offset:6144\n"
"v_mfma_f32_16x16x32_bf16 v[168:171], a[80:83], a[28:31], v[168:171]\n"
"s_and_b32 s70, s70, 0x7ff\n"
"buffer_load_dwordx4 v2, s[24:27], s70 offen lds\n"
"v_mfma_f32_16x16x32_bf16 v[164:167], a[76:79], a[28:31], v[164:167]\n"
"ds_read_b64_tr_b16 v[204:205], v13 offset:4096\n"
"v_mfma_f32_16x16x32_bf16 v[164:167], a[92:95], a[64:67], v[164:167]\n"
"ds_read_b64_tr_b16 v[206:207], v13 offset:6144\n"
"v_mfma_f32_16x16x32_bf16 v[160:163], a[88:91], a[64:67], v[160:163]\n"
"ds_read_b64_tr_b16 v[208:209], v14 offset:4096\n"
"v_mfma_f32_16x16x32_bf16 v[160:163], a[72:75], a[28:31], v[160:163]\n"
"ds_read_b64_tr_b16 v[210:211], v14 offset:6144\n"
"v_mfma_f32_16x16x32_bf16 v[176:179], a[72:75], a[32:35], v[176:179]\n"
"buffer_load_dwordx4 v3, s[24:27], s70 offen offset:1024 lds\n"
"v_mfma_f32_16x16x32_bf16 v[176:179], a[88:91], a[68:71], v[176:179]\n"
"v_mfma_f32_16x16x32_bf16 v[180:183], a[92:95], a[68:71], v[180:183]\n"
"v_mfma_f32_16x16x32_bf16 v[180:183], a[76:79], a[32:35], v[180:183]\n"
"v_mfma_f32_16x16x32_bf16 v[184:187], a[80:83], a[32:35], v[184:187]\n"
"v_mfma_f32_16x16x32_bf16 v[184:187], a[96:99], a[68:71], v[184:187]\n"
"buffer_load_dwordx4 v4, s[24:27], s70 offen offset:2048 lds\n"
"v_mfma_f32_16x16x32_bf16 v[188:191], a[100:103], a[68:71], v[188:191]\n"
"v_mfma_f32_16x16x32_bf16 v[188:191], a[84:87], a[32:35], v[188:191]\n"
"s_waitcnt lgkmcnt(5)\n"
"v_mfma_f32_16x16x32_bf16 v[48:51], a[232:235], a[0:3], v[48:51]\n"
"v_mfma_f32_16x16x32_bf16 v[48:51], a[248:251], a[36:39], v[48:51]\n"
"s_waitcnt lgkmcnt(0)\n"
"s_add_u32 m0, s65, 4096\n"
"ds_read_b128 a[52:55], v16 offset:8192\n"
"v_mfma_f32_16x16x32_bf16 v[52:55], a[252:255], a[36:39], v[52:55]\n"
"s_add_u32 s69, s34, 5767168\n"
"ds_read_b128 a[20:23], v15 offset:10240\n"
"v_mfma_f32_16x16x32_bf16 v[52:55], a[236:239], a[0:3], v[52:55]\n"
"s_and_b32 s69, s69, 0x7fffff\n"
"buffer_load_dwordx4 v7, s[28:31], s69 offen lds\n"
"v_mfma_f32_16x16x32_bf16 v[56:59], a[240:243], a[0:3], v[56:59]\n"
"ds_read_b128 a[56:59], v16 offset:10240\n"
"v_mfma_f32_16x16x32_bf16 v[56:59], v[204:207], a[36:39], v[56:59]\n"
"ds_read_b128 a[24:27], v15 offset:12288\n"
"v_mfma_f32_16x16x32_bf16 v[60:63], v[208:211], a[36:39], v[60:63]\n"
"ds_read_b128 a[60:63], v16 offset:12288\n"
"v_mfma_f32_16x16x32_bf16 v[60:63], a[244:247], a[0:3], v[60:63]\n"
"ds_read_b128 a[28:31], v15 offset:14336\n"
"v_mfma_f32_16x16x32_bf16 v[76:79], a[244:247], a[4:7], v[76:79]\n"
"buffer_load_dwordx4 v8, s[28:31], s69 offen offset:1024 lds\n"
"v_mfma_f32_16x16x32_bf16 v[76:79], v[208:211], a[40:43], v[76:79]\n"
"ds_read_b128 a[64:67], v16 offset:14336\n"
"v_mfma_f32_16x16x32_bf16 v[72:75], v[204:207], a[40:43], v[72:75]\n"
"ds_read_b128 a[32:35], v15 offset:51200\n"
"v_mfma_f32_16x16x32_bf16 v[72:75], a[240:243], a[4:7], v[72:75]\n"
"ds_read_b128 a[68:71], v16 offset:51200\n"
"v_mfma_f32_16x16x32_bf16 v[68:71], a[236:239], a[4:7], v[68:71]\n"
"v_mfma_f32_16x16x32_bf16 v[68:71], a[252:255], a[40:43], v[68:71]\n"
"buffer_load_dwordx4 v9, s[28:31], s69 offen offset:2048 lds\n"
"v_mfma_f32_16x16x32_bf16 v[64:67], a[248:251], a[40:43], v[64:67]\n"
"v_mfma_f32_16x16x32_bf16 v[64:67], a[232:235], a[4:7], v[64:67]\n"
"v_mfma_f32_16x16x32_bf16 v[80:83], a[232:235], a[8:11], v[80:83]\n"
"v_mfma_f32_16x16x32_bf16 v[80:83], a[248:251], a[44:47], v[80:83]\n"
"v_mfma_f32_16x16x32_bf16 v[84:87], a[252:255], a[44:47], v[84:87]\n"
"buffer_load_dwordx4 v10, s[28:31], s69 offen offset:3072 lds\n"
"v_mfma_f32_16x16x32_bf16 v[84:87], a[236:239], a[8:11], v[84:87]\n"
"s_waitcnt vmcnt(15)\n"
"ds_read_b64_tr_b16 a[72:73], v11 offset:8192\n"
"v_mfma_f32_16x16x32_bf16 v[88:91], a[240:243], a[8:11], v[88:91]\n"
"ds_read_b64_tr_b16 a[74:75], v11 offset:10240\n"
"v_mfma_f32_16x16x32_bf16 v[88:91], v[204:207], a[44:47], v[88:91]\n"
"s_add_u32 m0, s66, 32768\n"
"ds_read_b64_tr_b16 a[76:77], v12 offset:8192\n"
"v_mfma_f32_16x16x32_bf16 v[92:95], v[208:211], a[44:47], v[92:95]\n"
"s_add_u32 s70, s35, 1408\n"
"ds_read_b64_tr_b16 a[78:79], v12 offset:10240\n"
"v_mfma_f32_16x16x32_bf16 v[92:95], a[244:247], a[8:11], v[92:95]\n"
"s_and_b32 s70, s70, 0x7ff\n"
"buffer_load_dwordx4 v5, s[24:27], s70 offen offset:3072 lds\n"
"v_mfma_f32_16x16x32_bf16 v[108:111], a[244:247], a[12:15], v[108:111]\n"
"ds_read_b64_tr_b16 a[80:81], v13 offset:8192\n"
"v_mfma_f32_16x16x32_bf16 v[108:111], v[208:211], a[48:51], v[108:111]\n"
"ds_read_b64_tr_b16 a[82:83], v13 offset:10240\n"
"v_mfma_f32_16x16x32_bf16 v[104:107], v[204:207], a[48:51], v[104:107]\n"
"s_mul_i32 s71, s23, 6\n"
"ds_read_b64_tr_b16 a[84:85], v14 offset:8192\n"
"v_mfma_f32_16x16x32_bf16 v[104:107], a[240:243], a[12:15], v[104:107]\n"
"s_add_u32 s71, s77, s71\n"
"ds_read_b64_tr_b16 a[86:87], v14 offset:10240\n"
"v_mfma_f32_16x16x32_bf16 v[100:103], a[236:239], a[12:15], v[100:103]\n"
"s_and_b32 s71, s71, 0x1ffff\n"
"buffer_load_dwordx4 v[220:223], v23, s[40:43], s71 offen\n"
"v_mfma_f32_16x16x32_bf16 v[100:103], a[252:255], a[48:51], v[100:103]\n"
"v_mfma_f32_16x16x32_bf16 v[96:99], a[248:251], a[48:51], v[96:99]\n"
"v_mfma_f32_16x16x32_bf16 v[96:99], a[232:235], a[12:15], v[96:99]\n"
"v_mfma_f32_16x16x32_bf16 v[112:115], a[232:235], a[16:19], v[112:115]\n"
"s_waitcnt lgkmcnt(6)\n"
"v_mfma_f32_16x16x32_bf16 v[112:115], a[248:251], a[52:55], v[112:115]\n"
"buffer_load_dwordx4 v[224:227], v24, s[40:43], s71 offen offset:1024\n"
"v_mfma_f32_16x16x32_bf16 v[116:119], a[252:255], a[52:55], v[116:119]\n"
"v_mfma_f32_16x16x32_bf16 v[116:119], a[236:239], a[16:19], v[116:119]\n"
"v_mfma_f32_16x16x32_bf16 v[120:123], a[240:243], a[16:19], v[120:123]\n"
"s_waitcnt vmcnt(14)\n"
"s_barrier\n"
"v_mfma_f32_16x16x32_bf16 v[120:123], v[204:207], a[52:55], v[120:123]\n"
"s_waitcnt lgkmcnt(0)\n"
"s_add_u32 m0, s65, 8192\n"
"ds_read_b128 a[0:3], v15 offset:16384\n"
"v_mfma_f32_16x16x32_bf16 v[124:127], v[208:211], a[52:55], v[124:127]\n"
"s_add_u32 s69, s34, 6029312\n"
"ds_read_b128 a[36:39], v16 offset:16384\n"
"v_mfma_f32_16x16x32_bf16 v[124:127], a[244:247], a[16:19], v[124:127]\n"
"s_and_b32 s69, s69, 0x7fffff\n"
"buffer_load_dwordx4 v7, s[28:31], s69 offen lds\n"
"v_mfma_f32_16x16x32_bf16 v[140:143], a[244:247], a[20:23], v[140:143]\n"
"ds_read_b128 a[4:7], v15 offset:18432\n"
"v_mfma_f32_16x16x32_bf16 v[140:143], v[208:211], a[56:59], v[140:143]\n"
"ds_read_b128 a[40:43], v16 offset:18432\n"
"v_mfma_f32_16x16x32_bf16 v[136:139], v[204:207], a[56:59], v[136:139]\n"
"ds_read_b128 a[8:11], v15 offset:20480\n"
"v_mfma_f32_16x16x32_bf16 v[136:139], a[240:243], a[20:23], v[136:139]\n"
"ds_read_b128 a[44:47], v16 offset:20480\n"
"v_mfma_f32_16x16x32_bf16 v[132:135], a[236:239], a[20:23], v[132:135]\n"
"buffer_load_dwordx4 v8, s[28:31], s69 offen offset:1024 lds\n"
"v_mfma_f32_16x16x32_bf16 v[132:135], a[252:255], a[56:59], v[132:135]\n"
"ds_read_b128 a[12:15], v15 offset:22528\n"
"v_mfma_f32_16x16x32_bf16 v[128:131], a[248:251], a[56:59], v[128:131]\n"
"ds_read_b128 a[48:51], v16 offset:22528\n"
"v_mfma_f32_16x16x32_bf16 v[128:131], a[232:235], a[20:23], v[128:131]\n"
"ds_read_b128 a[16:19], v15 offset:24576\n"
"v_mfma_f32_16x16x32_bf16 v[144:147], a[232:235], a[24:27], v[144:147]\n"
"v_mfma_f32_16x16x32_bf16 v[144:147], a[248:251], a[60:63], v[144:147]\n"
"buffer_load_dwordx4 v9, s[28:31], s69 offen offset:2048 lds\n"
"v_mfma_f32_16x16x32_bf16 v[148:151], a[252:255], a[60:63], v[148:151]\n"
"v_mfma_f32_16x16x32_bf16 v[148:151], a[236:239], a[24:27], v[148:151]\n"
"v_mfma_f32_16x16x32_bf16 v[152:155], a[240:243], a[24:27], v[152:155]\n"
"v_mfma_f32_16x16x32_bf16 v[152:155], v[204:207], a[60:63], v[152:155]\n"
"v_mfma_f32_16x16x32_bf16 v[156:159], v[208:211], a[60:63], v[156:159]\n"
"buffer_load_dwordx4 v10, s[28:31], s69 offen offset:3072 lds\n"
"v_mfma_f32_16x16x32_bf16 v[156:159], a[244:247], a[24:27], v[156:159]\n"
"s_waitcnt vmcnt(14)\n"
"ds_read_b64_tr_b16 a[88:89], v11\n"
"v_mfma_f32_16x16x32_bf16 v[172:175], a[244:247], a[28:31], v[172:175]\n"
"ds_read_b64_tr_b16 a[90:91], v11 offset:2048\n"
"v_mfma_f32_16x16x32_bf16 v[172:175], v[208:211], a[64:67], v[172:175]\n"
"s_add_u32 m0, s66, 0\n"
"ds_read_b64_tr_b16 a[92:93], v12\n"
"v_mfma_f32_16x16x32_bf16 v[168:171], v[204:207], a[64:67], v[168:171]\n"
"s_add_u32 s70, s35, 1536\n"
"ds_read_b64_tr_b16 a[94:95], v12 offset:2048\n"
"v_mfma_f32_16x16x32_bf16 v[168:171], a[240:243], a[28:31], v[168:171]\n"
"s_and_b32 s70, s70, 0x7ff\n"
"buffer_load_dwordx4 v2, s[24:27], s70 offen lds\n"
"v_mfma_f32_16x16x32_bf16 v[164:167], a[236:239], a[28:31], v[164:167]\n"
"ds_read_b64_tr_b16 a[96:97], v13\n"
"v_mfma_f32_16x16x32_bf16 v[164:167], a[252:255], a[64:67], v[164:167]\n"
"ds_read_b64_tr_b16 a[98:99], v13 offset:2048\n"
"v_mfma_f32_16x16x32_bf16 v[160:163], a[248:251], a[64:67], v[160:163]\n"
"ds_read_b64_tr_b16 a[100:101], v14\n"
"v_mfma_f32_16x16x32_bf16 v[160:163], a[232:235], a[28:31], v[160:163]\n"
"ds_read_b64_tr_b16 a[102:103], v14 offset:2048\n"
"v_mfma_f32_16x16x32_bf16 v[176:179], a[232:235], a[32:35], v[176:179]\n"
"buffer_load_dwordx4 v3, s[24:27], s70 offen offset:1024 lds\n"
"v_mfma_f32_16x16x32_bf16 v[176:179], a[248:251], a[68:71], v[176:179]\n"
"v_mfma_f32_16x16x32_bf16 v[180:183], a[252:255], a[68:71], v[180:183]\n"
"v_mfma_f32_16x16x32_bf16 v[180:183], a[236:239], a[32:35], v[180:183]\n"
"v_mfma_f32_16x16x32_bf16 v[184:187], a[240:243], a[32:35], v[184:187]\n"
"v_mfma_f32_16x16x32_bf16 v[184:187], v[204:207], a[68:71], v[184:187]\n"
"buffer_load_dwordx4 v4, s[24:27], s70 offen offset:2048 lds\n"
"v_mfma_f32_16x16x32_bf16 v[188:191], v[208:211], a[68:71], v[188:191]\n"
"v_mfma_f32_16x16x32_bf16 v[188:191], a[244:247], a[32:35], v[188:191]\n"
"s_waitcnt lgkmcnt(5)\n"
"v_mfma_f32_16x16x32_bf16 v[48:51], a[72:75], a[0:3], v[48:51]\n"
"v_mfma_f32_16x16x32_bf16 v[48:51], a[88:91], a[36:39], v[48:51]\n"
"s_waitcnt lgkmcnt(0)\n"
"s_add_u32 m0, s65, 0\n"
"ds_read_b128 a[52:55], v16 offset:24576\n"
"v_mfma_f32_16x16x32_bf16 v[52:55], a[92:95], a[36:39], v[52:55]\n"
"s_add_u32 s69, s34, 6291456\n"
"ds_read_b128 a[20:23], v15 offset:26624\n"
"v_mfma_f32_16x16x32_bf16 v[52:55], a[76:79], a[0:3], v[52:55]\n"
"s_and_b32 s69, s69, 0x7fffff\n"
"buffer_load_dwordx4 v7, s[28:31], s69 offen lds\n"
"v_mfma_f32_16x16x32_bf16 v[56:59], a[80:83], a[0:3], v[56:59]\n"
"ds_read_b128 a[56:59], v16 offset:26624\n"
"v_mfma_f32_16x16x32_bf16 v[56:59], a[96:99], a[36:39], v[56:59]\n"
"ds_read_b128 a[24:27], v15 offset:28672\n"
"v_mfma_f32_16x16x32_bf16 v[60:63], a[100:103], a[36:39], v[60:63]\n"
"ds_read_b128 a[60:63], v16 offset:28672\n"
"v_mfma_f32_16x16x32_bf16 v[60:63], a[84:87], a[0:3], v[60:63]\n"
"ds_read_b128 a[28:31], v15 offset:30720\n"
"v_mfma_f32_16x16x32_bf16 v[76:79], a[84:87], a[4:7], v[76:79]\n"
"buffer_load_dwordx4 v8, s[28:31], s69 offen offset:1024 lds\n"
"v_mfma_f32_16x16x32_bf16 v[76:79], a[100:103], a[40:43], v[76:79]\n"
"ds_read_b128 a[64:67], v16 offset:30720\n"
"v_mfma_f32_16x16x32_bf16 v[72:75], a[96:99], a[40:43], v[72:75]\n"
"ds_read_b128 a[32:35], v15 offset:53248\n"
"v_mfma_f32_16x16x32_bf16 v[72:75], a[80:83], a[4:7], v[72:75]\n"
"ds_read_b128 a[68:71], v16 offset:53248\n"
"v_mfma_f32_16x16x32_bf16 v[68:71], a[76:79], a[4:7], v[68:71]\n"
"v_mfma_f32_16x16x32_bf16 v[68:71], a[92:95], a[40:43], v[68:71]\n"
"buffer_load_dwordx4 v9, s[28:31], s69 offen offset:2048 lds\n"
"v_mfma_f32_16x16x32_bf16 v[64:67], a[88:91], a[40:43], v[64:67]\n"
"v_mfma_f32_16x16x32_bf16 v[64:67], a[72:75], a[4:7], v[64:67]\n"
"v_mfma_f32_16x16x32_bf16 v[80:83], a[72:75], a[8:11], v[80:83]\n"
"v_mfma_f32_16x16x32_bf16 v[80:83], a[88:91], a[44:47], v[80:83]\n"
"v_mfma_f32_16x16x32_bf16 v[84:87], a[92:95], a[44:47], v[84:87]\n"
"buffer_load_dwordx4 v10, s[28:31], s69 offen offset:3072 lds\n"
"v_mfma_f32_16x16x32_bf16 v[84:87], a[76:79], a[8:11], v[84:87]\n"
"s_waitcnt vmcnt(14)\n"
"ds_read_b64_tr_b16 a[232:233], v11 offset:4096\n"
"v_mfma_f32_16x16x32_bf16 v[88:91], a[80:83], a[8:11], v[88:91]\n"
"ds_read_b64_tr_b16 a[234:235], v11 offset:6144\n"
"v_mfma_f32_16x16x32_bf16 v[88:91], a[96:99], a[44:47], v[88:91]\n"
"s_add_u32 m0, s66, 0\n"
"ds_read_b64_tr_b16 a[236:237], v12 offset:4096\n"
"v_mfma_f32_16x16x32_bf16 v[92:95], a[100:103], a[44:47], v[92:95]\n"
"s_add_u32 s70, s35, 1536\n"
"ds_read_b64_tr_b16 a[238:239], v12 offset:6144\n"
"v_mfma_f32_16x16x32_bf16 v[92:95], a[84:87], a[8:11], v[92:95]\n"
"s_and_b32 s70, s70, 0x7ff\n"
"buffer_load_dwordx4 v5, s[24:27], s70 offen offset:3072 lds\n"
"v_mfma_f32_16x16x32_bf16 v[108:111], a[84:87], a[12:15], v[108:111]\n"
"ds_read_b64_tr_b16 a[240:241], v13 offset:4096\n"
"v_mfma_f32_16x16x32_bf16 v[108:111], a[100:103], a[48:51], v[108:111]\n"
"ds_read_b64_tr_b16 a[242:243], v13 offset:6144\n"
"v_mfma_f32_16x16x32_bf16 v[104:107], a[96:99], a[48:51], v[104:107]\n"
"s_mul_i32 s71, s23, 8\n"
"ds_read_b64_tr_b16 a[244:245], v14 offset:4096\n"
"v_mfma_f32_16x16x32_bf16 v[104:107], a[80:83], a[12:15], v[104:107]\n"
"s_add_u32 s71, s77, s71\n"
"ds_read_b64_tr_b16 a[246:247], v14 offset:6144\n"
"v_mfma_f32_16x16x32_bf16 v[100:103], a[76:79], a[12:15], v[100:103]\n"
"s_and_b32 s71, s71, 0x1ffff\n"
"buffer_load_dwordx4 v[228:231], v23, s[40:43], s71 offen\n"
"v_mfma_f32_16x16x32_bf16 v[100:103], a[92:95], a[48:51], v[100:103]\n"
"v_mfma_f32_16x16x32_bf16 v[96:99], a[88:91], a[48:51], v[96:99]\n"
"v_mfma_f32_16x16x32_bf16 v[96:99], a[72:75], a[12:15], v[96:99]\n"
"v_mfma_f32_16x16x32_bf16 v[112:115], a[72:75], a[16:19], v[112:115]\n"
"s_waitcnt lgkmcnt(6)\n"
"v_mfma_f32_16x16x32_bf16 v[112:115], a[88:91], a[52:55], v[112:115]\n"
"buffer_load_dwordx4 v[232:235], v24, s[40:43], s71 offen offset:1024\n"
"v_mfma_f32_16x16x32_bf16 v[116:119], a[92:95], a[52:55], v[116:119]\n"
"s_add_u32 m0, s67, 0\n"
"s_add_u32 s70, s35, 1536\n"
"v_mfma_f32_16x16x32_bf16 v[116:119], a[76:79], a[16:19], v[116:119]\n"
"s_and_b32 s70, s70, 0x7ff\n"
"buffer_load_dwordx4 v6, s[24:27], s70 offen lds\n"
"v_mfma_f32_16x16x32_bf16 v[120:123], a[80:83], a[16:19], v[120:123]\n"
"s_waitcnt vmcnt(17)\n"
"s_barrier\n"
"v_mfma_f32_16x16x32_bf16 v[120:123], a[96:99], a[52:55], v[120:123]\n"
"s_waitcnt lgkmcnt(0)\n"
"s_add_u32 m0, s65, 4096\n"
"ds_read_b128 a[0:3], v15 offset:32768\n"
"v_mfma_f32_16x16x32_bf16 v[124:127], a[100:103], a[52:55], v[124:127]\n"
"s_add_u32 s69, s34, 6553600\n"
"ds_read_b128 a[36:39], v16 offset:32768\n"
"v_mfma_f32_16x16x32_bf16 v[124:127], a[84:87], a[16:19], v[124:127]\n"
"s_and_b32 s69, s69, 0x7fffff\n"
"buffer_load_dwordx4 v7, s[28:31], s69 offen lds\n"
"v_mfma_f32_16x16x32_bf16 v[140:143], a[84:87], a[20:23], v[140:143]\n"
"ds_read_b128 a[4:7], v15 offset:34816\n"
"v_mfma_f32_16x16x32_bf16 v[140:143], a[100:103], a[56:59], v[140:143]\n"
"ds_read_b128 a[40:43], v16 offset:34816\n"
"v_mfma_f32_16x16x32_bf16 v[136:139], a[96:99], a[56:59], v[136:139]\n"
"ds_read_b128 a[8:11], v15 offset:36864\n"
"v_mfma_f32_16x16x32_bf16 v[136:139], a[80:83], a[20:23], v[136:139]\n"
"ds_read_b128 a[44:47], v16 offset:36864\n"
"v_mfma_f32_16x16x32_bf16 v[132:135], a[76:79], a[20:23], v[132:135]\n"
"buffer_load_dwordx4 v8, s[28:31], s69 offen offset:1024 lds\n"
"v_mfma_f32_16x16x32_bf16 v[132:135], a[92:95], a[56:59], v[132:135]\n"
"ds_read_b128 a[12:15], v15 offset:38912\n"
"v_mfma_f32_16x16x32_bf16 v[128:131], a[88:91], a[56:59], v[128:131]\n"
"ds_read_b128 a[48:51], v16 offset:38912\n"
"v_mfma_f32_16x16x32_bf16 v[128:131], a[72:75], a[20:23], v[128:131]\n"
"ds_read_b128 a[16:19], v15 offset:40960\n"
"v_mfma_f32_16x16x32_bf16 v[144:147], a[72:75], a[24:27], v[144:147]\n"
"v_mfma_f32_16x16x32_bf16 v[144:147], a[88:91], a[60:63], v[144:147]\n"
"buffer_load_dwordx4 v9, s[28:31], s69 offen offset:2048 lds\n"
"v_mfma_f32_16x16x32_bf16 v[148:151], a[92:95], a[60:63], v[148:151]\n"
"v_mfma_f32_16x16x32_bf16 v[148:151], a[76:79], a[24:27], v[148:151]\n"
"v_mfma_f32_16x16x32_bf16 v[152:155], a[80:83], a[24:27], v[152:155]\n"
"v_mfma_f32_16x16x32_bf16 v[152:155], a[96:99], a[60:63], v[152:155]\n"
"v_mfma_f32_16x16x32_bf16 v[156:159], a[100:103], a[60:63], v[156:159]\n"
"buffer_load_dwordx4 v10, s[28:31], s69 offen offset:3072 lds\n"
"v_mfma_f32_16x16x32_bf16 v[156:159], a[84:87], a[24:27], v[156:159]\n"
"s_waitcnt vmcnt(15)\n"
"ds_read_b64_tr_b16 a[248:249], v11 offset:8192\n"
"v_mfma_f32_16x16x32_bf16 v[172:175], a[84:87], a[28:31], v[172:175]\n"
"ds_read_b64_tr_b16 a[250:251], v11 offset:10240\n"
"v_mfma_f32_16x16x32_bf16 v[172:175], a[100:103], a[64:67], v[172:175]\n"
"s_add_u32 m0, s66, 16384\n"
"ds_read_b64_tr_b16 a[252:253], v12 offset:8192\n"
"v_mfma_f32_16x16x32_bf16 v[168:171], a[96:99], a[64:67], v[168:171]\n"
"s_add_u32 s70, s35, 1664\n"
"ds_read_b64_tr_b16 a[254:255], v12 offset:10240\n"
"v_mfma_f32_16x16x32_bf16 v[168:171], a[80:83], a[28:31], v[168:171]\n"
"s_and_b32 s70, s70, 0x7ff\n"
"buffer_load_dwordx4 v2, s[24:27], s70 offen lds\n"
"v_mfma_f32_16x16x32_bf16 v[164:167], a[76:79], a[28:31], v[164:167]\n"
"ds_read_b64_tr_b16 v[204:205], v13 offset:8192\n"
"v_mfma_f32_16x16x32_bf16 v[164:167], a[92:95], a[64:67], v[164:167]\n"
"ds_read_b64_tr_b16 v[206:207], v13 offset:10240\n"
"v_mfma_f32_16x16x32_bf16 v[160:163], a[88:91], a[64:67], v[160:163]\n"
"ds_read_b64_tr_b16 v[208:209], v14 offset:8192\n"
"v_mfma_f32_16x16x32_bf16 v[160:163], a[72:75], a[28:31], v[160:163]\n"
"ds_read_b64_tr_b16 v[210:211], v14 offset:10240\n"
"v_mfma_f32_16x16x32_bf16 v[176:179], a[72:75], a[32:35], v[176:179]\n"
"buffer_load_dwordx4 v3, s[24:27], s70 offen offset:1024 lds\n"
"v_mfma_f32_16x16x32_bf16 v[176:179], a[88:91], a[68:71], v[176:179]\n"
"v_mfma_f32_16x16x32_bf16 v[180:183], a[92:95], a[68:71], v[180:183]\n"
"v_mfma_f32_16x16x32_bf16 v[180:183], a[76:79], a[32:35], v[180:183]\n"
"v_mfma_f32_16x16x32_bf16 v[184:187], a[80:83], a[32:35], v[184:187]\n"
"v_mfma_f32_16x16x32_bf16 v[184:187], a[96:99], a[68:71], v[184:187]\n"
"buffer_load_dwordx4 v4, s[24:27], s70 offen offset:2048 lds\n"
"v_mfma_f32_16x16x32_bf16 v[188:191], a[100:103], a[68:71], v[188:191]\n"
"v_mfma_f32_16x16x32_bf16 v[188:191], a[84:87], a[32:35], v[188:191]\n"
"s_waitcnt lgkmcnt(5)\n"
"v_mfma_f32_16x16x32_bf16 v[48:51], a[232:235], a[0:3], v[48:51]\n"
"v_mfma_f32_16x16x32_bf16 v[48:51], a[248:251], a[36:39], v[48:51]\n"
"s_waitcnt lgkmcnt(0)\n"
"s_add_u32 m0, s65, 8192\n"
"ds_read_b128 a[52:55], v16 offset:40960\n"
"v_mfma_f32_16x16x32_bf16 v[52:55], a[252:255], a[36:39], v[52:55]\n"
"s_add_u32 s69, s34, 6815744\n"
"ds_read_b128 a[20:23], v15 offset:43008\n"
"v_mfma_f32_16x16x32_bf16 v[52:55], a[236:239], a[0:3], v[52:55]\n"
"s_and_b32 s69, s69, 0x7fffff\n"
"buffer_load_dwordx4 v7, s[28:31], s69 offen lds\n"
"v_mfma_f32_16x16x32_bf16 v[56:59], a[240:243], a[0:3], v[56:59]\n"
"ds_read_b128 a[56:59], v16 offset:43008\n"
"v_mfma_f32_16x16x32_bf16 v[56:59], v[204:207], a[36:39], v[56:59]\n"
"ds_read_b128 a[24:27], v15 offset:45056\n"
"v_mfma_f32_16x16x32_bf16 v[60:63], v[208:211], a[36:39], v[60:63]\n"
"ds_read_b128 a[60:63], v16 offset:45056\n"
"v_mfma_f32_16x16x32_bf16 v[60:63], a[244:247], a[0:3], v[60:63]\n"
"ds_read_b128 a[28:31], v15 offset:47104\n"
"v_mfma_f32_16x16x32_bf16 v[76:79], a[244:247], a[4:7], v[76:79]\n"
"buffer_load_dwordx4 v8, s[28:31], s69 offen offset:1024 lds\n"
"v_mfma_f32_16x16x32_bf16 v[76:79], v[208:211], a[40:43], v[76:79]\n"
"ds_read_b128 a[64:67], v16 offset:47104\n"
"v_mfma_f32_16x16x32_bf16 v[72:75], v[204:207], a[40:43], v[72:75]\n"
"ds_read_b128 a[32:35], v15 offset:55296\n"
"v_mfma_f32_16x16x32_bf16 v[72:75], a[240:243], a[4:7], v[72:75]\n"
"ds_read_b128 a[68:71], v16 offset:55296\n"
"v_mfma_f32_16x16x32_bf16 v[68:71], a[236:239], a[4:7], v[68:71]\n"
"v_mfma_f32_16x16x32_bf16 v[68:71], a[252:255], a[40:43], v[68:71]\n"
"buffer_load_dwordx4 v9, s[28:31], s69 offen offset:2048 lds\n"
"v_mfma_f32_16x16x32_bf16 v[64:67], a[248:251], a[40:43], v[64:67]\n"
"v_mfma_f32_16x16x32_bf16 v[64:67], a[232:235], a[4:7], v[64:67]\n"
"v_mfma_f32_16x16x32_bf16 v[80:83], a[232:235], a[8:11], v[80:83]\n"
"v_mfma_f32_16x16x32_bf16 v[80:83], a[248:251], a[44:47], v[80:83]\n"
"v_mfma_f32_16x16x32_bf16 v[84:87], a[252:255], a[44:47], v[84:87]\n"
"buffer_load_dwordx4 v10, s[28:31], s69 offen offset:3072 lds\n"
"v_mfma_f32_16x16x32_bf16 v[84:87], a[236:239], a[8:11], v[84:87]\n"
"s_waitcnt vmcnt(15)\n"
"ds_read_b64_tr_b16 a[72:73], v11\n"
"v_mfma_f32_16x16x32_bf16 v[88:91], a[240:243], a[8:11], v[88:91]\n"
"ds_read_b64_tr_b16 a[74:75], v11 offset:2048\n"
"v_mfma_f32_16x16x32_bf16 v[88:91], v[204:207], a[44:47], v[88:91]\n"
"s_add_u32 m0, s66, 16384\n"
"ds_read_b64_tr_b16 a[76:77], v12\n"
"v_mfma_f32_16x16x32_bf16 v[92:95], v[208:211], a[44:47], v[92:95]\n"
"s_add_u32 s70, s35, 1664\n"
"ds_read_b64_tr_b16 a[78:79], v12 offset:2048\n"
"v_mfma_f32_16x16x32_bf16 v[92:95], a[244:247], a[8:11], v[92:95]\n"
"s_and_b32 s70, s70, 0x7ff\n"
"buffer_load_dwordx4 v5, s[24:27], s70 offen offset:3072 lds\n"
"v_mfma_f32_16x16x32_bf16 v[108:111], a[244:247], a[12:15], v[108:111]\n"
"ds_read_b64_tr_b16 a[80:81], v13\n"
"v_mfma_f32_16x16x32_bf16 v[108:111], v[208:211], a[48:51], v[108:111]\n"
"ds_read_b64_tr_b16 a[82:83], v13 offset:2048\n"
"v_mfma_f32_16x16x32_bf16 v[104:107], v[204:207], a[48:51], v[104:107]\n"
"s_mul_i32 s71, s23, 10\n"
"ds_read_b64_tr_b16 a[84:85], v14\n"
"v_mfma_f32_16x16x32_bf16 v[104:107], a[240:243], a[12:15], v[104:107]\n"
"s_add_u32 s71, s77, s71\n"
"ds_read_b64_tr_b16 a[86:87], v14 offset:2048\n"
"v_mfma_f32_16x16x32_bf16 v[100:103], a[236:239], a[12:15], v[100:103]\n"
"s_and_b32 s71, s71, 0x1ffff\n"
"buffer_load_dwordx4 v[236:239], v23, s[40:43], s71 offen\n"
"v_mfma_f32_16x16x32_bf16 v[100:103], a[252:255], a[48:51], v[100:103]\n"
"v_mfma_f32_16x16x32_bf16 v[96:99], a[248:251], a[48:51], v[96:99]\n"
"v_mfma_f32_16x16x32_bf16 v[96:99], a[232:235], a[12:15], v[96:99]\n"
"v_mfma_f32_16x16x32_bf16 v[112:115], a[232:235], a[16:19], v[112:115]\n"
"s_waitcnt lgkmcnt(6)\n"
"v_mfma_f32_16x16x32_bf16 v[112:115], a[248:251], a[52:55], v[112:115]\n"
"buffer_load_dwordx4 v[240:243], v24, s[40:43], s71 offen offset:1024\n"
"v_mfma_f32_16x16x32_bf16 v[116:119], a[252:255], a[52:55], v[116:119]\n"
"v_mfma_f32_16x16x32_bf16 v[116:119], a[236:239], a[16:19], v[116:119]\n"
"v_mfma_f32_16x16x32_bf16 v[120:123], a[240:243], a[16:19], v[120:123]\n"
"s_waitcnt vmcnt(14)\n"
"s_barrier\n"
"v_mfma_f32_16x16x32_bf16 v[120:123], v[204:207], a[52:55], v[120:123]\n"
"s_waitcnt lgkmcnt(0)\n"
"s_add_u32 m0, s65, 0\n"
"ds_read_b128 a[0:3], v15\n"
"v_mfma_f32_16x16x32_bf16 v[124:127], v[208:211], a[52:55], v[124:127]\n"
"s_add_u32 s69, s34, 7077888\n"
"ds_read_b128 a[36:39], v16\n"
"v_mfma_f32_16x16x32_bf16 v[124:127], a[244:247], a[16:19], v[124:127]\n"
"s_and_b32 s69, s69, 0x7fffff\n"
"buffer_load_dwordx4 v7, s[28:31], s69 offen lds\n"
"v_mfma_f32_16x16x32_bf16 v[140:143], a[244:247], a[20:23], v[140:143]\n"
"ds_read_b128 a[4:7], v15 offset:2048\n"
"v_mfma_f32_16x16x32_bf16 v[140:143], v[208:211], a[56:59], v[140:143]\n"
"ds_read_b128 a[40:43], v16 offset:2048\n"
"v_mfma_f32_16x16x32_bf16 v[136:139], v[204:207], a[56:59], v[136:139]\n"
"ds_read_b128 a[8:11], v15 offset:4096\n"
"v_mfma_f32_16x16x32_bf16 v[136:139], a[240:243], a[20:23], v[136:139]\n"
"ds_read_b128 a[44:47], v16 offset:4096\n"
"v_mfma_f32_16x16x32_bf16 v[132:135], a[236:239], a[20:23], v[132:135]\n"
"buffer_load_dwordx4 v8, s[28:31], s69 offen offset:1024 lds\n"
"v_mfma_f32_16x16x32_bf16 v[132:135], a[252:255], a[56:59], v[132:135]\n"
"ds_read_b128 a[12:15], v15 offset:6144\n"
"v_mfma_f32_16x16x32_bf16 v[128:131], a[248:251], a[56:59], v[128:131]\n"
"ds_read_b128 a[48:51], v16 offset:6144\n"
"v_mfma_f32_16x16x32_bf16 v[128:131], a[232:235], a[20:23], v[128:131]\n"
"ds_read_b128 a[16:19], v15 offset:8192\n"
"v_mfma_f32_16x16x32_bf16 v[144:147], a[232:235], a[24:27], v[144:147]\n"
"v_mfma_f32_16x16x32_bf16 v[144:147], a[248:251], a[60:63], v[144:147]\n"
"buffer_load_dwordx4 v9, s[28:31], s69 offen offset:2048 lds\n"
"v_mfma_f32_16x16x32_bf16 v[148:151], a[252:255], a[60:63], v[148:151]\n"
"v_mfma_f32_16x16x32_bf16 v[148:151], a[236:239], a[24:27], v[148:151]\n"
"v_mfma_f32_16x16x32_bf16 v[152:155], a[240:243], a[24:27], v[152:155]\n"
"v_mfma_f32_16x16x32_bf16 v[152:155], v[204:207], a[60:63], v[152:155]\n"
"v_mfma_f32_16x16x32_bf16 v[156:159], v[208:211], a[60:63], v[156:159]\n"
"buffer_load_dwordx4 v10, s[28:31], s69 offen offset:3072 lds\n"
"v_mfma_f32_16x16x32_bf16 v[156:159], a[244:247], a[24:27], v[156:159]\n"
"s_waitcnt vmcnt(14)\n"
"ds_read_b64_tr_b16 a[88:89], v11 offset:4096\n"
"v_mfma_f32_16x16x32_bf16 v[172:175], a[244:247], a[28:31], v[172:175]\n"
"ds_read_b64_tr_b16 a[90:91], v11 offset:6144\n"
"v_mfma_f32_16x16x32_bf16 v[172:175], v[208:211], a[64:67], v[172:175]\n"
"s_add_u32 m0, s66, 32768\n"
"ds_read_b64_tr_b16 a[92:93], v12 offset:4096\n"
"v_mfma_f32_16x16x32_bf16 v[168:171], v[204:207], a[64:67], v[168:171]\n"
"s_add_u32 s70, s35, 1792\n"
"ds_read_b64_tr_b16 a[94:95], v12 offset:6144\n"
"v_mfma_f32_16x16x32_bf16 v[168:171], a[240:243], a[28:31], v[168:171]\n"
"s_and_b32 s70, s70, 0x7ff\n"
"buffer_load_dwordx4 v2, s[24:27], s70 offen lds\n"
"v_mfma_f32_16x16x32_bf16 v[164:167], a[236:239], a[28:31], v[164:167]\n"
"ds_read_b64_tr_b16 a[96:97], v13 offset:4096\n"
"v_mfma_f32_16x16x32_bf16 v[164:167], a[252:255], a[64:67], v[164:167]\n"
"ds_read_b64_tr_b16 a[98:99], v13 offset:6144\n"
"v_mfma_f32_16x16x32_bf16 v[160:163], a[248:251], a[64:67], v[160:163]\n"
"ds_read_b64_tr_b16 a[100:101], v14 offset:4096\n"
"v_mfma_f32_16x16x32_bf16 v[160:163], a[232:235], a[28:31], v[160:163]\n"
"ds_read_b64_tr_b16 a[102:103], v14 offset:6144\n"
"v_mfma_f32_16x16x32_bf16 v[176:179], a[232:235], a[32:35], v[176:179]\n"
"buffer_load_dwordx4 v3, s[24:27], s70 offen offset:1024 lds\n"
"v_mfma_f32_16x16x32_bf16 v[176:179], a[248:251], a[68:71], v[176:179]\n"
"v_mfma_f32_16x16x32_bf16 v[180:183], a[252:255], a[68:71], v[180:183]\n"
"v_mfma_f32_16x16x32_bf16 v[180:183], a[236:239], a[32:35], v[180:183]\n"
"v_mfma_f32_16x16x32_bf16 v[184:187], a[240:243], a[32:35], v[184:187]\n"
"v_mfma_f32_16x16x32_bf16 v[184:187], v[204:207], a[68:71], v[184:187]\n"
"buffer_load_dwordx4 v4, s[24:27], s70 offen offset:2048 lds\n"
"v_mfma_f32_16x16x32_bf16 v[188:191], v[208:211], a[68:71], v[188:191]\n"
"v_mfma_f32_16x16x32_bf16 v[188:191], a[244:247], a[32:35], v[188:191]\n"
"s_waitcnt lgkmcnt(5)\n"
"v_mfma_f32_16x16x32_bf16 v[48:51], a[72:75], a[0:3], v[48:51]\n"
"v_mfma_f32_16x16x32_bf16 v[48:51], a[88:91], a[36:39], v[48:51]\n"
"s_waitcnt lgkmcnt(0)\n"
"s_add_u32 m0, s65, 4096\n"
"ds_read_b128 a[52:55], v16 offset:8192\n"
"v_mfma_f32_16x16x32_bf16 v[52:55], a[92:95], a[36:39], v[52:55]\n"
"s_add_u32 s69, s34, 7340032\n"
"ds_read_b128 a[20:23], v15 offset:10240\n"
"v_mfma_f32_16x16x32_bf16 v[52:55], a[76:79], a[0:3], v[52:55]\n"
"s_and_b32 s69, s69, 0x7fffff\n"
"buffer_load_dwordx4 v7, s[28:31], s69 offen lds\n"
"v_mfma_f32_16x16x32_bf16 v[56:59], a[80:83], a[0:3], v[56:59]\n"
"ds_read_b128 a[56:59], v16 offset:10240\n"
"v_mfma_f32_16x16x32_bf16 v[56:59], a[96:99], a[36:39], v[56:59]\n"
"ds_read_b128 a[24:27], v15 offset:12288\n"
"v_mfma_f32_16x16x32_bf16 v[60:63], a[100:103], a[36:39], v[60:63]\n"
"ds_read_b128 a[60:63], v16 offset:12288\n"
"v_mfma_f32_16x16x32_bf16 v[60:63], a[84:87], a[0:3], v[60:63]\n"
"ds_read_b128 a[28:31], v15 offset:14336\n"
"v_mfma_f32_16x16x32_bf16 v[76:79], a[84:87], a[4:7], v[76:79]\n"
"buffer_load_dwordx4 v8, s[28:31], s69 offen offset:1024 lds\n"
"v_mfma_f32_16x16x32_bf16 v[76:79], a[100:103], a[40:43], v[76:79]\n"
"ds_read_b128 a[64:67], v16 offset:14336\n"
"v_mfma_f32_16x16x32_bf16 v[72:75], a[96:99], a[40:43], v[72:75]\n"
"ds_read_b128 a[32:35], v15 offset:49152\n"
"v_mfma_f32_16x16x32_bf16 v[72:75], a[80:83], a[4:7], v[72:75]\n"
"ds_read_b128 a[68:71], v16 offset:49152\n"
"v_mfma_f32_16x16x32_bf16 v[68:71], a[76:79], a[4:7], v[68:71]\n"
"v_mfma_f32_16x16x32_bf16 v[68:71], a[92:95], a[40:43], v[68:71]\n"
"buffer_load_dwordx4 v9, s[28:31], s69 offen offset:2048 lds\n"
"v_mfma_f32_16x16x32_bf16 v[64:67], a[88:91], a[40:43], v[64:67]\n"
"v_mfma_f32_16x16x32_bf16 v[64:67], a[72:75], a[4:7], v[64:67]\n"
"v_mfma_f32_16x16x32_bf16 v[80:83], a[72:75], a[8:11], v[80:83]\n"
"v_mfma_f32_16x16x32_bf16 v[80:83], a[88:91], a[44:47], v[80:83]\n"
"v_mfma_f32_16x16x32_bf16 v[84:87], a[92:95], a[44:47], v[84:87]\n"
"buffer_load_dwordx4 v10, s[28:31], s69 offen offset:3072 lds\n"
"v_mfma_f32_16x16x32_bf16 v[84:87], a[76:79], a[8:11], v[84:87]\n"
"s_waitcnt vmcnt(14)\n"
"ds_read_b64_tr_b16 a[232:233], v11 offset:8192\n"
"v_mfma_f32_16x16x32_bf16 v[88:91], a[80:83], a[8:11], v[88:91]\n"
"ds_read_b64_tr_b16 a[234:235], v11 offset:10240\n"
"v_mfma_f32_16x16x32_bf16 v[88:91], a[96:99], a[44:47], v[88:91]\n"
"s_add_u32 m0, s66, 32768\n"
"ds_read_b64_tr_b16 a[236:237], v12 offset:8192\n"
"v_mfma_f32_16x16x32_bf16 v[92:95], a[100:103], a[44:47], v[92:95]\n"
"s_add_u32 s70, s35, 1792\n"
"ds_read_b64_tr_b16 a[238:239], v12 offset:10240\n"
"v_mfma_f32_16x16x32_bf16 v[92:95], a[84:87], a[8:11], v[92:95]\n"
"s_and_b32 s70, s70, 0x7ff\n"
"buffer_load_dwordx4 v5, s[24:27], s70 offen offset:3072 lds\n"
"v_mfma_f32_16x16x32_bf16 v[108:111], a[84:87], a[12:15], v[108:111]\n"
"ds_read_b64_tr_b16 a[240:241], v13 offset:8192\n"
"v_mfma_f32_16x16x32_bf16 v[108:111], a[100:103], a[48:51], v[108:111]\n"
"ds_read_b64_tr_b16 a[242:243], v13 offset:10240\n"
"v_mfma_f32_16x16x32_bf16 v[104:107], a[96:99], a[48:51], v[104:107]\n"
"s_mul_i32 s71, s23, 12\n"
"ds_read_b64_tr_b16 a[244:245], v14 offset:8192\n"
"v_mfma_f32_16x16x32_bf16 v[104:107], a[80:83], a[12:15], v[104:107]\n"
"s_add_u32 s71, s77, s71\n"
"ds_read_b64_tr_b16 a[246:247], v14 offset:10240\n"
"v_mfma_f32_16x16x32_bf16 v[100:103], a[76:79], a[12:15], v[100:103]\n"
"s_and_b32 s71, s71, 0x1ffff\n"
"buffer_load_dwordx4 v[244:247], v23, s[40:43], s71 offen nt\n"
"v_mfma_f32_16x16x32_bf16 v[100:103], a[92:95], a[48:51], v[100:103]\n"
"v_mfma_f32_16x16x32_bf16 v[96:99], a[88:91], a[48:51], v[96:99]\n"
"v_mfma_f32_16x16x32_bf16 v[96:99], a[72:75], a[12:15], v[96:99]\n"
"v_mfma_f32_16x16x32_bf16 v[112:115], a[72:75], a[16:19], v[112:115]\n"
"s_waitcnt lgkmcnt(6)\n"
"v_mfma_f32_16x16x32_bf16 v[112:115], a[88:91], a[52:55], v[112:115]\n"
"buffer_load_dwordx4 v[248:251], v24, s[40:43], s71 offen offset:1024 nt\n"
"v_mfma_f32_16x16x32_bf16 v[116:119], a[92:95], a[52:55], v[116:119]\n"
"s_add_u32 m0, s67, 4096\n"
"s_add_u32 s70, s35, 1792\n"
"v_mfma_f32_16x16x32_bf16 v[116:119], a[76:79], a[16:19], v[116:119]\n"
"s_and_b32 s70, s70, 0x7ff\n"
"buffer_load_dwordx4 v6, s[24:27], s70 offen lds\n"
"v_mfma_f32_16x16x32_bf16 v[120:123], a[80:83], a[16:19], v[120:123]\n"
"s_waitcnt vmcnt(17)\n"
"s_barrier\n"
"v_mfma_f32_16x16x32_bf16 v[120:123], a[96:99], a[52:55], v[120:123]\n"
"s_waitcnt lgkmcnt(0)\n"
"s_add_u32 m0, s65, 8192\n"
"ds_read_b128 a[0:3], v15 offset:16384\n"
"v_mfma_f32_16x16x32_bf16 v[124:127], a[100:103], a[52:55], v[124:127]\n"
"s_add_u32 s69, s34, 7602176\n"
"ds_read_b128 a[36:39], v16 offset:16384\n"
"v_mfma_f32_16x16x32_bf16 v[124:127], a[84:87], a[16:19], v[124:127]\n"
"s_and_b32 s69, s69, 0x7fffff\n"
"buffer_load_dwordx4 v7, s[28:31], s69 offen lds\n"
"v_mfma_f32_16x16x32_bf16 v[140:143], a[84:87], a[20:23], v[140:143]\n"
"ds_read_b128 a[4:7], v15 offset:18432\n"
"v_mfma_f32_16x16x32_bf16 v[140:143], a[100:103], a[56:59], v[140:143]\n"
"ds_read_b128 a[40:43], v16 offset:18432\n"
"v_mfma_f32_16x16x32_bf16 v[136:139], a[96:99], a[56:59], v[136:139]\n"
"ds_read_b128 a[8:11], v15 offset:20480\n"
"v_mfma_f32_16x16x32_bf16 v[136:139], a[80:83], a[20:23], v[136:139]\n"
"ds_read_b128 a[44:47], v16 offset:20480\n"
"v_mfma_f32_16x16x32_bf16 v[132:135], a[76:79], a[20:23], v[132:135]\n"
"buffer_load_dwordx4 v8, s[28:31], s69 offen offset:1024 lds\n"
"v_mfma_f32_16x16x32_bf16 v[132:135], a[92:95], a[56:59], v[132:135]\n"
"ds_read_b128 a[12:15], v15 offset:22528\n"
"v_mfma_f32_16x16x32_bf16 v[128:131], a[88:91], a[56:59], v[128:131]\n"
"ds_read_b128 a[48:51], v16 offset:22528\n"
"v_mfma_f32_16x16x32_bf16 v[128:131], a[72:75], a[20:23], v[128:131]\n"
"ds_read_b128 a[16:19], v15 offset:24576\n"
"v_mfma_f32_16x16x32_bf16 v[144:147], a[72:75], a[24:27], v[144:147]\n"
"v_mfma_f32_16x16x32_bf16 v[144:147], a[88:91], a[60:63], v[144:147]\n"
"buffer_load_dwordx4 v9, s[28:31], s69 offen offset:2048 lds\n"
"v_mfma_f32_16x16x32_bf16 v[148:151], a[92:95], a[60:63], v[148:151]\n"
"v_mfma_f32_16x16x32_bf16 v[148:151], a[76:79], a[24:27], v[148:151]\n"
"v_mfma_f32_16x16x32_bf16 v[152:155], a[80:83], a[24:27], v[152:155]\n"
"v_mfma_f32_16x16x32_bf16 v[152:155], a[96:99], a[60:63], v[152:155]\n"
"v_mfma_f32_16x16x32_bf16 v[156:159], a[100:103], a[60:63], v[156:159]\n"
"buffer_load_dwordx4 v10, s[28:31], s69 offen offset:3072 lds\n"
"v_mfma_f32_16x16x32_bf16 v[156:159], a[84:87], a[24:27], v[156:159]\n"
"s_waitcnt vmcnt(15)\n"
"ds_read_b64_tr_b16 a[248:249], v11\n"
"v_mfma_f32_16x16x32_bf16 v[172:175], a[84:87], a[28:31], v[172:175]\n"
"ds_read_b64_tr_b16 a[250:251], v11 offset:2048\n"
"v_mfma_f32_16x16x32_bf16 v[172:175], a[100:103], a[64:67], v[172:175]\n"
"s_add_u32 m0, s66, 0\n"
"ds_read_b64_tr_b16 a[252:253], v12\n"
"v_mfma_f32_16x16x32_bf16 v[168:171], a[96:99], a[64:67], v[168:171]\n"
"s_add_u32 s70, s35, 1920\n"
"ds_read_b64_tr_b16 a[254:255], v12 offset:2048\n"
"v_mfma_f32_16x16x32_bf16 v[168:171], a[80:83], a[28:31], v[168:171]\n"
"s_and_b32 s70, s70, 0x7ff\n"
"buffer_load_dwordx4 v2, s[24:27], s70 offen lds\n"
"v_mfma_f32_16x16x32_bf16 v[164:167], a[76:79], a[28:31], v[164:167]\n"
"ds_read_b64_tr_b16 v[204:205], v13\n"
"v_mfma_f32_16x16x32_bf16 v[164:167], a[92:95], a[64:67], v[164:167]\n"
"ds_read_b64_tr_b16 v[206:207], v13 offset:2048\n"
"v_mfma_f32_16x16x32_bf16 v[160:163], a[88:91], a[64:67], v[160:163]\n"
"ds_read_b64_tr_b16 v[208:209], v14\n"
"v_mfma_f32_16x16x32_bf16 v[160:163], a[72:75], a[28:31], v[160:163]\n"
"ds_read_b64_tr_b16 v[210:211], v14 offset:2048\n"
"v_mfma_f32_16x16x32_bf16 v[176:179], a[72:75], a[32:35], v[176:179]\n"
"buffer_load_dwordx4 v3, s[24:27], s70 offen offset:1024 lds\n"
"v_mfma_f32_16x16x32_bf16 v[176:179], a[88:91], a[68:71], v[176:179]\n"
"v_mfma_f32_16x16x32_bf16 v[180:183], a[92:95], a[68:71], v[180:183]\n"
"v_mfma_f32_16x16x32_bf16 v[180:183], a[76:79], a[32:35], v[180:183]\n"
"v_mfma_f32_16x16x32_bf16 v[184:187], a[80:83], a[32:35], v[184:187]\n"
"v_mfma_f32_16x16x32_bf16 v[184:187], a[96:99], a[68:71], v[184:187]\n"
"buffer_load_dwordx4 v4, s[24:27], s70 offen offset:2048 lds\n"
"v_mfma_f32_16x16x32_bf16 v[188:191], a[100:103], a[68:71], v[188:191]\n"
"v_mfma_f32_16x16x32_bf16 v[188:191], a[84:87], a[32:35], v[188:191]\n"
"s_waitcnt lgkmcnt(5)\n"
"v_mfma_f32_16x16x32_bf16 v[48:51], a[232:235], a[0:3], v[48:51]\n"
"v_mfma_f32_16x16x32_bf16 v[48:51], a[248:251], a[36:39], v[48:51]\n"
"s_waitcnt lgkmcnt(0)\n"
"s_add_u32 m0, s65, 0\n"
"ds_read_b128 a[52:55], v16 offset:24576\n"
"v_mfma_f32_16x16x32_bf16 v[52:55], a[252:255], a[36:39], v[52:55]\n"
"s_add_u32 s69, s34, 7864320\n"
"ds_read_b128 a[20:23], v15 offset:26624\n"
"v_mfma_f32_16x16x32_bf16 v[52:55], a[236:239], a[0:3], v[52:55]\n"
"s_and_b32 s69, s69, 0x7fffff\n"
"buffer_load_dwordx4 v7, s[28:31], s69 offen lds\n"
"v_mfma_f32_16x16x32_bf16 v[56:59], a[240:243], a[0:3], v[56:59]\n"
"ds_read_b128 a[56:59], v16 offset:26624\n"
"v_mfma_f32_16x16x32_bf16 v[56:59], v[204:207], a[36:39], v[56:59]\n"
"ds_read_b128 a[24:27], v15 offset:28672\n"
"v_mfma_f32_16x16x32_bf16 v[60:63], v[208:211], a[36:39], v[60:63]\n"
"ds_read_b128 a[60:63], v16 offset:28672\n"
"v_mfma_f32_16x16x32_bf16 v[60:63], a[244:247], a[0:3], v[60:63]\n"
"ds_read_b128 a[28:31], v15 offset:30720\n"
"v_mfma_f32_16x16x32_bf16 v[76:79], a[244:247], a[4:7], v[76:79]\n"
"buffer_load_dwordx4 v8, s[28:31], s69 offen offset:1024 lds\n"
"v_mfma_f32_16x16x32_bf16 v[76:79], v[208:211], a[40:43], v[76:79]\n"
"ds_read_b128 a[64:67], v16 offset:30720\n"
"v_mfma_f32_16x16x32_bf16 v[72:75], v[204:207], a[40:43], v[72:75]\n"
"ds_read_b128 a[32:35], v15 offset:51200\n"
"v_mfma_f32_16x16x32_bf16 v[72:75], a[240:243], a[4:7], v[72:75]\n"
"ds_read_b128 a[68:71], v16 offset:51200\n"
"v_mfma_f32_16x16x32_bf16 v[68:71], a[236:239], a[4:7], v[68:71]\n"
"v_mfma_f32_16x16x32_bf16 v[68:71], a[252:255], a[40:43], v[68:71]\n"
"buffer_load_dwordx4 v9, s[28:31], s69 offen offset:2048 lds\n"
"v_mfma_f32_16x16x32_bf16 v[64:67], a[248:251], a[40:43], v[64:67]\n"
"v_mfma_f32_16x16x32_bf16 v[64:67], a[232:235], a[4:7], v[64:67]\n"
"v_mfma_f32_16x16x32_bf16 v[80:83], a[232:235], a[8:11], v[80:83]\n"
"v_mfma_f32_16x16x32_bf16 v[80:83], a[248:251], a[44:47], v[80:83]\n"
"v_mfma_f32_16x16x32_bf16 v[84:87], a[252:255], a[44:47], v[84:87]\n"
"buffer_load_dwordx4 v10, s[28:31], s69 offen offset:3072 lds\n"
"v_mfma_f32_16x16x32_bf16 v[84:87], a[236:239], a[8:11], v[84:87]\n"
"s_waitcnt vmcnt(15)\n"
"ds_read_b64_tr_b16 a[72:73], v11 offset:4096\n"
"v_mfma_f32_16x16x32_bf16 v[88:91], a[240:243], a[8:11], v[88:91]\n"
"ds_read_b64_tr_b16 a[74:75], v11 offset:6144\n"
"v_mfma_f32_16x16x32_bf16 v[88:91], v[204:207], a[44:47], v[88:91]\n"
"s_add_u32 m0, s66, 0\n"
"ds_read_b64_tr_b16 a[76:77], v12 offset:4096\n"
"v_mfma_f32_16x16x32_bf16 v[92:95], v[208:211], a[44:47], v[92:95]\n"
"s_add_u32 s70, s35, 1920\n"
"ds_read_b64_tr_b16 a[78:79], v12 offset:6144\n"
"v_mfma_f32_16x16x32_bf16 v[92:95], a[244:247], a[8:11], v[92:95]\n"
"s_and_b32 s70, s70, 0x7ff\n"
"buffer_load_dwordx4 v5, s[24:27], s70 offen offset:3072 lds\n"
"v_mfma_f32_16x16x32_bf16 v[108:111], a[244:247], a[12:15], v[108:111]\n"
"ds_read_b64_tr_b16 a[80:81], v13 offset:4096\n"
"v_mfma_f32_16x16x32_bf16 v[108:111], v[208:211], a[48:51], v[108:111]\n"
"ds_read_b64_tr_b16 a[82:83], v13 offset:6144\n"
"v_mfma_f32_16x16x32_bf16 v[104:107], v[204:207], a[48:51], v[104:107]\n"
"s_mul_i32 s71, s23, 14\n"
"ds_read_b64_tr_b16 a[84:85], v14 offset:4096\n"
"v_mfma_f32_16x16x32_bf16 v[104:107], a[240:243], a[12:15], v[104:107]\n"
"s_add_u32 s71, s77, s71\n"
"ds_read_b64_tr_b16 a[86:87], v14 offset:6144\n"
"v_mfma_f32_16x16x32_bf16 v[100:103], a[236:239], a[12:15], v[100:103]\n"
"s_and_b32 s71, s71, 0x1ffff\n"
"buffer_load_dwordx4 v[252:255], v23, s[40:43], s71 offen nt\n"
"v_mfma_f32_16x16x32_bf16 v[100:103], a[252:255], a[48:51], v[100:103]\n"
"v_mfma_f32_16x16x32_bf16 v[96:99], a[248:251], a[48:51], v[96:99]\n"
"buffer_load_dword v44, v45, s[60:63], s78 offen\n"
"v_mfma_f32_16x16x32_bf16 v[96:99], a[232:235], a[12:15], v[96:99]\n"
"v_mfma_f32_16x16x32_bf16 v[112:115], a[232:235], a[16:19], v[112:115]\n"
"s_waitcnt lgkmcnt(5)\n"
"v_mfma_f32_16x16x32_bf16 v[112:115], a[248:251], a[52:55], v[112:115]\n"
"buffer_load_dwordx4 v[200:203], v24, s[40:43], s71 offen offset:1024 nt\n"
"v_mfma_f32_16x16x32_bf16 v[116:119], a[252:255], a[52:55], v[116:119]\n"
"v_mfma_f32_16x16x32_bf16 v[116:119], a[236:239], a[16:19], v[116:119]\n"
"v_mfma_f32_16x16x32_bf16 v[120:123], a[240:243], a[16:19], v[120:123]\n"
"s_waitcnt vmcnt(15)\n"
"s_barrier\n"
"v_mfma_f32_16x16x32_bf16 v[120:123], v[204:207], a[52:55], v[120:123]\n"
"s_waitcnt lgkmcnt(0)\n"
"s_add_u32 m0, s65, 4096\n"
"ds_read_b128 a[0:3], v15 offset:32768\n"
"v_mfma_f32_16x16x32_bf16 v[124:127], v[208:211], a[52:55], v[124:127]\n"
"s_add_u32 s69, s34, 8126464\n"
"ds_read_b128 a[36:39], v16 offset:32768\n"
"v_mfma_f32_16x16x32_bf16 v[124:127], a[244:247], a[16:19], v[124:127]\n"
"s_and_b32 s69, s69, 0x7fffff\n"
"buffer_load_dwordx4 v7, s[28:31], s69 offen lds\n"
"v_mfma_f32_16x16x32_bf16 v[140:143], a[244:247], a[20:23], v[140:143]\n"
"ds_read_b128 a[4:7], v15 offset:34816\n"
"v_mfma_f32_16x16x32_bf16 v[140:143], v[208:211], a[56:59], v[140:143]\n"
"ds_read_b128 a[40:43], v16 offset:34816\n"
"v_mfma_f32_16x16x32_bf16 v[136:139], v[204:207], a[56:59], v[136:139]\n"
"ds_read_b128 a[8:11], v15 offset:36864\n"
"v_mfma_f32_16x16x32_bf16 v[136:139], a[240:243], a[20:23], v[136:139]\n"
"ds_read_b128 a[44:47], v16 offset:36864\n"
"v_mfma_f32_16x16x32_bf16 v[132:135], a[236:239], a[20:23], v[132:135]\n"
"buffer_load_dwordx4 v8, s[28:31], s69 offen offset:1024 lds\n"
"v_mfma_f32_16x16x32_bf16 v[132:135], a[252:255], a[56:59], v[132:135]\n"
"ds_read_b128 a[12:15], v15 offset:38912\n"
"v_mfma_f32_16x16x32_bf16 v[128:131], a[248:251], a[56:59], v[128:131]\n"
"ds_read_b128 a[48:51], v16 offset:38912\n"
"v_mfma_f32_16x16x32_bf16 v[128:131], a[232:235], a[20:23], v[128:131]\n"
"ds_read_b128 a[16:19], v15 offset:40960\n"
"v_mfma_f32_16x16x32_bf16 v[144:147], a[232:235], a[24:27], v[144:147]\n"
"ds_read_b128 a[104:107], v25\n"
"v_mfma_f32_16x16x32_bf16 v[144:147], a[248:251], a[60:63], v[144:147]\n"
"buffer_load_dwordx4 v9, s[28:31], s69 offen offset:2048 lds\n"
"v_mfma_f32_16x16x32_bf16 v[148:151], a[252:255], a[60:63], v[148:151]\n"
"ds_read_b128 a[108:111], v26\n"
"v_mfma_f32_16x16x32_bf16 v[148:151], a[236:239], a[24:27], v[148:151]\n"
"ds_read_b128 a[112:115], v27\n"
"v_mfma_f32_16x16x32_bf16 v[152:155], a[240:243], a[24:27], v[152:155]\n"
"ds_read_b128 a[116:119], v28\n"
"v_mfma_f32_16x16x32_bf16 v[152:155], v[204:207], a[60:63], v[152:155]\n"
"ds_read_b128 a[132:135], v28 offset:4096\n"
"v_mfma_f32_16x16x32_bf16 v[156:159], v[208:211], a[60:63], v[156:159]\n"
"buffer_load_dwordx4 v10, s[28:31], s69 offen offset:3072 lds\n"
"v_mfma_f32_16x16x32_bf16 v[156:159], a[244:247], a[24:27], v[156:159]\n"
"s_waitcnt vmcnt(15)\n"
"ds_read_b64_tr_b16 a[88:89], v11 offset:8192\n"
"v_mfma_f32_16x16x32_bf16 v[172:175], a[244:247], a[28:31], v[172:175]\n"
"ds_read_b64_tr_b16 a[90:91], v11 offset:10240\n"
"v_mfma_f32_16x16x32_bf16 v[172:175], v[208:211], a[64:67], v[172:175]\n"
"ds_read_b64_tr_b16 a[92:93], v12 offset:8192\n"
"v_mfma_f32_16x16x32_bf16 v[168:171], v[204:207], a[64:67], v[168:171]\n"
"ds_read_b64_tr_b16 a[94:95], v12 offset:10240\n"
"v_mfma_f32_16x16x32_bf16 v[168:171], a[240:243], a[28:31], v[168:171]\n"
"ds_read_b64_tr_b16 a[96:97], v13 offset:8192\n"
"v_mfma_f32_16x16x32_bf16 v[164:167], a[236:239], a[28:31], v[164:167]\n"
"ds_read_b64_tr_b16 a[98:99], v13 offset:10240\n"
"v_mfma_f32_16x16x32_bf16 v[164:167], a[252:255], a[64:67], v[164:167]\n"
"buffer_load_dword v44, v216, s[48:51], 0 offen\n"
"v_mfma_f32_16x16x32_bf16 v[160:163], a[248:251], a[64:67], v[160:163]\n"
"ds_read_b64_tr_b16 a[100:101], v14 offset:8192\n"
"v_mfma_f32_16x16x32_bf16 v[160:163], a[232:235], a[28:31], v[160:163]\n"
"ds_read_b64_tr_b16 a[102:103], v14 offset:10240\n"
"v_mfma_f32_16x16x32_bf16 v[176:179], a[232:235], a[32:35], v[176:179]\n"
"ds_read_b128 a[128:131], v27 offset:4096\n"
"v_mfma_f32_16x16x32_bf16 v[176:179], a[248:251], a[68:71], v[176:179]\n"
"ds_read_b128 a[124:127], v26 offset:4096\n"
"v_mfma_f32_16x16x32_bf16 v[180:183], a[252:255], a[68:71], v[180:183]\n"
"ds_read_b128 a[120:123], v25 offset:4096\n"
"v_mfma_f32_16x16x32_bf16 v[180:183], a[236:239], a[32:35], v[180:183]\n"
"ds_read_b64_tr_b16 a[168:169], v29 offset:8192\n"
"v_mfma_f32_16x16x32_bf16 v[184:187], a[240:243], a[32:35], v[184:187]\n"
"ds_read_b64_tr_b16 a[170:171], v29 offset:12288\n"
"v_mfma_f32_16x16x32_bf16 v[184:187], v[204:207], a[68:71], v[184:187]\n"
"ds_read_b64_tr_b16 a[172:173], v30 offset:8192\n"
"v_mfma_f32_16x16x32_bf16 v[188:191], v[208:211], a[68:71], v[188:191]\n"
"ds_read_b64_tr_b16 a[174:175], v30 offset:12288\n"
"v_mfma_f32_16x16x32_bf16 v[188:191], a[244:247], a[32:35], v[188:191]\n"
"s_waitcnt lgkmcnt(11)\n"
"v_mfma_f32_16x16x32_bf16 v[48:51], a[72:75], a[0:3], v[48:51]\n"
"v_mfma_f32_16x16x32_bf16 v[48:51], a[88:91], a[36:39], v[48:51]\n"
"ds_read_b128 a[52:55], v16 offset:40960\n"
"v_mfma_f32_16x16x32_bf16 v[52:55], a[92:95], a[36:39], v[52:55]\n"
"ds_read_b128 a[20:23], v15 offset:43008\n"
"v_mfma_f32_16x16x32_bf16 v[52:55], a[76:79], a[0:3], v[52:55]\n"
"ds_read_b128 a[56:59], v16 offset:43008\n"
"v_mfma_f32_16x16x32_bf16 v[56:59], a[80:83], a[0:3], v[56:59]\n"
"s_waitcnt lgkmcnt(10)\n"
"ds_read_b128 a[24:27], v15 offset:45056\n"
"v_mfma_f32_16x16x32_bf16 v[56:59], a[96:99], a[36:39], v[56:59]\n"
"ds_read_b128 a[60:63], v16 offset:45056\n"
"v_mfma_f32_16x16x32_bf16 v[60:63], a[100:103], a[36:39], v[60:63]\n"
"ds_read_b128 a[28:31], v15 offset:47104\n"
"v_mfma_f32_16x16x32_bf16 v[60:63], a[84:87], a[0:3], v[60:63]\n"
"ds_read_b128 a[64:67], v16 offset:47104\n"
"v_mfma_f32_16x16x32_bf16 v[76:79], a[84:87], a[4:7], v[76:79]\n"
"ds_read_b128 a[32:35], v15 offset:53248\n"
"v_mfma_f32_16x16x32_bf16 v[76:79], a[100:103], a[40:43], v[76:79]\n"
"ds_read_b128 a[68:71], v16 offset:53248\n"
"v_mfma_f32_16x16x32_bf16 v[72:75], a[96:99], a[40:43], v[72:75]\n"
"ds_read_b64_tr_b16 a[176:177], v31 offset:8192\n"
"v_mfma_f32_16x16x32_bf16 v[72:75], a[80:83], a[4:7], v[72:75]\n"
"ds_read_b64_tr_b16 a[178:179], v31 offset:12288\n"
"v_mfma_f32_16x16x32_bf16 v[68:71], a[76:79], a[4:7], v[68:71]\n"
"ds_read_b64_tr_b16 a[180:181], v32 offset:8192\n"
"v_mfma_f32_16x16x32_bf16 v[68:71], a[92:95], a[40:43], v[68:71]\n"
"ds_read_b64_tr_b16 a[182:183], v32 offset:12288\n"
"v_mfma_f32_16x16x32_bf16 v[64:67], a[88:91], a[40:43], v[64:67]\n"
"ds_read_b64_tr_b16 a[184:185], v33 offset:8192\n"
"v_mfma_f32_16x16x32_bf16 v[64:67], a[72:75], a[4:7], v[64:67]\n"
"ds_read_b64_tr_b16 a[186:187], v33 offset:12288\n"
"v_mfma_f32_16x16x32_bf16 v[80:83], a[72:75], a[8:11], v[80:83]\n"
"ds_read_b64_tr_b16 a[188:189], v34 offset:8192\n"
"v_mfma_f32_16x16x32_bf16 v[80:83], a[88:91], a[44:47], v[80:83]\n"
"ds_read_b64_tr_b16 a[190:191], v34 offset:12288\n"
"v_mfma_f32_16x16x32_bf16 v[84:87], a[92:95], a[44:47], v[84:87]\n"
"ds_read_b64_tr_b16 a[192:193], v35 offset:8192\n"
"v_mfma_f32_16x16x32_bf16 v[84:87], a[76:79], a[8:11], v[84:87]\n"
"s_waitcnt vmcnt(9)\n"
"ds_read_b64_tr_b16 a[232:233], v11\n"
"v_mfma_f32_16x16x32_bf16 v[88:91], a[80:83], a[8:11], v[88:91]\n"
"ds_read_b64_tr_b16 a[234:235], v11 offset:2048\n"
"v_mfma_f32_16x16x32_bf16 v[88:91], a[96:99], a[44:47], v[88:91]\n"
"ds_read_b64_tr_b16 a[236:237], v12\n"
"v_mfma_f32_16x16x32_bf16 v[92:95], a[100:103], a[44:47], v[92:95]\n"
"ds_read_b64_tr_b16 a[238:239], v12 offset:2048\n"
"v_mfma_f32_16x16x32_bf16 v[92:95], a[84:87], a[8:11], v[92:95]\n"
"ds_read_b64_tr_b16 a[240:241], v13\n"
"v_mfma_f32_16x16x32_bf16 v[108:111], a[84:87], a[12:15], v[108:111]\n"
"ds_read_b64_tr_b16 a[242:243], v13 offset:2048\n"
"v_mfma_f32_16x16x32_bf16 v[108:111], a[100:103], a[48:51], v[108:111]\n"
"ds_read_b64_tr_b16 a[244:245], v14\n"
"v_mfma_f32_16x16x32_bf16 v[104:107], a[96:99], a[48:51], v[104:107]\n"
"ds_read_b64_tr_b16 a[246:247], v14 offset:2048\n"
"v_mfma_f32_16x16x32_bf16 v[104:107], a[80:83], a[12:15], v[104:107]\n"
"ds_read_b64_tr_b16 a[194:195], v35 offset:12288\n"
"v_mfma_f32_16x16x32_bf16 v[100:103], a[76:79], a[12:15], v[100:103]\n"
"ds_read_b64_tr_b16 a[196:197], v36 offset:8192\n"
"v_mfma_f32_16x16x32_bf16 v[100:103], a[92:95], a[48:51], v[100:103]\n"
"ds_read_b64_tr_b16 a[198:199], v36 offset:12288\n"
"v_mfma_f32_16x16x32_bf16 v[96:99], a[88:91], a[48:51], v[96:99]\n"
"v_mfma_f32_16x16x32_bf16 v[96:99], a[72:75], a[12:15], v[96:99]\n"
"v_mfma_f32_16x16x32_bf16 v[112:115], a[72:75], a[16:19], v[112:115]\n"
"s_waitcnt lgkmcnt(11)\n"
"v_mfma_f32_16x16x32_bf16 v[112:115], a[88:91], a[52:55], v[112:115]\n"
"v_mfma_f32_16x16x32_bf16 v[116:119], a[92:95], a[52:55], v[116:119]\n"
"v_mfma_f32_16x16x32_bf16 v[116:119], a[76:79], a[16:19], v[116:119]\n"
"v_mfma_f32_16x16x32_bf16 v[120:123], a[80:83], a[16:19], v[120:123]\n"
"s_waitcnt vmcnt(8)\n"
"s_barrier\n"
"v_mfma_f32_16x16x32_bf16 v[120:123], a[96:99], a[52:55], v[120:123]\n"
"ds_read_b128 a[0:3], v15\n"
"v_mfma_f32_16x16x32_bf16 v[124:127], a[100:103], a[52:55], v[124:127]\n"
"ds_read_b128 a[36:39], v16\n"
"v_mfma_f32_16x16x32_bf16 v[124:127], a[84:87], a[16:19], v[124:127]\n"
"ds_read_b128 a[4:7], v15 offset:2048\n"
"v_mfma_f32_16x16x32_bf16 v[140:143], a[84:87], a[20:23], v[140:143]\n"
"ds_read_b128 a[40:43], v16 offset:2048\n"
"v_mfma_f32_16x16x32_bf16 v[140:143], a[100:103], a[56:59], v[140:143]\n"
"ds_read_b128 a[8:11], v15 offset:4096\n"
"v_mfma_f32_16x16x32_bf16 v[136:139], a[96:99], a[56:59], v[136:139]\n"
"ds_read_b128 a[44:47], v16 offset:4096\n"
"v_mfma_f32_16x16x32_bf16 v[136:139], a[80:83], a[20:23], v[136:139]\n"
"ds_read_b128 a[12:15], v15 offset:6144\n"
"v_mfma_f32_16x16x32_bf16 v[132:135], a[76:79], a[20:23], v[132:135]\n"
"ds_read_b128 a[48:51], v16 offset:6144\n"
"v_mfma_f32_16x16x32_bf16 v[132:135], a[92:95], a[56:59], v[132:135]\n"
"ds_read_b128 a[16:19], v15 offset:8192\n"
"v_mfma_f32_16x16x32_bf16 v[128:131], a[88:91], a[56:59], v[128:131]\n"
"ds_read_b128 a[52:55], v16 offset:8192\n"
"v_mfma_f32_16x16x32_bf16 v[128:131], a[72:75], a[20:23], v[128:131]\n"
"s_waitcnt vmcnt(1)\n"
"ds_read_b64_tr_b16 a[248:249], v11 offset:4096\n"
"v_mfma_f32_16x16x32_bf16 v[144:147], a[72:75], a[24:27], v[144:147]\n"
"ds_read_b64_tr_b16 a[250:251], v11 offset:6144\n"
"v_mfma_f32_16x16x32_bf16 v[144:147], a[88:91], a[60:63], v[144:147]\n"
"ds_read_b64_tr_b16 a[252:253], v12 offset:4096\n"
"v_mfma_f32_16x16x32_bf16 v[148:151], a[92:95], a[60:63], v[148:151]\n"
"ds_read_b64_tr_b16 a[254:255], v12 offset:6144\n"
"v_mfma_f32_16x16x32_bf16 v[148:151], a[76:79], a[24:27], v[148:151]\n"
"ds_read_b64_tr_b16 v[204:205], v13 offset:4096\n"
"v_mfma_f32_16x16x32_bf16 v[152:155], a[80:83], a[24:27], v[152:155]\n"
"ds_read_b64_tr_b16 v[206:207], v13 offset:6144\n"
"v_mfma_f32_16x16x32_bf16 v[152:155], a[96:99], a[60:63], v[152:155]\n"
"ds_read_b64_tr_b16 v[208:209], v14 offset:4096\n"
"v_mfma_f32_16x16x32_bf16 v[156:159], a[100:103], a[60:63], v[156:159]\n"
"ds_read_b64_tr_b16 v[210:211], v14 offset:6144\n"
"v_mfma_f32_16x16x32_bf16 v[156:159], a[84:87], a[24:27], v[156:159]\n"
"ds_read_b128 a[20:23], v15 offset:10240\n"
"v_mfma_f32_16x16x32_bf16 v[172:175], a[84:87], a[28:31], v[172:175]\n"
"ds_read_b128 a[56:59], v16 offset:10240\n"
"v_mfma_f32_16x16x32_bf16 v[172:175], a[100:103], a[64:67], v[172:175]\n"
"ds_read_b128 a[24:27], v15 offset:12288\n"
"v_mfma_f32_16x16x32_bf16 v[168:171], a[96:99], a[64:67], v[168:171]\n"
"ds_read_b128 a[60:63], v16 offset:12288\n"
"v_mfma_f32_16x16x32_bf16 v[168:171], a[80:83], a[28:31], v[168:171]\n"
"v_mfma_f32_16x16x32_bf16 v[164:167], a[76:79], a[28:31], v[164:167]\n"
"v_mfma_f32_16x16x32_bf16 v[164:167], a[92:95], a[64:67], v[164:167]\n"
"v_mfma_f32_16x16x32_bf16 v[160:163], a[88:91], a[64:67], v[160:163]\n"
"v_mfma_f32_16x16x32_bf16 v[160:163], a[72:75], a[28:31], v[160:163]\n"
"v_mfma_f32_16x16x32_bf16 v[176:179], a[72:75], a[32:35], v[176:179]\n"
"ds_read_b128 a[28:31], v15 offset:14336\n"
"v_mfma_f32_16x16x32_bf16 v[176:179], a[88:91], a[68:71], v[176:179]\n"
"ds_read_b128 a[64:67], v16 offset:14336\n"
"v_mfma_f32_16x16x32_bf16 v[180:183], a[92:95], a[68:71], v[180:183]\n"
"v_mfma_f32_16x16x32_bf16 v[180:183], a[76:79], a[32:35], v[180:183]\n"
"v_mfma_f32_16x16x32_bf16 v[184:187], a[80:83], a[32:35], v[184:187]\n"
"v_mfma_f32_16x16x32_bf16 v[184:187], a[96:99], a[68:71], v[184:187]\n"
"v_mfma_f32_16x16x32_bf16 v[188:191], a[100:103], a[68:71], v[188:191]\n"
"v_mfma_f32_16x16x32_bf16 v[188:191], a[84:87], a[32:35], v[188:191]\n"
"s_waitcnt lgkmcnt(7)\n"
"ds_read_b128 a[32:35], v15 offset:55296\n"
"v_mfma_f32_16x16x32_bf16 v[48:51], a[232:235], a[0:3], v[48:51]\n"
"ds_read_b128 a[68:71], v16 offset:55296\n"
"v_mfma_f32_16x16x32_bf16 v[48:51], a[248:251], a[36:39], v[48:51]\n"
"v_mfma_f32_16x16x32_bf16 v[52:55], a[252:255], a[36:39], v[52:55]\n"
"v_mfma_f32_16x16x32_bf16 v[52:55], a[236:239], a[0:3], v[52:55]\n"
"v_mfma_f32_16x16x32_bf16 v[56:59], a[240:243], a[0:3], v[56:59]\n"
"v_mfma_f32_16x16x32_bf16 v[56:59], v[204:207], a[36:39], v[56:59]\n"
"s_waitcnt lgkmcnt(4)\n"
"v_mfma_f32_16x16x32_bf16 v[60:63], v[208:211], a[36:39], v[60:63]\n"
"v_mfma_f32_16x16x32_bf16 v[60:63], a[244:247], a[0:3], v[60:63]\n"
"s_waitcnt lgkmcnt(0)\n"
"s_barrier\n"
"v_mfma_f32_16x16x32_bf16 v[76:79], a[244:247], a[4:7], v[76:79]\n"
"s_add_u32 m0, s68, 32768\n"
"s_mul_i32 s71, s23, 8\n"
"v_mfma_f32_16x16x32_bf16 v[76:79], v[208:211], a[40:43], v[76:79]\n"
"s_add_u32 s71, s77, s71\n"
"v_mfma_f32_16x16x32_bf16 v[72:75], v[204:207], a[40:43], v[72:75]\n"
"s_and_b32 s71, s71, 0x1ffff\n"
"buffer_load_dwordx4 v21, s[36:39], s71 offen nt lds\n"
"v_mfma_f32_16x16x32_bf16 v[72:75], a[240:243], a[4:7], v[72:75]\n"
"v_mul_f32 v48, s72, v48\n"
"v_mul_f32 v49, s72, v49\n"
"v_mfma_f32_16x16x32_bf16 v[68:71], a[236:239], a[4:7], v[68:71]\n"
"v_mul_f32 v50, s72, v50\n"
"v_mul_f32 v51, s72, v51\n"
"v_mfma_f32_16x16x32_bf16 v[68:71], a[252:255], a[40:43], v[68:71]\n"
"v_mul_f32 v52, s72, v52\n"
"v_mul_f32 v53, s72, v53\n"
"v_mfma_f32_16x16x32_bf16 v[64:67], a[248:251], a[40:43], v[64:67]\n"
"v_mul_f32 v54, s72, v54\n"
"v_mul_f32 v55, s72, v55\n"
"v_mfma_f32_16x16x32_bf16 v[64:67], a[232:235], a[4:7], v[64:67]\n"
"v_cvt_pk_bf16_f32 v48, v48, v49\n"
"v_cvt_pk_bf16_f32 v49, v50, v51\n"
"v_mfma_f32_16x16x32_bf16 v[80:83], a[232:235], a[8:11], v[80:83]\n"
"v_cvt_pk_bf16_f32 v50, v52, v53\n"
"v_cvt_pk_bf16_f32 v51, v54, v55\n"
"v_mfma_f32_16x16x32_bf16 v[80:83], a[248:251], a[44:47], v[80:83]\n"
"ds_write_b128 v37, v[48:51]\n"
"v_mul_f32 v56, s72, v56\n"
"v_mfma_f32_16x16x32_bf16 v[84:87], a[252:255], a[44:47], v[84:87]\n"
"buffer_load_dwordx4 v22, s[36:39], s71 offen offset:1024 nt lds\n"
"v_mul_f32 v57, s72, v57\n"
"v_mfma_f32_16x16x32_bf16 v[84:87], a[236:239], a[8:11], v[84:87]\n"
"v_mul_f32 v58, s72, v58\n"
"v_mul_f32 v59, s72, v59\n"
"v_mfma_f32_16x16x32_bf16 v[88:91], a[240:243], a[8:11], v[88:91]\n"
"v_mul_f32 v60, s72, v60\n"
"v_mul_f32 v61, s72, v61\n"
"v_mfma_f32_16x16x32_bf16 v[88:91], v[204:207], a[44:47], v[88:91]\n"
"v_mul_f32 v62, s72, v62\n"
"v_mul_f32 v63, s72, v63\n"
"v_mfma_f32_16x16x32_bf16 v[92:95], v[208:211], a[44:47], v[92:95]\n"
"v_cvt_pk_bf16_f32 v56, v56, v57\n"
"v_cvt_pk_bf16_f32 v57, v58, v59\n"
"v_mfma_f32_16x16x32_bf16 v[92:95], a[244:247], a[8:11], v[92:95]\n"
"v_cvt_pk_bf16_f32 v58, v60, v61\n"
"v_cvt_pk_bf16_f32 v59, v62, v63\n"
"v_mfma_f32_16x16x32_bf16 v[108:111], a[244:247], a[12:15], v[108:111]\n"
"s_add_u32 m0, s68, 40960\n"
"s_mul_i32 s71, s23, 10\n"
"ds_write_b128 v37, v[56:59] offset:1024\n"
"v_mul_f32 v64, s72, v64\n"
"v_mfma_f32_16x16x32_bf16 v[108:111], v[208:211], a[48:51], v[108:111]\n"
"s_add_u32 s71, s77, s71\n"
"v_mul_f32 v65, s72, v65\n"
"v_mul_f32 v66, s72, v66\n"
"v_mfma_f32_16x16x32_bf16 v[104:107], v[204:207], a[48:51], v[104:107]\n"
"s_and_b32 s71, s71, 0x1ffff\n"
"buffer_load_dwordx4 v21, s[36:39], s71 offen nt lds\n"
"v_mul_f32 v67, s72, v67\n"
"v_mfma_f32_16x16x32_bf16 v[104:107], a[240:243], a[12:15], v[104:107]\n"
"v_mul_f32 v68, s72, v68\n"
"v_mul_f32 v69, s72, v69\n"
"v_mfma_f32_16x16x32_bf16 v[100:103], a[236:239], a[12:15], v[100:103]\n"
"v_mul_f32 v70, s72, v70\n"
"v_mul_f32 v71, s72, v71\n"
"v_mfma_f32_16x16x32_bf16 v[100:103], a[252:255], a[48:51], v[100:103]\n"
"v_cvt_pk_bf16_f32 v64, v64, v65\n"
"v_cvt_pk_bf16_f32 v65, v66, v67\n"
"v_mfma_f32_16x16x32_bf16 v[96:99], a[248:251], a[48:51], v[96:99]\n"
"v_cvt_pk_bf16_f32 v66, v68, v69\n"
"v_cvt_pk_bf16_f32 v67, v70, v71\n"
"v_mfma_f32_16x16x32_bf16 v[96:99], a[232:235], a[12:15], v[96:99]\n"
"ds_write_b128 v37, v[64:67] offset:4096\n"
"v_mul_f32 v72, s72, v72\n"
"v_mfma_f32_16x16x32_bf16 v[112:115], a[232:235], a[16:19], v[112:115]\n"
"v_mul_f32 v73, s72, v73\n"
"v_mul_f32 v74, s72, v74\n"
"v_mfma_f32_16x16x32_bf16 v[112:115], a[248:251], a[52:55], v[112:115]\n"
"v_mul_f32 v75, s72, v75\n"
"v_mul_f32 v76, s72, v76\n"
"v_mfma_f32_16x16x32_bf16 v[116:119], a[252:255], a[52:55], v[116:119]\n"
"buffer_load_dwordx4 v22, s[36:39], s71 offen offset:1024 nt lds\n"
"v_mul_f32 v77, s72, v77\n"
"v_mfma_f32_16x16x32_bf16 v[116:119], a[236:239], a[16:19], v[116:119]\n"
"v_mul_f32 v78, s72, v78\n"
"v_mul_f32 v79, s72, v79\n"
"v_mfma_f32_16x16x32_bf16 v[120:123], a[240:243], a[16:19], v[120:123]\n"
"v_cvt_pk_bf16_f32 v72, v72, v73\n"
"v_cvt_pk_bf16_f32 v73, v74, v75\n"
"v_mfma_f32_16x16x32_bf16 v[120:123], v[204:207], a[52:55], v[120:123]\n"
"v_cvt_pk_bf16_f32 v74, v76, v77\n"
"v_cvt_pk_bf16_f32 v75, v78, v79\n"
"v_mfma_f32_16x16x32_bf16 v[124:127], v[208:211], a[52:55], v[124:127]\n"
"ds_write_b128 v37, v[72:75] offset:5120\n"
"v_mul_f32 v80, s72, v80\n"
"v_mfma_f32_16x16x32_bf16 v[124:127], a[244:247], a[16:19], v[124:127]\n"
"v_mul_f32 v81, s72, v81\n"
"v_mul_f32 v82, s72, v82\n"
"v_mfma_f32_16x16x32_bf16 v[140:143], a[244:247], a[20:23], v[140:143]\n"
"v_mul_f32 v83, s72, v83\n"
"v_mul_f32 v84, s72, v84\n"
"v_mfma_f32_16x16x32_bf16 v[140:143], v[208:211], a[56:59], v[140:143]\n"
"v_mul_f32 v85, s72, v85\n"
"v_mul_f32 v86, s72, v86\n"
"v_mfma_f32_16x16x32_bf16 v[136:139], v[204:207], a[56:59], v[136:139]\n"
"v_mul_f32 v87, s72, v87\n"
"v_cvt_pk_bf16_f32 v80, v80, v81\n"
"v_mfma_f32_16x16x32_bf16 v[136:139], a[240:243], a[20:23], v[136:139]\n"
"v_cvt_pk_bf16_f32 v81, v82, v83\n"
"v_cvt_pk_bf16_f32 v82, v84, v85\n"
"v_mfma_f32_16x16x32_bf16 v[132:135], a[236:239], a[20:23], v[132:135]\n"
"v_cvt_pk_bf16_f32 v83, v86, v87\n"
"v_mfma_f32_16x16x32_bf16 v[132:135], a[252:255], a[56:59], v[132:135]\n"
"ds_write_b128 v37, v[80:83] offset:8192\n"
"v_mul_f32 v88, s72, v88\n"
"v_mfma_f32_16x16x32_bf16 v[128:131], a[248:251], a[56:59], v[128:131]\n"
"v_mul_f32 v89, s72, v89\n"
"v_mul_f32 v90, s72, v90\n"
"v_mfma_f32_16x16x32_bf16 v[128:131], a[232:235], a[20:23], v[128:131]\n"
"v_mul_f32 v91, s72, v91\n"
"v_mul_f32 v92, s72, v92\n"
"v_mfma_f32_16x16x32_bf16 v[144:147], a[232:235], a[24:27], v[144:147]\n"
"v_mul_f32 v93, s72, v93\n"
"v_mul_f32 v94, s72, v94\n"
"v_mfma_f32_16x16x32_bf16 v[144:147], a[248:251], a[60:63], v[144:147]\n"
"v_mul_f32 v95, s72, v95\n"
"v_cvt_pk_bf16_f32 v88, v88, v89\n"
"v_mfma_f32_16x16x32_bf16 v[148:151], a[252:255], a[60:63], v[148:151]\n"
"v_cvt_pk_bf16_f32 v89, v90, v91\n"
"v_cvt_pk_bf16_f32 v90, v92, v93\n"
"v_mfma_f32_16x16x32_bf16 v[148:151], a[236:239], a[24:27], v[148:151]\n"
"v_cvt_pk_bf16_f32 v91, v94, v95\n"
"v_mfma_f32_16x16x32_bf16 v[152:155], a[240:243], a[24:27], v[152:155]\n"
"ds_write_b128 v37, v[88:91] offset:9216\n"
"v_mul_f32 v96, s72, v96\n"
"v_mfma_f32_16x16x32_bf16 v[152:155], v[204:207], a[60:63], v[152:155]\n"
"v_mul_f32 v97, s72, v97\n"
"v_mul_f32 v98, s72, v98\n"
"v_mfma_f32_16x16x32_bf16 v[156:159], v[208:211], a[60:63], v[156:159]\n"
"v_mul_f32 v99, s72, v99\n"
"v_mul_f32 v100, s72, v100\n"
"v_mfma_f32_16x16x32_bf16 v[156:159], a[244:247], a[24:27], v[156:159]\n"
"v_mul_f32 v101, s72, v101\n"
"v_mul_f32 v102, s72, v102\n"
"v_mfma_f32_16x16x32_bf16 v[172:175], a[244:247], a[28:31], v[172:175]\n"
"v_mul_f32 v103, s72, v103\n"
"v_cvt_pk_bf16_f32 v96, v96, v97\n"
"v_mfma_f32_16x16x32_bf16 v[172:175], v[208:211], a[64:67], v[172:175]\n"
"v_cvt_pk_bf16_f32 v97, v98, v99\n"
"v_cvt_pk_bf16_f32 v98, v100, v101\n"
"v_mfma_f32_16x16x32_bf16 v[168:171], v[204:207], a[64:67], v[168:171]\n"
"v_cvt_pk_bf16_f32 v99, v102, v103\n"
"v_mfma_f32_16x16x32_bf16 v[168:171], a[240:243], a[28:31], v[168:171]\n"
"ds_write_b128 v37, v[96:99] offset:12288\n"
"v_mul_f32 v104, s72, v104\n"
"v_mfma_f32_16x16x32_bf16 v[164:167], a[236:239], a[28:31], v[164:167]\n"
"v_mul_f32 v105, s72, v105\n"
"v_mul_f32 v106, s72, v106\n"
"v_mfma_f32_16x16x32_bf16 v[164:167], a[252:255], a[64:67], v[164:167]\n"
"v_mul_f32 v107, s72, v107\n"
"v_mul_f32 v108, s72, v108\n"
"v_mfma_f32_16x16x32_bf16 v[160:163], a[248:251], a[64:67], v[160:163]\n"
"v_mul_f32 v109, s72, v109\n"
"v_mul_f32 v110, s72, v110\n"
"v_mfma_f32_16x16x32_bf16 v[160:163], a[232:235], a[28:31], v[160:163]\n"
"v_mul_f32 v111, s72, v111\n"
"v_cvt_pk_bf16_f32 v104, v104, v105\n"
"v_mfma_f32_16x16x32_bf16 v[176:179], a[232:235], a[32:35], v[176:179]\n"
"v_cvt_pk_bf16_f32 v105, v106, v107\n"
"v_cvt_pk_bf16_f32 v106, v108, v109\n"
"v_mfma_f32_16x16x32_bf16 v[176:179], a[248:251], a[68:71], v[176:179]\n"
"v_cvt_pk_bf16_f32 v107, v110, v111\n"
"v_mfma_f32_16x16x32_bf16 v[180:183], a[252:255], a[68:71], v[180:183]\n"
"ds_write_b128 v37, v[104:107] offset:13312\n"
"v_mul_f32 v112, s72, v112\n"
"v_mfma_f32_16x16x32_bf16 v[180:183], a[236:239], a[32:35], v[180:183]\n"
"v_mul_f32 v113, s72, v113\n"
"v_mul_f32 v114, s72, v114\n"
"v_mfma_f32_16x16x32_bf16 v[184:187], a[240:243], a[32:35], v[184:187]\n"
"v_mul_f32 v115, s72, v115\n"
"v_mul_f32 v116, s72, v116\n"
"v_mfma_f32_16x16x32_bf16 v[184:187], v[204:207], a[68:71], v[184:187]\n"
"v_mul_f32 v117, s72, v117\n"
"v_mul_f32 v118, s72, v118\n"
"v_mfma_f32_16x16x32_bf16 v[188:191], v[208:211], a[68:71], v[188:191]\n"
"v_mul_f32 v119, s72, v119\n"
"v_cvt_pk_bf16_f32 v112, v112, v113\n"
"v_mfma_f32_16x16x32_bf16 v[188:191], a[244:247], a[32:35], v[188:191]\n"
"v_cvt_pk_bf16_f32 v113, v114, v115\n"
"v_cvt_pk_bf16_f32 v114, v116, v117\n"
"v_cvt_pk_bf16_f32 v115, v118, v119\n"
"ds_write_b128 v37, v[112:115] offset:16384\n"
"v_mul_f32 v120, s72, v120\n"
"v_mul_f32 v121, s72, v121\n"
"v_mul_f32 v122, s72, v122\n"
"v_mul_f32 v123, s72, v123\n"
"v_mul_f32 v124, s72, v124\n"
"v_mul_f32 v125, s72, v125\n"
"v_mul_f32 v126, s72, v126\n"
"v_mul_f32 v127, s72, v127\n"
"v_cvt_pk_bf16_f32 v120, v120, v121\n"
"v_cvt_pk_bf16_f32 v121, v122, v123\n"
"v_cvt_pk_bf16_f32 v122, v124, v125\n"
"v_cvt_pk_bf16_f32 v123, v126, v127\n"
"ds_write_b128 v37, v[120:123] offset:17408\n"
"v_pk_mul_f32 v[128:129], v[128:129], s[72:73]\n"
"v_pk_mul_f32 v[130:131], v[130:131], s[72:73]\n"
"v_pk_mul_f32 v[132:133], v[132:133], s[72:73]\n"
"v_pk_mul_f32 v[134:135], v[134:135], s[72:73]\n"
"v_cvt_pk_bf16_f32 v128, v128, v129\n"
"v_cvt_pk_bf16_f32 v129, v130, v131\n"
"v_cvt_pk_bf16_f32 v130, v132, v133\n"
"v_cvt_pk_bf16_f32 v131, v134, v135\n"
"v_pk_mul_f32 v[136:137], v[136:137], s[72:73]\n"
"v_pk_mul_f32 v[138:139], v[138:139], s[72:73]\n"
"v_pk_mul_f32 v[140:141], v[140:141], s[72:73]\n"
"v_pk_mul_f32 v[142:143], v[142:143], s[72:73]\n"
"v_cvt_pk_bf16_f32 v136, v136, v137\n"
"v_cvt_pk_bf16_f32 v137, v138, v139\n"
"v_cvt_pk_bf16_f32 v138, v140, v141\n"
"v_cvt_pk_bf16_f32 v139, v142, v143\n"
"ds_write_b128 v37, v[128:131] offset:20480\n"
"ds_write_b128 v37, v[136:139] offset:21504\n"
"v_pk_mul_f32 v[144:145], v[144:145], s[72:73]\n"
"v_pk_mul_f32 v[146:147], v[146:147], s[72:73]\n"
"v_pk_mul_f32 v[148:149], v[148:149], s[72:73]\n"
"v_pk_mul_f32 v[150:151], v[150:151], s[72:73]\n"
"v_cvt_pk_bf16_f32 v144, v144, v145\n"
"v_cvt_pk_bf16_f32 v145, v146, v147\n"
"v_cvt_pk_bf16_f32 v146, v148, v149\n"
"v_cvt_pk_bf16_f32 v147, v150, v151\n"
"v_pk_mul_f32 v[152:153], v[152:153], s[72:73]\n"
"v_pk_mul_f32 v[154:155], v[154:155], s[72:73]\n"
"v_pk_mul_f32 v[156:157], v[156:157], s[72:73]\n"
"v_pk_mul_f32 v[158:159], v[158:159], s[72:73]\n"
"v_cvt_pk_bf16_f32 v152, v152, v153\n"
"v_cvt_pk_bf16_f32 v153, v154, v155\n"
"v_cvt_pk_bf16_f32 v154, v156, v157\n"
"v_cvt_pk_bf16_f32 v155, v158, v159\n"
"ds_write_b128 v37, v[144:147] offset:24576\n"
"ds_write_b128 v37, v[152:155] offset:25600\n"
"v_pk_mul_f32 v[160:161], v[160:161], s[72:73]\n"
"v_pk_mul_f32 v[162:163], v[162:163], s[72:73]\n"
"v_pk_mul_f32 v[164:165], v[164:165], s[72:73]\n"
"v_pk_mul_f32 v[166:167], v[166:167], s[72:73]\n"
"v_cvt_pk_bf16_f32 v160, v160, v161\n"
"v_cvt_pk_bf16_f32 v161, v162, v163\n"
"v_cvt_pk_bf16_f32 v162, v164, v165\n"
"v_cvt_pk_bf16_f32 v163, v166, v167\n"
"v_pk_mul_f32 v[168:169], v[168:169], s[72:73]\n"
"v_pk_mul_f32 v[170:171], v[170:171], s[72:73]\n"
"v_pk_mul_f32 v[172:173], v[172:173], s[72:73]\n"
"v_pk_mul_f32 v[174:175], v[174:175], s[72:73]\n"
"v_cvt_pk_bf16_f32 v168, v168, v169\n"
"v_cvt_pk_bf16_f32 v169, v170, v171\n"
"v_cvt_pk_bf16_f32 v170, v172, v173\n"
"v_cvt_pk_bf16_f32 v171, v174, v175\n"
"ds_write_b128 v37, v[160:163] offset:28672\n"
"ds_write_b128 v37, v[168:171] offset:29696\n"
"v_pk_mul_f32 v[176:177], v[176:177], s[72:73]\n"
"v_pk_mul_f32 v[178:179], v[178:179], s[72:73]\n"
"v_pk_mul_f32 v[180:181], v[180:181], s[72:73]\n"
"v_pk_mul_f32 v[182:183], v[182:183], s[72:73]\n"
"v_cvt_pk_bf16_f32 v176, v176, v177\n"
"v_cvt_pk_bf16_f32 v177, v178, v179\n"
"v_cvt_pk_bf16_f32 v178, v180, v181\n"
"v_cvt_pk_bf16_f32 v179, v182, v183\n"
"v_pk_mul_f32 v[184:185], v[184:185], s[72:73]\n"
"v_pk_mul_f32 v[186:187], v[186:187], s[72:73]\n"
"v_pk_mul_f32 v[188:189], v[188:189], s[72:73]\n"
"v_pk_mul_f32 v[190:191], v[190:191], s[72:73]\n"
"v_cvt_pk_bf16_f32 v184, v184, v185\n"
"v_cvt_pk_bf16_f32 v185, v186, v187\n"
"v_cvt_pk_bf16_f32 v186, v188, v189\n"
"v_cvt_pk_bf16_f32 v187, v190, v191\n"
"ds_write_b128 v37, v[176:179] offset:32768\n"
"ds_write_b128 v37, v[184:187] offset:33792\n"
"s_waitcnt lgkmcnt(0)\n"
"s_barrier\n"
"s_cmp_eq_u32 s64, 3\n"
"s_cbranch_scc1 L_helper_%=\n"
"L_att_%=:\n"
"ds_read2st64_b64 a[0:3], v38 offset0:0 offset1:1\n"
"ds_read2st64_b64 a[4:7], v38 offset0:2 offset1:3\n"
"ds_read2st64_b64 a[8:11], v38 offset0:4 offset1:5\n"
"ds_read2st64_b64 a[12:15], v38 offset0:6 offset1:7\n"
"ds_read2st64_b64 a[16:19], v38 offset0:8 offset1:9\n"
"ds_read2st64_b64 a[20:23], v38 offset0:10 offset1:11\n"
"ds_read2st64_b64 a[24:27], v38 offset0:12 offset1:13\n"
"ds_read2st64_b64 a[28:31], v38 offset0:14 offset1:15\n"
"s_add_u32 s24, s64, 1\n"
"s_and_b32 s24, s24, 3\n"
"s_mul_i32 s24, s24, 12288\n"
"s_mul_i32 s25, s64, 9216\n"
"s_lshl_b32 s26, s64, 16\n"
"s_lshl_b32 s69, s79, 7\n"
"s_add_u32 s26, s26, s69\n"
"s_mov_b32 s27, 0x4b800000\n"
"s_waitcnt lgkmcnt(7)\n"
"v_mfma_f32_16x16x32_bf16 v[168:171], a[104:107], a[0:3], 0\n"
"s_waitcnt lgkmcnt(6)\n"
"ds_read2st64_b64 a[32:35], v38 offset0:16 offset1:17\n"
"v_mfma_f32_16x16x32_bf16 v[168:171], a[108:111], a[4:7], v[168:171]\n"
"s_waitcnt lgkmcnt(6)\n"
"ds_read2st64_b64 a[36:39], v38 offset0:18 offset1:19\n"
"v_mfma_f32_16x16x32_bf16 v[168:171], a[112:115], a[8:11], v[168:171]\n"
"s_waitcnt lgkmcnt(6)\n"
"ds_read2st64_b64 a[40:43], v38 offset0:20 offset1:21\n"
"v_mfma_f32_16x16x32_bf16 v[168:171], a[116:119], a[12:15], v[168:171]\n"
"ds_read2st64_b64 a[44:47], v38 offset0:22 offset1:23\n"
"v_mfma_f32_16x16x32_bf16 v[172:175], a[132:135], a[12:15], 0\n"
"ds_read2st64_b64 a[48:51], v38 offset0:72 offset1:73\n"
"v_mfma_f32_16x16x32_bf16 v[172:175], a[128:131], a[8:11], v[172:175]\n"
"ds_read2st64_b64 a[52:55], v38 offset0:74 offset1:75\n"
"v_mfma_f32_16x16x32_bf16 v[172:175], a[124:127], a[4:7], v[172:175]\n"
"ds_read2st64_b64 a[56:59], v38 offset0:76 offset1:77\n"
"v_mfma_f32_16x16x32_bf16 v[172:175], a[120:123], a[0:3], v[172:175]\n"
"s_waitcnt lgkmcnt(7)\n"
"ds_read2st64_b64 a[60:63], v38 offset0:78 offset1:79\n"
"v_max3_f32 v212, v168, v169, v170\n"
"v_max_f32 v212, v212, v171\n"
"ds_read2st64_b64 a[64:67], v38 offset0:80 offset1:81\n"
"ds_read2st64_b64 a[68:71], v38 offset0:82 offset1:83\n"
"ds_read2st64_b64 a[72:75], v38 offset0:84 offset1:85\n"
"ds_read2st64_b64 a[76:79], v38 offset0:86 offset1:87\n"
"v_max3_f32 v212, v212, v172, v173\n"
"v_max3_f32 v212, v212, v174, v175\n"
"v_mov_b32 v213, v212\n"
"s_nop 1\n"
"v_permlane32_swap_b32 v212, v213\n"
"v_max_f32 v212, v212, v213\n"
"v_mov_b32 v213, v212\n"
"s_nop 1\n"
"v_permlane16_swap_b32 v212, v213\n"
"v_max_f32 v212, v212, v213\n"
"v_sub_f32 v168, v168, v212\n"
"v_exp_f32 v168, v168\n"
"v_sub_f32 v156, 0, v212\n"
"v_mfma_f32_16x16x32_bf16 v[176:179], a[104:107], a[16:19], 0\n"
"ds_read2st64_b64 a[80:83], v38 offset0:88 offset1:89\n"
"v_sub_f32 v169, v169, v212\n"
"v_exp_f32 v169, v169\n"
"v_sub_f32 v157, 0, v212\n"
"v_mfma_f32_16x16x32_bf16 v[176:179], a[108:111], a[20:23], v[176:179]\n"
"ds_read2st64_b64 a[84:87], v38 offset0:90 offset1:91\n"
"v_cvt_pk_bf16_f32 v184, v168, v169\n"
"v_sub_f32 v170, v170, v212\n"
"v_exp_f32 v170, v170\n"
"v_sub_f32 v158, 0, v212\n"
"v_mfma_f32_16x16x32_bf16 v[176:179], a[112:115], a[24:27], v[176:179]\n"
"ds_read2st64_b64 a[88:91], v38 offset0:92 offset1:93\n"
"v_sub_f32 v171, v171, v212\n"
"v_exp_f32 v171, v171\n"
"v_sub_f32 v159, 0, v212\n"
"v_mfma_f32_16x16x32_bf16 v[176:179], a[116:119], a[28:31], v[176:179]\n"
"ds_read2st64_b64 a[92:95], v38 offset0:94 offset1:95\n"
"v_cvt_pk_bf16_f32 v185, v170, v171\n"
"v_sub_f32 v172, v172, v212\n"
"v_exp_f32 v172, v172\n"
"v_mfma_f32_16x16x32_bf16 v[180:183], a[132:135], a[28:31], 0\n"
"v_sub_f32 v173, v173, v212\n"
"v_exp_f32 v173, v173\n"
"v_mfma_f32_16x16x32_bf16 v[180:183], a[128:131], a[24:27], v[180:183]\n"
"v_cvt_pk_bf16_f32 v186, v172, v173\n"
"v_sub_f32 v174, v174, v212\n"
"v_exp_f32 v174, v174\n"
"v_mfma_f32_16x16x32_bf16 v[180:183], a[124:127], a[20:23], v[180:183]\n"
"v_sub_f32 v175, v175, v212\n"
"v_exp_f32 v175, v175\n"
"v_mfma_f32_16x16x32_bf16 v[180:183], a[120:123], a[16:19], v[180:183]\n"
"v_cvt_pk_bf16_f32 v187, v174, v175\n"
"s_nop 1\n"
"v_mfma_f32_16x16x32_bf16 v[144:147], v[192:195], v[184:187], 0\n"
"s_waitcnt lgkmcnt(0)\n"
"s_barrier\n"
"v_max3_f32 v212, v176, v177, v178\n"
"v_max_f32 v212, v212, v179\n"
"v_mfma_f32_16x16x32_bf16 v[48:51], a[168:171], v[184:187], 0\n"
"ds_read_b128 a[136:139], v25 offset:8192\n"
"v_xor_b32 v29, s75, v29\n"
"v_mfma_f32_16x16x32_bf16 v[52:55], a[172:175], v[184:187], 0\n"
"ds_write_b128 v196, v[220:223]\n"
"v_mfma_f32_16x16x32_bf16 v[56:59], a[176:179], v[184:187], 0\n"
"ds_read_b128 a[140:143], v26 offset:8192\n"
"v_max3_f32 v212, v212, v180, v181\n"
"v_max3_f32 v212, v212, v182, v183\n"
"v_mfma_f32_16x16x32_bf16 v[60:63], a[180:183], v[184:187], 0\n"
"ds_write_b128 v196, v[224:227] offset:1024\n"
"v_mov_b32 v213, v212\n"
"v_mfma_f32_16x16x32_bf16 v[64:67], a[184:187], v[184:187], 0\n"
"ds_read_b128 a[144:147], v27 offset:8192\n"
"v_xor_b32 v30, s75, v30\n"
"v_permlane32_swap_b32 v212, v213\n"
"v_max_f32 v212, v212, v213\n"
"v_mfma_f32_16x16x32_bf16 v[68:71], a[188:191], v[184:187], 0\n"
"v_mov_b32 v213, v212\n"
"v_mfma_f32_16x16x32_bf16 v[72:75], a[192:195], v[184:187], 0\n"
"ds_read_b128 a[148:151], v28 offset:8192\n"
"v_permlane16_swap_b32 v212, v213\n"
"v_max_f32 v212, v212, v213\n"
"v_mfma_f32_16x16x32_bf16 v[76:79], a[196:199], v[184:187], 0\n"
"v_sub_f32 v176, v176, v212\n"
"v_exp_f32 v176, v176\n"
"v_sub_f32 v160, 0, v212\n"
"v_mfma_f32_16x16x32_bf16 v[168:171], a[104:107], a[32:35], 0\n"
"ds_read_b128 a[164:167], v28 offset:12288\n"
"v_xor_b32 v31, s75, v31\n"
"v_sub_f32 v177, v177, v212\n"
"v_exp_f32 v177, v177\n"
"v_sub_f32 v161, 0, v212\n"
"v_mfma_f32_16x16x32_bf16 v[168:171], a[108:111], a[36:39], v[168:171]\n"
"v_cvt_pk_bf16_f32 v188, v176, v177\n"
"v_sub_f32 v178, v178, v212\n"
"v_exp_f32 v178, v178\n"
"v_sub_f32 v162, 0, v212\n"
"v_mfma_f32_16x16x32_bf16 v[168:171], a[112:115], a[40:43], v[168:171]\n"
"ds_read_b128 a[160:163], v27 offset:12288\n"
"v_sub_f32 v179, v179, v212\n"
"v_exp_f32 v179, v179\n"
"v_sub_f32 v163, 0, v212\n"
"v_mfma_f32_16x16x32_bf16 v[168:171], a[116:119], a[44:47], v[168:171]\n"
"v_cvt_pk_bf16_f32 v189, v178, v179\n"
"v_sub_f32 v180, v180, v212\n"
"v_exp_f32 v180, v180\n"
"v_mfma_f32_16x16x32_bf16 v[172:175], a[132:135], a[44:47], 0\n"
"ds_read_b128 a[156:159], v26 offset:12288\n"
"v_xor_b32 v32, s75, v32\n"
"v_sub_f32 v181, v181, v212\n"
"v_exp_f32 v181, v181\n"
"v_mfma_f32_16x16x32_bf16 v[172:175], a[128:131], a[40:43], v[172:175]\n"
"v_cvt_pk_bf16_f32 v190, v180, v181\n"
"v_sub_f32 v182, v182, v212\n"
"v_exp_f32 v182, v182\n"
"v_mfma_f32_16x16x32_bf16 v[172:175], a[124:127], a[36:39], v[172:175]\n"
"ds_read_b128 a[152:155], v25 offset:12288\n"
"v_sub_f32 v183, v183, v212\n"
"v_exp_f32 v183, v183\n"
"v_mfma_f32_16x16x32_bf16 v[172:175], a[120:123], a[32:35], v[172:175]\n"
"v_cvt_pk_bf16_f32 v191, v182, v183\n"
"s_nop 1\n"
"v_mfma_f32_16x16x32_bf16 v[148:151], v[192:195], v[188:191], 0\n"
"v_max3_f32 v212, v168, v169, v170\n"
"v_max_f32 v212, v212, v171\n"
"v_mfma_f32_16x16x32_bf16 v[80:83], a[168:171], v[188:191], 0\n"
"ds_read_b64_tr_b16 a[200:201], v29\n"
"v_xor_b32 v33, s75, v33\n"
"v_mfma_f32_16x16x32_bf16 v[84:87], a[172:175], v[188:191], 0\n"
"ds_write_b128 v196, v[228:231] offset:8192\n"
"v_mfma_f32_16x16x32_bf16 v[88:91], a[176:179], v[188:191], 0\n"
"ds_read_b64_tr_b16 a[202:203], v29 offset:4096\n"
"v_max3_f32 v212, v212, v172, v173\n"
"v_max3_f32 v212, v212, v174, v175\n"
"v_mfma_f32_16x16x32_bf16 v[92:95], a[180:183], v[188:191], 0\n"
"ds_write_b128 v196, v[232:235] offset:9216\n"
"v_mov_b32 v213, v212\n"
"v_mfma_f32_16x16x32_bf16 v[96:99], a[184:187], v[188:191], 0\n"
"ds_read_b64_tr_b16 a[204:205], v30\n"
"v_xor_b32 v34, s75, v34\n"
"v_permlane32_swap_b32 v212, v213\n"
"v_max_f32 v212, v212, v213\n"
"v_mfma_f32_16x16x32_bf16 v[100:103], a[188:191], v[188:191], 0\n"
"v_mov_b32 v213, v212\n"
"v_mfma_f32_16x16x32_bf16 v[104:107], a[192:195], v[188:191], 0\n"
"ds_read_b64_tr_b16 a[206:207], v30 offset:4096\n"
"v_permlane16_swap_b32 v212, v213\n"
"v_max_f32 v212, v212, v213\n"
"v_mfma_f32_16x16x32_bf16 v[108:111], a[196:199], v[188:191], 0\n"
"s_waitcnt lgkmcnt(6)\n"
"v_sub_f32 v168, v168, v212\n"
"v_exp_f32 v168, v168\n"
"v_sub_f32 v164, 0, v212\n"
"v_mfma_f32_16x16x32_bf16 v[176:179], a[136:139], a[0:3], v[156:159]\n"
"ds_read_b64_tr_b16 a[208:209], v31\n"
"v_xor_b32 v35, s75, v35\n"
"v_sub_f32 v169, v169, v212\n"
"v_exp_f32 v169, v169\n"
"v_sub_f32 v165, 0, v212\n"
"v_mfma_f32_16x16x32_bf16 v[176:179], a[140:143], a[4:7], v[176:179]\n"
"v_cvt_pk_bf16_f32 v184, v168, v169\n"
"v_sub_f32 v170, v170, v212\n"
"v_exp_f32 v170, v170\n"
"v_sub_f32 v166, 0, v212\n"
"v_mfma_f32_16x16x32_bf16 v[176:179], a[144:147], a[8:11], v[176:179]\n"
"ds_read_b64_tr_b16 a[210:211], v31 offset:4096\n"
"v_sub_f32 v171, v171, v212\n"
"v_exp_f32 v171, v171\n"
"v_sub_f32 v167, 0, v212\n"
"v_mfma_f32_16x16x32_bf16 v[176:179], a[148:151], a[12:15], v[176:179]\n"
"v_cvt_pk_bf16_f32 v185, v170, v171\n"
"v_sub_f32 v172, v172, v212\n"
"v_exp_f32 v172, v172\n"
"v_mfma_f32_16x16x32_bf16 v[180:183], a[164:167], a[12:15], v[156:159]\n"
"ds_read_b64_tr_b16 a[212:213], v32\n"
"v_xor_b32 v36, s75, v36\n"
"v_sub_f32 v173, v173, v212\n"
"v_exp_f32 v173, v173\n"
"v_mfma_f32_16x16x32_bf16 v[180:183], a[160:163], a[8:11], v[180:183]\n"
"v_cvt_pk_bf16_f32 v186, v172, v173\n"
"v_sub_f32 v174, v174, v212\n"
"v_exp_f32 v174, v174\n"
"v_mfma_f32_16x16x32_bf16 v[180:183], a[156:159], a[4:7], v[180:183]\n"
"s_xor_b32 s75, s75, 0x38000\n"
"ds_read_b64_tr_b16 a[214:215], v32 offset:4096\n"
"v_sub_f32 v175, v175, v212\n"
"v_exp_f32 v175, v175\n"
"v_mfma_f32_16x16x32_bf16 v[180:183], a[152:155], a[0:3], v[180:183]\n"
"v_cvt_pk_bf16_f32 v187, v174, v175\n"
"s_nop 1\n"
"v_mfma_f32_16x16x32_bf16 v[152:155], v[192:195], v[184:187], 0\n"
"v_exp_f32 v176, v176\n"
"v_mfma_f32_16x16x32_bf16 v[112:115], a[168:171], v[184:187], 0\n"
"ds_read_b64_tr_b16 a[216:217], v33\n"
"v_xor_b32 v25, s74, v25\n"
"v_mfma_f32_16x16x32_bf16 v[116:119], a[172:175], v[184:187], 0\n"
"v_exp_f32 v177, v177\n"
"v_mfma_f32_16x16x32_bf16 v[120:123], a[176:179], v[184:187], 0\n"
"ds_read_b64_tr_b16 a[218:219], v33 offset:4096\n"
"v_cvt_pk_bf16_f32 v188, v176, v177\n"
"v_mfma_f32_16x16x32_bf16 v[124:127], a[180:183], v[184:187], 0\n"
"v_exp_f32 v178, v178\n"
"v_mfma_f32_16x16x32_bf16 v[128:131], a[184:187], v[184:187], 0\n"
"ds_read_b64_tr_b16 a[220:221], v34\n"
"ds_write_b128 v196, v[236:239] offset:16384\n"
"v_xor_b32 v26, s74, v26\n"
"v_mfma_f32_16x16x32_bf16 v[132:135], a[188:191], v[184:187], 0\n"
"v_exp_f32 v179, v179\n"
"v_mfma_f32_16x16x32_bf16 v[136:139], a[192:195], v[184:187], 0\n"
"ds_read_b64_tr_b16 a[222:223], v34 offset:4096\n"
"v_cvt_pk_bf16_f32 v189, v178, v179\n"
"v_mfma_f32_16x16x32_bf16 v[140:143], a[196:199], v[184:187], 0\n"
"v_exp_f32 v180, v180\n"
"v_mfma_f32_16x16x32_bf16 v[168:171], a[136:139], a[16:19], v[160:163]\n"
"ds_read_b64_tr_b16 a[224:225], v35\n"
"v_xor_b32 v27, s74, v27\n"
"v_mfma_f32_16x16x32_bf16 v[168:171], a[140:143], a[20:23], v[168:171]\n"
"v_exp_f32 v181, v181\n"
"v_mfma_f32_16x16x32_bf16 v[168:171], a[144:147], a[24:27], v[168:171]\n"
"ds_read_b64_tr_b16 a[226:227], v35 offset:4096\n"
"v_cvt_pk_bf16_f32 v190, v180, v181\n"
"v_mfma_f32_16x16x32_bf16 v[168:171], a[148:151], a[28:31], v[168:171]\n"
"v_exp_f32 v182, v182\n"
"v_mfma_f32_16x16x32_bf16 v[172:175], a[164:167], a[28:31], v[160:163]\n"
"ds_read_b64_tr_b16 a[228:229], v36\n"
"v_xor_b32 v28, s74, v28\n"
"v_mfma_f32_16x16x32_bf16 v[172:175], a[160:163], a[24:27], v[172:175]\n"
"v_exp_f32 v183, v183\n"
"v_mfma_f32_16x16x32_bf16 v[172:175], a[156:159], a[20:23], v[172:175]\n"
"s_xor_b32 s74, s74, 0x8000\n"
"ds_read_b64_tr_b16 a[230:231], v36 offset:4096\n"
"v_cvt_pk_bf16_f32 v191, v182, v183\n"
"v_mfma_f32_16x16x32_bf16 v[172:175], a[152:155], a[16:19], v[172:175]\n"
"ds_write_b128 v196, v[240:243] offset:17408\n"
"v_lshlrev_b32 v45, 4, v1\n"
"v_mfma_f32_16x16x32_bf16 v[144:147], v[192:195], v[188:191], v[144:147]\n"
"s_barrier\n"
"v_exp_f32 v168, v168\n"
"s_waitcnt lgkmcnt(4)\n"
"v_mfma_f32_16x16x32_bf16 v[48:51], a[200:203], v[188:191], v[48:51]\n"
"ds_read_b128 a[104:107], v25\n"
"s_add_u32 s69, s64, 0\n"
"s_and_b32 s69, s69, 3\n"
"s_lshl_b32 s69, s69, 11\n"
"v_add_u32 v2, s69, v45\n"
"v_mfma_f32_16x16x32_bf16 v[52:55], a[204:207], v[188:191], v[52:55]\n"
"v_exp_f32 v169, v169\n"
"v_mfma_f32_16x16x32_bf16 v[56:59], a[208:211], v[188:191], v[56:59]\n"
"ds_read_b128 a[108:111], v26\n"
"v_cvt_pk_bf16_f32 v184, v168, v169\n"
"v_mfma_f32_16x16x32_bf16 v[60:63], a[212:215], v[188:191], v[60:63]\n"
"v_exp_f32 v170, v170\n"
"v_mfma_f32_16x16x32_bf16 v[64:67], a[216:219], v[188:191], v[64:67]\n"
"ds_read_b128 a[112:115], v27\n"
"ds_write_b128 v196, v[244:247] offset:24576\n"
"v_mfma_f32_16x16x32_bf16 v[68:71], a[220:223], v[188:191], v[68:71]\n"
"s_waitcnt lgkmcnt(5)\n"
"v_exp_f32 v171, v171\n"
"v_mfma_f32_16x16x32_bf16 v[72:75], a[224:227], v[188:191], v[72:75]\n"
"ds_read_b128 a[116:119], v28\n"
"v_cvt_pk_bf16_f32 v185, v170, v171\n"
"v_mfma_f32_16x16x32_bf16 v[76:79], a[228:231], v[188:191], v[76:79]\n"
"v_exp_f32 v172, v172\n"
"v_mfma_f32_16x16x32_bf16 v[176:179], a[136:139], a[32:35], v[164:167]\n"
"ds_read_b128 a[132:135], v28 offset:4096\n"
"s_add_u32 s69, s64, 1\n"
"s_and_b32 s69, s69, 3\n"
"s_lshl_b32 s69, s69, 11\n"
"v_add_u32 v3, s69, v45\n"
"v_mfma_f32_16x16x32_bf16 v[176:179], a[140:143], a[36:39], v[176:179]\n"
"v_exp_f32 v173, v173\n"
"v_mfma_f32_16x16x32_bf16 v[176:179], a[144:147], a[40:43], v[176:179]\n"
"ds_read_b128 a[128:131], v27 offset:4096\n"
"v_cvt_pk_bf16_f32 v186, v172, v173\n"
"v_mfma_f32_16x16x32_bf16 v[176:179], a[148:151], a[44:47], v[176:179]\n"
"v_exp_f32 v174, v174\n"
"v_mfma_f32_16x16x32_bf16 v[180:183], a[164:167], a[44:47], v[164:167]\n"
"ds_read_b128 a[124:127], v26 offset:4096\n"
"s_add_u32 s69, s64, 2\n"
"s_and_b32 s69, s69, 3\n"
"s_lshl_b32 s69, s69, 11\n"
"v_add_u32 v4, s69, v45\n"
"v_mfma_f32_16x16x32_bf16 v[180:183], a[160:163], a[40:43], v[180:183]\n"
"v_exp_f32 v175, v175\n"
"v_mfma_f32_16x16x32_bf16 v[180:183], a[156:159], a[36:39], v[180:183]\n"
"ds_read_b128 a[120:123], v25 offset:4096\n"
"v_cvt_pk_bf16_f32 v187, v174, v175\n"
"v_mfma_f32_16x16x32_bf16 v[180:183], a[152:155], a[32:35], v[180:183]\n"
"ds_write_b128 v196, v[248:251] offset:25600\n"
"s_add_u32 s69, s64, 3\n"
"s_and_b32 s69, s69, 3\n"
"s_lshl_b32 s69, s69, 11\n"
"v_add_u32 v5, s69, v45\n"
"v_mfma_f32_16x16x32_bf16 v[148:151], v[192:195], v[184:187], v[148:151]\n"
"v_exp_f32 v176, v176\n"
"v_mfma_f32_16x16x32_bf16 v[80:83], a[200:203], v[184:187], v[80:83]\n"
"ds_read_b64_tr_b16 a[168:169], v29 offset:8192\n"
"v_add_u32 v6, 8192, v45\n"
"v_mfma_f32_16x16x32_bf16 v[84:87], a[204:207], v[184:187], v[84:87]\n"
"v_exp_f32 v177, v177\n"
"v_mfma_f32_16x16x32_bf16 v[88:91], a[208:211], v[184:187], v[88:91]\n"
"ds_read_b64_tr_b16 a[170:171], v29 offset:12288\n"
"v_cvt_pk_bf16_f32 v188, v176, v177\n"
"v_mfma_f32_16x16x32_bf16 v[92:95], a[212:215], v[184:187], v[92:95]\n"
"v_exp_f32 v178, v178\n"
"v_mfma_f32_16x16x32_bf16 v[96:99], a[216:219], v[184:187], v[96:99]\n"
"ds_read_b64_tr_b16 a[172:173], v30 offset:8192\n"
"ds_write_b128 v196, v[252:255] offset:32768\n"
"v_mfma_f32_16x16x32_bf16 v[100:103], a[220:223], v[184:187], v[100:103]\n"
"v_exp_f32 v179, v179\n"
"v_mfma_f32_16x16x32_bf16 v[104:107], a[224:227], v[184:187], v[104:107]\n"
"ds_read_b64_tr_b16 a[174:175], v30 offset:12288\n"
"v_cvt_pk_bf16_f32 v189, v178, v179\n"
"v_mfma_f32_16x16x32_bf16 v[108:111], a[228:231], v[184:187], v[108:111]\n"
"s_waitcnt lgkmcnt(5)\n"
"v_exp_f32 v180, v180\n"
"v_mfma_f32_16x16x32_bf16 v[168:171], a[104:107], a[0:3], v[156:159]\n"
"ds_read_b64_tr_b16 a[176:177], v31 offset:8192\n"
"s_add_u32 s69, s25, 65536\n"
"v_add_u32 v15, s69, v2\n"
"v_mfma_f32_16x16x32_bf16 v[168:171], a[108:111], a[4:7], v[168:171]\n"
"v_exp_f32 v181, v181\n"
"v_mfma_f32_16x16x32_bf16 v[168:171], a[112:115], a[8:11], v[168:171]\n"
"ds_read_b64_tr_b16 a[178:179], v31 offset:12288\n"
"v_cvt_pk_bf16_f32 v190, v180, v181\n"
"v_mfma_f32_16x16x32_bf16 v[168:171], a[116:119], a[12:15], v[168:171]\n"
"v_exp_f32 v182, v182\n"
"v_mfma_f32_16x16x32_bf16 v[172:175], a[132:135], a[12:15], v[156:159]\n"
"ds_read_b64_tr_b16 a[180:181], v32 offset:8192\n"
"v_add_u32 v16, s69, v3\n"
"v_mfma_f32_16x16x32_bf16 v[172:175], a[128:131], a[8:11], v[172:175]\n"
"v_exp_f32 v183, v183\n"
"v_mfma_f32_16x16x32_bf16 v[172:175], a[124:127], a[4:7], v[172:175]\n"
"ds_read_b64_tr_b16 a[182:183], v32 offset:12288\n"
"v_cvt_pk_bf16_f32 v191, v182, v183\n"
"v_mfma_f32_16x16x32_bf16 v[172:175], a[120:123], a[0:3], v[172:175]\n"
"ds_write_b128 v196, v[200:203] offset:33792\n"
"v_add_u32 v21, s69, v4\n"
"v_mfma_f32_16x16x32_bf16 v[152:155], v[192:195], v[188:191], v[152:155]\n"
"v_exp_f32 v168, v168\n"
"v_mfma_f32_16x16x32_bf16 v[112:115], a[200:203], v[188:191], v[112:115]\n"
"ds_read_b64_tr_b16 a[184:185], v33 offset:8192\n"
"v_add_u32 v22, s69, v5\n"
"v_mfma_f32_16x16x32_bf16 v[116:119], a[204:207], v[188:191], v[116:119]\n"
"v_exp_f32 v169, v169\n"
"v_mfma_f32_16x16x32_bf16 v[120:123], a[208:211], v[188:191], v[120:123]\n"
"ds_read_b64_tr_b16 a[186:187], v33 offset:12288\n"
"v_cvt_pk_bf16_f32 v184, v168, v169\n"
"v_mfma_f32_16x16x32_bf16 v[124:127], a[212:215], v[188:191], v[124:127]\n"
"v_exp_f32 v170, v170\n"
"v_mfma_f32_16x16x32_bf16 v[128:131], a[216:219], v[188:191], v[128:131]\n"
"ds_read_b64_tr_b16 a[188:189], v34 offset:8192\n"
"v_add_u32 v37, s69, v6\n"
"v_mfma_f32_16x16x32_bf16 v[132:135], a[220:223], v[188:191], v[132:135]\n"
"v_exp_f32 v171, v171\n"
"v_mfma_f32_16x16x32_bf16 v[136:139], a[224:227], v[188:191], v[136:139]\n"
"ds_read_b64_tr_b16 a[190:191], v34 offset:12288\n"
"v_cvt_pk_bf16_f32 v185, v170, v171\n"
"v_mfma_f32_16x16x32_bf16 v[140:143], a[228:231], v[188:191], v[140:143]\n"
"v_exp_f32 v172, v172\n"
"v_mfma_f32_16x16x32_bf16 v[176:179], a[104:107], a[16:19], v[160:163]\n"
"ds_read_b64_tr_b16 a[192:193], v35 offset:8192\n"
"v_lshrrev_b32 v40, 3, v1\n"
"v_mfma_f32_16x16x32_bf16 v[176:179], a[108:111], a[20:23], v[176:179]\n"
"v_exp_f32 v173, v173\n"
"v_mfma_f32_16x16x32_bf16 v[176:179], a[112:115], a[24:27], v[176:179]\n"
"ds_read_b64_tr_b16 a[194:195], v35 offset:12288\n"
"v_cvt_pk_bf16_f32 v186, v172, v173\n"
"v_mfma_f32_16x16x32_bf16 v[176:179], a[116:119], a[28:31], v[176:179]\n"
"v_exp_f32 v174, v174\n"
"v_mfma_f32_16x16x32_bf16 v[180:183], a[132:135], a[28:31], v[160:163]\n"
"ds_read_b64_tr_b16 a[196:197], v36 offset:8192\n"
"v_and_b32 v41, 7, v1\n"
"v_mfma_f32_16x16x32_bf16 v[180:183], a[128:131], a[24:27], v[180:183]\n"
"v_exp_f32 v175, v175\n"
"v_mfma_f32_16x16x32_bf16 v[180:183], a[124:127], a[20:23], v[180:183]\n"
"ds_read_b64_tr_b16 a[198:199], v36 offset:12288\n"
"v_cvt_pk_bf16_f32 v187, v174, v175\n"
"v_mfma_f32_16x16x32_bf16 v[180:183], a[120:123], a[16:19], v[180:183]\n"
"v_lshrrev_b32 v42, 1, v40\n"
"v_lshlrev_b32 v42, 1, v42\n"
"v_mfma_f32_16x16x32_bf16 v[144:147], v[192:195], v[184:187], v[144:147]\n"
"s_waitcnt vmcnt(0)\n"
"s_barrier\n"
"v_exp_f32 v176, v176\n"
"s_waitcnt lgkmcnt(4)\n"
"v_mfma_f32_16x16x32_bf16 v[48:51], a[168:171], v[184:187], v[48:51]\n"
"ds_read_b128 a[136:139], v25 offset:8192\n"
"v_xor_b32 v29, s75, v29\n"
"v_mfma_f32_16x16x32_bf16 v[52:55], a[172:175], v[184:187], v[52:55]\n"
"v_exp_f32 v177, v177\n"
"v_mfma_f32_16x16x32_bf16 v[56:59], a[176:179], v[184:187], v[56:59]\n"
"ds_read_b128 a[140:143], v26 offset:8192\n"
"v_cvt_pk_bf16_f32 v188, v176, v177\n"
"v_mfma_f32_16x16x32_bf16 v[60:63], a[180:183], v[184:187], v[60:63]\n"
"v_exp_f32 v178, v178\n"
"v_mfma_f32_16x16x32_bf16 v[64:67], a[184:187], v[184:187], v[64:67]\n"
"ds_read_b128 a[144:147], v27 offset:8192\n"
"v_xor_b32 v30, s75, v30\n"
"v_mfma_f32_16x16x32_bf16 v[68:71], a[188:191], v[184:187], v[68:71]\n"
"s_waitcnt lgkmcnt(4)\n"
"v_exp_f32 v179, v179\n"
"v_mfma_f32_16x16x32_bf16 v[72:75], a[192:195], v[184:187], v[72:75]\n"
"s_waitcnt lgkmcnt(3)\n"
"ds_read_b128 a[148:151], v28 offset:8192\n"
"v_cvt_pk_bf16_f32 v189, v178, v179\n"
"v_mfma_f32_16x16x32_bf16 v[76:79], a[196:199], v[184:187], v[76:79]\n"
"v_exp_f32 v180, v180\n"
"v_mfma_f32_16x16x32_bf16 v[168:171], a[104:107], a[32:35], v[164:167]\n"
"ds_read_b128 a[164:167], v28 offset:12288\n"
"v_xor_b32 v31, s75, v31\n"
"v_mfma_f32_16x16x32_bf16 v[168:171], a[108:111], a[36:39], v[168:171]\n"
"v_exp_f32 v181, v181\n"
"v_mfma_f32_16x16x32_bf16 v[168:171], a[112:115], a[40:43], v[168:171]\n"
"ds_read_b128 a[160:163], v27 offset:12288\n"
"v_cvt_pk_bf16_f32 v190, v180, v181\n"
"v_mfma_f32_16x16x32_bf16 v[168:171], a[116:119], a[44:47], v[168:171]\n"
"v_exp_f32 v182, v182\n"
"v_mfma_f32_16x16x32_bf16 v[172:175], a[132:135], a[44:47], v[164:167]\n"
"ds_read_b128 a[156:159], v26 offset:12288\n"
"v_xor_b32 v32, s75, v32\n"
"v_mfma_f32_16x16x32_bf16 v[172:175], a[128:131], a[40:43], v[172:175]\n"
"v_exp_f32 v183, v183\n"
"v_mfma_f32_16x16x32_bf16 v[172:175], a[124:127], a[36:39], v[172:175]\n"
"ds_read_b128 a[152:155], v25 offset:12288\n"
"v_cvt_pk_bf16_f32 v191, v182, v183\n"
"v_mfma_f32_16x16x32_bf16 v[172:175], a[120:123], a[32:35], v[172:175]\n"
"v_xor_b32 v42, v42, v41\n"
"v_lshlrev_b32 v42, 4, v42\n"
"v_mfma_f32_16x16x32_bf16 v[148:151], v[192:195], v[188:191], v[148:151]\n"
"v_exp_f32 v168, v168\n"
"v_mfma_f32_16x16x32_bf16 v[80:83], a[168:171], v[188:191], v[80:83]\n"
"ds_read_b64_tr_b16 a[200:201], v29\n"
"v_xor_b32 v33, s75, v33\n"
"v_mfma_f32_16x16x32_bf16 v[84:87], a[172:175], v[188:191], v[84:87]\n"
"v_exp_f32 v169, v169\n"
"v_mfma_f32_16x16x32_bf16 v[88:91], a[176:179], v[188:191], v[88:91]\n"
"ds_read_b64_tr_b16 a[202:203], v29 offset:4096\n"
"v_cvt_pk_bf16_f32 v184, v168, v169\n"
"v_mfma_f32_16x16x32_bf16 v[92:95], a[180:183], v[188:191], v[92:95]\n"
"v_exp_f32 v170, v170\n"
"v_mfma_f32_16x16x32_bf16 v[96:99], a[184:187], v[188:191], v[96:99]\n"
"ds_read_b64_tr_b16 a[204:205], v30\n"
"v_xor_b32 v34, s75, v34\n"
"v_mfma_f32_16x16x32_bf16 v[100:103], a[188:191], v[188:191], v[100:103]\n"
"v_exp_f32 v171, v171\n"
"v_mfma_f32_16x16x32_bf16 v[104:107], a[192:195], v[188:191], v[104:107]\n"
"ds_read_b64_tr_b16 a[206:207], v30 offset:4096\n"
"v_cvt_pk_bf16_f32 v185, v170, v171\n"
"v_mfma_f32_16x16x32_bf16 v[108:111], a[196:199], v[188:191], v[108:111]\n"
"s_waitcnt lgkmcnt(4)\n"
"v_exp_f32 v172, v172\n"
"v_mfma_f32_16x16x32_bf16 v[176:179], a[136:139], a[0:3], v[156:159]\n"
"ds_read_b64_tr_b16 a[208:209], v31\n"
"v_xor_b32 v35, s75, v35\n"
"v_mfma_f32_16x16x32_bf16 v[176:179], a[140:143], a[4:7], v[176:179]\n"
"v_exp_f32 v173, v173\n"
"v_mfma_f32_16x16x32_bf16 v[176:179], a[144:147], a[8:11], v[176:179]\n"
"ds_read_b64_tr_b16 a[210:211], v31 offset:4096\n"
"v_cvt_pk_bf16_f32 v186, v172, v173\n"
"v_mfma_f32_16x16x32_bf16 v[176:179], a[148:151], a[12:15], v[176:179]\n"
"v_exp_f32 v174, v174\n"
"v_mfma_f32_16x16x32_bf16 v[180:183], a[164:167], a[12:15], v[156:159]\n"
"ds_read_b64_tr_b16 a[212:213], v32\n"
"v_xor_b32 v36, s75, v36\n"
"v_mfma_f32_16x16x32_bf16 v[180:183], a[160:163], a[8:11], v[180:183]\n"
"v_exp_f32 v175, v175\n"
"v_mfma_f32_16x16x32_bf16 v[180:183], a[156:159], a[4:7], v[180:183]\n"
"s_xor_b32 s75, s75, 0x38000\n"
"ds_read_b64_tr_b16 a[214:215], v32 offset:4096\n"
"v_cvt_pk_bf16_f32 v187, v174, v175\n"
"v_mfma_f32_16x16x32_bf16 v[180:183], a[152:155], a[0:3], v[180:183]\n"
"v_lshlrev_b32 v43, 11, v40\n"
"v_add_u32 v7, v42, v43\n"
"v_mfma_f32_16x16x32_bf16 v[152:155], v[192:195], v[184:187], v[152:155]\n"
"v_exp_f32 v176, v176\n"
"v_mfma_f32_16x16x32_bf16 v[112:115], a[168:171], v[184:187], v[112:115]\n"
"ds_read_b64_tr_b16 a[216:217], v33\n"
"v_xor_b32 v25, s74, v25\n"
"v_add_u32 v8, 15360, v7\n"
"v_mfma_f32_16x16x32_bf16 v[116:119], a[172:175], v[184:187], v[116:119]\n"
"v_exp_f32 v177, v177\n"
"v_mfma_f32_16x16x32_bf16 v[120:123], a[176:179], v[184:187], v[120:123]\n"
"ds_read_b64_tr_b16 a[218:219], v33 offset:4096\n"
"v_cvt_pk_bf16_f32 v188, v176, v177\n"
"v_mfma_f32_16x16x32_bf16 v[124:127], a[180:183], v[184:187], v[124:127]\n"
"v_exp_f32 v178, v178\n"
"v_mfma_f32_16x16x32_bf16 v[128:131], a[184:187], v[184:187], v[128:131]\n"
"ds_read_b64_tr_b16 a[220:221], v34\n"
"v_xor_b32 v26, s74, v26\n"
"v_add_u32 v9, 30720, v7\n"
"v_mfma_f32_16x16x32_bf16 v[132:135], a[188:191], v[184:187], v[132:135]\n"
"v_exp_f32 v179, v179\n"
"v_mfma_f32_16x16x32_bf16 v[136:139], a[192:195], v[184:187], v[136:139]\n"
"ds_read_b64_tr_b16 a[222:223], v34 offset:4096\n"
"v_cvt_pk_bf16_f32 v189, v178, v179\n"
"v_mfma_f32_16x16x32_bf16 v[140:143], a[196:199], v[184:187], v[140:143]\n"
"v_exp_f32 v180, v180\n"
"v_mfma_f32_16x16x32_bf16 v[168:171], a[136:139], a[16:19], v[160:163]\n"
"ds_read_b64_tr_b16 a[224:225], v35\n"
"v_xor_b32 v27, s74, v27\n"
"v_add_u32 v10, 46080, v7\n"
"v_mfma_f32_16x16x32_bf16 v[168:171], a[140:143], a[20:23], v[168:171]\n"
"v_exp_f32 v181, v181\n"
"v_mfma_f32_16x16x32_bf16 v[168:171], a[144:147], a[24:27], v[168:171]\n"
"ds_read_b64_tr_b16 a[226:227], v35 offset:4096\n"
"v_cvt_pk_bf16_f32 v190, v180, v181\n"
"v_mfma_f32_16x16x32_bf16 v[168:171], a[148:151], a[28:31], v[168:171]\n"
"v_exp_f32 v182, v182\n"
"v_mfma_f32_16x16x32_bf16 v[172:175], a[164:167], a[28:31], v[160:163]\n"
"ds_read_b64_tr_b16 a[228:229], v36\n"
"v_xor_b32 v28, s74, v28\n"
"s_sub_u32 s69, s24, s65\n"
"v_add_u32 v11, s69, v11\n"
"v_add_u32 v12, s69, v12\n"
"v_add_u32 v13, s69, v13\n"
"v_add_u32 v14, s69, v14\n"
"s_xor_b32 s31, s26, 1792\n"
"s_and_b32 s65, s79, 7\n"
"s_and_b32 s66, s79, 8\n"
"s_lshl_b32 s68, s79, 2\n"
"s_add_u32 s68, s68, s64\n"
"s_xor_b32 s67, s68, 56\n"
"s_lshl_b32 s34, s79, 6\n"
"s_cmp_eq_u32 s64, 3\n"
"s_cselect_b32 s34, s34, 0x10000\n"
"s_cmp_eq_u32 s68, 3\n"
"s_cselect_b32 s35, 0, 0x10000\n"
"v_and_b32 v40, 7, v1\n"
"v_add_u32 v40, s66, v40\n"
"v_lshlrev_b32 v38, 6, v40\n"
"s_lshl_b32 s69, s64, 10\n"
"v_add_u32 v18, s69, v45\n"
"v_add_u32 v17, 65536, v18\n"
"s_lshr_b32 s69, s48, 7\n"
"s_sub_u32 s69, 3, s69\n"
"s_and_b32 s69, s69, 7\n"
"s_lshl_b32 s69, s69, 7\n"
"s_lshr_b32 s70, %1, 7\n"
"s_lshl_b32 s70, s70, 10\n"
"s_add_u32 s69, s69, s70\n"
"s_lshr_b32 s70, %1, 6\n"
"s_and_b32 s70, s70, 1\n"
"s_add_u32 s70, s70, s66\n"
"s_lshl_b32 s70, s70, 19\n"
"s_add_u32 s69, s69, s70\n"
"s_lshr_b32 s70, %1, 3\n"
"s_and_b32 s70, s70, 7\n"
"s_lshl_b32 s70, s70, 15\n"
"s_add_u32 s69, s69, s70\n"
"v_lshrrev_b32 v24, 2, v1\n"
"v_lshlrev_b32 v24, 11, v24\n"
"v_add_u32 v24, s69, v24\n"
"s_mul_i32 s12, s79, 73728\n"
"s_add_u32 s12, s12, 36864\n"
"s_add_u32 s13, s12, 9216\n"
"s_add_u32 s14, s12, 18432\n"
"s_add_u32 s15, s12, 27648\n"
"v_add_u32 v19, -65536, v39\n"
"v_mfma_f32_16x16x32_bf16 v[172:175], a[160:163], a[24:27], v[172:175]\n"
"v_exp_f32 v183, v183\n"
"v_mfma_f32_16x16x32_bf16 v[172:175], a[156:159], a[20:23], v[172:175]\n"
"s_xor_b32 s74, s74, 0x8000\n"
"ds_read_b64_tr_b16 a[230:231], v36 offset:4096\n"
"v_cvt_pk_bf16_f32 v191, v182, v183\n"
"v_mfma_f32_16x16x32_bf16 v[172:175], a[152:155], a[16:19], v[172:175]\n"
"s_nop 0\n"
"v_mfma_f32_16x16x32_bf16 v[144:147], v[192:195], v[188:191], v[144:147]\n"
"s_barrier\n"
"v_exp_f32 v168, v168\n"
"s_waitcnt lgkmcnt(1)\n"
"v_mfma_f32_16x16x32_bf16 v[48:51], a[200:203], v[188:191], v[48:51]\n"
"ds_read_b128 a[104:107], v25\n"
"v_mfma_f32_16x16x32_bf16 v[52:55], a[204:207], v[188:191], v[52:55]\n"
"v_exp_f32 v169, v169\n"
"v_mfma_f32_16x16x32_bf16 v[56:59], a[208:211], v[188:191], v[56:59]\n"
"ds_read_b128 a[108:111], v26\n"
"v_cvt_pk_bf16_f32 v184, v168, v169\n"
"v_mfma_f32_16x16x32_bf16 v[60:63], a[212:215], v[188:191], v[60:63]\n"
"v_exp_f32 v170, v170\n"
"v_mfma_f32_16x16x32_bf16 v[64:67], a[216:219], v[188:191], v[64:67]\n"
"ds_read_b128 a[112:115], v27\n"
"v_mfma_f32_16x16x32_bf16 v[68:71], a[220:223], v[188:191], v[68:71]\n"
"v_exp_f32 v171, v171\n"
"v_mfma_f32_16x16x32_bf16 v[72:75], a[224:227], v[188:191], v[72:75]\n"
"s_waitcnt lgkmcnt(3)\n"
"ds_read_b128 a[116:119], v28\n"
"v_cvt_pk_bf16_f32 v185, v170, v171\n"
"v_mfma_f32_16x16x32_bf16 v[76:79], a[228:231], v[188:191], v[76:79]\n"
"v_exp_f32 v172, v172\n"
"v_mfma_f32_16x16x32_bf16 v[176:179], a[136:139], a[32:35], v[164:167]\n"
"ds_read_b128 a[132:135], v28 offset:4096\n"
"v_mfma_f32_16x16x32_bf16 v[176:179], a[140:143], a[36:39], v[176:179]\n"
"v_exp_f32 v173, v173\n"
"v_mfma_f32_16x16x32_bf16 v[176:179], a[144:147], a[40:43], v[176:179]\n"
"ds_read_b128 a[128:131], v27 offset:4096\n"
"v_cvt_pk_bf16_f32 v186, v172, v173\n"
"v_mfma_f32_16x16x32_bf16 v[176:179], a[148:151], a[44:47], v[176:179]\n"
"v_exp_f32 v174, v174\n"
"v_mfma_f32_16x16x32_bf16 v[180:183], a[164:167], a[44:47], v[164:167]\n"
"ds_read_b128 a[124:127], v26 offset:4096\n"
"v_mfma_f32_16x16x32_bf16 v[180:183], a[160:163], a[40:43], v[180:183]\n"
"v_exp_f32 v175, v175\n"
"v_mfma_f32_16x16x32_bf16 v[180:183], a[156:159], a[36:39], v[180:183]\n"
"ds_read_b128 a[120:123], v25 offset:4096\n"
"v_cvt_pk_bf16_f32 v187, v174, v175\n"
"v_mfma_f32_16x16x32_bf16 v[180:183], a[152:155], a[32:35], v[180:183]\n"
"s_nop 0\n"
"v_mfma_f32_16x16x32_bf16 v[148:151], v[192:195], v[184:187], v[148:151]\n"
"v_exp_f32 v176, v176\n"
"v_mfma_f32_16x16x32_bf16 v[80:83], a[200:203], v[184:187], v[80:83]\n"
"ds_read_b64_tr_b16 a[168:169], v29 offset:8192\n"
"v_mfma_f32_16x16x32_bf16 v[84:87], a[204:207], v[184:187], v[84:87]\n"
"v_exp_f32 v177, v177\n"
"v_mfma_f32_16x16x32_bf16 v[88:91], a[208:211], v[184:187], v[88:91]\n"
"ds_read_b64_tr_b16 a[170:171], v29 offset:12288\n"
"v_cvt_pk_bf16_f32 v188, v176, v177\n"
"v_mfma_f32_16x16x32_bf16 v[92:95], a[212:215], v[184:187], v[92:95]\n"
"v_exp_f32 v178, v178\n"
"v_mfma_f32_16x16x32_bf16 v[96:99], a[216:219], v[184:187], v[96:99]\n"
"ds_read_b64_tr_b16 a[172:173], v30 offset:8192\n"
"v_mfma_f32_16x16x32_bf16 v[100:103], a[220:223], v[184:187], v[100:103]\n"
"v_exp_f32 v179, v179\n"
"v_mfma_f32_16x16x32_bf16 v[104:107], a[224:227], v[184:187], v[104:107]\n"
"ds_read_b64_tr_b16 a[174:175], v30 offset:12288\n"
"v_cvt_pk_bf16_f32 v189, v178, v179\n"
"v_mfma_f32_16x16x32_bf16 v[108:111], a[228:231], v[184:187], v[108:111]\n"
"s_waitcnt lgkmcnt(5)\n"
"v_exp_f32 v180, v180\n"
"v_mfma_f32_16x16x32_bf16 v[168:171], a[104:107], a[0:3], v[156:159]\n"
"ds_read_b64_tr_b16 a[176:177], v31 offset:8192\n"
"v_mfma_f32_16x16x32_bf16 v[168:171], a[108:111], a[4:7], v[168:171]\n"
"v_exp_f32 v181, v181\n"
"v_mfma_f32_16x16x32_bf16 v[168:171], a[112:115], a[8:11], v[168:171]\n"
"ds_read_b64_tr_b16 a[178:179], v31 offset:12288\n"
"v_cvt_pk_bf16_f32 v190, v180, v181\n"
"v_mfma_f32_16x16x32_bf16 v[168:171], a[116:119], a[12:15], v[168:171]\n"
"v_exp_f32 v182, v182\n"
"v_mfma_f32_16x16x32_bf16 v[172:175], a[132:135], a[12:15], v[156:159]\n"
"ds_read_b64_tr_b16 a[180:181], v32 offset:8192\n"
"v_mfma_f32_16x16x32_bf16 v[172:175], a[128:131], a[8:11], v[172:175]\n"
"v_exp_f32 v183, v183\n"
"v_mfma_f32_16x16x32_bf16 v[172:175], a[124:127], a[4:7], v[172:175]\n"
"s_waitcnt lgkmcnt(5)\n"
"ds_read_b64_tr_b16 a[182:183], v32 offset:12288\n"
"v_cvt_pk_bf16_f32 v191, v182, v183\n"
"v_mfma_f32_16x16x32_bf16 v[172:175], a[120:123], a[0:3], v[172:175]\n"
"s_nop 0\n"
"v_mfma_f32_16x16x32_bf16 v[152:155], v[192:195], v[188:191], v[152:155]\n"
"v_exp_f32 v168, v168\n"
"v_mfma_f32_16x16x32_bf16 v[112:115], a[200:203], v[188:191], v[112:115]\n"
"ds_read_b64_tr_b16 a[184:185], v33 offset:8192\n"
"v_mfma_f32_16x16x32_bf16 v[116:119], a[204:207], v[188:191], v[116:119]\n"
"v_exp_f32 v169, v169\n"
"v_mfma_f32_16x16x32_bf16 v[120:123], a[208:211], v[188:191], v[120:123]\n"
"ds_read_b64_tr_b16 a[186:187], v33 offset:12288\n"
"v_cvt_pk_bf16_f32 v184, v168, v169\n"
"v_mfma_f32_16x16x32_bf16 v[124:127], a[212:215], v[188:191], v[124:127]\n"
"v_exp_f32 v170, v170\n"
"v_mfma_f32_16x16x32_bf16 v[128:131], a[216:219], v[188:191], v[128:131]\n"
"ds_read_b64_tr_b16 a[188:189], v34 offset:8192\n"
"v_mfma_f32_16x16x32_bf16 v[132:135], a[220:223], v[188:191], v[132:135]\n"
"v_exp_f32 v171, v171\n"
"v_mfma_f32_16x16x32_bf16 v[136:139], a[224:227], v[188:191], v[136:139]\n"
"ds_read_b64_tr_b16 a[190:191], v34 offset:12288\n"
"v_cvt_pk_bf16_f32 v185, v170, v171\n"
"v_mfma_f32_16x16x32_bf16 v[140:143], a[228:231], v[188:191], v[140:143]\n"
"v_exp_f32 v172, v172\n"
"v_mfma_f32_16x16x32_bf16 v[176:179], a[104:107], a[16:19], v[160:163]\n"
"ds_read_b64_tr_b16 a[192:193], v35 offset:8192\n"
"v_mfma_f32_16x16x32_bf16 v[176:179], a[108:111], a[20:23], v[176:179]\n"
"v_exp_f32 v173, v173\n"
"v_mfma_f32_16x16x32_bf16 v[176:179], a[112:115], a[24:27], v[176:179]\n"
"ds_read_b64_tr_b16 a[194:195], v35 offset:12288\n"
"v_cvt_pk_bf16_f32 v186, v172, v173\n"
"v_mfma_f32_16x16x32_bf16 v[176:179], a[116:119], a[28:31], v[176:179]\n"
"v_exp_f32 v174, v174\n"
"v_mfma_f32_16x16x32_bf16 v[180:183], a[132:135], a[28:31], v[160:163]\n"
"ds_read_b64_tr_b16 a[196:197], v36 offset:8192\n"
"v_mfma_f32_16x16x32_bf16 v[180:183], a[128:131], a[24:27], v[180:183]\n"
"v_exp_f32 v175, v175\n"
"v_mfma_f32_16x16x32_bf16 v[180:183], a[124:127], a[20:23], v[180:183]\n"
"ds_read_b64_tr_b16 a[198:199], v36 offset:12288\n"
"v_cvt_pk_bf16_f32 v187, v174, v175\n"
"v_mfma_f32_16x16x32_bf16 v[180:183], a[120:123], a[16:19], v[180:183]\n"
"s_nop 0\n"
"v_mfma_f32_16x16x32_bf16 v[144:147], v[192:195], v[184:187], v[144:147]\n"
"s_barrier\n"
"v_exp_f32 v176, v176\n"
"v_mfma_f32_16x16x32_bf16 v[48:51], a[168:171], v[184:187], v[48:51]\n"
"s_waitcnt lgkmcnt(4)\n"
"ds_read_b128 a[136:139], v25 offset:8192\n"
"v_xor_b32 v29, s75, v29\n"
"v_mfma_f32_16x16x32_bf16 v[52:55], a[172:175], v[184:187], v[52:55]\n"
"v_exp_f32 v177, v177\n"
"v_mfma_f32_16x16x32_bf16 v[56:59], a[176:179], v[184:187], v[56:59]\n"
"ds_read_b128 a[140:143], v26 offset:8192\n"
"v_cvt_pk_bf16_f32 v188, v176, v177\n"
"v_mfma_f32_16x16x32_bf16 v[60:63], a[180:183], v[184:187], v[60:63]\n"
"v_exp_f32 v178, v178\n"
"v_mfma_f32_16x16x32_bf16 v[64:67], a[184:187], v[184:187], v[64:67]\n"
"ds_read_b128 a[144:147], v27 offset:8192\n"
"v_xor_b32 v30, s75, v30\n"
"v_mfma_f32_16x16x32_bf16 v[68:71], a[188:191], v[184:187], v[68:71]\n"
"s_waitcnt lgkmcnt(4)\n"
"v_exp_f32 v179, v179\n"
"v_mfma_f32_16x16x32_bf16 v[72:75], a[192:195], v[184:187], v[72:75]\n"
"s_waitcnt lgkmcnt(3)\n"
"ds_read_b128 a[148:151], v28 offset:8192\n"
"v_cvt_pk_bf16_f32 v189, v178, v179\n"
"v_mfma_f32_16x16x32_bf16 v[76:79], a[196:199], v[184:187], v[76:79]\n"
"v_exp_f32 v180, v180\n"
"v_mfma_f32_16x16x32_bf16 v[168:171], a[104:107], a[32:35], v[164:167]\n"
"ds_read_b128 a[164:167], v28 offset:12288\n"
"v_xor_b32 v31, s75, v31\n"
"v_mfma_f32_16x16x32_bf16 v[168:171], a[108:111], a[36:39], v[168:171]\n"
"v_exp_f32 v181, v181\n"
"v_mfma_f32_16x16x32_bf16 v[168:171], a[112:115], a[40:43], v[168:171]\n"
"ds_read_b128 a[160:163], v27 offset:12288\n"
"v_cvt_pk_bf16_f32 v190, v180, v181\n"
"v_mfma_f32_16x16x32_bf16 v[168:171], a[116:119], a[44:47], v[168:171]\n"
"v_exp_f32 v182, v182\n"
"v_mfma_f32_16x16x32_bf16 v[172:175], a[132:135], a[44:47], v[164:167]\n"
"ds_read_b128 a[156:159], v26 offset:12288\n"
"v_xor_b32 v32, s75, v32\n"
"v_mfma_f32_16x16x32_bf16 v[172:175], a[128:131], a[40:43], v[172:175]\n"
"v_exp_f32 v183, v183\n"
"v_mfma_f32_16x16x32_bf16 v[172:175], a[124:127], a[36:39], v[172:175]\n"
"ds_read_b128 a[152:155], v25 offset:12288\n"
"v_cvt_pk_bf16_f32 v191, v182, v183\n"
"v_mfma_f32_16x16x32_bf16 v[172:175], a[120:123], a[32:35], v[172:175]\n"
"s_nop 0\n"
"v_mfma_f32_16x16x32_bf16 v[148:151], v[192:195], v[188:191], v[148:151]\n"
"v_exp_f32 v168, v168\n"
"v_mfma_f32_16x16x32_bf16 v[80:83], a[168:171], v[188:191], v[80:83]\n"
"ds_read_b64_tr_b16 a[200:201], v29\n"
"v_xor_b32 v33, s75, v33\n"
"v_mfma_f32_16x16x32_bf16 v[84:87], a[172:175], v[188:191], v[84:87]\n"
"v_exp_f32 v169, v169\n"
"v_mfma_f32_16x16x32_bf16 v[88:91], a[176:179], v[188:191], v[88:91]\n"
"ds_read_b64_tr_b16 a[202:203], v29 offset:4096\n"
"v_cvt_pk_bf16_f32 v184, v168, v169\n"
"v_mfma_f32_16x16x32_bf16 v[92:95], a[180:183], v[188:191], v[92:95]\n"
"v_exp_f32 v170, v170\n"
"v_mfma_f32_16x16x32_bf16 v[96:99], a[184:187], v[188:191], v[96:99]\n"
"ds_read_b64_tr_b16 a[204:205], v30\n"
"v_xor_b32 v34, s75, v34\n"
"v_mfma_f32_16x16x32_bf16 v[100:103], a[188:191], v[188:191], v[100:103]\n"
"v_exp_f32 v171, v171\n"
"v_mfma_f32_16x16x32_bf16 v[104:107], a[192:195], v[188:191], v[104:107]\n"
"ds_read_b64_tr_b16 a[206:207], v30 offset:4096\n"
"v_cvt_pk_bf16_f32 v185, v170, v171\n"
"v_mfma_f32_16x16x32_bf16 v[108:111], a[196:199], v[188:191], v[108:111]\n"
"s_waitcnt lgkmcnt(5)\n"
"v_exp_f32 v172, v172\n"
"v_mfma_f32_16x16x32_bf16 v[176:179], a[136:139], a[0:3], v[156:159]\n"
"ds_read_b64_tr_b16 a[208:209], v31\n"
"v_xor_b32 v35, s75, v35\n"
"v_mfma_f32_16x16x32_bf16 v[176:179], a[140:143], a[4:7], v[176:179]\n"
"v_exp_f32 v173, v173\n"
"v_mfma_f32_16x16x32_bf16 v[176:179], a[144:147], a[8:11], v[176:179]\n"
"ds_read_b64_tr_b16 a[210:211], v31 offset:4096\n"
"v_cvt_pk_bf16_f32 v186, v172, v173\n"
"v_mfma_f32_16x16x32_bf16 v[176:179], a[148:151], a[12:15], v[176:179]\n"
"v_exp_f32 v174, v174\n"
"v_mfma_f32_16x16x32_bf16 v[180:183], a[164:167], a[12:15], v[156:159]\n"
"ds_read_b64_tr_b16 a[212:213], v32\n"
"v_xor_b32 v36, s75, v36\n"
"v_mfma_f32_16x16x32_bf16 v[180:183], a[160:163], a[8:11], v[180:183]\n"
"v_exp_f32 v175, v175\n"
"v_mfma_f32_16x16x32_bf16 v[180:183], a[156:159], a[4:7], v[180:183]\n"
"s_waitcnt lgkmcnt(4)\n"
"s_xor_b32 s75, s75, 0x38000\n"
"ds_read_b64_tr_b16 a[214:215], v32 offset:4096\n"
"v_cvt_pk_bf16_f32 v187, v174, v175\n"
"v_mfma_f32_16x16x32_bf16 v[180:183], a[152:155], a[0:3], v[180:183]\n"
"s_nop 0\n"
"v_mfma_f32_16x16x32_bf16 v[152:155], v[192:195], v[184:187], v[152:155]\n"
"v_exp_f32 v176, v176\n"
"v_mfma_f32_16x16x32_bf16 v[112:115], a[168:171], v[184:187], v[112:115]\n"
"ds_read_b64_tr_b16 a[216:217], v33\n"
"v_xor_b32 v25, s74, v25\n"
"v_mfma_f32_16x16x32_bf16 v[116:119], a[172:175], v[184:187], v[116:119]\n"
"v_exp_f32 v177, v177\n"
"v_mfma_f32_16x16x32_bf16 v[120:123], a[176:179], v[184:187], v[120:123]\n"
"ds_read_b64_tr_b16 a[218:219], v33 offset:4096\n"
"v_cvt_pk_bf16_f32 v188, v176, v177\n"
"v_mfma_f32_16x16x32_bf16 v[124:127], a[180:183], v[184:187], v[124:127]\n"
"v_exp_f32 v178, v178\n"
"v_mfma_f32_16x16x32_bf16 v[128:131], a[184:187], v[184:187], v[128:131]\n"
"ds_read_b64_tr_b16 a[220:221], v34\n"
"v_xor_b32 v26, s74, v26\n"
"v_mfma_f32_16x16x32_bf16 v[132:135], a[188:191], v[184:187], v[132:135]\n"
"v_exp_f32 v179, v179\n"
"v_mfma_f32_16x16x32_bf16 v[136:139], a[192:195], v[184:187], v[136:139]\n"
"ds_read_b64_tr_b16 a[222:223], v34 offset:4096\n"
"v_cvt_pk_bf16_f32 v189, v178, v179\n"
"v_mfma_f32_16x16x32_bf16 v[140:143], a[196:199], v[184:187], v[140:143]\n"
"v_exp_f32 v180, v180\n"
"v_mfma_f32_16x16x32_bf16 v[168:171], a[136:139], a[16:19], v[160:163]\n"
"ds_read_b64_tr_b16 a[224:225], v35\n"
"v_xor_b32 v27, s74, v27\n"
"v_mfma_f32_16x16x32_bf16 v[168:171], a[140:143], a[20:23], v[168:171]\n"
"v_exp_f32 v181, v181\n"
"v_mfma_f32_16x16x32_bf16 v[168:171], a[144:147], a[24:27], v[168:171]\n"
"ds_read_b64_tr_b16 a[226:227], v35 offset:4096\n"
"v_cvt_pk_bf16_f32 v190, v180, v181\n"
"v_mfma_f32_16x16x32_bf16 v[168:171], a[148:151], a[28:31], v[168:171]\n"
"v_exp_f32 v182, v182\n"
"v_mfma_f32_16x16x32_bf16 v[172:175], a[164:167], a[28:31], v[160:163]\n"
"ds_read_b64_tr_b16 a[228:229], v36\n"
"v_xor_b32 v28, s74, v28\n"
"v_mfma_f32_16x16x32_bf16 v[172:175], a[160:163], a[24:27], v[172:175]\n"
"v_exp_f32 v183, v183\n"
"v_mfma_f32_16x16x32_bf16 v[172:175], a[156:159], a[20:23], v[172:175]\n"
"s_xor_b32 s74, s74, 0x8000\n"
"ds_read_b64_tr_b16 a[230:231], v36 offset:4096\n"
"v_cvt_pk_bf16_f32 v191, v182, v183\n"
"v_mfma_f32_16x16x32_bf16 v[172:175], a[152:155], a[16:19], v[172:175]\n"
"s_mov_b32 s78, 4\n"
"L_loop1_%=:\n"
"s_nop 0\n"
"v_mfma_f32_16x16x32_bf16 v[144:147], v[192:195], v[188:191], v[144:147]\n"
"s_barrier\n"
"v_exp_f32 v168, v168\n"
"v_mfma_f32_16x16x32_bf16 v[48:51], a[200:203], v[188:191], v[48:51]\n"
"s_waitcnt lgkmcnt(4)\n"
"ds_read_b128 a[104:107], v25\n"
"v_mfma_f32_16x16x32_bf16 v[52:55], a[204:207], v[188:191], v[52:55]\n"
"v_exp_f32 v169, v169\n"
"v_mfma_f32_16x16x32_bf16 v[56:59], a[208:211], v[188:191], v[56:59]\n"
"ds_read_b128 a[108:111], v26\n"
"v_cvt_pk_bf16_f32 v184, v168, v169\n"
"v_mfma_f32_16x16x32_bf16 v[60:63], a[212:215], v[188:191], v[60:63]\n"
"v_exp_f32 v170, v170\n"
"v_mfma_f32_16x16x32_bf16 v[64:67], a[216:219], v[188:191], v[64:67]\n"
"ds_read_b128 a[112:115], v27\n"
"v_mfma_f32_16x16x32_bf16 v[68:71], a[220:223], v[188:191], v[68:71]\n"
"s_waitcnt lgkmcnt(4)\n"
"v_exp_f32 v171, v171\n"
"v_mfma_f32_16x16x32_bf16 v[72:75], a[224:227], v[188:191], v[72:75]\n"
"s_waitcnt lgkmcnt(3)\n"
"ds_read_b128 a[116:119], v28\n"
"v_cvt_pk_bf16_f32 v185, v170, v171\n"
"v_mfma_f32_16x16x32_bf16 v[76:79], a[228:231], v[188:191], v[76:79]\n"
"v_exp_f32 v172, v172\n"
"v_mfma_f32_16x16x32_bf16 v[176:179], a[136:139], a[32:35], v[164:167]\n"
"ds_read_b128 a[132:135], v28 offset:4096\n"
"v_mfma_f32_16x16x32_bf16 v[176:179], a[140:143], a[36:39], v[176:179]\n"
"v_exp_f32 v173, v173\n"
"v_mfma_f32_16x16x32_bf16 v[176:179], a[144:147], a[40:43], v[176:179]\n"
"ds_read_b128 a[128:131], v27 offset:4096\n"
"v_cvt_pk_bf16_f32 v186, v172, v173\n"
"v_mfma_f32_16x16x32_bf16 v[176:179], a[148:151], a[44:47], v[176:179]\n"
"v_exp_f32 v174, v174\n"
"v_mfma_f32_16x16x32_bf16 v[180:183], a[164:167], a[44:47], v[164:167]\n"
"ds_read_b128 a[124:127], v26 offset:4096\n"
"v_mfma_f32_16x16x32_bf16 v[180:183], a[160:163], a[40:43], v[180:183]\n"
"v_exp_f32 v175, v175\n"
"v_mfma_f32_16x16x32_bf16 v[180:183], a[156:159], a[36:39], v[180:183]\n"
"ds_read_b128 a[120:123], v25 offset:4096\n"
"v_cvt_pk_bf16_f32 v187, v174, v175\n"
"v_mfma_f32_16x16x32_bf16 v[180:183], a[152:155], a[32:35], v[180:183]\n"
"s_nop 0\n"
"v_mfma_f32_16x16x32_bf16 v[148:151], v[192:195], v[184:187], v[148:151]\n"
"v_exp_f32 v176, v176\n"
"v_mfma_f32_16x16x32_bf16 v[80:83], a[200:203], v[184:187], v[80:83]\n"
"ds_read_b64_tr_b16 a[168:169], v29 offset:8192\n"
"v_mfma_f32_16x16x32_bf16 v[84:87], a[204:207], v[184:187], v[84:87]\n"
"v_exp_f32 v177, v177\n"
"v_mfma_f32_16x16x32_bf16 v[88:91], a[208:211], v[184:187], v[88:91]\n"
"ds_read_b64_tr_b16 a[170:171], v29 offset:12288\n"
"v_cvt_pk_bf16_f32 v188, v176, v177\n"
"v_mfma_f32_16x16x32_bf16 v[92:95], a[212:215], v[184:187], v[92:95]\n"
"v_exp_f32 v178, v178\n"
"v_mfma_f32_16x16x32_bf16 v[96:99], a[216:219], v[184:187], v[96:99]\n"
"ds_read_b64_tr_b16 a[172:173], v30 offset:8192\n"
"v_mfma_f32_16x16x32_bf16 v[100:103], a[220:223], v[184:187], v[100:103]\n"
"v_exp_f32 v179, v179\n"
"v_mfma_f32_16x16x32_bf16 v[104:107], a[224:227], v[184:187], v[104:107]\n"
"ds_read_b64_tr_b16 a[174:175], v30 offset:12288\n"
"v_cvt_pk_bf16_f32 v189, v178, v179\n"
"v_mfma_f32_16x16x32_bf16 v[108:111], a[228:231], v[184:187], v[108:111]\n"
"s_waitcnt lgkmcnt(5)\n"
"v_exp_f32 v180, v180\n"
"v_mfma_f32_16x16x32_bf16 v[168:171], a[104:107], a[0:3], v[156:159]\n"
"ds_read_b64_tr_b16 a[176:177], v31 offset:8192\n"
"v_mfma_f32_16x16x32_bf16 v[168:171], a[108:111], a[4:7], v[168:171]\n"
"v_exp_f32 v181, v181\n"
"v_mfma_f32_16x16x32_bf16 v[168:171], a[112:115], a[8:11], v[168:171]\n"
"ds_read_b64_tr_b16 a[178:179], v31 offset:12288\n"
"v_cvt_pk_bf16_f32 v190, v180, v181\n"
"v_mfma_f32_16x16x32_bf16 v[168:171], a[116:119], a[12:15], v[168:171]\n"
"v_exp_f32 v182, v182\n"
"v_mfma_f32_16x16x32_bf16 v[172:175], a[132:135], a[12:15], v[156:159]\n"
"ds_read_b64_tr_b16 a[180:181], v32 offset:8192\n"
"v_mfma_f32_16x16x32_bf16 v[172:175], a[128:131], a[8:11], v[172:175]\n"
"v_exp_f32 v183, v183\n"
"v_mfma_f32_16x16x32_bf16 v[172:175], a[124:127], a[4:7], v[172:175]\n"
"s_waitcnt lgkmcnt(5)\n"
"ds_read_b64_tr_b16 a[182:183], v32 offset:12288\n"
"v_cvt_pk_bf16_f32 v191, v182, v183\n"
"v_mfma_f32_16x16x32_bf16 v[172:175], a[120:123], a[0:3], v[172:175]\n"
"s_nop 0\n"
"v_mfma_f32_16x16x32_bf16 v[152:155], v[192:195], v[188:191], v[152:155]\n"
"v_exp_f32 v168, v168\n"
"v_mfma_f32_16x16x32_bf16 v[112:115], a[200:203], v[188:191], v[112:115]\n"
"ds_read_b64_tr_b16 a[184:185], v33 offset:8192\n"
"v_mfma_f32_16x16x32_bf16 v[116:119], a[204:207], v[188:191], v[116:119]\n"
"v_exp_f32 v169, v169\n"
"v_mfma_f32_16x16x32_bf16 v[120:123], a[208:211], v[188:191], v[120:123]\n"
"ds_read_b64_tr_b16 a[186:187], v33 offset:12288\n"
"v_cvt_pk_bf16_f32 v184, v168, v169\n"
"v_mfma_f32_16x16x32_bf16 v[124:127], a[212:215], v[188:191], v[124:127]\n"
"v_exp_f32 v170, v170\n"
"v_mfma_f32_16x16x32_bf16 v[128:131], a[216:219], v[188:191], v[128:131]\n"
"ds_read_b64_tr_b16 a[188:189], v34 offset:8192\n"
"v_mfma_f32_16x16x32_bf16 v[132:135], a[220:223], v[188:191], v[132:135]\n"
"v_exp_f32 v171, v171\n"
"v_mfma_f32_16x16x32_bf16 v[136:139], a[224:227], v[188:191], v[136:139]\n"
"ds_read_b64_tr_b16 a[190:191], v34 offset:12288\n"
"v_cvt_pk_bf16_f32 v185, v170, v171\n"
"v_mfma_f32_16x16x32_bf16 v[140:143], a[228:231], v[188:191], v[140:143]\n"
"v_exp_f32 v172, v172\n"
"v_mfma_f32_16x16x32_bf16 v[176:179], a[104:107], a[16:19], v[160:163]\n"
"ds_read_b64_tr_b16 a[192:193], v35 offset:8192\n"
"v_mfma_f32_16x16x32_bf16 v[176:179], a[108:111], a[20:23], v[176:179]\n"
"v_exp_f32 v173, v173\n"
"v_mfma_f32_16x16x32_bf16 v[176:179], a[112:115], a[24:27], v[176:179]\n"
"ds_read_b64_tr_b16 a[194:195], v35 offset:12288\n"
"v_cvt_pk_bf16_f32 v186, v172, v173\n"
"v_mfma_f32_16x16x32_bf16 v[176:179], a[116:119], a[28:31], v[176:179]\n"
"v_exp_f32 v174, v174\n"
"v_mfma_f32_16x16x32_bf16 v[180:183], a[132:135], a[28:31], v[160:163]\n"
"ds_read_b64_tr_b16 a[196:197], v36 offset:8192\n"
"v_mfma_f32_16x16x32_bf16 v[180:183], a[128:131], a[24:27], v[180:183]\n"
"v_exp_f32 v175, v175\n"
"v_mfma_f32_16x16x32_bf16 v[180:183], a[124:127], a[20:23], v[180:183]\n"
"ds_read_b64_tr_b16 a[198:199], v36 offset:12288\n"
"v_cvt_pk_bf16_f32 v187, v174, v175\n"
"v_mfma_f32_16x16x32_bf16 v[180:183], a[120:123], a[16:19], v[180:183]\n"
"s_nop 0\n"
"v_mfma_f32_16x16x32_bf16 v[144:147], v[192:195], v[184:187], v[144:147]\n"
"s_barrier\n"
"v_exp_f32 v176, v176\n"
"v_mfma_f32_16x16x32_bf16 v[48:51], a[168:171], v[184:187], v[48:51]\n"
"s_waitcnt lgkmcnt(4)\n"
"ds_read_b128 a[136:139], v25 offset:8192\n"
"v_xor_b32 v29, s75, v29\n"
"v_mfma_f32_16x16x32_bf16 v[52:55], a[172:175], v[184:187], v[52:55]\n"
"v_exp_f32 v177, v177\n"
"v_mfma_f32_16x16x32_bf16 v[56:59], a[176:179], v[184:187], v[56:59]\n"
"ds_read_b128 a[140:143], v26 offset:8192\n"
"v_cvt_pk_bf16_f32 v188, v176, v177\n"
"v_mfma_f32_16x16x32_bf16 v[60:63], a[180:183], v[184:187], v[60:63]\n"
"v_exp_f32 v178, v178\n"
"v_mfma_f32_16x16x32_bf16 v[64:67], a[184:187], v[184:187], v[64:67]\n"
"ds_read_b128 a[144:147], v27 offset:8192\n"
"v_xor_b32 v30, s75, v30\n"
"v_mfma_f32_16x16x32_bf16 v[68:71], a[188:191], v[184:187], v[68:71]\n"
"s_waitcnt lgkmcnt(4)\n"
"v_exp_f32 v179, v179\n"
"v_mfma_f32_16x16x32_bf16 v[72:75], a[192:195], v[184:187], v[72:75]\n"
"s_waitcnt lgkmcnt(3)\n"
"ds_read_b128 a[148:151], v28 offset:8192\n"
"v_cvt_pk_bf16_f32 v189, v178, v179\n"
"v_mfma_f32_16x16x32_bf16 v[76:79], a[196:199], v[184:187], v[76:79]\n"
"v_exp_f32 v180, v180\n"
"v_mfma_f32_16x16x32_bf16 v[168:171], a[104:107], a[32:35], v[164:167]\n"
"ds_read_b128 a[164:167], v28 offset:12288\n"
"v_xor_b32 v31, s75, v31\n"
"v_mfma_f32_16x16x32_bf16 v[168:171], a[108:111], a[36:39], v[168:171]\n"
"v_exp_f32 v181, v181\n"
"v_mfma_f32_16x16x32_bf16 v[168:171], a[112:115], a[40:43], v[168:171]\n"
"ds_read_b128 a[160:163], v27 offset:12288\n"
"v_cvt_pk_bf16_f32 v190, v180, v181\n"
"v_mfma_f32_16x16x32_bf16 v[168:171], a[116:119], a[44:47], v[168:171]\n"
"v_exp_f32 v182, v182\n"
"v_mfma_f32_16x16x32_bf16 v[172:175], a[132:135], a[44:47], v[164:167]\n"
"ds_read_b128 a[156:159], v26 offset:12288\n"
"v_xor_b32 v32, s75, v32\n"
"v_mfma_f32_16x16x32_bf16 v[172:175], a[128:131], a[40:43], v[172:175]\n"
"v_exp_f32 v183, v183\n"
"v_mfma_f32_16x16x32_bf16 v[172:175], a[124:127], a[36:39], v[172:175]\n"
"ds_read_b128 a[152:155], v25 offset:12288\n"
"v_cvt_pk_bf16_f32 v191, v182, v183\n"
"v_mfma_f32_16x16x32_bf16 v[172:175], a[120:123], a[32:35], v[172:175]\n"
"s_nop 0\n"
"v_mfma_f32_16x16x32_bf16 v[148:151], v[192:195], v[188:191], v[148:151]\n"
"v_exp_f32 v168, v168\n"
"v_mfma_f32_16x16x32_bf16 v[80:83], a[168:171], v[188:191], v[80:83]\n"
"ds_read_b64_tr_b16 a[200:201], v29\n"
"v_xor_b32 v33, s75, v33\n"
"v_mfma_f32_16x16x32_bf16 v[84:87], a[172:175], v[188:191], v[84:87]\n"
"v_exp_f32 v169, v169\n"
"v_mfma_f32_16x16x32_bf16 v[88:91], a[176:179], v[188:191], v[88:91]\n"
"ds_read_b64_tr_b16 a[202:203], v29 offset:4096\n"
"v_cvt_pk_bf16_f32 v184, v168, v169\n"
"v_mfma_f32_16x16x32_bf16 v[92:95], a[180:183], v[188:191], v[92:95]\n"
"v_exp_f32 v170, v170\n"
"v_mfma_f32_16x16x32_bf16 v[96:99], a[184:187], v[188:191], v[96:99]\n"
"ds_read_b64_tr_b16 a[204:205], v30\n"
"v_xor_b32 v34, s75, v34\n"
"v_mfma_f32_16x16x32_bf16 v[100:103], a[188:191], v[188:191], v[100:103]\n"
"v_exp_f32 v171, v171\n"
"v_mfma_f32_16x16x32_bf16 v[104:107], a[192:195], v[188:191], v[104:107]\n"
"ds_read_b64_tr_b16 a[206:207], v30 offset:4096\n"
"v_cvt_pk_bf16_f32 v185, v170, v171\n"
"v_mfma_f32_16x16x32_bf16 v[108:111], a[196:199], v[188:191], v[108:111]\n"
"s_waitcnt lgkmcnt(5)\n"
"v_exp_f32 v172, v172\n"
"v_mfma_f32_16x16x32_bf16 v[176:179], a[136:139], a[0:3], v[156:159]\n"
"ds_read_b64_tr_b16 a[208:209], v31\n"
"v_xor_b32 v35, s75, v35\n"
"v_mfma_f32_16x16x32_bf16 v[176:179], a[140:143], a[4:7], v[176:179]\n"
"v_exp_f32 v173, v173\n"
"v_mfma_f32_16x16x32_bf16 v[176:179], a[144:147], a[8:11], v[176:179]\n"
"ds_read_b64_tr_b16 a[210:211], v31 offset:4096\n"
"v_cvt_pk_bf16_f32 v186, v172, v173\n"
"v_mfma_f32_16x16x32_bf16 v[176:179], a[148:151], a[12:15], v[176:179]\n"
"v_exp_f32 v174, v174\n"
"v_mfma_f32_16x16x32_bf16 v[180:183], a[164:167], a[12:15], v[156:159]\n"
"ds_read_b64_tr_b16 a[212:213], v32\n"
"v_xor_b32 v36, s75, v36\n"
"v_mfma_f32_16x16x32_bf16 v[180:183], a[160:163], a[8:11], v[180:183]\n"
"v_exp_f32 v175, v175\n"
"v_mfma_f32_16x16x32_bf16 v[180:183], a[156:159], a[4:7], v[180:183]\n"
"s_waitcnt lgkmcnt(4)\n"
"s_xor_b32 s75, s75, 0x38000\n"
"ds_read_b64_tr_b16 a[214:215], v32 offset:4096\n"
"v_cvt_pk_bf16_f32 v187, v174, v175\n"
"v_mfma_f32_16x16x32_bf16 v[180:183], a[152:155], a[0:3], v[180:183]\n"
"s_nop 0\n"
"v_mfma_f32_16x16x32_bf16 v[152:155], v[192:195], v[184:187], v[152:155]\n"
"v_exp_f32 v176, v176\n"
"v_mfma_f32_16x16x32_bf16 v[112:115], a[168:171], v[184:187], v[112:115]\n"
"ds_read_b64_tr_b16 a[216:217], v33\n"
"v_xor_b32 v25, s74, v25\n"
"v_mfma_f32_16x16x32_bf16 v[116:119], a[172:175], v[184:187], v[116:119]\n"
"v_exp_f32 v177, v177\n"
"v_mfma_f32_16x16x32_bf16 v[120:123], a[176:179], v[184:187], v[120:123]\n"
"ds_read_b64_tr_b16 a[218:219], v33 offset:4096\n"
"v_cvt_pk_bf16_f32 v188, v176, v177\n"
"v_mfma_f32_16x16x32_bf16 v[124:127], a[180:183], v[184:187], v[124:127]\n"
"v_exp_f32 v178, v178\n"
"v_mfma_f32_16x16x32_bf16 v[128:131], a[184:187], v[184:187], v[128:131]\n"
"ds_read_b64_tr_b16 a[220:221], v34\n"
"v_xor_b32 v26, s74, v26\n"
"v_mfma_f32_16x16x32_bf16 v[132:135], a[188:191], v[184:187], v[132:135]\n"
"v_exp_f32 v179, v179\n"
"v_mfma_f32_16x16x32_bf16 v[136:139], a[192:195], v[184:187], v[136:139]\n"
"ds_read_b64_tr_b16 a[222:223], v34 offset:4096\n"
"v_cvt_pk_bf16_f32 v189, v178, v179\n"
"v_mfma_f32_16x16x32_bf16 v[140:143], a[196:199], v[184:187], v[140:143]\n"
"v_exp_f32 v180, v180\n"
"v_mfma_f32_16x16x32_bf16 v[168:171], a[136:139], a[16:19], v[160:163]\n"
"ds_read_b64_tr_b16 a[224:225], v35\n"
"v_xor_b32 v27, s74, v27\n"
"v_mfma_f32_16x16x32_bf16 v[168:171], a[140:143], a[20:23], v[168:171]\n"
"v_exp_f32 v181, v181\n"
"v_mfma_f32_16x16x32_bf16 v[168:171], a[144:147], a[24:27], v[168:171]\n"
"ds_read_b64_tr_b16 a[226:227], v35 offset:4096\n"
"v_cvt_pk_bf16_f32 v190, v180, v181\n"
"v_mfma_f32_16x16x32_bf16 v[168:171], a[148:151], a[28:31], v[168:171]\n"
"v_exp_f32 v182, v182\n"
"v_mfma_f32_16x16x32_bf16 v[172:175], a[164:167], a[28:31], v[160:163]\n"
"ds_read_b64_tr_b16 a[228:229], v36\n"
"v_xor_b32 v28, s74, v28\n"
"v_mfma_f32_16x16x32_bf16 v[172:175], a[160:163], a[24:27], v[172:175]\n"
"v_exp_f32 v183, v183\n"
"v_mfma_f32_16x16x32_bf16 v[172:175], a[156:159], a[20:23], v[172:175]\n"
"s_xor_b32 s74, s74, 0x8000\n"
"ds_read_b64_tr_b16 a[230:231], v36 offset:4096\n"
"v_cvt_pk_bf16_f32 v191, v182, v183\n"
"v_mfma_f32_16x16x32_bf16 v[172:175], a[152:155], a[16:19], v[172:175]\n"
"s_sub_u32 s78, s78, 1\n"
"s_cbranch_scc0 L_loop1_%=\n"
"s_nop 0\n"
"v_mfma_f32_16x16x32_bf16 v[144:147], v[192:195], v[188:191], v[144:147]\n"
"s_barrier\n"
"v_exp_f32 v168, v168\n"
"v_mfma_f32_16x16x32_bf16 v[48:51], a[200:203], v[188:191], v[48:51]\n"
"s_waitcnt lgkmcnt(4)\n"
"ds_read_b128 a[104:107], v25\n"
"v_mfma_f32_16x16x32_bf16 v[52:55], a[204:207], v[188:191], v[52:55]\n"
"v_exp_f32 v169, v169\n"
"v_mfma_f32_16x16x32_bf16 v[56:59], a[208:211], v[188:191], v[56:59]\n"
"ds_read_b128 a[108:111], v26\n"
"v_cvt_pk_bf16_f32 v184, v168, v169\n"
"v_mfma_f32_16x16x32_bf16 v[60:63], a[212:215], v[188:191], v[60:63]\n"
"v_exp_f32 v170, v170\n"
"v_mfma_f32_16x16x32_bf16 v[64:67], a[216:219], v[188:191], v[64:67]\n"
"ds_read_b128 a[112:115], v27\n"
"v_mfma_f32_16x16x32_bf16 v[68:71], a[220:223], v[188:191], v[68:71]\n"
"s_waitcnt lgkmcnt(4)\n"
"v_exp_f32 v171, v171\n"
"v_mfma_f32_16x16x32_bf16 v[72:75], a[224:227], v[188:191], v[72:75]\n"
"s_waitcnt lgkmcnt(3)\n"
"ds_read_b128 a[116:119], v28\n"
"v_cvt_pk_bf16_f32 v185, v170, v171\n"
"v_mfma_f32_16x16x32_bf16 v[76:79], a[228:231], v[188:191], v[76:79]\n"
"v_exp_f32 v172, v172\n"
"v_mfma_f32_16x16x32_bf16 v[176:179], a[136:139], a[32:35], v[164:167]\n"
"ds_read_b128 a[132:135], v28 offset:4096\n"
"v_mfma_f32_16x16x32_bf16 v[176:179], a[140:143], a[36:39], v[176:179]\n"
"v_exp_f32 v173, v173\n"
"v_mfma_f32_16x16x32_bf16 v[176:179], a[144:147], a[40:43], v[176:179]\n"
"ds_read_b128 a[128:131], v27 offset:4096\n"
"v_cvt_pk_bf16_f32 v186, v172, v173\n"
"v_mfma_f32_16x16x32_bf16 v[176:179], a[148:151], a[44:47], v[176:179]\n"
"v_exp_f32 v174, v174\n"
"v_mfma_f32_16x16x32_bf16 v[180:183], a[164:167], a[44:47], v[164:167]\n"
"ds_read_b128 a[124:127], v26 offset:4096\n"
"v_cmp_nge_f32 vcc, s27, v144\n"
"v_mfma_f32_16x16x32_bf16 v[180:183], a[160:163], a[40:43], v[180:183]\n"
"v_exp_f32 v175, v175\n"
"v_mfma_f32_16x16x32_bf16 v[180:183], a[156:159], a[36:39], v[180:183]\n"
"ds_read_b128 a[120:123], v25 offset:4096\n"
"v_cvt_pk_bf16_f32 v187, v174, v175\n"
"v_mfma_f32_16x16x32_bf16 v[180:183], a[152:155], a[32:35], v[180:183]\n"
"s_cbranch_vccnz L_slow00_%=\n"
"L_slow00r_%=:\n"
"v_mfma_f32_16x16x32_bf16 v[148:151], v[192:195], v[184:187], v[148:151]\n"
"v_exp_f32 v176, v176\n"
"v_rcp_f32 v214, v144\n"
"s_nop 0\n"
"v_mul_f32 v48, v214, v48\n"
"v_mul_f32 v49, v214, v49\n"
"v_cvt_pk_bf16_f32 v196, v48, v49\n"
"v_mfma_f32_16x16x32_bf16 v[80:83], a[200:203], v[184:187], v[80:83]\n"
"ds_read_b64_tr_b16 a[168:169], v29 offset:8192\n"
"v_mul_f32 v50, v214, v50\n"
"v_mul_f32 v51, v214, v51\n"
"v_cvt_pk_bf16_f32 v197, v50, v51\n"
"v_mul_f32 v52, v214, v52\n"
"v_mfma_f32_16x16x32_bf16 v[84:87], a[204:207], v[184:187], v[84:87]\n"
"v_exp_f32 v177, v177\n"
"v_mul_f32 v53, v214, v53\n"
"v_cvt_pk_bf16_f32 v198, v52, v53\n"
"v_mul_f32 v54, v214, v54\n"
"v_mul_f32 v55, v214, v55\n"
"v_mfma_f32_16x16x32_bf16 v[88:91], a[208:211], v[184:187], v[88:91]\n"
"ds_read_b64_tr_b16 a[170:171], v29 offset:12288\n"
"v_cvt_pk_bf16_f32 v188, v176, v177\n"
"v_cvt_pk_bf16_f32 v199, v54, v55\n"
"v_mul_f32 v56, v214, v56\n"
"v_mul_f32 v57, v214, v57\n"
"v_mfma_f32_16x16x32_bf16 v[92:95], a[212:215], v[184:187], v[92:95]\n"
"ds_write_b128 v17, v[196:199]\n"
"v_exp_f32 v178, v178\n"
"v_cvt_pk_bf16_f32 v200, v56, v57\n"
"v_mul_f32 v58, v214, v58\n"
"v_mul_f32 v59, v214, v59\n"
"v_cvt_pk_bf16_f32 v201, v58, v59\n"
"v_mfma_f32_16x16x32_bf16 v[96:99], a[216:219], v[184:187], v[96:99]\n"
"ds_read_b64_tr_b16 a[172:173], v30 offset:8192\n"
"v_mul_f32 v60, v214, v60\n"
"v_mul_f32 v61, v214, v61\n"
"v_cvt_pk_bf16_f32 v202, v60, v61\n"
"v_mul_f32 v62, v214, v62\n"
"v_mfma_f32_16x16x32_bf16 v[100:103], a[220:223], v[184:187], v[100:103]\n"
"v_exp_f32 v179, v179\n"
"v_mul_f32 v63, v214, v63\n"
"v_cvt_pk_bf16_f32 v203, v62, v63\n"
"v_mul_f32 v64, v214, v64\n"
"v_mfma_f32_16x16x32_bf16 v[104:107], a[224:227], v[184:187], v[104:107]\n"
"ds_read_b64_tr_b16 a[174:175], v30 offset:12288\n"
"ds_write_b128 v17, v[200:203] offset:3072\n"
"v_cvt_pk_bf16_f32 v189, v178, v179\n"
"v_mul_f32 v65, v214, v65\n"
"v_cvt_pk_bf16_f32 v204, v64, v65\n"
"v_mul_f32 v66, v214, v66\n"
"v_mul_f32 v67, v214, v67\n"
"v_mfma_f32_16x16x32_bf16 v[108:111], a[228:231], v[184:187], v[108:111]\n"
"s_waitcnt lgkmcnt(3)\n"
"v_exp_f32 v180, v180\n"
"v_cvt_pk_bf16_f32 v205, v66, v67\n"
"v_mul_f32 v68, v214, v68\n"
"v_mul_f32 v69, v214, v69\n"
"v_cvt_pk_bf16_f32 v206, v68, v69\n"
"v_mfma_f32_16x16x32_bf16 v[168:171], a[104:107], a[48:51], 0\n"
"ds_read_b64_tr_b16 a[176:177], v31 offset:8192\n"
"v_mul_f32 v70, v214, v70\n"
"v_mul_f32 v71, v214, v71\n"
"v_cvt_pk_bf16_f32 v207, v70, v71\n"
"v_mfma_f32_16x16x32_bf16 v[168:171], a[108:111], a[52:55], v[168:171]\n"
"ds_write_b128 v17, v[204:207] offset:6144\n"
"v_exp_f32 v181, v181\n"
"v_mul_f32 v72, v214, v72\n"
"v_mul_f32 v73, v214, v73\n"
"v_cvt_pk_bf16_f32 v208, v72, v73\n"
"v_mul_f32 v74, v214, v74\n"
"v_mfma_f32_16x16x32_bf16 v[168:171], a[112:115], a[56:59], v[168:171]\n"
"ds_read_b64_tr_b16 a[178:179], v31 offset:12288\n"
"v_cvt_pk_bf16_f32 v190, v180, v181\n"
"v_mul_f32 v75, v214, v75\n"
"v_cvt_pk_bf16_f32 v209, v74, v75\n"
"v_mul_f32 v76, v214, v76\n"
"v_mul_f32 v77, v214, v77\n"
"v_mfma_f32_16x16x32_bf16 v[168:171], a[116:119], a[60:63], v[168:171]\n"
"v_exp_f32 v182, v182\n"
"v_cvt_pk_bf16_f32 v210, v76, v77\n"
"v_mul_f32 v78, v214, v78\n"
"v_mul_f32 v79, v214, v79\n"
"v_cvt_pk_bf16_f32 v211, v78, v79\n"
"v_mfma_f32_16x16x32_bf16 v[172:175], a[132:135], a[60:63], 0\n"
"ds_read_b64_tr_b16 a[180:181], v32 offset:8192\n"
"v_cmp_nge_f32 vcc, s27, v148\n"
"v_mfma_f32_16x16x32_bf16 v[172:175], a[128:131], a[56:59], v[172:175]\n"
"ds_write_b128 v17, v[208:211] offset:9216\n"
"v_exp_f32 v183, v183\n"
"v_mfma_f32_16x16x32_bf16 v[172:175], a[124:127], a[52:55], v[172:175]\n"
"ds_read_b64_tr_b16 a[182:183], v32 offset:12288\n"
"v_cvt_pk_bf16_f32 v191, v182, v183\n"
"v_mfma_f32_16x16x32_bf16 v[172:175], a[120:123], a[48:51], v[172:175]\n"
"s_cbranch_vccnz L_slow01_%=\n"
"L_slow01r_%=:\n"
"v_mfma_f32_16x16x32_bf16 v[152:155], v[192:195], v[188:191], v[152:155]\n"
"v_max3_f32 v212, v168, v169, v170\n"
"v_max_f32 v212, v212, v171\n"
"v_rcp_f32 v214, v148\n"
"s_nop 0\n"
"v_mul_f32 v80, v214, v80\n"
"v_mul_f32 v81, v214, v81\n"
"v_cvt_pk_bf16_f32 v196, v80, v81\n"
"v_mfma_f32_16x16x32_bf16 v[112:115], a[200:203], v[188:191], v[112:115]\n"
"ds_read_b64_tr_b16 a[184:185], v33 offset:8192\n"
"v_mul_f32 v82, v214, v82\n"
"v_mul_f32 v83, v214, v83\n"
"v_cvt_pk_bf16_f32 v197, v82, v83\n"
"v_mul_f32 v84, v214, v84\n"
"v_mfma_f32_16x16x32_bf16 v[116:119], a[204:207], v[188:191], v[116:119]\n"
"v_mul_f32 v85, v214, v85\n"
"v_cvt_pk_bf16_f32 v198, v84, v85\n"
"v_mul_f32 v86, v214, v86\n"
"v_mul_f32 v87, v214, v87\n"
"v_mfma_f32_16x16x32_bf16 v[120:123], a[208:211], v[188:191], v[120:123]\n"
"ds_read_b64_tr_b16 a[186:187], v33 offset:12288\n"
"v_max3_f32 v212, v212, v172, v173\n"
"v_max3_f32 v212, v212, v174, v175\n"
"v_cvt_pk_bf16_f32 v199, v86, v87\n"
"v_mul_f32 v88, v214, v88\n"
"v_mul_f32 v89, v214, v89\n"
"v_mfma_f32_16x16x32_bf16 v[124:127], a[212:215], v[188:191], v[124:127]\n"
"ds_write_b128 v17, v[196:199] offset:12288\n"
"v_mov_b32 v213, v212\n"
"v_cvt_pk_bf16_f32 v200, v88, v89\n"
"v_mul_f32 v90, v214, v90\n"
"v_mul_f32 v91, v214, v91\n"
"v_cvt_pk_bf16_f32 v201, v90, v91\n"
"v_mfma_f32_16x16x32_bf16 v[128:131], a[216:219], v[188:191], v[128:131]\n"
"ds_read_b64_tr_b16 a[188:189], v34 offset:8192\n"
"v_permlane32_swap_b32 v212, v213\n"
"v_max_f32 v212, v212, v213\n"
"v_mul_f32 v92, v214, v92\n"
"v_mul_f32 v93, v214, v93\n"
"v_cvt_pk_bf16_f32 v202, v92, v93\n"
"v_mul_f32 v94, v214, v94\n"
"v_mfma_f32_16x16x32_bf16 v[132:135], a[220:223], v[188:191], v[132:135]\n"
"v_mov_b32 v213, v212\n"
"v_mul_f32 v95, v214, v95\n"
"v_cvt_pk_bf16_f32 v203, v94, v95\n"
"v_mul_f32 v96, v214, v96\n"
"v_mfma_f32_16x16x32_bf16 v[136:139], a[224:227], v[188:191], v[136:139]\n"
"ds_read_b64_tr_b16 a[190:191], v34 offset:12288\n"
"ds_write_b128 v17, v[200:203] offset:15360\n"
"v_permlane16_swap_b32 v212, v213\n"
"v_max_f32 v212, v212, v213\n"
"v_mul_f32 v97, v214, v97\n"
"v_cvt_pk_bf16_f32 v204, v96, v97\n"
"v_mul_f32 v98, v214, v98\n"
"v_mul_f32 v99, v214, v99\n"
"v_mfma_f32_16x16x32_bf16 v[140:143], a[228:231], v[188:191], v[140:143]\n"
"v_sub_f32 v168, v168, v212\n"
"v_exp_f32 v168, v168\n"
"v_sub_f32 v156, 0, v212\n"
"v_cvt_pk_bf16_f32 v205, v98, v99\n"
"v_mul_f32 v100, v214, v100\n"
"v_mul_f32 v101, v214, v101\n"
"v_cvt_pk_bf16_f32 v206, v100, v101\n"
"v_mfma_f32_16x16x32_bf16 v[176:179], a[104:107], a[64:67], 0\n"
"ds_read_b64_tr_b16 a[192:193], v35 offset:8192\n"
"v_sub_f32 v169, v169, v212\n"
"v_exp_f32 v169, v169\n"
"v_sub_f32 v157, 0, v212\n"
"v_mul_f32 v102, v214, v102\n"
"v_mul_f32 v103, v214, v103\n"
"v_cvt_pk_bf16_f32 v207, v102, v103\n"
"v_mfma_f32_16x16x32_bf16 v[176:179], a[108:111], a[68:71], v[176:179]\n"
"ds_write_b128 v17, v[204:207] offset:18432\n"
"v_cvt_pk_bf16_f32 v184, v168, v169\n"
"v_sub_f32 v170, v170, v212\n"
"v_exp_f32 v170, v170\n"
"v_sub_f32 v158, 0, v212\n"
"v_mul_f32 v104, v214, v104\n"
"v_mul_f32 v105, v214, v105\n"
"v_cvt_pk_bf16_f32 v208, v104, v105\n"
"v_mul_f32 v106, v214, v106\n"
"v_mfma_f32_16x16x32_bf16 v[176:179], a[112:115], a[72:75], v[176:179]\n"
"ds_read_b64_tr_b16 a[194:195], v35 offset:12288\n"
"v_sub_f32 v171, v171, v212\n"
"v_exp_f32 v171, v171\n"
"v_sub_f32 v159, 0, v212\n"
"v_mul_f32 v107, v214, v107\n"
"v_cvt_pk_bf16_f32 v209, v106, v107\n"
"v_mul_f32 v108, v214, v108\n"
"v_mul_f32 v109, v214, v109\n"
"v_mfma_f32_16x16x32_bf16 v[176:179], a[116:119], a[76:79], v[176:179]\n"
"v_cvt_pk_bf16_f32 v185, v170, v171\n"
"v_sub_f32 v172, v172, v212\n"
"v_exp_f32 v172, v172\n"
"v_cvt_pk_bf16_f32 v210, v108, v109\n"
"v_mul_f32 v110, v214, v110\n"
"v_mul_f32 v111, v214, v111\n"
"v_cvt_pk_bf16_f32 v211, v110, v111\n"
"v_mfma_f32_16x16x32_bf16 v[180:183], a[132:135], a[76:79], 0\n"
"ds_read_b64_tr_b16 a[196:197], v36 offset:8192\n"
"v_sub_f32 v173, v173, v212\n"
"v_exp_f32 v173, v173\n"
"v_cmp_nge_f32 vcc, s27, v152\n"
"v_mfma_f32_16x16x32_bf16 v[180:183], a[128:131], a[72:75], v[180:183]\n"
"ds_write_b128 v17, v[208:211] offset:21504\n"
"v_cvt_pk_bf16_f32 v186, v172, v173\n"
"v_sub_f32 v174, v174, v212\n"
"v_exp_f32 v174, v174\n"
"v_mfma_f32_16x16x32_bf16 v[180:183], a[124:127], a[68:71], v[180:183]\n"
"ds_read_b64_tr_b16 a[198:199], v36 offset:12288\n"
"v_sub_f32 v175, v175, v212\n"
"v_exp_f32 v175, v175\n"
"v_mfma_f32_16x16x32_bf16 v[180:183], a[120:123], a[64:67], v[180:183]\n"
"s_cbranch_vccnz L_slow02_%=\n"
"L_slow02r_%=:\n"
"v_cvt_pk_bf16_f32 v187, v174, v175\n"
"s_nop 1\n"
"v_mfma_f32_16x16x32_bf16 v[144:147], v[192:195], v[184:187], 0\n"
"s_waitcnt lgkmcnt(1)\n"
"s_barrier\n"
"v_max3_f32 v212, v176, v177, v178\n"
"v_max_f32 v212, v212, v179\n"
"v_rcp_f32 v214, v152\n"
"s_nop 0\n"
"v_mul_f32 v112, v214, v112\n"
"v_mul_f32 v113, v214, v113\n"
"v_cvt_pk_bf16_f32 v196, v112, v113\n"
"v_mfma_f32_16x16x32_bf16 v[48:51], a[168:171], v[184:187], 0\n"
"ds_read_b128 a[136:139], v25 offset:8192\n"
"v_xor_b32 v29, s75, v29\n"
"v_mul_f32 v114, v214, v114\n"
"v_mul_f32 v115, v214, v115\n"
"v_cvt_pk_bf16_f32 v197, v114, v115\n"
"v_mul_f32 v116, v214, v116\n"
"v_mfma_f32_16x16x32_bf16 v[52:55], a[172:175], v[184:187], 0\n"
"v_mul_f32 v117, v214, v117\n"
"v_cvt_pk_bf16_f32 v198, v116, v117\n"
"v_mul_f32 v118, v214, v118\n"
"v_mul_f32 v119, v214, v119\n"
"v_mfma_f32_16x16x32_bf16 v[56:59], a[176:179], v[184:187], 0\n"
"ds_read_b128 a[140:143], v26 offset:8192\n"
"v_max3_f32 v212, v212, v180, v181\n"
"v_max3_f32 v212, v212, v182, v183\n"
"v_cvt_pk_bf16_f32 v199, v118, v119\n"
"v_mul_f32 v120, v214, v120\n"
"v_mul_f32 v121, v214, v121\n"
"v_mfma_f32_16x16x32_bf16 v[60:63], a[180:183], v[184:187], 0\n"
"ds_write_b128 v17, v[196:199] offset:24576\n"
"v_mov_b32 v213, v212\n"
"v_cvt_pk_bf16_f32 v200, v120, v121\n"
"v_mul_f32 v122, v214, v122\n"
"v_mul_f32 v123, v214, v123\n"
"v_cvt_pk_bf16_f32 v201, v122, v123\n"
"v_mfma_f32_16x16x32_bf16 v[64:67], a[184:187], v[184:187], 0\n"
"ds_read_b128 a[144:147], v27 offset:8192\n"
"v_xor_b32 v30, s75, v30\n"
"v_permlane32_swap_b32 v212, v213\n"
"v_max_f32 v212, v212, v213\n"
"v_mul_f32 v124, v214, v124\n"
"v_mul_f32 v125, v214, v125\n"
"v_cvt_pk_bf16_f32 v202, v124, v125\n"
"v_mul_f32 v126, v214, v126\n"
"v_mfma_f32_16x16x32_bf16 v[68:71], a[188:191], v[184:187], 0\n"
"v_mov_b32 v213, v212\n"
"v_mul_f32 v127, v214, v127\n"
"v_cvt_pk_bf16_f32 v203, v126, v127\n"
"v_mul_f32 v128, v214, v128\n"
"v_mfma_f32_16x16x32_bf16 v[72:75], a[192:195], v[184:187], 0\n"
"s_waitcnt lgkmcnt(2)\n"
"ds_read_b128 a[148:151], v28 offset:8192\n"
"ds_write_b128 v17, v[200:203] offset:27648\n"
"v_permlane16_swap_b32 v212, v213\n"
"v_max_f32 v212, v212, v213\n"
"v_mul_f32 v129, v214, v129\n"
"v_cvt_pk_bf16_f32 v204, v128, v129\n"
"v_mul_f32 v130, v214, v130\n"
"v_mul_f32 v131, v214, v131\n"
"v_mfma_f32_16x16x32_bf16 v[76:79], a[196:199], v[184:187], 0\n"
"v_sub_f32 v176, v176, v212\n"
"v_exp_f32 v176, v176\n"
"v_sub_f32 v160, 0, v212\n"
"v_cvt_pk_bf16_f32 v205, v130, v131\n"
"v_mul_f32 v132, v214, v132\n"
"v_mul_f32 v133, v214, v133\n"
"v_cvt_pk_bf16_f32 v206, v132, v133\n"
"v_mfma_f32_16x16x32_bf16 v[168:171], a[104:107], a[80:83], 0\n"
"ds_read_b128 a[164:167], v28 offset:12288\n"
"v_xor_b32 v31, s75, v31\n"
"v_sub_f32 v177, v177, v212\n"
"v_exp_f32 v177, v177\n"
"v_sub_f32 v161, 0, v212\n"
"v_mul_f32 v134, v214, v134\n"
"v_mul_f32 v135, v214, v135\n"
"v_cvt_pk_bf16_f32 v207, v134, v135\n"
"v_mfma_f32_16x16x32_bf16 v[168:171], a[108:111], a[84:87], v[168:171]\n"
"ds_write_b128 v18, v[204:207]\n"
"v_cvt_pk_bf16_f32 v188, v176, v177\n"
"v_sub_f32 v178, v178, v212\n"
"v_exp_f32 v178, v178\n"
"v_sub_f32 v162, 0, v212\n"
"v_mul_f32 v136, v214, v136\n"
"v_mul_f32 v137, v214, v137\n"
"v_cvt_pk_bf16_f32 v208, v136, v137\n"
"v_mul_f32 v138, v214, v138\n"
"v_mfma_f32_16x16x32_bf16 v[168:171], a[112:115], a[88:91], v[168:171]\n"
"ds_read_b128 a[160:163], v27 offset:12288\n"
"v_sub_f32 v179, v179, v212\n"
"v_exp_f32 v179, v179\n"
"v_sub_f32 v163, 0, v212\n"
"v_mul_f32 v139, v214, v139\n"
"v_cvt_pk_bf16_f32 v209, v138, v139\n"
"v_mul_f32 v140, v214, v140\n"
"v_mul_f32 v141, v214, v141\n"
"v_mfma_f32_16x16x32_bf16 v[168:171], a[116:119], a[92:95], v[168:171]\n"
"v_cvt_pk_bf16_f32 v189, v178, v179\n"
"v_sub_f32 v180, v180, v212\n"
"v_exp_f32 v180, v180\n"
"v_cvt_pk_bf16_f32 v210, v140, v141\n"
"v_mul_f32 v142, v214, v142\n"
"v_mul_f32 v143, v214, v143\n"
"v_cvt_pk_bf16_f32 v211, v142, v143\n"
"v_mfma_f32_16x16x32_bf16 v[172:175], a[132:135], a[92:95], 0\n"
"ds_read_b128 a[156:159], v26 offset:12288\n"
"v_xor_b32 v32, s75, v32\n"
"v_sub_f32 v181, v181, v212\n"
"v_exp_f32 v181, v181\n"
"v_mfma_f32_16x16x32_bf16 v[172:175], a[128:131], a[88:91], v[172:175]\n"
"ds_write_b128 v18, v[208:211] offset:3072\n"
"v_cvt_pk_bf16_f32 v190, v180, v181\n"
"v_sub_f32 v182, v182, v212\n"
"v_exp_f32 v182, v182\n"
"v_mfma_f32_16x16x32_bf16 v[172:175], a[124:127], a[84:87], v[172:175]\n"
"ds_read_b128 a[152:155], v25 offset:12288\n"
"v_sub_f32 v183, v183, v212\n"
"v_exp_f32 v183, v183\n"
"v_mfma_f32_16x16x32_bf16 v[172:175], a[120:123], a[80:83], v[172:175]\n"
"v_cvt_pk_bf16_f32 v191, v182, v183\n"
"s_nop 1\n"
"v_mfma_f32_16x16x32_bf16 v[148:151], v[192:195], v[188:191], 0\n"
"v_max3_f32 v212, v168, v169, v170\n"
"v_max_f32 v212, v212, v171\n"
"v_mfma_f32_16x16x32_bf16 v[80:83], a[168:171], v[188:191], 0\n"
"ds_read_b64_tr_b16 a[200:201], v29\n"
"v_xor_b32 v33, s75, v33\n"
"v_mfma_f32_16x16x32_bf16 v[84:87], a[172:175], v[188:191], 0\n"
"v_mfma_f32_16x16x32_bf16 v[88:91], a[176:179], v[188:191], 0\n"
"ds_read_b64_tr_b16 a[202:203], v29 offset:4096\n"
"v_max3_f32 v212, v212, v172, v173\n"
"v_max3_f32 v212, v212, v174, v175\n"
"v_mfma_f32_16x16x32_bf16 v[92:95], a[180:183], v[188:191], 0\n"
"v_mov_b32 v213, v212\n"
"v_mfma_f32_16x16x32_bf16 v[96:99], a[184:187], v[188:191], 0\n"
"ds_read_b64_tr_b16 a[204:205], v30\n"
"v_xor_b32 v34, s75, v34\n"
"v_permlane32_swap_b32 v212, v213\n"
"v_max_f32 v212, v212, v213\n"
"v_mfma_f32_16x16x32_bf16 v[100:103], a[188:191], v[188:191], 0\n"
"v_mov_b32 v213, v212\n"
"v_mfma_f32_16x16x32_bf16 v[104:107], a[192:195], v[188:191], 0\n"
"ds_read_b64_tr_b16 a[206:207], v30 offset:4096\n"
"v_permlane16_swap_b32 v212, v213\n"
"v_max_f32 v212, v212, v213\n"
"v_mfma_f32_16x16x32_bf16 v[108:111], a[196:199], v[188:191], 0\n"
"v_sub_f32 v168, v168, v212\n"
"v_exp_f32 v168, v168\n"
"v_sub_f32 v164, 0, v212\n"
"v_mfma_f32_16x16x32_bf16 v[176:179], a[136:139], a[48:51], v[156:159]\n"
"ds_read_b64_tr_b16 a[208:209], v31\n"
"v_xor_b32 v35, s75, v35\n"
"v_sub_f32 v169, v169, v212\n"
"v_exp_f32 v169, v169\n"
"v_sub_f32 v165, 0, v212\n"
"v_mfma_f32_16x16x32_bf16 v[176:179], a[140:143], a[52:55], v[176:179]\n"
"s_waitcnt lgkmcnt(3)\n"
"v_cvt_pk_bf16_f32 v184, v168, v169\n"
"v_sub_f32 v170, v170, v212\n"
"v_exp_f32 v170, v170\n"
"v_sub_f32 v166, 0, v212\n"
"v_mfma_f32_16x16x32_bf16 v[176:179], a[144:147], a[56:59], v[176:179]\n"
"ds_read_b64_tr_b16 a[210:211], v31 offset:4096\n"
"v_sub_f32 v171, v171, v212\n"
"v_exp_f32 v171, v171\n"
"v_sub_f32 v167, 0, v212\n"
"v_mfma_f32_16x16x32_bf16 v[176:179], a[148:151], a[60:63], v[176:179]\n"
"v_cvt_pk_bf16_f32 v185, v170, v171\n"
"v_sub_f32 v172, v172, v212\n"
"v_exp_f32 v172, v172\n"
"v_mfma_f32_16x16x32_bf16 v[180:183], a[164:167], a[60:63], v[156:159]\n"
"ds_read_b64_tr_b16 a[212:213], v32\n"
"v_xor_b32 v36, s75, v36\n"
"v_sub_f32 v173, v173, v212\n"
"v_exp_f32 v173, v173\n"
"v_mfma_f32_16x16x32_bf16 v[180:183], a[160:163], a[56:59], v[180:183]\n"
"v_cvt_pk_bf16_f32 v186, v172, v173\n"
"v_sub_f32 v174, v174, v212\n"
"v_exp_f32 v174, v174\n"
"v_mfma_f32_16x16x32_bf16 v[180:183], a[156:159], a[52:55], v[180:183]\n"
"s_xor_b32 s75, s75, 0x38000\n"
"ds_read_b64_tr_b16 a[214:215], v32 offset:4096\n"
"v_sub_f32 v175, v175, v212\n"
"v_exp_f32 v175, v175\n"
"v_mfma_f32_16x16x32_bf16 v[180:183], a[152:155], a[48:51], v[180:183]\n"
"v_cvt_pk_bf16_f32 v187, v174, v175\n"
"s_nop 1\n"
"v_mfma_f32_16x16x32_bf16 v[152:155], v[192:195], v[184:187], 0\n"
"v_exp_f32 v176, v176\n"
"v_mfma_f32_16x16x32_bf16 v[112:115], a[168:171], v[184:187], 0\n"
"ds_read_b64_tr_b16 a[216:217], v33\n"
"v_xor_b32 v25, s74, v25\n"
"v_mfma_f32_16x16x32_bf16 v[116:119], a[172:175], v[184:187], 0\n"
"v_exp_f32 v177, v177\n"
"v_mfma_f32_16x16x32_bf16 v[120:123], a[176:179], v[184:187], 0\n"
"ds_read_b64_tr_b16 a[218:219], v33 offset:4096\n"
"v_cvt_pk_bf16_f32 v188, v176, v177\n"
"v_mfma_f32_16x16x32_bf16 v[124:127], a[180:183], v[184:187], 0\n"
"v_exp_f32 v178, v178\n"
"v_mfma_f32_16x16x32_bf16 v[128:131], a[184:187], v[184:187], 0\n"
"ds_read_b64_tr_b16 a[220:221], v34\n"
"v_xor_b32 v26, s74, v26\n"
"v_mfma_f32_16x16x32_bf16 v[132:135], a[188:191], v[184:187], 0\n"
"v_exp_f32 v179, v179\n"
"v_mfma_f32_16x16x32_bf16 v[136:139], a[192:195], v[184:187], 0\n"
"ds_read_b64_tr_b16 a[222:223], v34 offset:4096\n"
"v_cvt_pk_bf16_f32 v189, v178, v179\n"
"v_mfma_f32_16x16x32_bf16 v[140:143], a[196:199], v[184:187], 0\n"
"v_exp_f32 v180, v180\n"
"v_mfma_f32_16x16x32_bf16 v[168:171], a[136:139], a[64:67], v[160:163]\n"
"ds_read_b64_tr_b16 a[224:225], v35\n"
"v_xor_b32 v27, s74, v27\n"
"v_mfma_f32_16x16x32_bf16 v[168:171], a[140:143], a[68:71], v[168:171]\n"
"v_exp_f32 v181, v181\n"
"v_mfma_f32_16x16x32_bf16 v[168:171], a[144:147], a[72:75], v[168:171]\n"
"ds_read_b64_tr_b16 a[226:227], v35 offset:4096\n"
"v_cvt_pk_bf16_f32 v190, v180, v181\n"
"v_mfma_f32_16x16x32_bf16 v[168:171], a[148:151], a[76:79], v[168:171]\n"
"v_exp_f32 v182, v182\n"
"v_mfma_f32_16x16x32_bf16 v[172:175], a[164:167], a[76:79], v[160:163]\n"
"ds_read_b64_tr_b16 a[228:229], v36\n"
"v_xor_b32 v28, s74, v28\n"
"v_mfma_f32_16x16x32_bf16 v[172:175], a[160:163], a[72:75], v[172:175]\n"
"v_exp_f32 v183, v183\n"
"v_mfma_f32_16x16x32_bf16 v[172:175], a[156:159], a[68:71], v[172:175]\n"
"s_xor_b32 s74, s74, 0x8000\n"
"ds_read_b64_tr_b16 a[230:231], v36 offset:4096\n"
"v_cvt_pk_bf16_f32 v191, v182, v183\n"
"v_mfma_f32_16x16x32_bf16 v[172:175], a[152:155], a[64:67], v[172:175]\n"
"s_mov_b32 s78, 5\n"
"L_loop2_%=:\n"
"s_nop 0\n"
"v_mfma_f32_16x16x32_bf16 v[144:147], v[192:195], v[188:191], v[144:147]\n"
"s_barrier\n"
"v_exp_f32 v168, v168\n"
"v_mfma_f32_16x16x32_bf16 v[48:51], a[200:203], v[188:191], v[48:51]\n"
"s_waitcnt lgkmcnt(4)\n"
"ds_read_b128 a[104:107], v25\n"
"v_mfma_f32_16x16x32_bf16 v[52:55], a[204:207], v[188:191], v[52:55]\n"
"v_exp_f32 v169, v169\n"
"v_mfma_f32_16x16x32_bf16 v[56:59], a[208:211], v[188:191], v[56:59]\n"
"ds_read_b128 a[108:111], v26\n"
"v_cvt_pk_bf16_f32 v184, v168, v169\n"
"v_mfma_f32_16x16x32_bf16 v[60:63], a[212:215], v[188:191], v[60:63]\n"
"v_exp_f32 v170, v170\n"
"v_mfma_f32_16x16x32_bf16 v[64:67], a[216:219], v[188:191], v[64:67]\n"
"ds_read_b128 a[112:115], v27\n"
"v_mfma_f32_16x16x32_bf16 v[68:71], a[220:223], v[188:191], v[68:71]\n"
"s_waitcnt lgkmcnt(4)\n"
"v_exp_f32 v171, v171\n"
"v_mfma_f32_16x16x32_bf16 v[72:75], a[224:227], v[188:191], v[72:75]\n"
"s_waitcnt lgkmcnt(3)\n"
"ds_read_b128 a[116:119], v28\n"
"v_cvt_pk_bf16_f32 v185, v170, v171\n"
"v_mfma_f32_16x16x32_bf16 v[76:79], a[228:231], v[188:191], v[76:79]\n"
"v_exp_f32 v172, v172\n"
"v_mfma_f32_16x16x32_bf16 v[176:179], a[136:139], a[80:83], v[164:167]\n"
"ds_read_b128 a[132:135], v28 offset:4096\n"
"v_mfma_f32_16x16x32_bf16 v[176:179], a[140:143], a[84:87], v[176:179]\n"
"v_exp_f32 v173, v173\n"
"v_mfma_f32_16x16x32_bf16 v[176:179], a[144:147], a[88:91], v[176:179]\n"
"ds_read_b128 a[128:131], v27 offset:4096\n"
"v_cvt_pk_bf16_f32 v186, v172, v173\n"
"v_mfma_f32_16x16x32_bf16 v[176:179], a[148:151], a[92:95], v[176:179]\n"
"v_exp_f32 v174, v174\n"
"v_mfma_f32_16x16x32_bf16 v[180:183], a[164:167], a[92:95], v[164:167]\n"
"ds_read_b128 a[124:127], v26 offset:4096\n"
"v_mfma_f32_16x16x32_bf16 v[180:183], a[160:163], a[88:91], v[180:183]\n"
"v_exp_f32 v175, v175\n"
"v_mfma_f32_16x16x32_bf16 v[180:183], a[156:159], a[84:87], v[180:183]\n"
"ds_read_b128 a[120:123], v25 offset:4096\n"
"v_cvt_pk_bf16_f32 v187, v174, v175\n"
"v_mfma_f32_16x16x32_bf16 v[180:183], a[152:155], a[80:83], v[180:183]\n"
"s_nop 0\n"
"v_mfma_f32_16x16x32_bf16 v[148:151], v[192:195], v[184:187], v[148:151]\n"
"v_exp_f32 v176, v176\n"
"v_mfma_f32_16x16x32_bf16 v[80:83], a[200:203], v[184:187], v[80:83]\n"
"ds_read_b64_tr_b16 a[168:169], v29 offset:8192\n"
"v_mfma_f32_16x16x32_bf16 v[84:87], a[204:207], v[184:187], v[84:87]\n"
"v_exp_f32 v177, v177\n"
"v_mfma_f32_16x16x32_bf16 v[88:91], a[208:211], v[184:187], v[88:91]\n"
"ds_read_b64_tr_b16 a[170:171], v29 offset:12288\n"
"v_cvt_pk_bf16_f32 v188, v176, v177\n"
"v_mfma_f32_16x16x32_bf16 v[92:95], a[212:215], v[184:187], v[92:95]\n"
"v_exp_f32 v178, v178\n"
"v_mfma_f32_16x16x32_bf16 v[96:99], a[216:219], v[184:187], v[96:99]\n"
"ds_read_b64_tr_b16 a[172:173], v30 offset:8192\n"
"v_mfma_f32_16x16x32_bf16 v[100:103], a[220:223], v[184:187], v[100:103]\n"
"v_exp_f32 v179, v179\n"
"v_mfma_f32_16x16x32_bf16 v[104:107], a[224:227], v[184:187], v[104:107]\n"
"ds_read_b64_tr_b16 a[174:175], v30 offset:12288\n"
"v_cvt_pk_bf16_f32 v189, v178, v179\n"
"v_mfma_f32_16x16x32_bf16 v[108:111], a[228:231], v[184:187], v[108:111]\n"
"s_waitcnt lgkmcnt(5)\n"
"v_exp_f32 v180, v180\n"
"v_mfma_f32_16x16x32_bf16 v[168:171], a[104:107], a[48:51], v[156:159]\n"
"ds_read_b64_tr_b16 a[176:177], v31 offset:8192\n"
"v_mfma_f32_16x16x32_bf16 v[168:171], a[108:111], a[52:55], v[168:171]\n"
"v_exp_f32 v181, v181\n"
"v_mfma_f32_16x16x32_bf16 v[168:171], a[112:115], a[56:59], v[168:171]\n"
"ds_read_b64_tr_b16 a[178:179], v31 offset:12288\n"
"v_cvt_pk_bf16_f32 v190, v180, v181\n"
"v_mfma_f32_16x16x32_bf16 v[168:171], a[116:119], a[60:63], v[168:171]\n"
"v_exp_f32 v182, v182\n"
"v_mfma_f32_16x16x32_bf16 v[172:175], a[132:135], a[60:63], v[156:159]\n"
"ds_read_b64_tr_b16 a[180:181], v32 offset:8192\n"
"v_mfma_f32_16x16x32_bf16 v[172:175], a[128:131], a[56:59], v[172:175]\n"
"v_exp_f32 v183, v183\n"
"v_mfma_f32_16x16x32_bf16 v[172:175], a[124:127], a[52:55], v[172:175]\n"
"s_waitcnt lgkmcnt(5)\n"
"ds_read_b64_tr_b16 a[182:183], v32 offset:12288\n"
"v_cvt_pk_bf16_f32 v191, v182, v183\n"
"v_mfma_f32_16x16x32_bf16 v[172:175], a[120:123], a[48:51], v[172:175]\n"
"s_nop 0\n"
"v_mfma_f32_16x16x32_bf16 v[152:155], v[192:195], v[188:191], v[152:155]\n"
"v_exp_f32 v168, v168\n"
"v_mfma_f32_16x16x32_bf16 v[112:115], a[200:203], v[188:191], v[112:115]\n"
"ds_read_b64_tr_b16 a[184:185], v33 offset:8192\n"
"v_mfma_f32_16x16x32_bf16 v[116:119], a[204:207], v[188:191], v[116:119]\n"
"v_exp_f32 v169, v169\n"
"v_mfma_f32_16x16x32_bf16 v[120:123], a[208:211], v[188:191], v[120:123]\n"
"ds_read_b64_tr_b16 a[186:187], v33 offset:12288\n"
"v_cvt_pk_bf16_f32 v184, v168, v169\n"
"v_mfma_f32_16x16x32_bf16 v[124:127], a[212:215], v[188:191], v[124:127]\n"
"v_exp_f32 v170, v170\n"
"v_mfma_f32_16x16x32_bf16 v[128:131], a[216:219], v[188:191], v[128:131]\n"
"ds_read_b64_tr_b16 a[188:189], v34 offset:8192\n"
"v_mfma_f32_16x16x32_bf16 v[132:135], a[220:223], v[188:191], v[132:135]\n"
"v_exp_f32 v171, v171\n"
"v_mfma_f32_16x16x32_bf16 v[136:139], a[224:227], v[188:191], v[136:139]\n"
"ds_read_b64_tr_b16 a[190:191], v34 offset:12288\n"
"v_cvt_pk_bf16_f32 v185, v170, v171\n"
"v_mfma_f32_16x16x32_bf16 v[140:143], a[228:231], v[188:191], v[140:143]\n"
"v_exp_f32 v172, v172\n"
"v_mfma_f32_16x16x32_bf16 v[176:179], a[104:107], a[64:67], v[160:163]\n"
"ds_read_b64_tr_b16 a[192:193], v35 offset:8192\n"
"v_mfma_f32_16x16x32_bf16 v[176:179], a[108:111], a[68:71], v[176:179]\n"
"v_exp_f32 v173, v173\n"
"v_mfma_f32_16x16x32_bf16 v[176:179], a[112:115], a[72:75], v[176:179]\n"
"ds_read_b64_tr_b16 a[194:195], v35 offset:12288\n"
"v_cvt_pk_bf16_f32 v186, v172, v173\n"
"v_mfma_f32_16x16x32_bf16 v[176:179], a[116:119], a[76:79], v[176:179]\n"
"v_exp_f32 v174, v174\n"
"v_mfma_f32_16x16x32_bf16 v[180:183], a[132:135], a[76:79], v[160:163]\n"
"ds_read_b64_tr_b16 a[196:197], v36 offset:8192\n"
"v_mfma_f32_16x16x32_bf16 v[180:183], a[128:131], a[72:75], v[180:183]\n"
"v_exp_f32 v175, v175\n"
"v_mfma_f32_16x16x32_bf16 v[180:183], a[124:127], a[68:71], v[180:183]\n"
"ds_read_b64_tr_b16 a[198:199], v36 offset:12288\n"
"v_cvt_pk_bf16_f32 v187, v174, v175\n"
"v_mfma_f32_16x16x32_bf16 v[180:183], a[120:123], a[64:67], v[180:183]\n"
"s_nop 0\n"
"v_mfma_f32_16x16x32_bf16 v[144:147], v[192:195], v[184:187], v[144:147]\n"
"s_barrier\n"
"v_exp_f32 v176, v176\n"
"v_mfma_f32_16x16x32_bf16 v[48:51], a[168:171], v[184:187], v[48:51]\n"
"s_waitcnt lgkmcnt(4)\n"
"ds_read_b128 a[136:139], v25 offset:8192\n"
"v_xor_b32 v29, s75, v29\n"
"v_mfma_f32_16x16x32_bf16 v[52:55], a[172:175], v[184:187], v[52:55]\n"
"v_exp_f32 v177, v177\n"
"v_mfma_f32_16x16x32_bf16 v[56:59], a[176:179], v[184:187], v[56:59]\n"
"ds_read_b128 a[140:143], v26 offset:8192\n"
"v_cvt_pk_bf16_f32 v188, v176, v177\n"
"v_mfma_f32_16x16x32_bf16 v[60:63], a[180:183], v[184:187], v[60:63]\n"
"v_exp_f32 v178, v178\n"
"v_mfma_f32_16x16x32_bf16 v[64:67], a[184:187], v[184:187], v[64:67]\n"
"ds_read_b128 a[144:147], v27 offset:8192\n"
"v_xor_b32 v30, s75, v30\n"
"v_mfma_f32_16x16x32_bf16 v[68:71], a[188:191], v[184:187], v[68:71]\n"
"s_waitcnt lgkmcnt(4)\n"
"v_exp_f32 v179, v179\n"
"v_mfma_f32_16x16x32_bf16 v[72:75], a[192:195], v[184:187], v[72:75]\n"
"s_waitcnt lgkmcnt(3)\n"
"ds_read_b128 a[148:151], v28 offset:8192\n"
"v_cvt_pk_bf16_f32 v189, v178, v179\n"
"v_mfma_f32_16x16x32_bf16 v[76:79], a[196:199], v[184:187], v[76:79]\n"
"v_exp_f32 v180, v180\n"
"v_mfma_f32_16x16x32_bf16 v[168:171], a[104:107], a[80:83], v[164:167]\n"
"ds_read_b128 a[164:167], v28 offset:12288\n"
"v_xor_b32 v31, s75, v31\n"
"v_mfma_f32_16x16x32_bf16 v[168:171], a[108:111], a[84:87], v[168:171]\n"
"v_exp_f32 v181, v181\n"
"v_mfma_f32_16x16x32_bf16 v[168:171], a[112:115], a[88:91], v[168:171]\n"
"ds_read_b128 a[160:163], v27 offset:12288\n"
"v_cvt_pk_bf16_f32 v190, v180, v181\n"
"v_mfma_f32_16x16x32_bf16 v[168:171], a[116:119], a[92:95], v[168:171]\n"
"v_exp_f32 v182, v182\n"
"v_mfma_f32_16x16x32_bf16 v[172:175], a[132:135], a[92:95], v[164:167]\n"
"ds_read_b128 a[156:159], v26 offset:12288\n"
"v_xor_b32 v32, s75, v32\n"
"v_mfma_f32_16x16x32_bf16 v[172:175], a[128:131], a[88:91], v[172:175]\n"
"v_exp_f32 v183, v183\n"
"v_mfma_f32_16x16x32_bf16 v[172:175], a[124:127], a[84:87], v[172:175]\n"
"ds_read_b128 a[152:155], v25 offset:12288\n"
"v_cvt_pk_bf16_f32 v191, v182, v183\n"
"v_mfma_f32_16x16x32_bf16 v[172:175], a[120:123], a[80:83], v[172:175]\n"
"s_nop 0\n"
"v_mfma_f32_16x16x32_bf16 v[148:151], v[192:195], v[188:191], v[148:151]\n"
"v_exp_f32 v168, v168\n"
"v_mfma_f32_16x16x32_bf16 v[80:83], a[168:171], v[188:191], v[80:83]\n"
"ds_read_b64_tr_b16 a[200:201], v29\n"
"v_xor_b32 v33, s75, v33\n"
"v_mfma_f32_16x16x32_bf16 v[84:87], a[172:175], v[188:191], v[84:87]\n"
"v_exp_f32 v169, v169\n"
"v_mfma_f32_16x16x32_bf16 v[88:91], a[176:179], v[188:191], v[88:91]\n"
"ds_read_b64_tr_b16 a[202:203], v29 offset:4096\n"
"v_cvt_pk_bf16_f32 v184, v168, v169\n"
"v_mfma_f32_16x16x32_bf16 v[92:95], a[180:183], v[188:191], v[92:95]\n"
"v_exp_f32 v170, v170\n"
"v_mfma_f32_16x16x32_bf16 v[96:99], a[184:187], v[188:191], v[96:99]\n"
"ds_read_b64_tr_b16 a[204:205], v30\n"
"v_xor_b32 v34, s75, v34\n"
"v_mfma_f32_16x16x32_bf16 v[100:103], a[188:191], v[188:191], v[100:103]\n"
"v_exp_f32 v171, v171\n"
"v_mfma_f32_16x16x32_bf16 v[104:107], a[192:195], v[188:191], v[104:107]\n"
"ds_read_b64_tr_b16 a[206:207], v30 offset:4096\n"
"v_cvt_pk_bf16_f32 v185, v170, v171\n"
"v_mfma_f32_16x16x32_bf16 v[108:111], a[196:199], v[188:191], v[108:111]\n"
"s_waitcnt lgkmcnt(5)\n"
"v_exp_f32 v172, v172\n"
"v_mfma_f32_16x16x32_bf16 v[176:179], a[136:139], a[48:51], v[156:159]\n"
"ds_read_b64_tr_b16 a[208:209], v31\n"
"v_xor_b32 v35, s75, v35\n"
"v_mfma_f32_16x16x32_bf16 v[176:179], a[140:143], a[52:55], v[176:179]\n"
"v_exp_f32 v173, v173\n"
"v_mfma_f32_16x16x32_bf16 v[176:179], a[144:147], a[56:59], v[176:179]\n"
"ds_read_b64_tr_b16 a[210:211], v31 offset:4096\n"
"v_cvt_pk_bf16_f32 v186, v172, v173\n"
"v_mfma_f32_16x16x32_bf16 v[176:179], a[148:151], a[60:63], v[176:179]\n"
"v_exp_f32 v174, v174\n"
"v_mfma_f32_16x16x32_bf16 v[180:183], a[164:167], a[60:63], v[156:159]\n"
"ds_read_b64_tr_b16 a[212:213], v32\n"
"v_xor_b32 v36, s75, v36\n"
"v_mfma_f32_16x16x32_bf16 v[180:183], a[160:163], a[56:59], v[180:183]\n"
"v_exp_f32 v175, v175\n"
"v_mfma_f32_16x16x32_bf16 v[180:183], a[156:159], a[52:55], v[180:183]\n"
"s_waitcnt lgkmcnt(4)\n"
"s_xor_b32 s75, s75, 0x38000\n"
"ds_read_b64_tr_b16 a[214:215], v32 offset:4096\n"
"v_cvt_pk_bf16_f32 v187, v174, v175\n"
"v_mfma_f32_16x16x32_bf16 v[180:183], a[152:155], a[48:51], v[180:183]\n"
"s_nop 0\n"
"v_mfma_f32_16x16x32_bf16 v[152:155], v[192:195], v[184:187], v[152:155]\n"
"v_exp_f32 v176, v176\n"
"v_mfma_f32_16x16x32_bf16 v[112:115], a[168:171], v[184:187], v[112:115]\n"
"ds_read_b64_tr_b16 a[216:217], v33\n"
"v_xor_b32 v25, s74, v25\n"
"v_mfma_f32_16x16x32_bf16 v[116:119], a[172:175], v[184:187], v[116:119]\n"
"v_exp_f32 v177, v177\n"
"v_mfma_f32_16x16x32_bf16 v[120:123], a[176:179], v[184:187], v[120:123]\n"
"ds_read_b64_tr_b16 a[218:219], v33 offset:4096\n"
"v_cvt_pk_bf16_f32 v188, v176, v177\n"
"v_mfma_f32_16x16x32_bf16 v[124:127], a[180:183], v[184:187], v[124:127]\n"
"v_exp_f32 v178, v178\n"
"v_mfma_f32_16x16x32_bf16 v[128:131], a[184:187], v[184:187], v[128:131]\n"
"ds_read_b64_tr_b16 a[220:221], v34\n"
"v_xor_b32 v26, s74, v26\n"
"v_mfma_f32_16x16x32_bf16 v[132:135], a[188:191], v[184:187], v[132:135]\n"
"v_exp_f32 v179, v179\n"
"v_mfma_f32_16x16x32_bf16 v[136:139], a[192:195], v[184:187], v[136:139]\n"
"ds_read_b64_tr_b16 a[222:223], v34 offset:4096\n"
"v_cvt_pk_bf16_f32 v189, v178, v179\n"
"v_mfma_f32_16x16x32_bf16 v[140:143], a[196:199], v[184:187], v[140:143]\n"
"v_exp_f32 v180, v180\n"
"v_mfma_f32_16x16x32_bf16 v[168:171], a[136:139], a[64:67], v[160:163]\n"
"ds_read_b64_tr_b16 a[224:225], v35\n"
"v_xor_b32 v27, s74, v27\n"
"v_mfma_f32_16x16x32_bf16 v[168:171], a[140:143], a[68:71], v[168:171]\n"
"v_exp_f32 v181, v181\n"
"v_mfma_f32_16x16x32_bf16 v[168:171], a[144:147], a[72:75], v[168:171]\n"
"ds_read_b64_tr_b16 a[226:227], v35 offset:4096\n"
"v_cvt_pk_bf16_f32 v190, v180, v181\n"
"v_mfma_f32_16x16x32_bf16 v[168:171], a[148:151], a[76:79], v[168:171]\n"
"v_exp_f32 v182, v182\n"
"v_mfma_f32_16x16x32_bf16 v[172:175], a[164:167], a[76:79], v[160:163]\n"
"ds_read_b64_tr_b16 a[228:229], v36\n"
"v_xor_b32 v28, s74, v28\n"
"v_mfma_f32_16x16x32_bf16 v[172:175], a[160:163], a[72:75], v[172:175]\n"
"v_exp_f32 v183, v183\n"
"v_mfma_f32_16x16x32_bf16 v[172:175], a[156:159], a[68:71], v[172:175]\n"
"s_xor_b32 s74, s74, 0x8000\n"
"ds_read_b64_tr_b16 a[230:231], v36 offset:4096\n"
"v_cvt_pk_bf16_f32 v191, v182, v183\n"
"v_mfma_f32_16x16x32_bf16 v[172:175], a[152:155], a[64:67], v[172:175]\n"
"s_sub_u32 s78, s78, 1\n"
"s_cbranch_scc0 L_loop2_%=\n"
"s_nop 0\n"
"v_mfma_f32_16x16x32_bf16 v[144:147], v[192:195], v[188:191], v[144:147]\n"
"s_barrier\n"
"v_exp_f32 v168, v168\n"
"v_mfma_f32_16x16x32_bf16 v[48:51], a[200:203], v[188:191], v[48:51]\n"
"s_waitcnt lgkmcnt(4)\n"
"ds_read_b128 a[104:107], v25\n"
"v_mfma_f32_16x16x32_bf16 v[52:55], a[204:207], v[188:191], v[52:55]\n"
"v_exp_f32 v169, v169\n"
"v_mfma_f32_16x16x32_bf16 v[56:59], a[208:211], v[188:191], v[56:59]\n"
"ds_read_b128 a[108:111], v26\n"
"v_cvt_pk_bf16_f32 v184, v168, v169\n"
"v_mfma_f32_16x16x32_bf16 v[60:63], a[212:215], v[188:191], v[60:63]\n"
"v_exp_f32 v170, v170\n"
"v_mfma_f32_16x16x32_bf16 v[64:67], a[216:219], v[188:191], v[64:67]\n"
"ds_read_b128 a[112:115], v27\n"
"v_mfma_f32_16x16x32_bf16 v[68:71], a[220:223], v[188:191], v[68:71]\n"
"s_waitcnt lgkmcnt(4)\n"
"v_exp_f32 v171, v171\n"
"v_mfma_f32_16x16x32_bf16 v[72:75], a[224:227], v[188:191], v[72:75]\n"
"s_waitcnt lgkmcnt(3)\n"
"ds_read_b128 a[116:119], v28\n"
"v_cvt_pk_bf16_f32 v185, v170, v171\n"
"v_mfma_f32_16x16x32_bf16 v[76:79], a[228:231], v[188:191], v[76:79]\n"
"v_exp_f32 v172, v172\n"
"v_mfma_f32_16x16x32_bf16 v[176:179], a[136:139], a[80:83], v[164:167]\n"
"ds_read_b128 a[132:135], v28 offset:4096\n"
"v_mfma_f32_16x16x32_bf16 v[176:179], a[140:143], a[84:87], v[176:179]\n"
"v_exp_f32 v173, v173\n"
"v_mfma_f32_16x16x32_bf16 v[176:179], a[144:147], a[88:91], v[176:179]\n"
"ds_read_b128 a[128:131], v27 offset:4096\n"
"v_cvt_pk_bf16_f32 v186, v172, v173\n"
"v_mfma_f32_16x16x32_bf16 v[176:179], a[148:151], a[92:95], v[176:179]\n"
"v_exp_f32 v174, v174\n"
"v_mfma_f32_16x16x32_bf16 v[180:183], a[164:167], a[92:95], v[164:167]\n"
"ds_read_b128 a[124:127], v26 offset:4096\n"
"v_mfma_f32_16x16x32_bf16 v[180:183], a[160:163], a[88:91], v[180:183]\n"
"v_exp_f32 v175, v175\n"
"v_mfma_f32_16x16x32_bf16 v[180:183], a[156:159], a[84:87], v[180:183]\n"
"ds_read_b128 a[120:123], v25 offset:4096\n"
"v_cvt_pk_bf16_f32 v187, v174, v175\n"
"v_mfma_f32_16x16x32_bf16 v[180:183], a[152:155], a[80:83], v[180:183]\n"
"s_nop 0\n"
"v_mfma_f32_16x16x32_bf16 v[148:151], v[192:195], v[184:187], v[148:151]\n"
"v_exp_f32 v176, v176\n"
"v_mfma_f32_16x16x32_bf16 v[80:83], a[200:203], v[184:187], v[80:83]\n"
"ds_read_b64_tr_b16 a[168:169], v29 offset:8192\n"
"v_mfma_f32_16x16x32_bf16 v[84:87], a[204:207], v[184:187], v[84:87]\n"
"v_exp_f32 v177, v177\n"
"v_mfma_f32_16x16x32_bf16 v[88:91], a[208:211], v[184:187], v[88:91]\n"
"ds_read_b64_tr_b16 a[170:171], v29 offset:12288\n"
"v_cvt_pk_bf16_f32 v188, v176, v177\n"
"v_mfma_f32_16x16x32_bf16 v[92:95], a[212:215], v[184:187], v[92:95]\n"
"v_exp_f32 v178, v178\n"
"v_mfma_f32_16x16x32_bf16 v[96:99], a[216:219], v[184:187], v[96:99]\n"
"ds_read_b64_tr_b16 a[172:173], v30 offset:8192\n"
"v_mfma_f32_16x16x32_bf16 v[100:103], a[220:223], v[184:187], v[100:103]\n"
"v_exp_f32 v179, v179\n"
"v_mfma_f32_16x16x32_bf16 v[104:107], a[224:227], v[184:187], v[104:107]\n"
"ds_read_b64_tr_b16 a[174:175], v30 offset:12288\n"
"v_cvt_pk_bf16_f32 v189, v178, v179\n"
"v_mfma_f32_16x16x32_bf16 v[108:111], a[228:231], v[184:187], v[108:111]\n"
"s_waitcnt lgkmcnt(5)\n"
"v_exp_f32 v180, v180\n"
"v_mfma_f32_16x16x32_bf16 v[168:171], a[104:107], a[48:51], v[156:159]\n"
"ds_read_b64_tr_b16 a[176:177], v31 offset:8192\n"
"v_mfma_f32_16x16x32_bf16 v[168:171], a[108:111], a[52:55], v[168:171]\n"
"v_exp_f32 v181, v181\n"
"v_mfma_f32_16x16x32_bf16 v[168:171], a[112:115], a[56:59], v[168:171]\n"
"ds_read_b64_tr_b16 a[178:179], v31 offset:12288\n"
"v_cvt_pk_bf16_f32 v190, v180, v181\n"
"v_mfma_f32_16x16x32_bf16 v[168:171], a[116:119], a[60:63], v[168:171]\n"
"v_exp_f32 v182, v182\n"
"v_mfma_f32_16x16x32_bf16 v[172:175], a[132:135], a[60:63], v[156:159]\n"
"ds_read_b64_tr_b16 a[180:181], v32 offset:8192\n"
"v_mfma_f32_16x16x32_bf16 v[172:175], a[128:131], a[56:59], v[172:175]\n"
"v_exp_f32 v183, v183\n"
"v_mfma_f32_16x16x32_bf16 v[172:175], a[124:127], a[52:55], v[172:175]\n"
"s_waitcnt lgkmcnt(5)\n"
"ds_read_b64_tr_b16 a[182:183], v32 offset:12288\n"
"v_cvt_pk_bf16_f32 v191, v182, v183\n"
"v_mfma_f32_16x16x32_bf16 v[172:175], a[120:123], a[48:51], v[172:175]\n"
"s_nop 0\n"
"v_mfma_f32_16x16x32_bf16 v[152:155], v[192:195], v[188:191], v[152:155]\n"
"v_exp_f32 v168, v168\n"
"v_mfma_f32_16x16x32_bf16 v[112:115], a[200:203], v[188:191], v[112:115]\n"
"ds_read_b64_tr_b16 a[184:185], v33 offset:8192\n"
"v_mfma_f32_16x16x32_bf16 v[116:119], a[204:207], v[188:191], v[116:119]\n"
"v_exp_f32 v169, v169\n"
"v_mfma_f32_16x16x32_bf16 v[120:123], a[208:211], v[188:191], v[120:123]\n"
"ds_read_b64_tr_b16 a[186:187], v33 offset:12288\n"
"v_cvt_pk_bf16_f32 v184, v168, v169\n"
"v_mfma_f32_16x16x32_bf16 v[124:127], a[212:215], v[188:191], v[124:127]\n"
"v_exp_f32 v170, v170\n"
"v_mfma_f32_16x16x32_bf16 v[128:131], a[216:219], v[188:191], v[128:131]\n"
"ds_read_b64_tr_b16 a[188:189], v34 offset:8192\n"
"v_mfma_f32_16x16x32_bf16 v[132:135], a[220:223], v[188:191], v[132:135]\n"
"v_exp_f32 v171, v171\n"
"v_mfma_f32_16x16x32_bf16 v[136:139], a[224:227], v[188:191], v[136:139]\n"
"ds_read_b64_tr_b16 a[190:191], v34 offset:12288\n"
"v_cvt_pk_bf16_f32 v185, v170, v171\n"
"v_mfma_f32_16x16x32_bf16 v[140:143], a[228:231], v[188:191], v[140:143]\n"
"v_exp_f32 v172, v172\n"
"v_mfma_f32_16x16x32_bf16 v[176:179], a[104:107], a[64:67], v[160:163]\n"
"ds_read_b64_tr_b16 a[192:193], v35 offset:8192\n"
"v_mfma_f32_16x16x32_bf16 v[176:179], a[108:111], a[68:71], v[176:179]\n"
"v_exp_f32 v173, v173\n"
"v_mfma_f32_16x16x32_bf16 v[176:179], a[112:115], a[72:75], v[176:179]\n"
"ds_read_b64_tr_b16 a[194:195], v35 offset:12288\n"
"v_cvt_pk_bf16_f32 v186, v172, v173\n"
"v_mfma_f32_16x16x32_bf16 v[176:179], a[116:119], a[76:79], v[176:179]\n"
"v_exp_f32 v174, v174\n"
"v_mfma_f32_16x16x32_bf16 v[180:183], a[132:135], a[76:79], v[160:163]\n"
"ds_read_b64_tr_b16 a[196:197], v36 offset:8192\n"
"v_mfma_f32_16x16x32_bf16 v[180:183], a[128:131], a[72:75], v[180:183]\n"
"v_exp_f32 v175, v175\n"
"v_mfma_f32_16x16x32_bf16 v[180:183], a[124:127], a[68:71], v[180:183]\n"
"ds_read_b64_tr_b16 a[198:199], v36 offset:12288\n"
"v_cvt_pk_bf16_f32 v187, v174, v175\n"
"v_mfma_f32_16x16x32_bf16 v[180:183], a[120:123], a[64:67], v[180:183]\n"
"s_nop 0\n"
"v_mfma_f32_16x16x32_bf16 v[144:147], v[192:195], v[184:187], v[144:147]\n"
"s_barrier\n"
"v_exp_f32 v176, v176\n"
"v_mfma_f32_16x16x32_bf16 v[48:51], a[168:171], v[184:187], v[48:51]\n"
"s_waitcnt lgkmcnt(4)\n"
"ds_read_b128 a[136:139], v25 offset:8192\n"
"v_xor_b32 v29, s75, v29\n"
"v_mfma_f32_16x16x32_bf16 v[52:55], a[172:175], v[184:187], v[52:55]\n"
"v_exp_f32 v177, v177\n"
"v_mfma_f32_16x16x32_bf16 v[56:59], a[176:179], v[184:187], v[56:59]\n"
"ds_read_b128 a[140:143], v26 offset:8192\n"
"v_cvt_pk_bf16_f32 v188, v176, v177\n"
"v_mfma_f32_16x16x32_bf16 v[60:63], a[180:183], v[184:187], v[60:63]\n"
"v_exp_f32 v178, v178\n"
"v_mfma_f32_16x16x32_bf16 v[64:67], a[184:187], v[184:187], v[64:67]\n"
"ds_read_b128 a[144:147], v27 offset:8192\n"
"v_xor_b32 v30, s75, v30\n"
"v_mfma_f32_16x16x32_bf16 v[68:71], a[188:191], v[184:187], v[68:71]\n"
"s_waitcnt lgkmcnt(4)\n"
"v_exp_f32 v179, v179\n"
"v_mfma_f32_16x16x32_bf16 v[72:75], a[192:195], v[184:187], v[72:75]\n"
"s_waitcnt lgkmcnt(3)\n"
"ds_read_b128 a[148:151], v28 offset:8192\n"
"v_cvt_pk_bf16_f32 v189, v178, v179\n"
"v_mfma_f32_16x16x32_bf16 v[76:79], a[196:199], v[184:187], v[76:79]\n"
"v_exp_f32 v180, v180\n"
"v_mfma_f32_16x16x32_bf16 v[168:171], a[104:107], a[80:83], v[164:167]\n"
"ds_read_b128 a[164:167], v28 offset:12288\n"
"v_xor_b32 v31, s75, v31\n"
"v_mfma_f32_16x16x32_bf16 v[168:171], a[108:111], a[84:87], v[168:171]\n"
"v_exp_f32 v181, v181\n"
"v_mfma_f32_16x16x32_bf16 v[168:171], a[112:115], a[88:91], v[168:171]\n"
"ds_read_b128 a[160:163], v27 offset:12288\n"
"v_cvt_pk_bf16_f32 v190, v180, v181\n"
"v_mfma_f32_16x16x32_bf16 v[168:171], a[116:119], a[92:95], v[168:171]\n"
"v_exp_f32 v182, v182\n"
"v_mfma_f32_16x16x32_bf16 v[172:175], a[132:135], a[92:95], v[164:167]\n"
"ds_read_b128 a[156:159], v26 offset:12288\n"
"v_xor_b32 v32, s75, v32\n"
"v_mfma_f32_16x16x32_bf16 v[172:175], a[128:131], a[88:91], v[172:175]\n"
"v_exp_f32 v183, v183\n"
"v_mfma_f32_16x16x32_bf16 v[172:175], a[124:127], a[84:87], v[172:175]\n"
"ds_read_b128 a[152:155], v25 offset:12288\n"
"v_cvt_pk_bf16_f32 v191, v182, v183\n"
"v_mfma_f32_16x16x32_bf16 v[172:175], a[120:123], a[80:83], v[172:175]\n"
"s_nop 0\n"
"v_mfma_f32_16x16x32_bf16 v[148:151], v[192:195], v[188:191], v[148:151]\n"
"v_exp_f32 v168, v168\n"
"v_mfma_f32_16x16x32_bf16 v[80:83], a[168:171], v[188:191], v[80:83]\n"
"s_cmp_le_u32 s65, 0\n"
"s_cselect_b32 s70, 1, 0\n"
"ds_read_b64_tr_b16 a[200:201], v29\n"
"v_xor_b32 v33, s75, v33\n"
"v_mfma_f32_16x16x32_bf16 v[84:87], a[172:175], v[188:191], v[84:87]\n"
"s_add_u32 s70, s70, s66\n"
"v_exp_f32 v169, v169\n"
"v_mfma_f32_16x16x32_bf16 v[88:91], a[176:179], v[188:191], v[88:91]\n"
"s_mul_i32 s70, s70, 73728\n"
"ds_read_b64_tr_b16 a[202:203], v29 offset:4096\n"
"v_cvt_pk_bf16_f32 v184, v168, v169\n"
"v_mfma_f32_16x16x32_bf16 v[92:95], a[180:183], v[188:191], v[92:95]\n"
"s_add_u32 s70, s70, s25\n"
"v_exp_f32 v170, v170\n"
"v_mfma_f32_16x16x32_bf16 v[96:99], a[184:187], v[188:191], v[96:99]\n"
"ds_read_b64_tr_b16 a[204:205], v30\n"
"buffer_load_dwordx4 a[0:3], v2, s[44:47], s70 offen sc1\n"
"v_xor_b32 v34, s75, v34\n"
"v_mfma_f32_16x16x32_bf16 v[100:103], a[188:191], v[188:191], v[100:103]\n"
"v_exp_f32 v171, v171\n"
"v_mfma_f32_16x16x32_bf16 v[104:107], a[192:195], v[188:191], v[104:107]\n"
"ds_read_b64_tr_b16 a[206:207], v30 offset:4096\n"
"v_cvt_pk_bf16_f32 v185, v170, v171\n"
"v_mfma_f32_16x16x32_bf16 v[108:111], a[196:199], v[188:191], v[108:111]\n"
"s_waitcnt lgkmcnt(4)\n"
"v_exp_f32 v172, v172\n"
"v_mfma_f32_16x16x32_bf16 v[176:179], a[136:139], a[48:51], v[156:159]\n"
"ds_read_b64_tr_b16 a[208:209], v31\n"
"buffer_load_dwordx4 a[4:7], v2, s[44:47], s70 offen offset:1024 sc1\n"
"v_xor_b32 v35, s75, v35\n"
"v_mfma_f32_16x16x32_bf16 v[176:179], a[140:143], a[52:55], v[176:179]\n"
"v_exp_f32 v173, v173\n"
"v_mfma_f32_16x16x32_bf16 v[176:179], a[144:147], a[56:59], v[176:179]\n"
"ds_read_b64_tr_b16 a[210:211], v31 offset:4096\n"
"v_cvt_pk_bf16_f32 v186, v172, v173\n"
"v_mfma_f32_16x16x32_bf16 v[176:179], a[148:151], a[60:63], v[176:179]\n"
"v_exp_f32 v174, v174\n"
"v_mfma_f32_16x16x32_bf16 v[180:183], a[164:167], a[60:63], v[156:159]\n"
"ds_read_b64_tr_b16 a[212:213], v32\n"
"buffer_load_dwordx4 a[8:11], v3, s[44:47], s70 offen sc1\n"
"v_xor_b32 v36, s75, v36\n"
"v_mfma_f32_16x16x32_bf16 v[180:183], a[160:163], a[56:59], v[180:183]\n"
"v_exp_f32 v175, v175\n"
"v_mfma_f32_16x16x32_bf16 v[180:183], a[156:159], a[52:55], v[180:183]\n"
"s_xor_b32 s75, s75, 0x38000\n"
"ds_read_b64_tr_b16 a[214:215], v32 offset:4096\n"
"v_cvt_pk_bf16_f32 v187, v174, v175\n"
"v_mfma_f32_16x16x32_bf16 v[180:183], a[152:155], a[48:51], v[180:183]\n"
"s_nop 0\n"
"v_mfma_f32_16x16x32_bf16 v[152:155], v[192:195], v[184:187], v[152:155]\n"
"v_exp_f32 v176, v176\n"
"v_mfma_f32_16x16x32_bf16 v[112:115], a[168:171], v[184:187], v[112:115]\n"
"ds_read_b64_tr_b16 a[216:217], v33\n"
"v_mfma_f32_16x16x32_bf16 v[116:119], a[172:175], v[184:187], v[116:119]\n"
"v_exp_f32 v177, v177\n"
"v_mfma_f32_16x16x32_bf16 v[120:123], a[176:179], v[184:187], v[120:123]\n"
"ds_read_b64_tr_b16 a[218:219], v33 offset:4096\n"
"v_cvt_pk_bf16_f32 v188, v176, v177\n"
"v_mfma_f32_16x16x32_bf16 v[124:127], a[180:183], v[184:187], v[124:127]\n"
"v_exp_f32 v178, v178\n"
"v_mfma_f32_16x16x32_bf16 v[128:131], a[184:187], v[184:187], v[128:131]\n"
"ds_read_b64_tr_b16 a[220:221], v34\n"
"buffer_load_dwordx4 a[12:15], v3, s[44:47], s70 offen offset:1024 sc1\n"
"v_mfma_f32_16x16x32_bf16 v[132:135], a[188:191], v[184:187], v[132:135]\n"
"v_exp_f32 v179, v179\n"
"v_mfma_f32_16x16x32_bf16 v[136:139], a[192:195], v[184:187], v[136:139]\n"
"ds_read_b64_tr_b16 a[222:223], v34 offset:4096\n"
"v_cvt_pk_bf16_f32 v189, v178, v179\n"
"v_mfma_f32_16x16x32_bf16 v[140:143], a[196:199], v[184:187], v[140:143]\n"
"v_exp_f32 v180, v180\n"
"v_mfma_f32_16x16x32_bf16 v[168:171], a[136:139], a[64:67], v[160:163]\n"
"ds_read_b64_tr_b16 a[224:225], v35\n"
"buffer_load_dwordx4 a[16:19], v4, s[44:47], s70 offen sc1\n"
"v_mfma_f32_16x16x32_bf16 v[168:171], a[140:143], a[68:71], v[168:171]\n"
"v_exp_f32 v181, v181\n"
"v_mfma_f32_16x16x32_bf16 v[168:171], a[144:147], a[72:75], v[168:171]\n"
"ds_read_b64_tr_b16 a[226:227], v35 offset:4096\n"
"v_cvt_pk_bf16_f32 v190, v180, v181\n"
"v_mfma_f32_16x16x32_bf16 v[168:171], a[148:151], a[76:79], v[168:171]\n"
"v_exp_f32 v182, v182\n"
"v_mfma_f32_16x16x32_bf16 v[172:175], a[164:167], a[76:79], v[160:163]\n"
"ds_read_b64_tr_b16 a[228:229], v36\n"
"buffer_load_dwordx4 a[20:23], v4, s[44:47], s70 offen offset:1024 sc1\n"
"v_mfma_f32_16x16x32_bf16 v[172:175], a[160:163], a[72:75], v[172:175]\n"
"v_exp_f32 v183, v183\n"
"v_mfma_f32_16x16x32_bf16 v[172:175], a[156:159], a[68:71], v[172:175]\n"
"ds_read_b64_tr_b16 a[230:231], v36 offset:4096\n"
"v_cvt_pk_bf16_f32 v191, v182, v183\n"
"v_mfma_f32_16x16x32_bf16 v[172:175], a[152:155], a[64:67], v[172:175]\n"
"s_nop 0\n"
"v_mfma_f32_16x16x32_bf16 v[144:147], v[192:195], v[188:191], v[144:147]\n"
"s_barrier\n"
"v_exp_f32 v168, v168\n"
"s_waitcnt lgkmcnt(4)\n"
"v_mfma_f32_16x16x32_bf16 v[48:51], a[200:203], v[188:191], v[48:51]\n"
"ds_read_b64_tr_b16 a[36:37], v11\n"
"v_mfma_f32_16x16x32_bf16 v[52:55], a[204:207], v[188:191], v[52:55]\n"
"v_exp_f32 v169, v169\n"
"v_mfma_f32_16x16x32_bf16 v[56:59], a[208:211], v[188:191], v[56:59]\n"
"ds_read_b64_tr_b16 a[38:39], v11 offset:2048\n"
"v_cvt_pk_bf16_f32 v184, v168, v169\n"
"v_mfma_f32_16x16x32_bf16 v[60:63], a[212:215], v[188:191], v[60:63]\n"
"v_exp_f32 v170, v170\n"
"v_mfma_f32_16x16x32_bf16 v[64:67], a[216:219], v[188:191], v[64:67]\n"
"ds_read_b64_tr_b16 a[40:41], v12\n"
"buffer_load_dwordx4 a[24:27], v5, s[44:47], s70 offen sc1\n"
"v_mfma_f32_16x16x32_bf16 v[68:71], a[220:223], v[188:191], v[68:71]\n"
"s_waitcnt lgkmcnt(4)\n"
"v_exp_f32 v171, v171\n"
"v_mfma_f32_16x16x32_bf16 v[72:75], a[224:227], v[188:191], v[72:75]\n"
"s_waitcnt lgkmcnt(3)\n"
"ds_read_b64_tr_b16 a[42:43], v12 offset:2048\n"
"v_cvt_pk_bf16_f32 v185, v170, v171\n"
"v_mfma_f32_16x16x32_bf16 v[76:79], a[228:231], v[188:191], v[76:79]\n"
"v_exp_f32 v172, v172\n"
"v_mfma_f32_16x16x32_bf16 v[176:179], a[136:139], a[80:83], v[164:167]\n"
"ds_read_b64_tr_b16 a[44:45], v13\n"
"buffer_load_dwordx4 a[28:31], v5, s[44:47], s70 offen offset:1024 sc1\n"
"v_mfma_f32_16x16x32_bf16 v[176:179], a[140:143], a[84:87], v[176:179]\n"
"v_exp_f32 v173, v173\n"
"v_mfma_f32_16x16x32_bf16 v[176:179], a[144:147], a[88:91], v[176:179]\n"
"ds_read_b64_tr_b16 a[46:47], v13 offset:2048\n"
"v_cvt_pk_bf16_f32 v186, v172, v173\n"
"v_mfma_f32_16x16x32_bf16 v[176:179], a[148:151], a[92:95], v[176:179]\n"
"v_exp_f32 v174, v174\n"
"v_mfma_f32_16x16x32_bf16 v[180:183], a[164:167], a[92:95], v[164:167]\n"
"ds_read_b64_tr_b16 a[96:97], v14\n"
"buffer_load_dwordx4 a[32:35], v6, s[44:47], s70 offen sc1\n"
"v_cmp_nge_f32 vcc, s27, v144\n"
"v_mfma_f32_16x16x32_bf16 v[180:183], a[160:163], a[88:91], v[180:183]\n"
"v_exp_f32 v175, v175\n"
"v_mfma_f32_16x16x32_bf16 v[180:183], a[156:159], a[84:87], v[180:183]\n"
"ds_read_b64_tr_b16 a[98:99], v14 offset:2048\n"
"v_cvt_pk_bf16_f32 v187, v174, v175\n"
"v_mfma_f32_16x16x32_bf16 v[180:183], a[152:155], a[80:83], v[180:183]\n"
"s_cbranch_vccnz L_slow10_%=\n"
"L_slow10r_%=:\n"
"v_mfma_f32_16x16x32_bf16 v[148:151], v[192:195], v[184:187], v[148:151]\n"
"v_exp_f32 v176, v176\n"
"v_rcp_f32 v214, v144\n"
"s_nop 0\n"
"v_mul_f32 v48, v214, v48\n"
"v_mul_f32 v49, v214, v49\n"
"v_cvt_pk_bf16_f32 v196, v48, v49\n"
"v_mfma_f32_16x16x32_bf16 v[80:83], a[200:203], v[184:187], v[80:83]\n"
"s_waitcnt lgkmcnt(0)\n"
"s_add_u32 m0, s24, 0\n"
"s_mov_b32 s69, s79\n"
"s_lshl_b32 s69, s69, 19\n"
"s_add_u32 s69, s69, s31\n"
"s_add_i32 s69, s69, 262144\n"
"v_mul_f32 v50, v214, v50\n"
"v_mul_f32 v51, v214, v51\n"
"v_cvt_pk_bf16_f32 v197, v50, v51\n"
"v_mul_f32 v52, v214, v52\n"
"v_mfma_f32_16x16x32_bf16 v[84:87], a[204:207], v[184:187], v[84:87]\n"
"buffer_load_dwordx4 v7, s[48:51], s69 offen lds\n"
"v_exp_f32 v177, v177\n"
"v_mul_f32 v53, v214, v53\n"
"v_cvt_pk_bf16_f32 v198, v52, v53\n"
"v_mul_f32 v54, v214, v54\n"
"v_mul_f32 v55, v214, v55\n"
"v_mfma_f32_16x16x32_bf16 v[88:91], a[208:211], v[184:187], v[88:91]\n"
"buffer_load_dwordx4 v8, s[48:51], s69 offen offset:1024 lds\n"
"v_cvt_pk_bf16_f32 v188, v176, v177\n"
"v_cvt_pk_bf16_f32 v199, v54, v55\n"
"v_mul_f32 v56, v214, v56\n"
"v_mfma_f32_16x16x32_bf16 v[92:95], a[212:215], v[184:187], v[92:95]\n"
"buffer_load_dwordx4 v9, s[48:51], s69 offen offset:2048 lds\n"
"ds_write_b128 v39, v[196:199]\n"
"v_exp_f32 v178, v178\n"
"v_mul_f32 v57, v214, v57\n"
"v_cvt_pk_bf16_f32 v200, v56, v57\n"
"v_mul_f32 v58, v214, v58\n"
"v_mul_f32 v59, v214, v59\n"
"v_mfma_f32_16x16x32_bf16 v[96:99], a[216:219], v[184:187], v[96:99]\n"
"buffer_load_dwordx4 v10, s[48:51], s69 offen offset:3072 lds\n"
"buffer_store_dwordx4 v[196:199], v19, s[44:47], s12 offen sc1\n"
"v_cvt_pk_bf16_f32 v201, v58, v59\n"
"v_mul_f32 v60, v214, v60\n"
"v_mul_f32 v61, v214, v61\n"
"v_cvt_pk_bf16_f32 v202, v60, v61\n"
"v_mfma_f32_16x16x32_bf16 v[100:103], a[220:223], v[184:187], v[100:103]\n"
"v_exp_f32 v179, v179\n"
"v_mul_f32 v62, v214, v62\n"
"v_mul_f32 v63, v214, v63\n"
"v_cvt_pk_bf16_f32 v203, v62, v63\n"
"v_mfma_f32_16x16x32_bf16 v[104:107], a[224:227], v[184:187], v[104:107]\n"
"ds_write_b128 v39, v[200:203] offset:9216\n"
"v_cvt_pk_bf16_f32 v189, v178, v179\n"
"v_mul_f32 v64, v214, v64\n"
"v_mul_f32 v65, v214, v65\n"
"v_cvt_pk_bf16_f32 v204, v64, v65\n"
"v_mfma_f32_16x16x32_bf16 v[108:111], a[228:231], v[184:187], v[108:111]\n"
"v_exp_f32 v180, v180\n"
"v_mul_f32 v66, v214, v66\n"
"v_mul_f32 v67, v214, v67\n"
"v_cvt_pk_bf16_f32 v205, v66, v67\n"
"v_mul_f32 v68, v214, v68\n"
"buffer_store_dwordx4 v[200:203], v19, s[44:47], s13 offen sc1\n"
"v_mul_f32 v69, v214, v69\n"
"v_cvt_pk_bf16_f32 v206, v68, v69\n"
"v_mul_f32 v70, v214, v70\n"
"v_mul_f32 v71, v214, v71\n"
"v_exp_f32 v181, v181\n"
"v_cvt_pk_bf16_f32 v207, v70, v71\n"
"v_mul_f32 v72, v214, v72\n"
"ds_write_b128 v39, v[204:207] offset:18432\n"
"v_cvt_pk_bf16_f32 v190, v180, v181\n"
"v_mul_f32 v73, v214, v73\n"
"v_cvt_pk_bf16_f32 v208, v72, v73\n"
"v_mul_f32 v74, v214, v74\n"
"v_mul_f32 v75, v214, v75\n"
"buffer_store_dwordx4 v[204:207], v19, s[44:47], s14 offen sc1\n"
"v_exp_f32 v182, v182\n"
"v_cvt_pk_bf16_f32 v209, v74, v75\n"
"v_mul_f32 v76, v214, v76\n"
"v_mul_f32 v77, v214, v77\n"
"v_cvt_pk_bf16_f32 v210, v76, v77\n"
"v_mul_f32 v78, v214, v78\n"
"v_mul_f32 v79, v214, v79\n"
"v_cvt_pk_bf16_f32 v211, v78, v79\n"
"v_cmp_nge_f32 vcc, s27, v148\n"
"ds_write_b128 v39, v[208:211] offset:27648\n"
"v_exp_f32 v183, v183\n"
"s_nop 0\n"
"v_cvt_pk_bf16_f32 v191, v182, v183\n"
"s_cbranch_vccnz L_slow11_%=\n"
"L_slow11r_%=:\n"
"buffer_store_dwordx4 v[208:211], v19, s[44:47], s15 offen sc1\n"
"v_mfma_f32_16x16x32_bf16 v[152:155], v[192:195], v[188:191], v[152:155]\n"
"v_rcp_f32 v214, v148\n"
"s_nop 0\n"
"v_mul_f32 v80, v214, v80\n"
"v_mul_f32 v81, v214, v81\n"
"v_cvt_pk_bf16_f32 v196, v80, v81\n"
"v_mfma_f32_16x16x32_bf16 v[112:115], a[200:203], v[188:191], v[112:115]\n"
"v_mul_f32 v82, v214, v82\n"
"v_mul_f32 v83, v214, v83\n"
"v_cvt_pk_bf16_f32 v197, v82, v83\n"
"v_mul_f32 v84, v214, v84\n"
"v_mfma_f32_16x16x32_bf16 v[116:119], a[204:207], v[188:191], v[116:119]\n"
"v_mul_f32 v85, v214, v85\n"
"v_cvt_pk_bf16_f32 v198, v84, v85\n"
"v_mul_f32 v86, v214, v86\n"
"v_mul_f32 v87, v214, v87\n"
"v_mfma_f32_16x16x32_bf16 v[120:123], a[208:211], v[188:191], v[120:123]\n"
"v_cvt_pk_bf16_f32 v199, v86, v87\n"
"v_mul_f32 v88, v214, v88\n"
"v_mfma_f32_16x16x32_bf16 v[124:127], a[212:215], v[188:191], v[124:127]\n"
"ds_write_b128 v39, v[196:199] offset:1024\n"
"v_mul_f32 v89, v214, v89\n"
"v_cvt_pk_bf16_f32 v200, v88, v89\n"
"v_mul_f32 v90, v214, v90\n"
"v_mul_f32 v91, v214, v91\n"
"v_mfma_f32_16x16x32_bf16 v[128:131], a[216:219], v[188:191], v[128:131]\n"
"buffer_store_dwordx4 v[196:199], v19, s[44:47], s12 offen offset:1024 sc1\n"
"v_cvt_pk_bf16_f32 v201, v90, v91\n"
"v_mul_f32 v92, v214, v92\n"
"v_mul_f32 v93, v214, v93\n"
"v_cvt_pk_bf16_f32 v202, v92, v93\n"
"v_mfma_f32_16x16x32_bf16 v[132:135], a[220:223], v[188:191], v[132:135]\n"
"v_mul_f32 v94, v214, v94\n"
"v_mul_f32 v95, v214, v95\n"
"v_cvt_pk_bf16_f32 v203, v94, v95\n"
"v_mfma_f32_16x16x32_bf16 v[136:139], a[224:227], v[188:191], v[136:139]\n"
"ds_write_b128 v39, v[200:203] offset:10240\n"
"v_mul_f32 v96, v214, v96\n"
"v_mul_f32 v97, v214, v97\n"
"v_cvt_pk_bf16_f32 v204, v96, v97\n"
"v_mfma_f32_16x16x32_bf16 v[140:143], a[228:231], v[188:191], v[140:143]\n"
"v_mul_f32 v98, v214, v98\n"
"v_mul_f32 v99, v214, v99\n"
"v_cvt_pk_bf16_f32 v205, v98, v99\n"
"v_mul_f32 v100, v214, v100\n"
"buffer_store_dwordx4 v[200:203], v19, s[44:47], s13 offen offset:1024 sc1\n"
"v_mul_f32 v101, v214, v101\n"
"v_cvt_pk_bf16_f32 v206, v100, v101\n"
"v_mul_f32 v102, v214, v102\n"
"v_mul_f32 v103, v214, v103\n"
"v_cvt_pk_bf16_f32 v207, v102, v103\n"
"v_mul_f32 v104, v214, v104\n"
"ds_write_b128 v39, v[204:207] offset:19456\n"
"v_mul_f32 v105, v214, v105\n"
"v_cvt_pk_bf16_f32 v208, v104, v105\n"
"v_mul_f32 v106, v214, v106\n"
"v_mul_f32 v107, v214, v107\n"
"buffer_store_dwordx4 v[204:207], v19, s[44:47], s14 offen offset:1024 sc1\n"
"v_cvt_pk_bf16_f32 v209, v106, v107\n"
"v_mul_f32 v108, v214, v108\n"
"v_mul_f32 v109, v214, v109\n"
"v_cvt_pk_bf16_f32 v210, v108, v109\n"
"v_mul_f32 v110, v214, v110\n"
"v_mul_f32 v111, v214, v111\n"
"v_cvt_pk_bf16_f32 v211, v110, v111\n"
"v_cmp_nge_f32 vcc, s27, v152\n"
"ds_write_b128 v39, v[208:211] offset:28672\n"
"s_cbranch_vccnz L_slow12_%=\n"
"L_slow12r_%=:\n"
"buffer_store_dwordx4 v[208:211], v19, s[44:47], s15 offen offset:1024 sc1\n"
"v_rcp_f32 v214, v152\n"
"s_nop 0\n"
"v_mul_f32 v112, v214, v112\n"
"v_mul_f32 v113, v214, v113\n"
"v_cvt_pk_bf16_f32 v196, v112, v113\n"
"v_mul_f32 v114, v214, v114\n"
"v_mul_f32 v115, v214, v115\n"
"v_cvt_pk_bf16_f32 v197, v114, v115\n"
"v_mul_f32 v116, v214, v116\n"
"v_mul_f32 v117, v214, v117\n"
"v_cvt_pk_bf16_f32 v198, v116, v117\n"
"v_mul_f32 v118, v214, v118\n"
"v_mul_f32 v119, v214, v119\n"
"v_cvt_pk_bf16_f32 v199, v118, v119\n"
"ds_write_b128 v39, v[196:199] offset:2048\n"
"buffer_store_dwordx4 v[196:199], v19, s[44:47], s12 offen offset:2048 sc1\n"
"v_mul_f32 v120, v214, v120\n"
"v_mul_f32 v121, v214, v121\n"
"v_cvt_pk_bf16_f32 v200, v120, v121\n"
"v_mul_f32 v122, v214, v122\n"
"v_mul_f32 v123, v214, v123\n"
"v_cvt_pk_bf16_f32 v201, v122, v123\n"
"v_mul_f32 v124, v214, v124\n"
"v_mul_f32 v125, v214, v125\n"
"v_cvt_pk_bf16_f32 v202, v124, v125\n"
"v_mul_f32 v126, v214, v126\n"
"v_mul_f32 v127, v214, v127\n"
"v_cvt_pk_bf16_f32 v203, v126, v127\n"
"ds_write_b128 v39, v[200:203] offset:11264\n"
"buffer_store_dwordx4 v[200:203], v19, s[44:47], s13 offen offset:2048 sc1\n"
"v_mul_f32 v128, v214, v128\n"
"v_mul_f32 v129, v214, v129\n"
"v_cvt_pk_bf16_f32 v204, v128, v129\n"
"v_mul_f32 v130, v214, v130\n"
"v_mul_f32 v131, v214, v131\n"
"v_cvt_pk_bf16_f32 v205, v130, v131\n"
"v_mul_f32 v132, v214, v132\n"
"v_mul_f32 v133, v214, v133\n"
"v_cvt_pk_bf16_f32 v206, v132, v133\n"
"v_mul_f32 v134, v214, v134\n"
"v_mul_f32 v135, v214, v135\n"
"v_cvt_pk_bf16_f32 v207, v134, v135\n"
"ds_write_b128 v39, v[204:207] offset:20480\n"
"buffer_store_dwordx4 v[204:207], v19, s[44:47], s14 offen offset:2048 sc1\n"
"v_mul_f32 v136, v214, v136\n"
"v_mul_f32 v137, v214, v137\n"
"v_cvt_pk_bf16_f32 v208, v136, v137\n"
"v_mul_f32 v138, v214, v138\n"
"v_mul_f32 v139, v214, v139\n"
"v_cvt_pk_bf16_f32 v209, v138, v139\n"
"v_mul_f32 v140, v214, v140\n"
"v_mul_f32 v141, v214, v141\n"
"v_cvt_pk_bf16_f32 v210, v140, v141\n"
"v_mul_f32 v142, v214, v142\n"
"v_mul_f32 v143, v214, v143\n"
"v_cvt_pk_bf16_f32 v211, v142, v143\n"
"ds_write_b128 v39, v[208:211] offset:29696\n"
"buffer_store_dwordx4 v[208:211], v19, s[44:47], s15 offen offset:2048 sc1\n"
"s_waitcnt lgkmcnt(0)\n"
"s_barrier\n"
"s_waitcnt vmcnt(24)\n"
"v_mfma_f32_16x16x32_bf16 v[48:51], a[36:39], a[0:3], 0\n"
"ds_read_b64_tr_b16 a[216:217], v11 offset:4096\n"
"v_mfma_f32_16x16x32_bf16 v[52:55], a[40:43], a[0:3], 0\n"
"ds_read_b64_tr_b16 a[218:219], v11 offset:6144\n"
"v_mfma_f32_16x16x32_bf16 v[56:59], a[44:47], a[0:3], 0\n"
"ds_read_b64_tr_b16 a[220:221], v12 offset:4096\n"
"v_mfma_f32_16x16x32_bf16 v[60:63], a[96:99], a[0:3], 0\n"
"s_waitcnt vmcnt(23)\n"
"ds_read_b64_tr_b16 a[222:223], v12 offset:6144\n"
"v_mfma_f32_16x16x32_bf16 v[76:79], a[96:99], a[4:7], 0\n"
"ds_read_b64_tr_b16 a[224:225], v13 offset:4096\n"
"v_mfma_f32_16x16x32_bf16 v[72:75], a[44:47], a[4:7], 0\n"
"ds_read_b64_tr_b16 a[226:227], v13 offset:6144\n"
"v_mfma_f32_16x16x32_bf16 v[68:71], a[40:43], a[4:7], 0\n"
"ds_read_b64_tr_b16 a[228:229], v14 offset:4096\n"
"v_mfma_f32_16x16x32_bf16 v[64:67], a[36:39], a[4:7], 0\n"
"s_waitcnt vmcnt(22)\n"
"ds_read_b64_tr_b16 a[230:231], v14 offset:6144\n"
"v_mfma_f32_16x16x32_bf16 v[80:83], a[36:39], a[8:11], 0\n"
"ds_read_b64_tr_b16 a[232:233], v11 offset:8192\n"
"v_mfma_f32_16x16x32_bf16 v[84:87], a[40:43], a[8:11], 0\n"
"ds_read_b64_tr_b16 a[234:235], v11 offset:10240\n"
"v_mfma_f32_16x16x32_bf16 v[88:91], a[44:47], a[8:11], 0\n"
"s_cmp_le_u32 s65, 1\n"
"s_cselect_b32 s70, 2, 1\n"
"ds_read_b64_tr_b16 a[236:237], v12 offset:8192\n"
"v_mfma_f32_16x16x32_bf16 v[92:95], a[96:99], a[8:11], 0\n"
"s_waitcnt vmcnt(21)\n"
"s_add_u32 s70, s70, s66\n"
"ds_read_b64_tr_b16 a[238:239], v12 offset:10240\n"
"v_mfma_f32_16x16x32_bf16 v[108:111], a[96:99], a[12:15], 0\n"
"s_mul_i32 s70, s70, 73728\n"
"ds_read_b64_tr_b16 a[240:241], v13 offset:8192\n"
"v_mfma_f32_16x16x32_bf16 v[104:107], a[44:47], a[12:15], 0\n"
"s_add_u32 s70, s70, s25\n"
"ds_read_b64_tr_b16 a[242:243], v13 offset:10240\n"
"v_mfma_f32_16x16x32_bf16 v[100:103], a[40:43], a[12:15], 0\n"
"s_waitcnt lgkmcnt(6)\n"
"ds_read_b64_tr_b16 a[244:245], v14 offset:8192\n"
"buffer_load_dwordx4 a[84:87], v2, s[44:47], s70 offen sc1\n"
"v_mfma_f32_16x16x32_bf16 v[96:99], a[36:39], a[12:15], 0\n"
"s_waitcnt vmcnt(21)\n"
"s_add_u32 m0, s24, 4096\n"
"ds_read_b64_tr_b16 a[246:247], v14 offset:10240\n"
"v_mfma_f32_16x16x32_bf16 v[112:115], a[36:39], a[16:19], 0\n"
"s_cmp_le_u32 s65, 1\n"
"s_cselect_b32 s69, 2, 1\n"
"ds_read_b128 a[48:51], v15\n"
"v_mfma_f32_16x16x32_bf16 v[116:119], a[40:43], a[16:19], 0\n"
"s_add_u32 s69, s69, s66\n"
"buffer_load_dwordx4 a[88:91], v2, s[44:47], s70 offen offset:1024 sc1\n"
"v_mfma_f32_16x16x32_bf16 v[120:123], a[44:47], a[16:19], 0\n"
"s_lshl_b32 s69, s69, 19\n"
"ds_read_b128 a[52:55], v15 offset:1024\n"
"v_mfma_f32_16x16x32_bf16 v[124:127], a[96:99], a[16:19], 0\n"
"s_waitcnt vmcnt(21)\n"
"s_add_u32 s69, s69, s26\n"
"ds_read_b128 a[56:59], v16\n"
"v_mfma_f32_16x16x32_bf16 v[140:143], a[96:99], a[20:23], 0\n"
"buffer_load_dwordx4 v7, s[48:51], s69 offen lds\n"
"v_mfma_f32_16x16x32_bf16 v[136:139], a[44:47], a[20:23], 0\n"
"ds_read_b128 a[60:63], v16 offset:1024\n"
"v_mfma_f32_16x16x32_bf16 v[132:135], a[40:43], a[20:23], 0\n"
"ds_read_b128 a[64:67], v21\n"
"v_mfma_f32_16x16x32_bf16 v[128:131], a[36:39], a[20:23], 0\n"
"s_waitcnt vmcnt(21)\n"
"ds_read_b128 a[68:71], v21 offset:1024\n"
"v_mfma_f32_16x16x32_bf16 v[144:147], a[36:39], a[24:27], 0\n"
"buffer_load_dwordx4 v8, s[48:51], s69 offen offset:1024 lds\n"
"v_mfma_f32_16x16x32_bf16 v[148:151], a[40:43], a[24:27], 0\n"
"ds_read_b128 a[72:75], v22\n"
"v_mfma_f32_16x16x32_bf16 v[152:155], a[44:47], a[24:27], 0\n"
"ds_read_b128 a[76:79], v22 offset:1024\n"
"v_mfma_f32_16x16x32_bf16 v[156:159], a[96:99], a[24:27], 0\n"
"s_waitcnt vmcnt(21)\n"
"buffer_load_dwordx4 v9, s[48:51], s69 offen offset:2048 lds\n"
"v_mfma_f32_16x16x32_bf16 v[172:175], a[96:99], a[28:31], 0\n"
"ds_read_b128 a[80:83], v37\n"
"v_mfma_f32_16x16x32_bf16 v[168:171], a[44:47], a[28:31], 0\n"
"v_mfma_f32_16x16x32_bf16 v[164:167], a[40:43], a[28:31], 0\n"
"v_mfma_f32_16x16x32_bf16 v[160:163], a[36:39], a[28:31], 0\n"
"s_waitcnt vmcnt(21)\n"
"buffer_load_dwordx4 v10, s[48:51], s69 offen offset:3072 lds\n"
"v_mfma_f32_16x16x32_bf16 v[176:179], a[36:39], a[32:35], 0\n"
"v_mfma_f32_16x16x32_bf16 v[180:183], a[40:43], a[32:35], 0\n"
"s_mul_i32 s29, s79, 73728\n"
"v_mfma_f32_16x16x32_bf16 v[184:187], a[44:47], a[32:35], 0\n"
"s_add_u32 s29, s29, s25\n"
"buffer_load_dwordx4 a[92:95], v3, s[44:47], s70 offen sc1\n"
"v_mfma_f32_16x16x32_bf16 v[188:191], a[96:99], a[32:35], 0\n"
"s_waitcnt lgkmcnt(3)\n"
"s_add_u32 s29, s29, 36864\n"
"v_mfma_f32_16x16x32_bf16 v[48:51], a[216:219], a[48:51], v[48:51]\n"
"s_add_u32 m0, s24, 8192\n"
"v_mfma_f32_16x16x32_bf16 v[52:55], a[220:223], a[48:51], v[52:55]\n"
"s_cmp_le_u32 s65, 2\n"
"s_cselect_b32 s69, 3, 2\n"
"buffer_load_dwordx4 a[100:103], v3, s[44:47], s70 offen offset:1024 sc1\n"
"v_mfma_f32_16x16x32_bf16 v[56:59], a[224:227], a[48:51], v[56:59]\n"
"s_add_u32 s69, s69, s66\n"
"v_mfma_f32_16x16x32_bf16 v[60:63], a[228:231], a[48:51], v[60:63]\n"
"s_lshl_b32 s69, s69, 19\n"
"v_mfma_f32_16x16x32_bf16 v[76:79], a[228:231], a[52:55], v[76:79]\n"
"s_add_u32 s69, s69, s26\n"
"v_mfma_f32_16x16x32_bf16 v[72:75], a[224:227], a[52:55], v[72:75]\n"
"buffer_load_dwordx4 v7, s[48:51], s69 offen lds\n"
"v_mfma_f32_16x16x32_bf16 v[68:71], a[220:223], a[52:55], v[68:71]\n"
"v_mfma_f32_16x16x32_bf16 v[64:67], a[216:219], a[52:55], v[64:67]\n"
"v_mfma_f32_16x16x32_bf16 v[80:83], a[216:219], a[56:59], v[80:83]\n"
"buffer_load_dwordx4 v8, s[48:51], s69 offen offset:1024 lds\n"
"v_mfma_f32_16x16x32_bf16 v[84:87], a[220:223], a[56:59], v[84:87]\n"
"v_mfma_f32_16x16x32_bf16 v[88:91], a[224:227], a[56:59], v[88:91]\n"
"v_mfma_f32_16x16x32_bf16 v[92:95], a[228:231], a[56:59], v[92:95]\n"
"v_mfma_f32_16x16x32_bf16 v[108:111], a[228:231], a[60:63], v[108:111]\n"
"buffer_load_dwordx4 v9, s[48:51], s69 offen offset:2048 lds\n"
"v_mfma_f32_16x16x32_bf16 v[104:107], a[224:227], a[60:63], v[104:107]\n"
"s_waitcnt vmcnt(23)\n"
"ds_read_b64_tr_b16 v[224:225], v11\n"
"v_mfma_f32_16x16x32_bf16 v[100:103], a[220:223], a[60:63], v[100:103]\n"
"ds_read_b64_tr_b16 v[226:227], v11 offset:2048\n"
"v_mfma_f32_16x16x32_bf16 v[96:99], a[216:219], a[60:63], v[96:99]\n"
"buffer_load_dwordx4 v10, s[48:51], s69 offen offset:3072 lds\n"
"v_mfma_f32_16x16x32_bf16 v[112:115], a[216:219], a[64:67], v[112:115]\n"
"ds_read_b64_tr_b16 v[228:229], v12\n"
"v_mfma_f32_16x16x32_bf16 v[116:119], a[220:223], a[64:67], v[116:119]\n"
"ds_read_b64_tr_b16 v[230:231], v12 offset:2048\n"
"v_mfma_f32_16x16x32_bf16 v[120:123], a[224:227], a[64:67], v[120:123]\n"
"buffer_load_dwordx4 a[248:251], v4, s[44:47], s70 offen sc1\n"
"v_mfma_f32_16x16x32_bf16 v[124:127], a[228:231], a[64:67], v[124:127]\n"
"ds_read_b64_tr_b16 v[232:233], v13\n"
"v_mfma_f32_16x16x32_bf16 v[140:143], a[228:231], a[68:71], v[140:143]\n"
"ds_read_b64_tr_b16 v[234:235], v13 offset:2048\n"
"v_mfma_f32_16x16x32_bf16 v[136:139], a[224:227], a[68:71], v[136:139]\n"
"ds_read_b64_tr_b16 v[236:237], v14\n"
"v_mfma_f32_16x16x32_bf16 v[132:135], a[220:223], a[68:71], v[132:135]\n"
"buffer_load_dwordx4 a[252:255], v4, s[44:47], s70 offen offset:1024 sc1\n"
"v_mfma_f32_16x16x32_bf16 v[128:131], a[216:219], a[68:71], v[128:131]\n"
"s_waitcnt lgkmcnt(7)\n"
"ds_read_b64_tr_b16 v[238:239], v14 offset:2048\n"
"v_mfma_f32_16x16x32_bf16 v[144:147], a[216:219], a[72:75], v[144:147]\n"
"v_mfma_f32_16x16x32_bf16 v[148:151], a[220:223], a[72:75], v[148:151]\n"
"buffer_load_dwordx4 v[244:247], v5, s[44:47], s70 offen sc1\n"
"v_mfma_f32_16x16x32_bf16 v[152:155], a[224:227], a[72:75], v[152:155]\n"
"v_mfma_f32_16x16x32_bf16 v[156:159], a[228:231], a[72:75], v[156:159]\n"
"v_mfma_f32_16x16x32_bf16 v[172:175], a[228:231], a[76:79], v[172:175]\n"
"v_mfma_f32_16x16x32_bf16 v[168:171], a[224:227], a[76:79], v[168:171]\n"
"buffer_load_dwordx4 v[248:251], v5, s[44:47], s70 offen offset:1024 sc1\n"
"v_mfma_f32_16x16x32_bf16 v[164:167], a[220:223], a[76:79], v[164:167]\n"
"v_mfma_f32_16x16x32_bf16 v[160:163], a[216:219], a[76:79], v[160:163]\n"
"v_mfma_f32_16x16x32_bf16 v[176:179], a[216:219], a[80:83], v[176:179]\n"
"buffer_load_dwordx4 v[252:255], v6, s[44:47], s70 offen sc1\n"
"v_mfma_f32_16x16x32_bf16 v[180:183], a[220:223], a[80:83], v[180:183]\n"
"v_mfma_f32_16x16x32_bf16 v[184:187], a[224:227], a[80:83], v[184:187]\n"
"v_mfma_f32_16x16x32_bf16 v[188:191], a[228:231], a[80:83], v[188:191]\n"
"v_mfma_f32_16x16x32_bf16 v[192:195], a[232:235], a[0:3], 0\n"
"s_waitcnt lgkmcnt(0)\n"
"s_add_u32 m0, s24, 0\n"
"s_cmp_le_u32 s65, 1\n"
"s_cselect_b32 s69, 2, 1\n"
"v_mfma_f32_16x16x32_bf16 v[192:195], v[224:227], a[48:51], v[192:195]\n"
"s_add_u32 s69, s69, s66\n"
"v_mfma_f32_16x16x32_bf16 v[196:199], v[228:231], a[48:51], 0\n"
"s_mov_b32 s30, 3145728\n"
"s_lshl_b32 s69, s69, 19\n"
"v_mfma_f32_16x16x32_bf16 v[196:199], a[236:239], a[0:3], v[196:199]\n"
"s_add_u32 s69, s69, s31\n"
"buffer_load_dword v44, v24, s[48:51], s30 offen\n"
"v_mfma_f32_16x16x32_bf16 v[200:203], a[240:243], a[0:3], 0\n"
"s_cmp_le_u32 s65, 2\n"
"s_cselect_b32 s71, 3, 2\n"
"buffer_load_dwordx4 v7, s[48:51], s69 offen lds\n"
"v_mfma_f32_16x16x32_bf16 v[200:203], v[232:235], a[48:51], v[200:203]\n"
"s_add_u32 s71, s71, s66\n"
"v_mfma_f32_16x16x32_bf16 v[204:207], v[236:239], a[48:51], 0\n"
"s_mul_i32 s71, s71, 73728\n"
"v_mfma_f32_16x16x32_bf16 v[204:207], a[244:247], a[0:3], v[204:207]\n"
"s_add_u32 s71, s71, s25\n"
"v_mfma_f32_16x16x32_bf16 v[220:223], a[244:247], a[4:7], 0\n"
"buffer_load_dwordx4 a[0:3], v2, s[44:47], s71 offen sc1\n"
"v_mfma_f32_16x16x32_bf16 v[220:223], v[236:239], a[52:55], v[220:223]\n"
"v_mfma_f32_16x16x32_bf16 v[216:219], v[232:235], a[52:55], 0\n"
"v_mfma_f32_16x16x32_bf16 v[216:219], a[240:243], a[4:7], v[216:219]\n"
"v_mfma_f32_16x16x32_bf16 v[212:215], a[236:239], a[4:7], 0\n"
"buffer_load_dwordx4 v8, s[48:51], s69 offen offset:1024 lds\n"
"v_mfma_f32_16x16x32_bf16 v[212:215], v[228:231], a[52:55], v[212:215]\n"
"s_waitcnt vmcnt(15)\n"
"ds_read_b64_tr_b16 a[36:37], v11 offset:4096\n"
"v_mfma_f32_16x16x32_bf16 v[208:211], v[224:227], a[52:55], 0\n"
"ds_read_b64_tr_b16 a[38:39], v11 offset:6144\n"
"v_mfma_f32_16x16x32_bf16 v[208:211], a[232:235], a[4:7], v[208:211]\n"
"ds_read_b64_tr_b16 a[40:41], v12 offset:4096\n"
"v_mfma_f32_16x16x32_bf16 a[104:107], a[232:235], a[8:11], 0\n"
"buffer_load_dwordx4 a[4:7], v2, s[44:47], s71 offen offset:1024 sc1\n"
"v_mfma_f32_16x16x32_bf16 a[104:107], v[224:227], a[56:59], a[104:107]\n"
"ds_read_b64_tr_b16 a[42:43], v12 offset:6144\n"
"v_mfma_f32_16x16x32_bf16 a[108:111], v[228:231], a[56:59], 0\n"
"ds_read_b64_tr_b16 a[44:45], v13 offset:4096\n"
"v_mfma_f32_16x16x32_bf16 a[108:111], a[236:239], a[8:11], a[108:111]\n"
"ds_read_b64_tr_b16 a[46:47], v13 offset:6144\n"
"v_mfma_f32_16x16x32_bf16 a[112:115], a[240:243], a[8:11], 0\n"
"buffer_load_dwordx4 v9, s[48:51], s69 offen offset:2048 lds\n"
"v_mfma_f32_16x16x32_bf16 a[112:115], v[232:235], a[56:59], a[112:115]\n"
"ds_read_b64_tr_b16 a[96:97], v14 offset:4096\n"
"v_mfma_f32_16x16x32_bf16 a[116:119], v[236:239], a[56:59], 0\n"
"ds_read_b64_tr_b16 a[98:99], v14 offset:6144\n"
"v_mfma_f32_16x16x32_bf16 a[116:119], a[244:247], a[8:11], a[116:119]\n"
"v_mfma_f32_16x16x32_bf16 a[132:135], a[244:247], a[12:15], 0\n"
"buffer_load_dwordx4 a[8:11], v3, s[44:47], s71 offen sc1\n"
"v_mfma_f32_16x16x32_bf16 a[132:135], v[236:239], a[60:63], a[132:135]\n"
"v_mfma_f32_16x16x32_bf16 a[128:131], v[232:235], a[60:63], 0\n"
"v_mfma_f32_16x16x32_bf16 a[128:131], a[240:243], a[12:15], a[128:131]\n"
"v_mfma_f32_16x16x32_bf16 a[124:127], a[236:239], a[12:15], 0\n"
"buffer_load_dwordx4 v10, s[48:51], s69 offen offset:3072 lds\n"
"v_mfma_f32_16x16x32_bf16 a[124:127], v[228:231], a[60:63], a[124:127]\n"
"v_mfma_f32_16x16x32_bf16 a[120:123], v[224:227], a[60:63], 0\n"
"v_mfma_f32_16x16x32_bf16 a[120:123], a[232:235], a[12:15], a[120:123]\n"
"v_mfma_f32_16x16x32_bf16 a[136:139], a[232:235], a[16:19], 0\n"
"buffer_load_dwordx4 a[12:15], v3, s[44:47], s71 offen offset:1024 sc1\n"
"v_mfma_f32_16x16x32_bf16 a[136:139], v[224:227], a[64:67], a[136:139]\n"
"v_mfma_f32_16x16x32_bf16 a[140:143], v[228:231], a[64:67], 0\n"
"v_mfma_f32_16x16x32_bf16 a[140:143], a[236:239], a[16:19], a[140:143]\n"
"v_mfma_f32_16x16x32_bf16 a[144:147], a[240:243], a[16:19], 0\n"
"v_mfma_f32_16x16x32_bf16 a[144:147], v[232:235], a[64:67], a[144:147]\n"
"s_barrier\n"
"v_mfma_f32_16x16x32_bf16 a[148:151], v[236:239], a[64:67], 0\n"
"s_waitcnt lgkmcnt(0)\n"
"v_mfma_f32_16x16x32_bf16 a[148:151], a[244:247], a[16:19], a[148:151]\n"
"s_add_u32 m0, s24, 4096\n"
"v_mfma_f32_16x16x32_bf16 a[164:167], a[244:247], a[20:23], 0\n"
"s_cmp_le_u32 s65, 2\n"
"s_cselect_b32 s69, 3, 2\n"
"buffer_load_dwordx4 a[16:19], v4, s[44:47], s71 offen sc1\n"
"v_mfma_f32_16x16x32_bf16 a[164:167], v[236:239], a[68:71], a[164:167]\n"
"s_add_u32 s69, s69, s66\n"
"v_mfma_f32_16x16x32_bf16 a[160:163], v[232:235], a[68:71], 0\n"
"s_lshl_b32 s69, s69, 19\n"
"v_mfma_f32_16x16x32_bf16 a[160:163], a[240:243], a[20:23], a[160:163]\n"
"s_add_u32 s69, s69, s31\n"
"v_mfma_f32_16x16x32_bf16 a[156:159], a[236:239], a[20:23], 0\n"
"buffer_load_dwordx4 v7, s[48:51], s69 offen lds\n"
"v_mfma_f32_16x16x32_bf16 a[156:159], v[228:231], a[68:71], a[156:159]\n"
"v_mfma_f32_16x16x32_bf16 a[152:155], v[224:227], a[68:71], 0\n"
"v_mfma_f32_16x16x32_bf16 a[152:155], a[232:235], a[20:23], a[152:155]\n"
"v_mfma_f32_16x16x32_bf16 a[168:171], a[232:235], a[24:27], 0\n"
"buffer_load_dwordx4 a[20:23], v4, s[44:47], s71 offen offset:1024 sc1\n"
"v_mfma_f32_16x16x32_bf16 a[168:171], v[224:227], a[72:75], a[168:171]\n"
"s_waitcnt vmcnt(17)\n"
"ds_read_b64_tr_b16 a[216:217], v11 offset:8192\n"
"v_mfma_f32_16x16x32_bf16 a[172:175], v[228:231], a[72:75], 0\n"
"ds_read_b64_tr_b16 a[218:219], v11 offset:10240\n"
"v_mfma_f32_16x16x32_bf16 a[172:175], a[236:239], a[24:27], a[172:175]\n"
"ds_read_b64_tr_b16 a[220:221], v12 offset:8192\n"
"v_mfma_f32_16x16x32_bf16 a[176:179], a[240:243], a[24:27], 0\n"
"buffer_load_dwordx4 v8, s[48:51], s69 offen offset:1024 lds\n"
"v_mfma_f32_16x16x32_bf16 a[176:179], v[232:235], a[72:75], a[176:179]\n"
"ds_read_b64_tr_b16 a[222:223], v12 offset:10240\n"
"v_mfma_f32_16x16x32_bf16 a[180:183], v[236:239], a[72:75], 0\n"
"ds_read_b64_tr_b16 a[224:225], v13 offset:8192\n"
"v_mfma_f32_16x16x32_bf16 a[180:183], a[244:247], a[24:27], a[180:183]\n"
"ds_read_b64_tr_b16 a[226:227], v13 offset:10240\n"
"v_mfma_f32_16x16x32_bf16 a[196:199], a[244:247], a[28:31], 0\n"
"buffer_load_dwordx4 a[24:27], v5, s[44:47], s71 offen sc1\n"
"v_mfma_f32_16x16x32_bf16 a[196:199], v[236:239], a[76:79], a[196:199]\n"
"ds_read_b64_tr_b16 a[228:229], v14 offset:8192\n"
"v_mfma_f32_16x16x32_bf16 a[192:195], v[232:235], a[76:79], 0\n"
"ds_read_b64_tr_b16 a[230:231], v14 offset:10240\n"
"v_mfma_f32_16x16x32_bf16 a[192:195], a[240:243], a[28:31], a[192:195]\n"
"v_mfma_f32_16x16x32_bf16 a[188:191], a[236:239], a[28:31], 0\n"
"buffer_load_dwordx4 v9, s[48:51], s69 offen offset:2048 lds\n"
"v_mfma_f32_16x16x32_bf16 a[188:191], v[228:231], a[76:79], a[188:191]\n"
"v_mfma_f32_16x16x32_bf16 a[184:187], v[224:227], a[76:79], 0\n"
"v_mfma_f32_16x16x32_bf16 a[184:187], a[232:235], a[28:31], a[184:187]\n"
"v_mfma_f32_16x16x32_bf16 a[200:203], a[232:235], a[32:35], 0\n"
"buffer_load_dwordx4 a[28:31], v5, s[44:47], s71 offen offset:1024 sc1\n"
"v_mfma_f32_16x16x32_bf16 a[200:203], v[224:227], a[80:83], a[200:203]\n"
"v_mfma_f32_16x16x32_bf16 a[204:207], v[228:231], a[80:83], 0\n"
"v_mfma_f32_16x16x32_bf16 a[204:207], a[236:239], a[32:35], a[204:207]\n"
"v_mfma_f32_16x16x32_bf16 a[208:211], a[240:243], a[32:35], 0\n"
"buffer_load_dwordx4 v10, s[48:51], s69 offen offset:3072 lds\n"
"v_mfma_f32_16x16x32_bf16 a[208:211], v[232:235], a[80:83], a[208:211]\n"
"v_mfma_f32_16x16x32_bf16 a[212:215], v[236:239], a[80:83], 0\n"
"v_mfma_f32_16x16x32_bf16 a[212:215], a[244:247], a[32:35], a[212:215]\n"
"s_waitcnt vmcnt(16)\n"
"s_waitcnt lgkmcnt(0)\n"
"L_join_%=:\n"
"v_mfma_f32_16x16x32_bf16 v[48:51], a[36:39], a[84:87], v[48:51]\n"
"s_waitcnt vmcnt(14)\n"
"buffer_load_dwordx4 a[32:35], v6, s[44:47], s71 offen sc1\n"
"v_mfma_f32_16x16x32_bf16 v[48:51], a[216:219], a[0:3], v[48:51]\n"
"v_mfma_f32_16x16x32_bf16 v[52:55], a[220:223], a[0:3], v[52:55]\n"
"s_cmp_le_u32 s65, 3\n"
"s_cselect_b32 s70, 4, 3\n"
"v_mfma_f32_16x16x32_bf16 v[52:55], a[40:43], a[84:87], v[52:55]\n"
"s_add_u32 s70, s70, s66\n"
"v_mfma_f32_16x16x32_bf16 v[56:59], a[44:47], a[84:87], v[56:59]\n"
"s_mul_i32 s70, s70, 73728\n"
"v_mfma_f32_16x16x32_bf16 v[56:59], a[224:227], a[0:3], v[56:59]\n"
"s_add_u32 s70, s70, s25\n"
"s_add_u32 m0, s24, 8192\n"
"v_mfma_f32_16x16x32_bf16 v[60:63], a[228:231], a[0:3], v[60:63]\n"
"s_cmp_le_u32 s65, 3\n"
"s_cselect_b32 s69, 4, 3\n"
"buffer_load_dwordx4 a[48:51], v2, s[44:47], s70 offen sc1\n"
"v_mfma_f32_16x16x32_bf16 v[60:63], a[96:99], a[84:87], v[60:63]\n"
"s_add_u32 s69, s69, s66\n"
"v_mfma_f32_16x16x32_bf16 v[76:79], a[96:99], a[88:91], v[76:79]\n"
"s_waitcnt vmcnt(14)\n"
"s_lshl_b32 s69, s69, 19\n"
"v_mfma_f32_16x16x32_bf16 v[76:79], a[228:231], a[4:7], v[76:79]\n"
"s_add_u32 s69, s69, s26\n"
"v_mfma_f32_16x16x32_bf16 v[72:75], a[224:227], a[4:7], v[72:75]\n"
"buffer_load_dwordx4 v7, s[48:51], s69 offen lds\n"
"v_mfma_f32_16x16x32_bf16 v[72:75], a[44:47], a[88:91], v[72:75]\n"
"v_mfma_f32_16x16x32_bf16 v[68:71], a[40:43], a[88:91], v[68:71]\n"
"s_waitcnt vmcnt(12)\n"
"ds_read_b64_tr_b16 a[232:233], v11\n"
"v_mfma_f32_16x16x32_bf16 v[68:71], a[220:223], a[4:7], v[68:71]\n"
"ds_read_b64_tr_b16 a[234:235], v11 offset:2048\n"
"v_mfma_f32_16x16x32_bf16 v[64:67], a[216:219], a[4:7], v[64:67]\n"
"buffer_load_dwordx4 a[52:55], v2, s[44:47], s70 offen offset:1024 sc1\n"
"v_mfma_f32_16x16x32_bf16 v[64:67], a[36:39], a[88:91], v[64:67]\n"
"ds_read_b64_tr_b16 a[236:237], v12\n"
"v_mfma_f32_16x16x32_bf16 v[80:83], a[36:39], a[92:95], v[80:83]\n"
"ds_read_b64_tr_b16 a[238:239], v12 offset:2048\n"
"v_mfma_f32_16x16x32_bf16 v[80:83], a[216:219], a[8:11], v[80:83]\n"
"ds_read_b64_tr_b16 a[240:241], v13\n"
"v_mfma_f32_16x16x32_bf16 v[84:87], a[220:223], a[8:11], v[84:87]\n"
"buffer_load_dwordx4 v8, s[48:51], s69 offen offset:1024 lds\n"
"v_mfma_f32_16x16x32_bf16 v[84:87], a[40:43], a[92:95], v[84:87]\n"
"ds_read_b64_tr_b16 a[242:243], v13 offset:2048\n"
"v_mfma_f32_16x16x32_bf16 v[88:91], a[44:47], a[92:95], v[88:91]\n"
"ds_read_b64_tr_b16 a[244:245], v14\n"
"v_mfma_f32_16x16x32_bf16 v[88:91], a[224:227], a[8:11], v[88:91]\n"
"ds_read_b64_tr_b16 a[246:247], v14 offset:2048\n"
"v_mfma_f32_16x16x32_bf16 v[92:95], a[228:231], a[8:11], v[92:95]\n"
"buffer_load_dwordx4 a[56:59], v3, s[44:47], s70 offen sc1\n"
"v_mfma_f32_16x16x32_bf16 v[92:95], a[96:99], a[92:95], v[92:95]\n"
"v_mfma_f32_16x16x32_bf16 v[108:111], a[96:99], a[100:103], v[108:111]\n"
"s_waitcnt vmcnt(14)\n"
"v_mfma_f32_16x16x32_bf16 v[108:111], a[228:231], a[12:15], v[108:111]\n"
"v_mfma_f32_16x16x32_bf16 v[104:107], a[224:227], a[12:15], v[104:107]\n"
"buffer_load_dwordx4 v9, s[48:51], s69 offen offset:2048 lds\n"
"v_mfma_f32_16x16x32_bf16 v[104:107], a[44:47], a[100:103], v[104:107]\n"
"v_mfma_f32_16x16x32_bf16 v[100:103], a[40:43], a[100:103], v[100:103]\n"
"v_mfma_f32_16x16x32_bf16 v[100:103], a[220:223], a[12:15], v[100:103]\n"
"v_mfma_f32_16x16x32_bf16 v[96:99], a[216:219], a[12:15], v[96:99]\n"
"buffer_load_dwordx4 a[60:63], v3, s[44:47], s70 offen offset:1024 sc1\n"
"v_mfma_f32_16x16x32_bf16 v[96:99], a[36:39], a[100:103], v[96:99]\n"
"v_mfma_f32_16x16x32_bf16 v[112:115], a[36:39], a[248:251], v[112:115]\n"
"s_waitcnt vmcnt(15)\n"
"v_mfma_f32_16x16x32_bf16 v[112:115], a[216:219], a[16:19], v[112:115]\n"
"v_mfma_f32_16x16x32_bf16 v[116:119], a[220:223], a[16:19], v[116:119]\n"
"buffer_load_dwordx4 v10, s[48:51], s69 offen offset:3072 lds\n"
"v_mfma_f32_16x16x32_bf16 v[116:119], a[40:43], a[248:251], v[116:119]\n"
"v_mfma_f32_16x16x32_bf16 v[120:123], a[44:47], a[248:251], v[120:123]\n"
"s_waitcnt lgkmcnt(0)\n"
"v_mfma_f32_16x16x32_bf16 v[120:123], a[224:227], a[16:19], v[120:123]\n"
"s_add_u32 m0, s24, 0\n"
"v_mfma_f32_16x16x32_bf16 v[124:127], a[228:231], a[16:19], v[124:127]\n"
"s_cmp_le_u32 s65, 4\n"
"s_cselect_b32 s69, 5, 4\n"
"buffer_load_dwordx4 a[64:67], v4, s[44:47], s70 offen sc1\n"
"v_mfma_f32_16x16x32_bf16 v[124:127], a[96:99], a[248:251], v[124:127]\n"
"s_add_u32 s69, s69, s66\n"
"v_mfma_f32_16x16x32_bf16 v[140:143], a[96:99], a[252:255], v[140:143]\n"
"s_waitcnt vmcnt(15)\n"
"s_lshl_b32 s69, s69, 19\n"
"v_mfma_f32_16x16x32_bf16 v[140:143], a[228:231], a[20:23], v[140:143]\n"
"s_add_u32 s69, s69, s26\n"
"v_mfma_f32_16x16x32_bf16 v[136:139], a[224:227], a[20:23], v[136:139]\n"
"buffer_load_dwordx4 v7, s[48:51], s69 offen lds\n"
"v_mfma_f32_16x16x32_bf16 v[136:139], a[44:47], a[252:255], v[136:139]\n"
"v_mfma_f32_16x16x32_bf16 v[132:135], a[40:43], a[252:255], v[132:135]\n"
"v_mfma_f32_16x16x32_bf16 v[132:135], a[220:223], a[20:23], v[132:135]\n"
"v_mfma_f32_16x16x32_bf16 v[128:131], a[216:219], a[20:23], v[128:131]\n"
"buffer_load_dwordx4 a[68:71], v4, s[44:47], s70 offen offset:1024 sc1\n"
"v_mfma_f32_16x16x32_bf16 v[128:131], a[36:39], a[252:255], v[128:131]\n"
"v_mfma_f32_16x16x32_bf16 v[144:147], a[36:39], v[244:247], v[144:147]\n"
"s_waitcnt vmcnt(12)\n"
"ds_read_b64_tr_b16 v[224:225], v11 offset:4096\n"
"v_mfma_f32_16x16x32_bf16 v[144:147], a[216:219], a[24:27], v[144:147]\n"
"ds_read_b64_tr_b16 v[226:227], v11 offset:6144\n"
"v_mfma_f32_16x16x32_bf16 v[148:151], a[220:223], a[24:27], v[148:151]\n"
"buffer_load_dwordx4 v8, s[48:51], s69 offen offset:1024 lds\n"
"v_mfma_f32_16x16x32_bf16 v[148:151], a[40:43], v[244:247], v[148:151]\n"
"ds_read_b64_tr_b16 v[228:229], v12 offset:4096\n"
"v_mfma_f32_16x16x32_bf16 v[152:155], a[44:47], v[244:247], v[152:155]\n"
"ds_read_b64_tr_b16 v[230:231], v12 offset:6144\n"
"v_mfma_f32_16x16x32_bf16 v[152:155], a[224:227], a[24:27], v[152:155]\n"
"ds_read_b64_tr_b16 v[232:233], v13 offset:4096\n"
"v_mfma_f32_16x16x32_bf16 v[156:159], a[228:231], a[24:27], v[156:159]\n"
"buffer_load_dwordx4 a[72:75], v5, s[44:47], s70 offen sc1\n"
"v_mfma_f32_16x16x32_bf16 v[156:159], a[96:99], v[244:247], v[156:159]\n"
"ds_read_b64_tr_b16 v[234:235], v13 offset:6144\n"
"v_mfma_f32_16x16x32_bf16 v[172:175], a[96:99], v[248:251], v[172:175]\n"
"ds_read_b64_tr_b16 v[236:237], v14 offset:4096\n"
"v_mfma_f32_16x16x32_bf16 v[172:175], a[228:231], a[28:31], v[172:175]\n"
"ds_read_b64_tr_b16 v[238:239], v14 offset:6144\n"
"v_mfma_f32_16x16x32_bf16 v[168:171], a[224:227], a[28:31], v[168:171]\n"
"buffer_load_dwordx4 v9, s[48:51], s69 offen offset:2048 lds\n"
"v_mfma_f32_16x16x32_bf16 v[168:171], a[44:47], v[248:251], v[168:171]\n"
"v_mfma_f32_16x16x32_bf16 v[164:167], a[40:43], v[248:251], v[164:167]\n"
"v_mfma_f32_16x16x32_bf16 v[164:167], a[220:223], a[28:31], v[164:167]\n"
"v_mfma_f32_16x16x32_bf16 v[160:163], a[216:219], a[28:31], v[160:163]\n"
"buffer_load_dwordx4 a[76:79], v5, s[44:47], s70 offen offset:1024 sc1\n"
"v_mfma_f32_16x16x32_bf16 v[160:163], a[36:39], v[248:251], v[160:163]\n"
"v_mfma_f32_16x16x32_bf16 v[176:179], a[36:39], v[252:255], v[176:179]\n"
"s_waitcnt vmcnt(15)\n"
"v_mfma_f32_16x16x32_bf16 v[176:179], a[216:219], a[32:35], v[176:179]\n"
"v_mfma_f32_16x16x32_bf16 v[180:183], a[220:223], a[32:35], v[180:183]\n"
"buffer_load_dwordx4 v10, s[48:51], s69 offen offset:3072 lds\n"
"v_mfma_f32_16x16x32_bf16 v[180:183], a[40:43], v[252:255], v[180:183]\n"
"v_mfma_f32_16x16x32_bf16 v[184:187], a[44:47], v[252:255], v[184:187]\n"
"v_mfma_f32_16x16x32_bf16 v[184:187], a[224:227], a[32:35], v[184:187]\n"
"v_mfma_f32_16x16x32_bf16 v[188:191], a[228:231], a[32:35], v[188:191]\n"
"buffer_load_dwordx4 a[80:83], v6, s[44:47], s70 offen sc1\n"
"v_mfma_f32_16x16x32_bf16 v[188:191], a[96:99], v[252:255], v[188:191]\n"
"v_mfma_f32_16x16x32_bf16 v[192:195], a[232:235], a[84:87], v[192:195]\n"
"s_waitcnt lgkmcnt(0)\n"
"s_add_u32 m0, s24, 4096\n"
"s_cmp_le_u32 s65, 3\n"
"s_cselect_b32 s69, 4, 3\n"
"v_mfma_f32_16x16x32_bf16 v[192:195], v[224:227], a[0:3], v[192:195]\n"
"s_add_u32 s69, s69, s66\n"
"v_mfma_f32_16x16x32_bf16 v[196:199], v[228:231], a[0:3], v[196:199]\n"
"s_mov_b32 s30, 262144\n"
"s_lshl_b32 s69, s69, 19\n"
"v_mfma_f32_16x16x32_bf16 v[196:199], a[236:239], a[84:87], v[196:199]\n"
"s_add_u32 s69, s69, s31\n"
"buffer_load_dword v44, v24, s[48:51], s30 offen\n"
"v_mfma_f32_16x16x32_bf16 v[200:203], a[240:243], a[84:87], v[200:203]\n"
"s_cmp_le_u32 s65, 4\n"
"s_cselect_b32 s71, 5, 4\n"
"buffer_load_dwordx4 v7, s[48:51], s69 offen lds\n"
"v_mfma_f32_16x16x32_bf16 v[200:203], v[232:235], a[0:3], v[200:203]\n"
"s_add_u32 s71, s71, s66\n"
"v_mfma_f32_16x16x32_bf16 v[204:207], v[236:239], a[0:3], v[204:207]\n"
"s_mul_i32 s71, s71, 73728\n"
"v_mfma_f32_16x16x32_bf16 v[204:207], a[244:247], a[84:87], v[204:207]\n"
"s_add_u32 s71, s71, s25\n"
"v_mfma_f32_16x16x32_bf16 v[220:223], a[244:247], a[88:91], v[220:223]\n"
"buffer_load_dwordx4 a[84:87], v2, s[44:47], s71 offen sc1\n"
"v_mfma_f32_16x16x32_bf16 v[220:223], v[236:239], a[4:7], v[220:223]\n"
"v_mfma_f32_16x16x32_bf16 v[216:219], v[232:235], a[4:7], v[216:219]\n"
"v_mfma_f32_16x16x32_bf16 v[216:219], a[240:243], a[88:91], v[216:219]\n"
"v_mfma_f32_16x16x32_bf16 v[212:215], a[236:239], a[88:91], v[212:215]\n"
"buffer_load_dwordx4 v8, s[48:51], s69 offen offset:1024 lds\n"
"v_mfma_f32_16x16x32_bf16 v[212:215], v[228:231], a[4:7], v[212:215]\n"
"s_waitcnt vmcnt(13)\n"
"ds_read_b64_tr_b16 a[36:37], v11 offset:8192\n"
"v_mfma_f32_16x16x32_bf16 v[208:211], v[224:227], a[4:7], v[208:211]\n"
"ds_read_b64_tr_b16 a[38:39], v11 offset:10240\n"
"v_mfma_f32_16x16x32_bf16 v[208:211], a[232:235], a[88:91], v[208:211]\n"
"ds_read_b64_tr_b16 a[40:41], v12 offset:8192\n"
"v_mfma_f32_16x16x32_bf16 a[104:107], a[232:235], a[92:95], a[104:107]\n"
"buffer_load_dwordx4 a[88:91], v2, s[44:47], s71 offen offset:1024 sc1\n"
"v_mfma_f32_16x16x32_bf16 a[104:107], v[224:227], a[8:11], a[104:107]\n"
"ds_read_b64_tr_b16 a[42:43], v12 offset:10240\n"
"v_mfma_f32_16x16x32_bf16 a[108:111], v[228:231], a[8:11], a[108:111]\n"
"ds_read_b64_tr_b16 a[44:45], v13 offset:8192\n"
"v_mfma_f32_16x16x32_bf16 a[108:111], a[236:239], a[92:95], a[108:111]\n"
"ds_read_b64_tr_b16 a[46:47], v13 offset:10240\n"
"v_mfma_f32_16x16x32_bf16 a[112:115], a[240:243], a[92:95], a[112:115]\n"
"buffer_load_dwordx4 v9, s[48:51], s69 offen offset:2048 lds\n"
"v_mfma_f32_16x16x32_bf16 a[112:115], v[232:235], a[8:11], a[112:115]\n"
"ds_read_b64_tr_b16 a[96:97], v14 offset:8192\n"
"v_mfma_f32_16x16x32_bf16 a[116:119], v[236:239], a[8:11], a[116:119]\n"
"ds_read_b64_tr_b16 a[98:99], v14 offset:10240\n"
"v_mfma_f32_16x16x32_bf16 a[116:119], a[244:247], a[92:95], a[116:119]\n"
"v_mfma_f32_16x16x32_bf16 a[132:135], a[244:247], a[100:103], a[132:135]\n"
"buffer_load_dwordx4 a[92:95], v3, s[44:47], s71 offen sc1\n"
"v_mfma_f32_16x16x32_bf16 a[132:135], v[236:239], a[12:15], a[132:135]\n"
"v_mfma_f32_16x16x32_bf16 a[128:131], v[232:235], a[12:15], a[128:131]\n"
"v_mfma_f32_16x16x32_bf16 a[128:131], a[240:243], a[100:103], a[128:131]\n"
"v_mfma_f32_16x16x32_bf16 a[124:127], a[236:239], a[100:103], a[124:127]\n"
"buffer_load_dwordx4 v10, s[48:51], s69 offen offset:3072 lds\n"
"v_mfma_f32_16x16x32_bf16 a[124:127], v[228:231], a[12:15], a[124:127]\n"
"v_mfma_f32_16x16x32_bf16 a[120:123], v[224:227], a[12:15], a[120:123]\n"
"v_mfma_f32_16x16x32_bf16 a[120:123], a[232:235], a[100:103], a[120:123]\n"
"v_mfma_f32_16x16x32_bf16 a[136:139], a[232:235], a[248:251], a[136:139]\n"
"buffer_load_dwordx4 a[100:103], v3, s[44:47], s71 offen offset:1024 sc1\n"
"v_mfma_f32_16x16x32_bf16 a[136:139], v[224:227], a[16:19], a[136:139]\n"
"v_mfma_f32_16x16x32_bf16 a[140:143], v[228:231], a[16:19], a[140:143]\n"
"v_mfma_f32_16x16x32_bf16 a[140:143], a[236:239], a[248:251], a[140:143]\n"
"v_mfma_f32_16x16x32_bf16 a[144:147], a[240:243], a[248:251], a[144:147]\n"
"v_mfma_f32_16x16x32_bf16 a[144:147], v[232:235], a[16:19], a[144:147]\n"
"v_mfma_f32_16x16x32_bf16 a[148:151], v[236:239], a[16:19], a[148:151]\n"
"s_waitcnt lgkmcnt(0)\n"
"v_mfma_f32_16x16x32_bf16 a[148:151], a[244:247], a[248:251], a[148:151]\n"
"s_add_u32 m0, s24, 8192\n"
"v_mfma_f32_16x16x32_bf16 a[164:167], a[244:247], a[252:255], a[164:167]\n"
"s_cmp_le_u32 s65, 4\n"
"s_cselect_b32 s69, 5, 4\n"
"buffer_load_dwordx4 a[248:251], v4, s[44:47], s71 offen sc1\n"
"v_mfma_f32_16x16x32_bf16 a[164:167], v[236:239], a[20:23], a[164:167]\n"
"s_add_u32 s69, s69, s66\n"
"v_mfma_f32_16x16x32_bf16 a[160:163], v[232:235], a[20:23], a[160:163]\n"
"s_lshl_b32 s69, s69, 19\n"
"v_mfma_f32_16x16x32_bf16 a[160:163], a[240:243], a[252:255], a[160:163]\n"
"s_add_u32 s69, s69, s31\n"
"v_mfma_f32_16x16x32_bf16 a[156:159], a[236:239], a[252:255], a[156:159]\n"
"buffer_load_dwordx4 v7, s[48:51], s69 offen lds\n"
"v_mfma_f32_16x16x32_bf16 a[156:159], v[228:231], a[20:23], a[156:159]\n"
"v_mfma_f32_16x16x32_bf16 a[152:155], v[224:227], a[20:23], a[152:155]\n"
"v_mfma_f32_16x16x32_bf16 a[152:155], a[232:235], a[252:255], a[152:155]\n"
"v_mfma_f32_16x16x32_bf16 a[168:171], a[232:235], v[244:247], a[168:171]\n"
"buffer_load_dwordx4 a[252:255], v4, s[44:47], s71 offen offset:1024 sc1\n"
"v_mfma_f32_16x16x32_bf16 a[168:171], v[224:227], a[24:27], a[168:171]\n"
"s_waitcnt vmcnt(13)\n"
"ds_read_b64_tr_b16 a[216:217], v11\n"
"v_mfma_f32_16x16x32_bf16 a[172:175], v[228:231], a[24:27], a[172:175]\n"
"ds_read_b64_tr_b16 a[218:219], v11 offset:2048\n"
"v_mfma_f32_16x16x32_bf16 a[172:175], a[236:239], v[244:247], a[172:175]\n"
"ds_read_b64_tr_b16 a[220:221], v12\n"
"v_mfma_f32_16x16x32_bf16 a[176:179], a[240:243], v[244:247], a[176:179]\n"
"buffer_load_dwordx4 v8, s[48:51], s69 offen offset:1024 lds\n"
"v_mfma_f32_16x16x32_bf16 a[176:179], v[232:235], a[24:27], a[176:179]\n"
"ds_read_b64_tr_b16 a[222:223], v12 offset:2048\n"
"v_mfma_f32_16x16x32_bf16 a[180:183], v[236:239], a[24:27], a[180:183]\n"
"ds_read_b64_tr_b16 a[224:225], v13\n"
"v_mfma_f32_16x16x32_bf16 a[180:183], a[244:247], v[244:247], a[180:183]\n"
"ds_read_b64_tr_b16 a[226:227], v13 offset:2048\n"
"v_mfma_f32_16x16x32_bf16 a[196:199], a[244:247], v[248:251], a[196:199]\n"
"buffer_load_dwordx4 v[244:247], v5, s[44:47], s71 offen sc1\n"
"v_mfma_f32_16x16x32_bf16 a[196:199], v[236:239], a[28:31], a[196:199]\n"
"ds_read_b64_tr_b16 a[228:229], v14\n"
"v_mfma_f32_16x16x32_bf16 a[192:195], v[232:235], a[28:31], a[192:195]\n"
"ds_read_b64_tr_b16 a[230:231], v14 offset:2048\n"
"v_mfma_f32_16x16x32_bf16 a[192:195], a[240:243], v[248:251], a[192:195]\n"
"v_mfma_f32_16x16x32_bf16 a[188:191], a[236:239], v[248:251], a[188:191]\n"
"buffer_load_dwordx4 v9, s[48:51], s69 offen offset:2048 lds\n"
"v_mfma_f32_16x16x32_bf16 a[188:191], v[228:231], a[28:31], a[188:191]\n"
"v_mfma_f32_16x16x32_bf16 a[184:187], v[224:227], a[28:31], a[184:187]\n"
"v_mfma_f32_16x16x32_bf16 a[184:187], a[232:235], v[248:251], a[184:187]\n"
"v_mfma_f32_16x16x32_bf16 a[200:203], a[232:235], v[252:255], a[200:203]\n"
"buffer_load_dwordx4 v[248:251], v5, s[44:47], s71 offen offset:1024 sc1\n"
"v_mfma_f32_16x16x32_bf16 a[200:203], v[224:227], a[32:35], a[200:203]\n"
"v_mfma_f32_16x16x32_bf16 a[204:207], v[228:231], a[32:35], a[204:207]\n"
"v_mfma_f32_16x16x32_bf16 a[204:207], a[236:239], v[252:255], a[204:207]\n"
"v_mfma_f32_16x16x32_bf16 a[208:211], a[240:243], v[252:255], a[208:211]\n"
"buffer_load_dwordx4 v10, s[48:51], s69 offen offset:3072 lds\n"
"v_mfma_f32_16x16x32_bf16 a[208:211], v[232:235], a[32:35], a[208:211]\n"
"v_mfma_f32_16x16x32_bf16 a[212:215], v[236:239], a[32:35], a[212:215]\n"
"v_mfma_f32_16x16x32_bf16 a[212:215], a[244:247], v[252:255], a[212:215]\n"
"v_mfma_f32_16x16x32_bf16 v[48:51], a[36:39], a[48:51], v[48:51]\n"
"s_waitcnt vmcnt(14) lgkmcnt(3)\n"
"buffer_load_dwordx4 v[252:255], v6, s[44:47], s71 offen sc1\n"
"v_mfma_f32_16x16x32_bf16 v[48:51], a[216:219], a[84:87], v[48:51]\n"
"v_mfma_f32_16x16x32_bf16 v[52:55], a[220:223], a[84:87], v[52:55]\n"
"s_cmp_le_u32 s65, 5\n"
"s_cselect_b32 s70, 6, 5\n"
"v_mfma_f32_16x16x32_bf16 v[52:55], a[40:43], a[48:51], v[52:55]\n"
"s_add_u32 s70, s70, s66\n"
"v_mfma_f32_16x16x32_bf16 v[56:59], a[44:47], a[48:51], v[56:59]\n"
"s_mul_i32 s70, s70, 73728\n"
"s_waitcnt lgkmcnt(0)\n"
"v_mfma_f32_16x16x32_bf16 v[56:59], a[224:227], a[84:87], v[56:59]\n"
"s_add_u32 s70, s70, s25\n"
"s_add_u32 m0, s24, 0\n"
"v_mfma_f32_16x16x32_bf16 v[60:63], a[228:231], a[84:87], v[60:63]\n"
"s_cmp_le_u32 s65, 5\n"
"s_cselect_b32 s69, 6, 5\n"
"buffer_load_dwordx4 a[0:3], v2, s[44:47], s70 offen sc1\n"
"v_mfma_f32_16x16x32_bf16 v[60:63], a[96:99], a[48:51], v[60:63]\n"
"s_add_u32 s69, s69, s66\n"
"v_mfma_f32_16x16x32_bf16 v[76:79], a[96:99], a[52:55], v[76:79]\n"
"s_waitcnt vmcnt(14)\n"
"s_lshl_b32 s69, s69, 19\n"
"v_mfma_f32_16x16x32_bf16 v[76:79], a[228:231], a[88:91], v[76:79]\n"
"s_add_u32 s69, s69, s26\n"
"v_mfma_f32_16x16x32_bf16 v[72:75], a[224:227], a[88:91], v[72:75]\n"
"buffer_load_dwordx4 v7, s[48:51], s69 offen lds\n"
"v_mfma_f32_16x16x32_bf16 v[72:75], a[44:47], a[52:55], v[72:75]\n"
"v_mfma_f32_16x16x32_bf16 v[68:71], a[40:43], a[52:55], v[68:71]\n"
"s_waitcnt vmcnt(12)\n"
"ds_read_b64_tr_b16 a[232:233], v11 offset:4096\n"
"v_mfma_f32_16x16x32_bf16 v[68:71], a[220:223], a[88:91], v[68:71]\n"
"ds_read_b64_tr_b16 a[234:235], v11 offset:6144\n"
"v_mfma_f32_16x16x32_bf16 v[64:67], a[216:219], a[88:91], v[64:67]\n"
"buffer_load_dwordx4 a[4:7], v2, s[44:47], s70 offen offset:1024 sc1\n"
"v_mfma_f32_16x16x32_bf16 v[64:67], a[36:39], a[52:55], v[64:67]\n"
"ds_read_b64_tr_b16 a[236:237], v12 offset:4096\n"
"v_mfma_f32_16x16x32_bf16 v[80:83], a[36:39], a[56:59], v[80:83]\n"
"ds_read_b64_tr_b16 a[238:239], v12 offset:6144\n"
"v_mfma_f32_16x16x32_bf16 v[80:83], a[216:219], a[92:95], v[80:83]\n"
"ds_read_b64_tr_b16 a[240:241], v13 offset:4096\n"
"v_mfma_f32_16x16x32_bf16 v[84:87], a[220:223], a[92:95], v[84:87]\n"
"buffer_load_dwordx4 v8, s[48:51], s69 offen offset:1024 lds\n"
"v_mfma_f32_16x16x32_bf16 v[84:87], a[40:43], a[56:59], v[84:87]\n"
"ds_read_b64_tr_b16 a[242:243], v13 offset:6144\n"
"v_mfma_f32_16x16x32_bf16 v[88:91], a[44:47], a[56:59], v[88:91]\n"
"ds_read_b64_tr_b16 a[244:245], v14 offset:4096\n"
"v_mfma_f32_16x16x32_bf16 v[88:91], a[224:227], a[92:95], v[88:91]\n"
"ds_read_b64_tr_b16 a[246:247], v14 offset:6144\n"
"v_mfma_f32_16x16x32_bf16 v[92:95], a[228:231], a[92:95], v[92:95]\n"
"buffer_load_dwordx4 a[8:11], v3, s[44:47], s70 offen sc1\n"
"v_mfma_f32_16x16x32_bf16 v[92:95], a[96:99], a[56:59], v[92:95]\n"
"v_mfma_f32_16x16x32_bf16 v[108:111], a[96:99], a[60:63], v[108:111]\n"
"s_waitcnt vmcnt(14)\n"
"v_mfma_f32_16x16x32_bf16 v[108:111], a[228:231], a[100:103], v[108:111]\n"
"v_mfma_f32_16x16x32_bf16 v[104:107], a[224:227], a[100:103], v[104:107]\n"
"buffer_load_dwordx4 v9, s[48:51], s69 offen offset:2048 lds\n"
"v_mfma_f32_16x16x32_bf16 v[104:107], a[44:47], a[60:63], v[104:107]\n"
"v_mfma_f32_16x16x32_bf16 v[100:103], a[40:43], a[60:63], v[100:103]\n"
"v_mfma_f32_16x16x32_bf16 v[100:103], a[220:223], a[100:103], v[100:103]\n"
"v_mfma_f32_16x16x32_bf16 v[96:99], a[216:219], a[100:103], v[96:99]\n"
"buffer_load_dwordx4 a[12:15], v3, s[44:47], s70 offen offset:1024 sc1\n"
"v_mfma_f32_16x16x32_bf16 v[96:99], a[36:39], a[60:63], v[96:99]\n"
"v_mfma_f32_16x16x32_bf16 v[112:115], a[36:39], a[64:67], v[112:115]\n"
"s_waitcnt vmcnt(15)\n"
"v_mfma_f32_16x16x32_bf16 v[112:115], a[216:219], a[248:251], v[112:115]\n"
"v_mfma_f32_16x16x32_bf16 v[116:119], a[220:223], a[248:251], v[116:119]\n"
"buffer_load_dwordx4 v10, s[48:51], s69 offen offset:3072 lds\n"
"v_mfma_f32_16x16x32_bf16 v[116:119], a[40:43], a[64:67], v[116:119]\n"
"v_mfma_f32_16x16x32_bf16 v[120:123], a[44:47], a[64:67], v[120:123]\n"
"s_waitcnt lgkmcnt(0)\n"
"v_mfma_f32_16x16x32_bf16 v[120:123], a[224:227], a[248:251], v[120:123]\n"
"s_add_u32 m0, s24, 4096\n"
"v_mfma_f32_16x16x32_bf16 v[124:127], a[228:231], a[248:251], v[124:127]\n"
"s_cmp_le_u32 s65, 6\n"
"s_cselect_b32 s69, 7, 6\n"
"buffer_load_dwordx4 a[16:19], v4, s[44:47], s70 offen sc1\n"
"v_mfma_f32_16x16x32_bf16 v[124:127], a[96:99], a[64:67], v[124:127]\n"
"s_add_u32 s69, s69, s66\n"
"v_mfma_f32_16x16x32_bf16 v[140:143], a[96:99], a[68:71], v[140:143]\n"
"s_waitcnt vmcnt(15)\n"
"s_lshl_b32 s69, s69, 19\n"
"v_mfma_f32_16x16x32_bf16 v[140:143], a[228:231], a[252:255], v[140:143]\n"
"s_add_u32 s69, s69, s26\n"
"v_mfma_f32_16x16x32_bf16 v[136:139], a[224:227], a[252:255], v[136:139]\n"
"buffer_load_dwordx4 v7, s[48:51], s69 offen lds\n"
"v_mfma_f32_16x16x32_bf16 v[136:139], a[44:47], a[68:71], v[136:139]\n"
"v_mfma_f32_16x16x32_bf16 v[132:135], a[40:43], a[68:71], v[132:135]\n"
"v_mfma_f32_16x16x32_bf16 v[132:135], a[220:223], a[252:255], v[132:135]\n"
"v_mfma_f32_16x16x32_bf16 v[128:131], a[216:219], a[252:255], v[128:131]\n"
"buffer_load_dwordx4 a[20:23], v4, s[44:47], s70 offen offset:1024 sc1\n"
"v_mfma_f32_16x16x32_bf16 v[128:131], a[36:39], a[68:71], v[128:131]\n"
"v_mfma_f32_16x16x32_bf16 v[144:147], a[36:39], a[72:75], v[144:147]\n"
"s_waitcnt vmcnt(12)\n"
"ds_read_b64_tr_b16 v[224:225], v11 offset:8192\n"
"v_mfma_f32_16x16x32_bf16 v[144:147], a[216:219], v[244:247], v[144:147]\n"
"ds_read_b64_tr_b16 v[226:227], v11 offset:10240\n"
"v_mfma_f32_16x16x32_bf16 v[148:151], a[220:223], v[244:247], v[148:151]\n"
"buffer_load_dwordx4 v8, s[48:51], s69 offen offset:1024 lds\n"
"v_mfma_f32_16x16x32_bf16 v[148:151], a[40:43], a[72:75], v[148:151]\n"
"ds_read_b64_tr_b16 v[228:229], v12 offset:8192\n"
"v_mfma_f32_16x16x32_bf16 v[152:155], a[44:47], a[72:75], v[152:155]\n"
"ds_read_b64_tr_b16 v[230:231], v12 offset:10240\n"
"v_mfma_f32_16x16x32_bf16 v[152:155], a[224:227], v[244:247], v[152:155]\n"
"ds_read_b64_tr_b16 v[232:233], v13 offset:8192\n"
"v_mfma_f32_16x16x32_bf16 v[156:159], a[228:231], v[244:247], v[156:159]\n"
"buffer_load_dwordx4 a[24:27], v5, s[44:47], s70 offen sc1\n"
"v_mfma_f32_16x16x32_bf16 v[156:159], a[96:99], a[72:75], v[156:159]\n"
"ds_read_b64_tr_b16 v[234:235], v13 offset:10240\n"
"v_mfma_f32_16x16x32_bf16 v[172:175], a[96:99], a[76:79], v[172:175]\n"
"ds_read_b64_tr_b16 v[236:237], v14 offset:8192\n"
"v_mfma_f32_16x16x32_bf16 v[172:175], a[228:231], v[248:251], v[172:175]\n"
"ds_read_b64_tr_b16 v[238:239], v14 offset:10240\n"
"v_mfma_f32_16x16x32_bf16 v[168:171], a[224:227], v[248:251], v[168:171]\n"
"buffer_load_dwordx4 v9, s[48:51], s69 offen offset:2048 lds\n"
"v_mfma_f32_16x16x32_bf16 v[168:171], a[44:47], a[76:79], v[168:171]\n"
"v_mfma_f32_16x16x32_bf16 v[164:167], a[40:43], a[76:79], v[164:167]\n"
"v_mfma_f32_16x16x32_bf16 v[164:167], a[220:223], v[248:251], v[164:167]\n"
"v_mfma_f32_16x16x32_bf16 v[160:163], a[216:219], v[248:251], v[160:163]\n"
"buffer_load_dwordx4 a[28:31], v5, s[44:47], s70 offen offset:1024 sc1\n"
"v_mfma_f32_16x16x32_bf16 v[160:163], a[36:39], a[76:79], v[160:163]\n"
"v_mfma_f32_16x16x32_bf16 v[176:179], a[36:39], a[80:83], v[176:179]\n"
"s_waitcnt vmcnt(15)\n"
"v_mfma_f32_16x16x32_bf16 v[176:179], a[216:219], v[252:255], v[176:179]\n"
"v_mfma_f32_16x16x32_bf16 v[180:183], a[220:223], v[252:255], v[180:183]\n"
"buffer_load_dwordx4 v10, s[48:51], s69 offen offset:3072 lds\n"
"v_mfma_f32_16x16x32_bf16 v[180:183], a[40:43], a[80:83], v[180:183]\n"
"v_mfma_f32_16x16x32_bf16 v[184:187], a[44:47], a[80:83], v[184:187]\n"
"v_mfma_f32_16x16x32_bf16 v[184:187], a[224:227], v[252:255], v[184:187]\n"
"v_mfma_f32_16x16x32_bf16 v[188:191], a[228:231], v[252:255], v[188:191]\n"
"buffer_load_dwordx4 a[32:35], v6, s[44:47], s70 offen sc1\n"
"v_mfma_f32_16x16x32_bf16 v[188:191], a[96:99], a[80:83], v[188:191]\n"
"v_mfma_f32_16x16x32_bf16 v[192:195], a[232:235], a[48:51], v[192:195]\n"
"s_waitcnt lgkmcnt(0)\n"
"s_add_u32 m0, s24, 8192\n"
"s_cmp_le_u32 s65, 5\n"
"s_cselect_b32 s69, 6, 5\n"
"v_mfma_f32_16x16x32_bf16 v[192:195], v[224:227], a[84:87], v[192:195]\n"
"s_add_u32 s69, s69, s66\n"
"v_mfma_f32_16x16x32_bf16 v[196:199], v[228:231], a[84:87], v[196:199]\n"
"s_mov_b32 s30, 1310720\n"
"s_lshl_b32 s69, s69, 19\n"
"v_mfma_f32_16x16x32_bf16 v[196:199], a[236:239], a[48:51], v[196:199]\n"
"s_add_u32 s69, s69, s31\n"
"buffer_load_dword v44, v24, s[48:51], s30 offen\n"
"v_mfma_f32_16x16x32_bf16 v[200:203], a[240:243], a[48:51], v[200:203]\n"
"s_cmp_le_u32 s65, 6\n"
"s_cselect_b32 s71, 7, 6\n"
"buffer_load_dwordx4 v7, s[48:51], s69 offen lds\n"
"v_mfma_f32_16x16x32_bf16 v[200:203], v[232:235], a[84:87], v[200:203]\n"
"s_add_u32 s71, s71, s66\n"
"v_mfma_f32_16x16x32_bf16 v[204:207], v[236:239], a[84:87], v[204:207]\n"
"s_mul_i32 s71, s71, 73728\n"
"v_mfma_f32_16x16x32_bf16 v[204:207], a[244:247], a[48:51], v[204:207]\n"
"s_add_u32 s71, s71, s25\n"
"v_mfma_f32_16x16x32_bf16 v[220:223], a[244:247], a[52:55], v[220:223]\n"
"buffer_load_dwordx4 a[48:51], v2, s[44:47], s71 offen sc1\n"
"v_mfma_f32_16x16x32_bf16 v[220:223], v[236:239], a[88:91], v[220:223]\n"
"v_mfma_f32_16x16x32_bf16 v[216:219], v[232:235], a[88:91], v[216:219]\n"
"v_mfma_f32_16x16x32_bf16 v[216:219], a[240:243], a[52:55], v[216:219]\n"
"v_mfma_f32_16x16x32_bf16 v[212:215], a[236:239], a[52:55], v[212:215]\n"
"buffer_load_dwordx4 v8, s[48:51], s69 offen offset:1024 lds\n"
"v_mfma_f32_16x16x32_bf16 v[212:215], v[228:231], a[88:91], v[212:215]\n"
"s_waitcnt vmcnt(13)\n"
"ds_read_b64_tr_b16 a[36:37], v11\n"
"v_mfma_f32_16x16x32_bf16 v[208:211], v[224:227], a[88:91], v[208:211]\n"
"ds_read_b64_tr_b16 a[38:39], v11 offset:2048\n"
"v_mfma_f32_16x16x32_bf16 v[208:211], a[232:235], a[52:55], v[208:211]\n"
"ds_read_b64_tr_b16 a[40:41], v12\n"
"v_mfma_f32_16x16x32_bf16 a[104:107], a[232:235], a[56:59], a[104:107]\n"
"buffer_load_dwordx4 a[52:55], v2, s[44:47], s71 offen offset:1024 sc1\n"
"v_mfma_f32_16x16x32_bf16 a[104:107], v[224:227], a[92:95], a[104:107]\n"
"ds_read_b64_tr_b16 a[42:43], v12 offset:2048\n"
"v_mfma_f32_16x16x32_bf16 a[108:111], v[228:231], a[92:95], a[108:111]\n"
"ds_read_b64_tr_b16 a[44:45], v13\n"
"v_mfma_f32_16x16x32_bf16 a[108:111], a[236:239], a[56:59], a[108:111]\n"
"ds_read_b64_tr_b16 a[46:47], v13 offset:2048\n"
"v_mfma_f32_16x16x32_bf16 a[112:115], a[240:243], a[56:59], a[112:115]\n"
"buffer_load_dwordx4 v9, s[48:51], s69 offen offset:2048 lds\n"
"v_mfma_f32_16x16x32_bf16 a[112:115], v[232:235], a[92:95], a[112:115]\n"
"ds_read_b64_tr_b16 a[96:97], v14\n"
"v_mfma_f32_16x16x32_bf16 a[116:119], v[236:239], a[92:95], a[116:119]\n"
"ds_read_b64_tr_b16 a[98:99], v14 offset:2048\n"
"v_mfma_f32_16x16x32_bf16 a[116:119], a[244:247], a[56:59], a[116:119]\n"
"v_mfma_f32_16x16x32_bf16 a[132:135], a[244:247], a[60:63], a[132:135]\n"
"buffer_load_dwordx4 a[56:59], v3, s[44:47], s71 offen sc1\n"
"v_mfma_f32_16x16x32_bf16 a[132:135], v[236:239], a[100:103], a[132:135]\n"
"v_mfma_f32_16x16x32_bf16 a[128:131], v[232:235], a[100:103], a[128:131]\n"
"v_mfma_f32_16x16x32_bf16 a[128:131], a[240:243], a[60:63], a[128:131]\n"
"v_mfma_f32_16x16x32_bf16 a[124:127], a[236:239], a[60:63], a[124:127]\n"
"buffer_load_dwordx4 v10, s[48:51], s69 offen offset:3072 lds\n"
"v_mfma_f32_16x16x32_bf16 a[124:127], v[228:231], a[100:103], a[124:127]\n"
"v_mfma_f32_16x16x32_bf16 a[120:123], v[224:227], a[100:103], a[120:123]\n"
"v_mfma_f32_16x16x32_bf16 a[120:123], a[232:235], a[60:63], a[120:123]\n"
"v_mfma_f32_16x16x32_bf16 a[136:139], a[232:235], a[64:67], a[136:139]\n"
"buffer_load_dwordx4 a[60:63], v3, s[44:47], s71 offen offset:1024 sc1\n"
"v_mfma_f32_16x16x32_bf16 a[136:139], v[224:227], a[248:251], a[136:139]\n"
"v_mfma_f32_16x16x32_bf16 a[140:143], v[228:231], a[248:251], a[140:143]\n"
"v_mfma_f32_16x16x32_bf16 a[140:143], a[236:239], a[64:67], a[140:143]\n"
"v_mfma_f32_16x16x32_bf16 a[144:147], a[240:243], a[64:67], a[144:147]\n"
"v_mfma_f32_16x16x32_bf16 a[144:147], v[232:235], a[248:251], a[144:147]\n"
"v_mfma_f32_16x16x32_bf16 a[148:151], v[236:239], a[248:251], a[148:151]\n"
"s_waitcnt lgkmcnt(0)\n"
"buffer_load_dwordx2 v[34:35], v38, s[56:59], 0 offen offset:2048 sc1\n"
"v_mfma_f32_16x16x32_bf16 a[148:151], a[244:247], a[64:67], a[148:151]\n"
"s_add_u32 m0, s24, 0\n"
"v_mfma_f32_16x16x32_bf16 a[164:167], a[244:247], a[68:71], a[164:167]\n"
"s_cmp_le_u32 s65, 6\n"
"s_cselect_b32 s69, 7, 6\n"
"buffer_load_dwordx4 a[64:67], v4, s[44:47], s71 offen sc1\n"
"v_mfma_f32_16x16x32_bf16 a[164:167], v[236:239], a[252:255], a[164:167]\n"
"s_add_u32 s69, s69, s66\n"
"v_mfma_f32_16x16x32_bf16 a[160:163], v[232:235], a[252:255], a[160:163]\n"
"s_lshl_b32 s69, s69, 19\n"
"v_mfma_f32_16x16x32_bf16 a[160:163], a[240:243], a[68:71], a[160:163]\n"
"s_add_u32 s69, s69, s31\n"
"v_mfma_f32_16x16x32_bf16 a[156:159], a[236:239], a[68:71], a[156:159]\n"
"buffer_load_dwordx4 v7, s[48:51], s69 offen lds\n"
"v_mfma_f32_16x16x32_bf16 a[156:159], v[228:231], a[252:255], a[156:159]\n"
"v_mfma_f32_16x16x32_bf16 a[152:155], v[224:227], a[252:255], a[152:155]\n"
"v_mfma_f32_16x16x32_bf16 a[152:155], a[232:235], a[68:71], a[152:155]\n"
"v_mfma_f32_16x16x32_bf16 a[168:171], a[232:235], a[72:75], a[168:171]\n"
"buffer_load_dwordx4 a[68:71], v4, s[44:47], s71 offen offset:1024 sc1\n"
"v_mfma_f32_16x16x32_bf16 a[168:171], v[224:227], v[244:247], a[168:171]\n"
"s_waitcnt vmcnt(14)\n"
"ds_read_b64_tr_b16 a[216:217], v11 offset:4096\n"
"v_mfma_f32_16x16x32_bf16 a[172:175], v[228:231], v[244:247], a[172:175]\n"
"ds_read_b64_tr_b16 a[218:219], v11 offset:6144\n"
"v_mfma_f32_16x16x32_bf16 a[172:175], a[236:239], a[72:75], a[172:175]\n"
"ds_read_b64_tr_b16 a[220:221], v12 offset:4096\n"
"v_mfma_f32_16x16x32_bf16 a[176:179], a[240:243], a[72:75], a[176:179]\n"
"buffer_load_dwordx4 v8, s[48:51], s69 offen offset:1024 lds\n"
"v_mfma_f32_16x16x32_bf16 a[176:179], v[232:235], v[244:247], a[176:179]\n"
"ds_read_b64_tr_b16 a[222:223], v12 offset:6144\n"
"v_mfma_f32_16x16x32_bf16 a[180:183], v[236:239], v[244:247], a[180:183]\n"
"ds_read_b64_tr_b16 a[224:225], v13 offset:4096\n"
"v_mfma_f32_16x16x32_bf16 a[180:183], a[244:247], a[72:75], a[180:183]\n"
"ds_read_b64_tr_b16 a[226:227], v13 offset:6144\n"
"v_mfma_f32_16x16x32_bf16 a[196:199], a[244:247], a[76:79], a[196:199]\n"
"buffer_load_dwordx4 a[72:75], v5, s[44:47], s71 offen sc1\n"
"v_mfma_f32_16x16x32_bf16 a[196:199], v[236:239], v[248:251], a[196:199]\n"
"ds_read_b64_tr_b16 a[228:229], v14 offset:4096\n"
"v_mfma_f32_16x16x32_bf16 a[192:195], v[232:235], v[248:251], a[192:195]\n"
"ds_read_b64_tr_b16 a[230:231], v14 offset:6144\n"
"v_mfma_f32_16x16x32_bf16 a[192:195], a[240:243], a[76:79], a[192:195]\n"
"v_mfma_f32_16x16x32_bf16 a[188:191], a[236:239], a[76:79], a[188:191]\n"
"buffer_load_dwordx4 v9, s[48:51], s69 offen offset:2048 lds\n"
"v_mfma_f32_16x16x32_bf16 a[188:191], v[228:231], v[248:251], a[188:191]\n"
"v_mfma_f32_16x16x32_bf16 a[184:187], v[224:227], v[248:251], a[184:187]\n"
"v_mfma_f32_16x16x32_bf16 a[184:187], a[232:235], a[76:79], a[184:187]\n"
"v_mfma_f32_16x16x32_bf16 a[200:203], a[232:235], a[80:83], a[200:203]\n"
"buffer_load_dwordx4 a[76:79], v5, s[44:47], s71 offen offset:1024 sc1\n"
"v_mfma_f32_16x16x32_bf16 a[200:203], v[224:227], v[252:255], a[200:203]\n"
"v_mfma_f32_16x16x32_bf16 a[204:207], v[228:231], v[252:255], a[204:207]\n"
"v_mfma_f32_16x16x32_bf16 a[204:207], a[236:239], a[80:83], a[204:207]\n"
"v_mfma_f32_16x16x32_bf16 a[208:211], a[240:243], a[80:83], a[208:211]\n"
"buffer_load_dwordx4 v10, s[48:51], s69 offen offset:3072 lds\n"
"v_mfma_f32_16x16x32_bf16 a[208:211], v[232:235], v[252:255], a[208:211]\n"
"v_mfma_f32_16x16x32_bf16 a[212:215], v[236:239], v[252:255], a[212:215]\n"
"v_mfma_f32_16x16x32_bf16 a[212:215], a[244:247], a[80:83], a[212:215]\n"
"v_mfma_f32_16x16x32_bf16 v[48:51], a[36:39], a[0:3], v[48:51]\n"
"s_waitcnt vmcnt(15) lgkmcnt(3)\n"
"buffer_load_dwordx4 a[80:83], v6, s[44:47], s71 offen sc1\n"
"v_mfma_f32_16x16x32_bf16 v[48:51], a[216:219], a[48:51], v[48:51]\n"
"s_cmp_le_u32 s65, 0\n"
"s_cselect_b32 s70, 1, 0\n"
"s_waitcnt vmcnt(9)\n"
"v_cmp_ne_u64 vcc, v[34:35], v[46:47]\n"
"v_mfma_f32_16x16x32_bf16 v[52:55], a[220:223], a[48:51], v[52:55]\n"
"s_add_u32 s70, s70, s66\n"
"v_mfma_f32_16x16x32_bf16 v[52:55], a[40:43], a[0:3], v[52:55]\n"
"s_mul_i32 s70, s70, 73728\n"
"s_cbranch_vccnz L_spin1_%=\n"
"L_spinr1_%=:\n"
"s_waitcnt lgkmcnt(0)\n"
"v_mfma_f32_16x16x32_bf16 v[56:59], a[44:47], a[0:3], v[56:59]\n"
"s_add_u32 s70, s70, s25\n"
"s_add_u32 m0, s24, 4096\n"
"v_mfma_f32_16x16x32_bf16 v[56:59], a[224:227], a[48:51], v[56:59]\n"
"s_add_u32 s70, s70, 36864\n"
"s_cmp_le_u32 s65, 0\n"
"s_cselect_b32 s69, 1, 0\n"
"v_mfma_f32_16x16x32_bf16 v[60:63], a[228:231], a[48:51], v[60:63]\n"
"s_add_u32 s69, s69, s66\n"
"buffer_load_dwordx4 a[84:87], v2, s[44:47], s70 offen sc1\n"
"v_mfma_f32_16x16x32_bf16 v[60:63], a[96:99], a[0:3], v[60:63]\n"
"s_lshl_b32 s69, s69, 19\n"
"v_mfma_f32_16x16x32_bf16 v[76:79], a[96:99], a[4:7], v[76:79]\n"
"s_add_u32 s69, s69, s26\n"
"v_mfma_f32_16x16x32_bf16 v[76:79], a[228:231], a[52:55], v[76:79]\n"
"s_add_i32 s69, s69, 262144\n"
"v_mfma_f32_16x16x32_bf16 v[72:75], a[224:227], a[52:55], v[72:75]\n"
"buffer_load_dwordx4 v7, s[48:51], s69 offen lds\n"
"v_mfma_f32_16x16x32_bf16 v[72:75], a[44:47], a[4:7], v[72:75]\n"
"v_mfma_f32_16x16x32_bf16 v[68:71], a[40:43], a[4:7], v[68:71]\n"
"ds_read_b64_tr_b16 a[232:233], v11 offset:8192\n"
"v_mfma_f32_16x16x32_bf16 v[68:71], a[220:223], a[52:55], v[68:71]\n"
"ds_read_b64_tr_b16 a[234:235], v11 offset:10240\n"
"v_mfma_f32_16x16x32_bf16 v[64:67], a[216:219], a[52:55], v[64:67]\n"
"buffer_load_dwordx4 a[88:91], v2, s[44:47], s70 offen offset:1024 sc1\n"
"v_mfma_f32_16x16x32_bf16 v[64:67], a[36:39], a[4:7], v[64:67]\n"
"ds_read_b64_tr_b16 a[236:237], v12 offset:8192\n"
"v_mfma_f32_16x16x32_bf16 v[80:83], a[36:39], a[8:11], v[80:83]\n"
"ds_read_b64_tr_b16 a[238:239], v12 offset:10240\n"
"v_mfma_f32_16x16x32_bf16 v[80:83], a[216:219], a[56:59], v[80:83]\n"
"ds_read_b64_tr_b16 a[240:241], v13 offset:8192\n"
"v_mfma_f32_16x16x32_bf16 v[84:87], a[220:223], a[56:59], v[84:87]\n"
"buffer_load_dwordx4 v8, s[48:51], s69 offen offset:1024 lds\n"
"v_mfma_f32_16x16x32_bf16 v[84:87], a[40:43], a[8:11], v[84:87]\n"
"ds_read_b64_tr_b16 a[242:243], v13 offset:10240\n"
"v_mfma_f32_16x16x32_bf16 v[88:91], a[44:47], a[8:11], v[88:91]\n"
"ds_read_b64_tr_b16 a[244:245], v14 offset:8192\n"
"v_mfma_f32_16x16x32_bf16 v[88:91], a[224:227], a[56:59], v[88:91]\n"
"ds_read_b64_tr_b16 a[246:247], v14 offset:10240\n"
"v_mfma_f32_16x16x32_bf16 v[92:95], a[228:231], a[56:59], v[92:95]\n"
"buffer_load_dwordx4 a[92:95], v3, s[44:47], s70 offen sc1\n"
"v_mfma_f32_16x16x32_bf16 v[92:95], a[96:99], a[8:11], v[92:95]\n"
"v_mfma_f32_16x16x32_bf16 v[108:111], a[96:99], a[12:15], v[108:111]\n"
"v_mfma_f32_16x16x32_bf16 v[108:111], a[228:231], a[60:63], v[108:111]\n"
"v_mfma_f32_16x16x32_bf16 v[104:107], a[224:227], a[60:63], v[104:107]\n"
"buffer_load_dwordx4 v9, s[48:51], s69 offen offset:2048 lds\n"
"v_mfma_f32_16x16x32_bf16 v[104:107], a[44:47], a[12:15], v[104:107]\n"
"v_mfma_f32_16x16x32_bf16 v[100:103], a[40:43], a[12:15], v[100:103]\n"
"v_mfma_f32_16x16x32_bf16 v[100:103], a[220:223], a[60:63], v[100:103]\n"
"v_mfma_f32_16x16x32_bf16 v[96:99], a[216:219], a[60:63], v[96:99]\n"
"buffer_load_dwordx4 a[100:103], v3, s[44:47], s70 offen offset:1024 sc1\n"
"v_mfma_f32_16x16x32_bf16 v[96:99], a[36:39], a[12:15], v[96:99]\n"
"v_mfma_f32_16x16x32_bf16 v[112:115], a[36:39], a[16:19], v[112:115]\n"
"s_waitcnt vmcnt(15)\n"
"v_mfma_f32_16x16x32_bf16 v[112:115], a[216:219], a[64:67], v[112:115]\n"
"v_mfma_f32_16x16x32_bf16 v[116:119], a[220:223], a[64:67], v[116:119]\n"
"buffer_load_dwordx4 v10, s[48:51], s69 offen offset:3072 lds\n"
"v_mfma_f32_16x16x32_bf16 v[116:119], a[40:43], a[16:19], v[116:119]\n"
"v_mfma_f32_16x16x32_bf16 v[120:123], a[44:47], a[16:19], v[120:123]\n"
"s_waitcnt lgkmcnt(0)\n"
"s_add_u32 m0, s24, 8192\n"
"v_mfma_f32_16x16x32_bf16 v[120:123], a[224:227], a[64:67], v[120:123]\n"
"s_cmp_le_u32 s65, 1\n"
"s_cselect_b32 s69, 2, 1\n"
"v_mfma_f32_16x16x32_bf16 v[124:127], a[228:231], a[64:67], v[124:127]\n"
"s_add_u32 s69, s69, s66\n"
"buffer_load_dwordx4 a[248:251], v4, s[44:47], s70 offen sc1\n"
"v_mfma_f32_16x16x32_bf16 v[124:127], a[96:99], a[16:19], v[124:127]\n"
"s_lshl_b32 s69, s69, 19\n"
"v_mfma_f32_16x16x32_bf16 v[140:143], a[96:99], a[20:23], v[140:143]\n"
"s_waitcnt vmcnt(15)\n"
"s_add_u32 s69, s69, s26\n"
"v_mfma_f32_16x16x32_bf16 v[140:143], a[228:231], a[68:71], v[140:143]\n"
"s_add_i32 s69, s69, 262144\n"
"v_mfma_f32_16x16x32_bf16 v[136:139], a[224:227], a[68:71], v[136:139]\n"
"buffer_load_dwordx4 v7, s[48:51], s69 offen lds\n"
"v_mfma_f32_16x16x32_bf16 v[136:139], a[44:47], a[20:23], v[136:139]\n"
"v_mfma_f32_16x16x32_bf16 v[132:135], a[40:43], a[20:23], v[132:135]\n"
"v_mfma_f32_16x16x32_bf16 v[132:135], a[220:223], a[68:71], v[132:135]\n"
"v_mfma_f32_16x16x32_bf16 v[128:131], a[216:219], a[68:71], v[128:131]\n"
"buffer_load_dwordx4 a[252:255], v4, s[44:47], s70 offen offset:1024 sc1\n"
"v_mfma_f32_16x16x32_bf16 v[128:131], a[36:39], a[20:23], v[128:131]\n"
"v_mfma_f32_16x16x32_bf16 v[144:147], a[36:39], a[24:27], v[144:147]\n"
"s_waitcnt vmcnt(12)\n"
"ds_read_b64_tr_b16 v[224:225], v11\n"
"v_mfma_f32_16x16x32_bf16 v[144:147], a[216:219], a[72:75], v[144:147]\n"
"ds_read_b64_tr_b16 v[226:227], v11 offset:2048\n"
"v_mfma_f32_16x16x32_bf16 v[148:151], a[220:223], a[72:75], v[148:151]\n"
"buffer_load_dwordx4 v8, s[48:51], s69 offen offset:1024 lds\n"
"v_mfma_f32_16x16x32_bf16 v[148:151], a[40:43], a[24:27], v[148:151]\n"
"ds_read_b64_tr_b16 v[228:229], v12\n"
"v_mfma_f32_16x16x32_bf16 v[152:155], a[44:47], a[24:27], v[152:155]\n"
"ds_read_b64_tr_b16 v[230:231], v12 offset:2048\n"
"v_mfma_f32_16x16x32_bf16 v[152:155], a[224:227], a[72:75], v[152:155]\n"
"ds_read_b64_tr_b16 v[232:233], v13\n"
"v_mfma_f32_16x16x32_bf16 v[156:159], a[228:231], a[72:75], v[156:159]\n"
"buffer_load_dwordx4 v[244:247], v5, s[44:47], s70 offen sc1\n"
"v_mfma_f32_16x16x32_bf16 v[156:159], a[96:99], a[24:27], v[156:159]\n"
"ds_read_b64_tr_b16 v[234:235], v13 offset:2048\n"
"v_mfma_f32_16x16x32_bf16 v[172:175], a[96:99], a[28:31], v[172:175]\n"
"ds_read_b64_tr_b16 v[236:237], v14\n"
"v_mfma_f32_16x16x32_bf16 v[172:175], a[228:231], a[76:79], v[172:175]\n"
"ds_read_b64_tr_b16 v[238:239], v14 offset:2048\n"
"v_mfma_f32_16x16x32_bf16 v[168:171], a[224:227], a[76:79], v[168:171]\n"
"buffer_load_dwordx4 v9, s[48:51], s69 offen offset:2048 lds\n"
"v_mfma_f32_16x16x32_bf16 v[168:171], a[44:47], a[28:31], v[168:171]\n"
"v_mfma_f32_16x16x32_bf16 v[164:167], a[40:43], a[28:31], v[164:167]\n"
"v_mfma_f32_16x16x32_bf16 v[164:167], a[220:223], a[76:79], v[164:167]\n"
"v_mfma_f32_16x16x32_bf16 v[160:163], a[216:219], a[76:79], v[160:163]\n"
"buffer_load_dwordx4 v[248:251], v5, s[44:47], s70 offen offset:1024 sc1\n"
"v_mfma_f32_16x16x32_bf16 v[160:163], a[36:39], a[28:31], v[160:163]\n"
"v_mfma_f32_16x16x32_bf16 v[176:179], a[36:39], a[32:35], v[176:179]\n"
"s_waitcnt vmcnt(15)\n"
"v_mfma_f32_16x16x32_bf16 v[176:179], a[216:219], a[80:83], v[176:179]\n"
"v_mfma_f32_16x16x32_bf16 v[180:183], a[220:223], a[80:83], v[180:183]\n"
"buffer_load_dwordx4 v10, s[48:51], s69 offen offset:3072 lds\n"
"v_mfma_f32_16x16x32_bf16 v[180:183], a[40:43], a[32:35], v[180:183]\n"
"v_mfma_f32_16x16x32_bf16 v[184:187], a[44:47], a[32:35], v[184:187]\n"
"v_mfma_f32_16x16x32_bf16 v[184:187], a[224:227], a[80:83], v[184:187]\n"
"v_mfma_f32_16x16x32_bf16 v[188:191], a[228:231], a[80:83], v[188:191]\n"
"buffer_load_dwordx4 v[252:255], v6, s[44:47], s70 offen sc1\n"
"v_mfma_f32_16x16x32_bf16 v[188:191], a[96:99], a[32:35], v[188:191]\n"
"v_mfma_f32_16x16x32_bf16 v[192:195], a[232:235], a[0:3], v[192:195]\n"
"s_waitcnt lgkmcnt(0)\n"
"s_add_u32 m0, s24, 0\n"
"s_cmp_le_u32 s65, 0\n"
"s_cselect_b32 s69, 1, 0\n"
"s_add_u32 s69, s69, s66\n"
"v_mfma_f32_16x16x32_bf16 v[192:195], v[224:227], a[48:51], v[192:195]\n"
"s_lshl_b32 s69, s69, 19\n"
"v_mfma_f32_16x16x32_bf16 v[196:199], v[228:231], a[48:51], v[196:199]\n"
"s_mov_b32 s30, 2359296\n"
"s_add_u32 s69, s69, s31\n"
"v_mfma_f32_16x16x32_bf16 v[196:199], a[236:239], a[0:3], v[196:199]\n"
"s_cmp_le_u32 s65, 1\n"
"s_cselect_b32 s71, 2, 1\n"
"s_add_i32 s69, s69, 262144\n"
"buffer_load_dword v44, v24, s[48:51], s30 offen\n"
"v_mfma_f32_16x16x32_bf16 v[200:203], a[240:243], a[0:3], v[200:203]\n"
"s_add_u32 s71, s71, s66\n"
"buffer_load_dwordx4 v7, s[48:51], s69 offen lds\n"
"v_mfma_f32_16x16x32_bf16 v[200:203], v[232:235], a[48:51], v[200:203]\n"
"s_mul_i32 s71, s71, 73728\n"
"v_mfma_f32_16x16x32_bf16 v[204:207], v[236:239], a[48:51], v[204:207]\n"
"s_add_u32 s71, s71, s25\n"
"v_mfma_f32_16x16x32_bf16 v[204:207], a[244:247], a[0:3], v[204:207]\n"
"s_add_u32 s71, s71, 36864\n"
"v_mfma_f32_16x16x32_bf16 v[220:223], a[244:247], a[4:7], v[220:223]\n"
"buffer_load_dwordx4 a[0:3], v2, s[44:47], s71 offen sc1\n"
"v_mfma_f32_16x16x32_bf16 v[220:223], v[236:239], a[52:55], v[220:223]\n"
"v_mfma_f32_16x16x32_bf16 v[216:219], v[232:235], a[52:55], v[216:219]\n"
"v_mfma_f32_16x16x32_bf16 v[216:219], a[240:243], a[4:7], v[216:219]\n"
"v_mfma_f32_16x16x32_bf16 v[212:215], a[236:239], a[4:7], v[212:215]\n"
"buffer_load_dwordx4 v8, s[48:51], s69 offen offset:1024 lds\n"
"v_mfma_f32_16x16x32_bf16 v[212:215], v[228:231], a[52:55], v[212:215]\n"
"s_waitcnt vmcnt(13)\n"
"ds_read_b64_tr_b16 a[36:37], v11 offset:4096\n"
"v_mfma_f32_16x16x32_bf16 v[208:211], v[224:227], a[52:55], v[208:211]\n"
"ds_read_b64_tr_b16 a[38:39], v11 offset:6144\n"
"v_mfma_f32_16x16x32_bf16 v[208:211], a[232:235], a[4:7], v[208:211]\n"
"ds_read_b64_tr_b16 a[40:41], v12 offset:4096\n"
"v_mfma_f32_16x16x32_bf16 a[104:107], a[232:235], a[8:11], a[104:107]\n"
"buffer_load_dwordx4 a[4:7], v2, s[44:47], s71 offen offset:1024 sc1\n"
"v_mfma_f32_16x16x32_bf16 a[104:107], v[224:227], a[56:59], a[104:107]\n"
"ds_read_b64_tr_b16 a[42:43], v12 offset:6144\n"
"v_mfma_f32_16x16x32_bf16 a[108:111], v[228:231], a[56:59], a[108:111]\n"
"ds_read_b64_tr_b16 a[44:45], v13 offset:4096\n"
"v_mfma_f32_16x16x32_bf16 a[108:111], a[236:239], a[8:11], a[108:111]\n"
"ds_read_b64_tr_b16 a[46:47], v13 offset:6144\n"
"v_mfma_f32_16x16x32_bf16 a[112:115], a[240:243], a[8:11], a[112:115]\n"
"buffer_load_dwordx4 v9, s[48:51], s69 offen offset:2048 lds\n"
"v_mfma_f32_16x16x32_bf16 a[112:115], v[232:235], a[56:59], a[112:115]\n"
"ds_read_b64_tr_b16 a[96:97], v14 offset:4096\n"
"v_mfma_f32_16x16x32_bf16 a[116:119], v[236:239], a[56:59], a[116:119]\n"
"ds_read_b64_tr_b16 a[98:99], v14 offset:6144\n"
"v_mfma_f32_16x16x32_bf16 a[116:119], a[244:247], a[8:11], a[116:119]\n"
"v_mfma_f32_16x16x32_bf16 a[132:135], a[244:247], a[12:15], a[132:135]\n"
"buffer_load_dwordx4 a[8:11], v3, s[44:47], s71 offen sc1\n"
"v_mfma_f32_16x16x32_bf16 a[132:135], v[236:239], a[60:63], a[132:135]\n"
"v_mfma_f32_16x16x32_bf16 a[128:131], v[232:235], a[60:63], a[128:131]\n"
"v_mfma_f32_16x16x32_bf16 a[128:131], a[240:243], a[12:15], a[128:131]\n"
"v_mfma_f32_16x16x32_bf16 a[124:127], a[236:239], a[12:15], a[124:127]\n"
"buffer_load_dwordx4 v10, s[48:51], s69 offen offset:3072 lds\n"
"v_mfma_f32_16x16x32_bf16 a[124:127], v[228:231], a[60:63], a[124:127]\n"
"v_mfma_f32_16x16x32_bf16 a[120:123], v[224:227], a[60:63], a[120:123]\n"
"v_mfma_f32_16x16x32_bf16 a[120:123], a[232:235], a[12:15], a[120:123]\n"
"v_mfma_f32_16x16x32_bf16 a[136:139], a[232:235], a[16:19], a[136:139]\n"
"buffer_load_dwordx4 a[12:15], v3, s[44:47], s71 offen offset:1024 sc1\n"
"v_mfma_f32_16x16x32_bf16 a[136:139], v[224:227], a[64:67], a[136:139]\n"
"v_mfma_f32_16x16x32_bf16 a[140:143], v[228:231], a[64:67], a[140:143]\n"
"v_mfma_f32_16x16x32_bf16 a[140:143], a[236:239], a[16:19], a[140:143]\n"
"v_mfma_f32_16x16x32_bf16 a[144:147], a[240:243], a[16:19], a[144:147]\n"
"v_mfma_f32_16x16x32_bf16 a[144:147], v[232:235], a[64:67], a[144:147]\n"
"s_waitcnt lgkmcnt(0)\n"
"v_mfma_f32_16x16x32_bf16 a[148:151], v[236:239], a[64:67], a[148:151]\n"
"s_add_u32 m0, s24, 4096\n"
"v_mfma_f32_16x16x32_bf16 a[148:151], a[244:247], a[16:19], a[148:151]\n"
"s_cmp_le_u32 s65, 1\n"
"s_cselect_b32 s69, 2, 1\n"
"v_mfma_f32_16x16x32_bf16 a[164:167], a[244:247], a[20:23], a[164:167]\n"
"s_add_u32 s69, s69, s66\n"
"buffer_load_dwordx4 a[16:19], v4, s[44:47], s71 offen sc1\n"
"v_mfma_f32_16x16x32_bf16 a[164:167], v[236:239], a[68:71], a[164:167]\n"
"s_lshl_b32 s69, s69, 19\n"
"v_mfma_f32_16x16x32_bf16 a[160:163], v[232:235], a[68:71], a[160:163]\n"
"s_add_u32 s69, s69, s31\n"
"v_mfma_f32_16x16x32_bf16 a[160:163], a[240:243], a[20:23], a[160:163]\n"
"s_add_i32 s69, s69, 262144\n"
"v_mfma_f32_16x16x32_bf16 a[156:159], a[236:239], a[20:23], a[156:159]\n"
"buffer_load_dwordx4 v7, s[48:51], s69 offen lds\n"
"v_mfma_f32_16x16x32_bf16 a[156:159], v[228:231], a[68:71], a[156:159]\n"
"v_mfma_f32_16x16x32_bf16 a[152:155], v[224:227], a[68:71], a[152:155]\n"
"v_mfma_f32_16x16x32_bf16 a[152:155], a[232:235], a[20:23], a[152:155]\n"
"v_mfma_f32_16x16x32_bf16 a[168:171], a[232:235], a[24:27], a[168:171]\n"
"buffer_load_dwordx4 a[20:23], v4, s[44:47], s71 offen offset:1024 sc1\n"
"v_mfma_f32_16x16x32_bf16 a[168:171], v[224:227], a[72:75], a[168:171]\n"
"s_waitcnt vmcnt(13)\n"
"ds_read_b64_tr_b16 a[216:217], v11 offset:8192\n"
"v_mfma_f32_16x16x32_bf16 a[172:175], v[228:231], a[72:75], a[172:175]\n"
"ds_read_b64_tr_b16 a[218:219], v11 offset:10240\n"
"v_mfma_f32_16x16x32_bf16 a[172:175], a[236:239], a[24:27], a[172:175]\n"
"ds_read_b64_tr_b16 a[220:221], v12 offset:8192\n"
"v_mfma_f32_16x16x32_bf16 a[176:179], a[240:243], a[24:27], a[176:179]\n"
"buffer_load_dwordx4 v8, s[48:51], s69 offen offset:1024 lds\n"
"v_mfma_f32_16x16x32_bf16 a[176:179], v[232:235], a[72:75], a[176:179]\n"
"ds_read_b64_tr_b16 a[222:223], v12 offset:10240\n"
"v_mfma_f32_16x16x32_bf16 a[180:183], v[236:239], a[72:75], a[180:183]\n"
"ds_read_b64_tr_b16 a[224:225], v13 offset:8192\n"
"v_mfma_f32_16x16x32_bf16 a[180:183], a[244:247], a[24:27], a[180:183]\n"
"ds_read_b64_tr_b16 a[226:227], v13 offset:10240\n"
"v_mfma_f32_16x16x32_bf16 a[196:199], a[244:247], a[28:31], a[196:199]\n"
"buffer_load_dwordx4 a[24:27], v5, s[44:47], s71 offen sc1\n"
"v_mfma_f32_16x16x32_bf16 a[196:199], v[236:239], a[76:79], a[196:199]\n"
"ds_read_b64_tr_b16 a[228:229], v14 offset:8192\n"
"v_mfma_f32_16x16x32_bf16 a[192:195], v[232:235], a[76:79], a[192:195]\n"
"ds_read_b64_tr_b16 a[230:231], v14 offset:10240\n"
"v_mfma_f32_16x16x32_bf16 a[192:195], a[240:243], a[28:31], a[192:195]\n"
"v_mfma_f32_16x16x32_bf16 a[188:191], a[236:239], a[28:31], a[188:191]\n"
"buffer_load_dwordx4 v9, s[48:51], s69 offen offset:2048 lds\n"
"v_mfma_f32_16x16x32_bf16 a[188:191], v[228:231], a[76:79], a[188:191]\n"
"v_mfma_f32_16x16x32_bf16 a[184:187], v[224:227], a[76:79], a[184:187]\n"
"v_mfma_f32_16x16x32_bf16 a[184:187], a[232:235], a[28:31], a[184:187]\n"
"v_mfma_f32_16x16x32_bf16 a[200:203], a[232:235], a[32:35], a[200:203]\n"
"buffer_load_dwordx4 a[28:31], v5, s[44:47], s71 offen offset:1024 sc1\n"
"v_mfma_f32_16x16x32_bf16 a[200:203], v[224:227], a[80:83], a[200:203]\n"
"v_mfma_f32_16x16x32_bf16 a[204:207], v[228:231], a[80:83], a[204:207]\n"
"v_mfma_f32_16x16x32_bf16 a[204:207], a[236:239], a[32:35], a[204:207]\n"
"v_mfma_f32_16x16x32_bf16 a[208:211], a[240:243], a[32:35], a[208:211]\n"
"buffer_load_dwordx4 v10, s[48:51], s69 offen offset:3072 lds\n"
"v_mfma_f32_16x16x32_bf16 a[208:211], v[232:235], a[80:83], a[208:211]\n"
"v_mfma_f32_16x16x32_bf16 a[212:215], v[236:239], a[80:83], a[212:215]\n"
"v_mfma_f32_16x16x32_bf16 a[212:215], a[244:247], a[32:35], a[212:215]\n"
"v_mfma_f32_16x16x32_bf16 v[48:51], a[36:39], a[84:87], v[48:51]\n"
"s_waitcnt vmcnt(14) lgkmcnt(3)\n"
"buffer_load_dwordx4 a[32:35], v6, s[44:47], s71 offen sc1\n"
"v_mfma_f32_16x16x32_bf16 v[48:51], a[216:219], a[0:3], v[48:51]\n"
"s_cmp_le_u32 s65, 2\n"
"s_cselect_b32 s70, 3, 2\n"
"v_mfma_f32_16x16x32_bf16 v[52:55], a[220:223], a[0:3], v[52:55]\n"
"s_add_u32 s70, s70, s66\n"
"v_mfma_f32_16x16x32_bf16 v[52:55], a[40:43], a[84:87], v[52:55]\n"
"s_mul_i32 s70, s70, 73728\n"
"s_waitcnt lgkmcnt(0)\n"
"v_mfma_f32_16x16x32_bf16 v[56:59], a[44:47], a[84:87], v[56:59]\n"
"s_add_u32 s70, s70, s25\n"
"s_add_u32 m0, s24, 8192\n"
"v_mfma_f32_16x16x32_bf16 v[56:59], a[224:227], a[0:3], v[56:59]\n"
"s_add_u32 s70, s70, 36864\n"
"s_cmp_le_u32 s65, 2\n"
"s_cselect_b32 s69, 3, 2\n"
"v_mfma_f32_16x16x32_bf16 v[60:63], a[228:231], a[0:3], v[60:63]\n"
"s_add_u32 s69, s69, s66\n"
"buffer_load_dwordx4 a[48:51], v2, s[44:47], s70 offen sc1\n"
"v_mfma_f32_16x16x32_bf16 v[60:63], a[96:99], a[84:87], v[60:63]\n"
"s_lshl_b32 s69, s69, 19\n"
"v_mfma_f32_16x16x32_bf16 v[76:79], a[96:99], a[88:91], v[76:79]\n"
"s_waitcnt vmcnt(14)\n"
"s_add_u32 s69, s69, s26\n"
"v_mfma_f32_16x16x32_bf16 v[76:79], a[228:231], a[4:7], v[76:79]\n"
"s_add_i32 s69, s69, 262144\n"
"v_mfma_f32_16x16x32_bf16 v[72:75], a[224:227], a[4:7], v[72:75]\n"
"buffer_load_dwordx4 v7, s[48:51], s69 offen lds\n"
"v_mfma_f32_16x16x32_bf16 v[72:75], a[44:47], a[88:91], v[72:75]\n"
"v_mfma_f32_16x16x32_bf16 v[68:71], a[40:43], a[88:91], v[68:71]\n"
"s_waitcnt vmcnt(12)\n"
"ds_read_b64_tr_b16 a[232:233], v11\n"
"v_mfma_f32_16x16x32_bf16 v[68:71], a[220:223], a[4:7], v[68:71]\n"
"ds_read_b64_tr_b16 a[234:235], v11 offset:2048\n"
"v_mfma_f32_16x16x32_bf16 v[64:67], a[216:219], a[4:7], v[64:67]\n"
"buffer_load_dwordx4 a[52:55], v2, s[44:47], s70 offen offset:1024 sc1\n"
"v_mfma_f32_16x16x32_bf16 v[64:67], a[36:39], a[88:91], v[64:67]\n"
"ds_read_b64_tr_b16 a[236:237], v12\n"
"v_mfma_f32_16x16x32_bf16 v[80:83], a[36:39], a[92:95], v[80:83]\n"
"ds_read_b64_tr_b16 a[238:239], v12 offset:2048\n"
"v_mfma_f32_16x16x32_bf16 v[80:83], a[216:219], a[8:11], v[80:83]\n"
"ds_read_b64_tr_b16 a[240:241], v13\n"
"v_mfma_f32_16x16x32_bf16 v[84:87], a[220:223], a[8:11], v[84:87]\n"
"buffer_load_dwordx4 v8, s[48:51], s69 offen offset:1024 lds\n"
"v_mfma_f32_16x16x32_bf16 v[84:87], a[40:43], a[92:95], v[84:87]\n"
"ds_read_b64_tr_b16 a[242:243], v13 offset:2048\n"
"v_mfma_f32_16x16x32_bf16 v[88:91], a[44:47], a[92:95], v[88:91]\n"
"ds_read_b64_tr_b16 a[244:245], v14\n"
"v_mfma_f32_16x16x32_bf16 v[88:91], a[224:227], a[8:11], v[88:91]\n"
"ds_read_b64_tr_b16 a[246:247], v14 offset:2048\n"
"v_mfma_f32_16x16x32_bf16 v[92:95], a[228:231], a[8:11], v[92:95]\n"
"buffer_load_dwordx4 a[56:59], v3, s[44:47], s70 offen sc1\n"
"v_mfma_f32_16x16x32_bf16 v[92:95], a[96:99], a[92:95], v[92:95]\n"
"v_mfma_f32_16x16x32_bf16 v[108:111], a[96:99], a[100:103], v[108:111]\n"
"s_waitcnt vmcnt(14)\n"
"v_mfma_f32_16x16x32_bf16 v[108:111], a[228:231], a[12:15], v[108:111]\n"
"v_mfma_f32_16x16x32_bf16 v[104:107], a[224:227], a[12:15], v[104:107]\n"
"buffer_load_dwordx4 v9, s[48:51], s69 offen offset:2048 lds\n"
"v_mfma_f32_16x16x32_bf16 v[104:107], a[44:47], a[100:103], v[104:107]\n"
"v_mfma_f32_16x16x32_bf16 v[100:103], a[40:43], a[100:103], v[100:103]\n"
"v_mfma_f32_16x16x32_bf16 v[100:103], a[220:223], a[12:15], v[100:103]\n"
"v_mfma_f32_16x16x32_bf16 v[96:99], a[216:219], a[12:15], v[96:99]\n"
"buffer_load_dwordx4 a[60:63], v3, s[44:47], s70 offen offset:1024 sc1\n"
"v_mfma_f32_16x16x32_bf16 v[96:99], a[36:39], a[100:103], v[96:99]\n"
"v_mfma_f32_16x16x32_bf16 v[112:115], a[36:39], a[248:251], v[112:115]\n"
"s_waitcnt vmcnt(15)\n"
"v_mfma_f32_16x16x32_bf16 v[112:115], a[216:219], a[16:19], v[112:115]\n"
"v_mfma_f32_16x16x32_bf16 v[116:119], a[220:223], a[16:19], v[116:119]\n"
"buffer_load_dwordx4 v10, s[48:51], s69 offen offset:3072 lds\n"
"v_mfma_f32_16x16x32_bf16 v[116:119], a[40:43], a[248:251], v[116:119]\n"
"v_mfma_f32_16x16x32_bf16 v[120:123], a[44:47], a[248:251], v[120:123]\n"
"s_waitcnt lgkmcnt(0)\n"
"s_add_u32 m0, s24, 0\n"
"v_mfma_f32_16x16x32_bf16 v[120:123], a[224:227], a[16:19], v[120:123]\n"
"s_cmp_le_u32 s65, 3\n"
"s_cselect_b32 s69, 4, 3\n"
"v_mfma_f32_16x16x32_bf16 v[124:127], a[228:231], a[16:19], v[124:127]\n"
"s_add_u32 s69, s69, s66\n"
"buffer_load_dwordx4 a[64:67], v4, s[44:47], s70 offen sc1\n"
"v_mfma_f32_16x16x32_bf16 v[124:127], a[96:99], a[248:251], v[124:127]\n"
"s_lshl_b32 s69, s69, 19\n"
"v_mfma_f32_16x16x32_bf16 v[140:143], a[96:99], a[252:255], v[140:143]\n"
"s_waitcnt vmcnt(15)\n"
"s_add_u32 s69, s69, s26\n"
"v_mfma_f32_16x16x32_bf16 v[140:143], a[228:231], a[20:23], v[140:143]\n"
"s_add_i32 s69, s69, 262144\n"
"v_mfma_f32_16x16x32_bf16 v[136:139], a[224:227], a[20:23], v[136:139]\n"
"buffer_load_dwordx4 v7, s[48:51], s69 offen lds\n"
"v_mfma_f32_16x16x32_bf16 v[136:139], a[44:47], a[252:255], v[136:139]\n"
"v_mfma_f32_16x16x32_bf16 v[132:135], a[40:43], a[252:255], v[132:135]\n"
"v_mfma_f32_16x16x32_bf16 v[132:135], a[220:223], a[20:23], v[132:135]\n"
"v_mfma_f32_16x16x32_bf16 v[128:131], a[216:219], a[20:23], v[128:131]\n"
"buffer_load_dwordx4 a[68:71], v4, s[44:47], s70 offen offset:1024 sc1\n"
"v_mfma_f32_16x16x32_bf16 v[128:131], a[36:39], a[252:255], v[128:131]\n"
"v_mfma_f32_16x16x32_bf16 v[144:147], a[36:39], v[244:247], v[144:147]\n"
"s_waitcnt vmcnt(12)\n"
"ds_read_b64_tr_b16 v[224:225], v11 offset:4096\n"
"v_mfma_f32_16x16x32_bf16 v[144:147], a[216:219], a[24:27], v[144:147]\n"
"ds_read_b64_tr_b16 v[226:227], v11 offset:6144\n"
"v_mfma_f32_16x16x32_bf16 v[148:151], a[220:223], a[24:27], v[148:151]\n"
"buffer_load_dwordx4 v8, s[48:51], s69 offen offset:1024 lds\n"
"v_mfma_f32_16x16x32_bf16 v[148:151], a[40:43], v[244:247], v[148:151]\n"
"ds_read_b64_tr_b16 v[228:229], v12 offset:4096\n"
"v_mfma_f32_16x16x32_bf16 v[152:155], a[44:47], v[244:247], v[152:155]\n"
"ds_read_b64_tr_b16 v[230:231], v12 offset:6144\n"
"v_mfma_f32_16x16x32_bf16 v[152:155], a[224:227], a[24:27], v[152:155]\n"
"ds_read_b64_tr_b16 v[232:233], v13 offset:4096\n"
"v_mfma_f32_16x16x32_bf16 v[156:159], a[228:231], a[24:27], v[156:159]\n"
"buffer_load_dwordx4 a[72:75], v5, s[44:47], s70 offen sc1\n"
"v_mfma_f32_16x16x32_bf16 v[156:159], a[96:99], v[244:247], v[156:159]\n"
"ds_read_b64_tr_b16 v[234:235], v13 offset:6144\n"
"v_mfma_f32_16x16x32_bf16 v[172:175], a[96:99], v[248:251], v[172:175]\n"
"ds_read_b64_tr_b16 v[236:237], v14 offset:4096\n"
"v_mfma_f32_16x16x32_bf16 v[172:175], a[228:231], a[28:31], v[172:175]\n"
"ds_read_b64_tr_b16 v[238:239], v14 offset:6144\n"
"v_mfma_f32_16x16x32_bf16 v[168:171], a[224:227], a[28:31], v[168:171]\n"
"buffer_load_dwordx4 v9, s[48:51], s69 offen offset:2048 lds\n"
"v_mfma_f32_16x16x32_bf16 v[168:171], a[44:47], v[248:251], v[168:171]\n"
"v_mfma_f32_16x16x32_bf16 v[164:167], a[40:43], v[248:251], v[164:167]\n"
"v_mfma_f32_16x16x32_bf16 v[164:167], a[220:223], a[28:31], v[164:167]\n"
"v_mfma_f32_16x16x32_bf16 v[160:163], a[216:219], a[28:31], v[160:163]\n"
"buffer_load_dwordx4 a[76:79], v5, s[44:47], s70 offen offset:1024 sc1\n"
"v_mfma_f32_16x16x32_bf16 v[160:163], a[36:39], v[248:251], v[160:163]\n"
"v_mfma_f32_16x16x32_bf16 v[176:179], a[36:39], v[252:255], v[176:179]\n"
"s_waitcnt vmcnt(15)\n"
"v_mfma_f32_16x16x32_bf16 v[176:179], a[216:219], a[32:35], v[176:179]\n"
"v_mfma_f32_16x16x32_bf16 v[180:183], a[220:223], a[32:35], v[180:183]\n"
"buffer_load_dwordx4 v10, s[48:51], s69 offen offset:3072 lds\n"
"v_mfma_f32_16x16x32_bf16 v[180:183], a[40:43], v[252:255], v[180:183]\n"
"v_mfma_f32_16x16x32_bf16 v[184:187], a[44:47], v[252:255], v[184:187]\n"
"v_mfma_f32_16x16x32_bf16 v[184:187], a[224:227], a[32:35], v[184:187]\n"
"v_mfma_f32_16x16x32_bf16 v[188:191], a[228:231], a[32:35], v[188:191]\n"
"buffer_load_dwordx4 a[80:83], v6, s[44:47], s70 offen sc1\n"
"v_mfma_f32_16x16x32_bf16 v[188:191], a[96:99], v[252:255], v[188:191]\n"
"v_mfma_f32_16x16x32_bf16 v[192:195], a[232:235], a[84:87], v[192:195]\n"
"s_waitcnt lgkmcnt(0)\n"
"s_add_u32 m0, s24, 4096\n"
"s_cmp_le_u32 s65, 2\n"
"s_cselect_b32 s69, 3, 2\n"
"s_add_u32 s69, s69, s66\n"
"v_mfma_f32_16x16x32_bf16 v[192:195], v[224:227], a[0:3], v[192:195]\n"
"s_lshl_b32 s69, s69, 19\n"
"v_add_u32 v33, 4096, v45\n"
"v_mfma_f32_16x16x32_bf16 v[196:199], v[228:231], a[0:3], v[196:199]\n"
"s_mov_b32 s30, 3407872\n"
"s_add_u32 s69, s69, s31\n"
"v_mfma_f32_16x16x32_bf16 v[196:199], a[236:239], a[84:87], v[196:199]\n"
"s_cmp_le_u32 s65, 3\n"
"s_cselect_b32 s71, 4, 3\n"
"s_add_i32 s69, s69, 262144\n"
"buffer_load_dword v44, v24, s[48:51], s30 offen\n"
"v_add_u32 v36, 8192, v45\n"
"v_mfma_f32_16x16x32_bf16 v[200:203], a[240:243], a[84:87], v[200:203]\n"
"s_add_u32 s71, s71, s66\n"
"buffer_load_dwordx4 v7, s[48:51], s69 offen lds\n"
"v_mfma_f32_16x16x32_bf16 v[200:203], v[232:235], a[0:3], v[200:203]\n"
"s_mul_i32 s71, s71, 73728\n"
"s_add_u32 s29, s64, 1\n"
"s_and_b32 s29, s29, 3\n"
"s_mul_i32 s29, s29, 24576\n"
"s_add_u32 s29, s29, 49152\n"
"v_add_u32 v26, s29, v45\n"
"v_mfma_f32_16x16x32_bf16 v[204:207], v[236:239], a[0:3], v[204:207]\n"
"s_add_u32 s71, s71, s25\n"
"v_mfma_f32_16x16x32_bf16 v[204:207], a[244:247], a[84:87], v[204:207]\n"
"s_add_u32 s71, s71, 36864\n"
"s_add_u32 s29, s64, 2\n"
"s_and_b32 s29, s29, 3\n"
"s_mul_i32 s29, s29, 24576\n"
"s_add_u32 s29, s29, 57344\n"
"v_add_u32 v27, s29, v45\n"
"v_mfma_f32_16x16x32_bf16 v[220:223], a[244:247], a[88:91], v[220:223]\n"
"buffer_load_dwordx4 a[84:87], v2, s[44:47], s71 offen sc1\n"
"v_mfma_f32_16x16x32_bf16 v[220:223], v[236:239], a[4:7], v[220:223]\n"
"s_add_u32 s29, s64, 3\n"
"s_and_b32 s29, s29, 3\n"
"s_mul_i32 s29, s29, 24576\n"
"s_add_u32 s29, s29, 65536\n"
"v_add_u32 v28, s29, v45\n"
"v_mfma_f32_16x16x32_bf16 v[216:219], v[232:235], a[4:7], v[216:219]\n"
"v_mfma_f32_16x16x32_bf16 v[216:219], a[240:243], a[88:91], v[216:219]\n"
"s_lshl_b32 s29, s64, 10\n"
"s_add_u32 s29, s29, 147456\n"
"v_add_u32 v25, s29, v45\n"
"v_mfma_f32_16x16x32_bf16 v[212:215], a[236:239], a[88:91], v[212:215]\n"
"buffer_load_dwordx4 v8, s[48:51], s69 offen offset:1024 lds\n"
"v_mfma_f32_16x16x32_bf16 v[212:215], v[228:231], a[4:7], v[212:215]\n"
"s_waitcnt vmcnt(13)\n"
"s_mul_i32 s29, s64, 24576\n"
"s_add_u32 s29, s29, 49152\n"
"ds_read_b64_tr_b16 a[36:37], v11 offset:8192\n"
"v_add_u32 v29, s29, v45\n"
"v_mfma_f32_16x16x32_bf16 v[208:211], v[224:227], a[4:7], v[208:211]\n"
"ds_read_b64_tr_b16 a[38:39], v11 offset:10240\n"
"v_mfma_f32_16x16x32_bf16 v[208:211], a[232:235], a[88:91], v[208:211]\n"
"s_lshl_b32 s29, s64, 12\n"
"s_add_u32 s29, s29, 147456\n"
"ds_read_b64_tr_b16 a[40:41], v12 offset:8192\n"
"v_add_u32 v30, s29, v45\n"
"v_mfma_f32_16x16x32_bf16 a[104:107], a[232:235], a[92:95], a[104:107]\n"
"buffer_load_dwordx4 a[88:91], v2, s[44:47], s71 offen offset:1024 sc1\n"
"v_mfma_f32_16x16x32_bf16 a[104:107], v[224:227], a[8:11], a[104:107]\n"
"ds_read_b64_tr_b16 a[42:43], v12 offset:10240\n"
"v_and_b32 v40, 15, v1\n"
"v_mfma_f32_16x16x32_bf16 a[108:111], v[228:231], a[8:11], a[108:111]\n"
"ds_read_b64_tr_b16 a[44:45], v13 offset:8192\n"
"v_mfma_f32_16x16x32_bf16 a[108:111], a[236:239], a[92:95], a[108:111]\n"
"ds_read_b64_tr_b16 a[46:47], v13 offset:10240\n"
"v_mul_u32_u24 v40, 144, v40\n"
"v_mfma_f32_16x16x32_bf16 a[112:115], a[240:243], a[92:95], a[112:115]\n"
"buffer_load_dwordx4 v9, s[48:51], s69 offen offset:2048 lds\n"
"v_mfma_f32_16x16x32_bf16 a[112:115], v[232:235], a[8:11], a[112:115]\n"
"ds_read_b64_tr_b16 a[96:97], v14 offset:8192\n"
"v_lshrrev_b32 v41, 4, v1\n"
"v_mfma_f32_16x16x32_bf16 a[116:119], v[236:239], a[8:11], a[116:119]\n"
"ds_read_b64_tr_b16 a[98:99], v14 offset:10240\n"
"v_mfma_f32_16x16x32_bf16 a[116:119], a[244:247], a[92:95], a[116:119]\n"
"v_lshlrev_b32 v41, 3, v41\n"
"v_mfma_f32_16x16x32_bf16 a[132:135], a[244:247], a[100:103], a[132:135]\n"
"buffer_load_dwordx4 a[92:95], v3, s[44:47], s71 offen sc1\n"
"v_mfma_f32_16x16x32_bf16 a[132:135], v[236:239], a[12:15], a[132:135]\n"
"v_add3_u32 v15, v40, v41, s24\n"
"v_mfma_f32_16x16x32_bf16 a[128:131], v[232:235], a[12:15], a[128:131]\n"
"v_mfma_f32_16x16x32_bf16 a[128:131], a[240:243], a[100:103], a[128:131]\n"
"v_lshrrev_b32 v40, 3, v1\n"
"v_mfma_f32_16x16x32_bf16 a[124:127], a[236:239], a[100:103], a[124:127]\n"
"buffer_load_dwordx4 v10, s[48:51], s69 offen offset:3072 lds\n"
"v_mfma_f32_16x16x32_bf16 a[124:127], v[228:231], a[12:15], a[124:127]\n"
"v_and_b32 v41, 7, v1\n"
"v_mfma_f32_16x16x32_bf16 a[120:123], v[224:227], a[12:15], a[120:123]\n"
"v_mfma_f32_16x16x32_bf16 a[120:123], a[232:235], a[100:103], a[120:123]\n"
"v_lshlrev_b32 v41, 4, v41\n"
"v_mfma_f32_16x16x32_bf16 a[136:139], a[232:235], a[248:251], a[136:139]\n"
"buffer_load_dwordx4 a[100:103], v3, s[44:47], s71 offen offset:1024 sc1\n"
"v_mfma_f32_16x16x32_bf16 a[136:139], v[224:227], a[16:19], a[136:139]\n"
"v_mul_u32_u24 v42, 144, v40\n"
"v_mfma_f32_16x16x32_bf16 a[140:143], v[228:231], a[16:19], a[140:143]\n"
"v_mfma_f32_16x16x32_bf16 a[140:143], a[236:239], a[248:251], a[140:143]\n"
"v_add3_u32 v37, v42, v41, s24\n"
"v_mfma_f32_16x16x32_bf16 a[144:147], a[240:243], a[248:251], a[144:147]\n"
"v_mfma_f32_16x16x32_bf16 a[144:147], v[232:235], a[16:19], a[144:147]\n"
"s_waitcnt lgkmcnt(0)\n"
"v_lshlrev_b32 v40, 11, v40\n"
"v_mfma_f32_16x16x32_bf16 a[148:151], v[236:239], a[16:19], a[148:151]\n"
"s_add_u32 m0, s24, 8192\n"
"v_mfma_f32_16x16x32_bf16 a[148:151], a[244:247], a[248:251], a[148:151]\n"
"s_cmp_le_u32 s65, 3\n"
"s_cselect_b32 s69, 4, 3\n"
"s_lshl_b32 s29, s64, 16\n"
"v_add3_u32 v31, v40, v41, s29\n"
"v_mfma_f32_16x16x32_bf16 a[164:167], a[244:247], a[252:255], a[164:167]\n"
"s_add_u32 s69, s69, s66\n"
"buffer_load_dwordx4 a[248:251], v4, s[44:47], s71 offen sc1\n"
"v_mfma_f32_16x16x32_bf16 a[164:167], v[236:239], a[20:23], a[164:167]\n"
"s_lshl_b32 s69, s69, 19\n"
"v_lshrrev_b32 v40, 1, v1\n"
"v_mfma_f32_16x16x32_bf16 a[160:163], v[232:235], a[20:23], a[160:163]\n"
"s_add_u32 s69, s69, s31\n"
"v_mfma_f32_16x16x32_bf16 a[160:163], a[240:243], a[252:255], a[160:163]\n"
"s_add_i32 s69, s69, 262144\n"
"v_and_b32 v41, 1, v1\n"
"v_mfma_f32_16x16x32_bf16 a[156:159], a[236:239], a[252:255], a[156:159]\n"
"buffer_load_dwordx4 v7, s[48:51], s69 offen lds\n"
"v_mfma_f32_16x16x32_bf16 a[156:159], v[228:231], a[20:23], a[156:159]\n"
"v_lshlrev_b32 v41, 4, v41\n"
"v_mfma_f32_16x16x32_bf16 a[152:155], v[224:227], a[20:23], a[152:155]\n"
"v_mfma_f32_16x16x32_bf16 a[152:155], a[232:235], a[252:255], a[152:155]\n"
"v_mul_u32_u24 v42, 144, v40\n"
"v_mfma_f32_16x16x32_bf16 a[168:171], a[232:235], v[244:247], a[168:171]\n"
"buffer_load_dwordx4 a[252:255], v4, s[44:47], s71 offen offset:1024 sc1\n"
"v_mfma_f32_16x16x32_bf16 a[168:171], v[224:227], a[24:27], a[168:171]\n"
"s_waitcnt vmcnt(13)\n"
"ds_read_b64_tr_b16 a[216:217], v11\n"
"v_add3_u32 v39, v42, v41, s24\n"
"v_mfma_f32_16x16x32_bf16 a[172:175], v[228:231], a[24:27], a[172:175]\n"
"ds_read_b64_tr_b16 a[218:219], v11 offset:2048\n"
"v_mfma_f32_16x16x32_bf16 a[172:175], a[236:239], v[244:247], a[172:175]\n"
"ds_read_b64_tr_b16 a[220:221], v12\n"
"v_lshlrev_b32 v40, 11, v40\n"
"v_mfma_f32_16x16x32_bf16 a[176:179], a[240:243], v[244:247], a[176:179]\n"
"buffer_load_dwordx4 v8, s[48:51], s69 offen offset:1024 lds\n"
"v_mfma_f32_16x16x32_bf16 a[176:179], v[232:235], a[24:27], a[176:179]\n"
"s_lshl_b32 s29, s64, 5\n"
"s_add_u32 s29, s29, 262144\n"
"ds_read_b64_tr_b16 a[222:223], v12 offset:2048\n"
"v_add3_u32 v32, v40, v41, s29\n"
"v_mfma_f32_16x16x32_bf16 a[180:183], v[236:239], a[24:27], a[180:183]\n"
"ds_read_b64_tr_b16 a[224:225], v13\n"
"v_mfma_f32_16x16x32_bf16 a[180:183], a[244:247], v[244:247], a[180:183]\n"
"ds_read_b64_tr_b16 a[226:227], v13 offset:2048\n"
"v_mfma_f32_16x16x32_bf16 a[196:199], a[244:247], v[248:251], a[196:199]\n"
"buffer_load_dwordx4 v[244:247], v5, s[44:47], s71 offen sc1\n"
"v_mfma_f32_16x16x32_bf16 a[196:199], v[236:239], a[28:31], a[196:199]\n"
"ds_read_b64_tr_b16 a[228:229], v14\n"
"v_mfma_f32_16x16x32_bf16 a[192:195], v[232:235], a[28:31], a[192:195]\n"
"ds_read_b64_tr_b16 a[230:231], v14 offset:2048\n"
"v_mfma_f32_16x16x32_bf16 a[192:195], a[240:243], v[248:251], a[192:195]\n"
"v_mfma_f32_16x16x32_bf16 a[188:191], a[236:239], v[248:251], a[188:191]\n"
"buffer_load_dwordx4 v9, s[48:51], s69 offen offset:2048 lds\n"
"v_mfma_f32_16x16x32_bf16 a[188:191], v[228:231], a[28:31], a[188:191]\n"
"v_mfma_f32_16x16x32_bf16 a[184:187], v[224:227], a[28:31], a[184:187]\n"
"v_mfma_f32_16x16x32_bf16 a[184:187], a[232:235], v[248:251], a[184:187]\n"
"v_mfma_f32_16x16x32_bf16 a[200:203], a[232:235], v[252:255], a[200:203]\n"
"buffer_load_dwordx4 v[248:251], v5, s[44:47], s71 offen offset:1024 sc1\n"
"v_mfma_f32_16x16x32_bf16 a[200:203], v[224:227], a[32:35], a[200:203]\n"
"v_mfma_f32_16x16x32_bf16 a[204:207], v[228:231], a[32:35], a[204:207]\n"
"v_mfma_f32_16x16x32_bf16 a[204:207], a[236:239], v[252:255], a[204:207]\n"
"v_mfma_f32_16x16x32_bf16 a[208:211], a[240:243], v[252:255], a[208:211]\n"
"buffer_load_dwordx4 v10, s[48:51], s69 offen offset:3072 lds\n"
"v_mfma_f32_16x16x32_bf16 a[208:211], v[232:235], a[32:35], a[208:211]\n"
"v_mfma_f32_16x16x32_bf16 a[212:215], v[236:239], a[32:35], a[212:215]\n"
"v_mfma_f32_16x16x32_bf16 a[212:215], a[244:247], v[252:255], a[212:215]\n"
"v_mfma_f32_16x16x32_bf16 v[48:51], a[36:39], a[48:51], v[48:51]\n"
"s_waitcnt vmcnt(14) lgkmcnt(3)\n"
"buffer_load_dwordx4 v[252:255], v6, s[44:47], s71 offen sc1\n"
"v_mfma_f32_16x16x32_bf16 v[48:51], a[216:219], a[84:87], v[48:51]\n"
"s_cmp_le_u32 s65, 4\n"
"s_cselect_b32 s70, 5, 4\n"
"v_mfma_f32_16x16x32_bf16 v[52:55], a[220:223], a[84:87], v[52:55]\n"
"s_add_u32 s70, s70, s66\n"
"v_mfma_f32_16x16x32_bf16 v[52:55], a[40:43], a[48:51], v[52:55]\n"
"s_mul_i32 s70, s70, 73728\n"
"s_waitcnt lgkmcnt(0)\n"
"v_mfma_f32_16x16x32_bf16 v[56:59], a[44:47], a[48:51], v[56:59]\n"
"s_add_u32 s70, s70, s25\n"
"s_add_u32 m0, s24, 0\n"
"v_mfma_f32_16x16x32_bf16 v[56:59], a[224:227], a[84:87], v[56:59]\n"
"s_add_u32 s70, s70, 36864\n"
"s_cmp_le_u32 s65, 4\n"
"s_cselect_b32 s69, 5, 4\n"
"v_mfma_f32_16x16x32_bf16 v[60:63], a[228:231], a[84:87], v[60:63]\n"
"s_add_u32 s69, s69, s66\n"
"buffer_load_dwordx4 a[8:11], v3, s[44:47], s70 offen sc1\n"
"v_mfma_f32_16x16x32_bf16 v[60:63], a[96:99], a[48:51], v[60:63]\n"
"s_lshl_b32 s69, s69, 19\n"
"v_mfma_f32_16x16x32_bf16 v[76:79], a[96:99], a[52:55], v[76:79]\n"
"s_waitcnt vmcnt(14)\n"
"s_add_u32 s69, s69, s31\n"
"v_mfma_f32_16x16x32_bf16 v[76:79], a[228:231], a[88:91], v[76:79]\n"
"s_add_i32 s69, s69, 262144\n"
"v_mfma_f32_16x16x32_bf16 v[72:75], a[224:227], a[88:91], v[72:75]\n"
"buffer_load_dwordx4 v7, s[48:51], s69 offen lds\n"
"v_mfma_f32_16x16x32_bf16 v[72:75], a[44:47], a[52:55], v[72:75]\n"
"v_mfma_f32_16x16x32_bf16 v[68:71], a[40:43], a[52:55], v[68:71]\n"
"s_waitcnt vmcnt(12)\n"
"ds_read_b64_tr_b16 a[232:233], v11 offset:4096\n"
"v_mfma_f32_16x16x32_bf16 v[68:71], a[220:223], a[88:91], v[68:71]\n"
"ds_read_b64_tr_b16 a[234:235], v11 offset:6144\n"
"v_mfma_f32_16x16x32_bf16 v[64:67], a[216:219], a[88:91], v[64:67]\n"
"buffer_load_dwordx4 a[12:15], v3, s[44:47], s70 offen offset:1024 sc1\n"
"v_mfma_f32_16x16x32_bf16 v[64:67], a[36:39], a[52:55], v[64:67]\n"
"ds_read_b64_tr_b16 a[236:237], v12 offset:4096\n"
"v_mfma_f32_16x16x32_bf16 v[80:83], a[36:39], a[56:59], v[80:83]\n"
"ds_read_b64_tr_b16 a[238:239], v12 offset:6144\n"
"v_mfma_f32_16x16x32_bf16 v[80:83], a[216:219], a[92:95], v[80:83]\n"
"ds_read_b64_tr_b16 a[240:241], v13 offset:4096\n"
"v_mfma_f32_16x16x32_bf16 v[84:87], a[220:223], a[92:95], v[84:87]\n"
"buffer_load_dwordx4 v8, s[48:51], s69 offen offset:1024 lds\n"
"v_mfma_f32_16x16x32_bf16 v[84:87], a[40:43], a[56:59], v[84:87]\n"
"ds_read_b64_tr_b16 a[242:243], v13 offset:6144\n"
"v_mfma_f32_16x16x32_bf16 v[88:91], a[44:47], a[56:59], v[88:91]\n"
"ds_read_b64_tr_b16 a[244:245], v14 offset:4096\n"
"v_mfma_f32_16x16x32_bf16 v[88:91], a[224:227], a[92:95], v[88:91]\n"
"ds_read_b64_tr_b16 a[246:247], v14 offset:6144\n"
"v_mfma_f32_16x16x32_bf16 v[92:95], a[228:231], a[92:95], v[92:95]\n"
"buffer_load_dwordx4 a[16:19], v4, s[44:47], s70 offen sc1\n"
"v_mfma_f32_16x16x32_bf16 v[92:95], a[96:99], a[56:59], v[92:95]\n"
"v_mfma_f32_16x16x32_bf16 v[108:111], a[96:99], a[60:63], v[108:111]\n"
"s_waitcnt vmcnt(14)\n"
"v_mfma_f32_16x16x32_bf16 v[108:111], a[228:231], a[100:103], v[108:111]\n"
"v_mfma_f32_16x16x32_bf16 v[104:107], a[224:227], a[100:103], v[104:107]\n"
"buffer_load_dwordx4 v9, s[48:51], s69 offen offset:2048 lds\n"
"v_mfma_f32_16x16x32_bf16 v[104:107], a[44:47], a[60:63], v[104:107]\n"
"v_mfma_f32_16x16x32_bf16 v[100:103], a[40:43], a[60:63], v[100:103]\n"
"v_mfma_f32_16x16x32_bf16 v[100:103], a[220:223], a[100:103], v[100:103]\n"
"v_mfma_f32_16x16x32_bf16 v[96:99], a[216:219], a[100:103], v[96:99]\n"
"buffer_load_dwordx4 a[20:23], v4, s[44:47], s70 offen offset:1024 sc1\n"
"v_mfma_f32_16x16x32_bf16 v[96:99], a[36:39], a[60:63], v[96:99]\n"
"v_mfma_f32_16x16x32_bf16 v[112:115], a[36:39], a[64:67], v[112:115]\n"
"s_waitcnt vmcnt(15)\n"
"v_mfma_f32_16x16x32_bf16 v[112:115], a[216:219], a[248:251], v[112:115]\n"
"v_mfma_f32_16x16x32_bf16 v[116:119], a[220:223], a[248:251], v[116:119]\n"
"buffer_load_dwordx4 v10, s[48:51], s69 offen offset:3072 lds\n"
"v_mfma_f32_16x16x32_bf16 v[116:119], a[40:43], a[64:67], v[116:119]\n"
"v_mfma_f32_16x16x32_bf16 v[120:123], a[44:47], a[64:67], v[120:123]\n"
"s_waitcnt lgkmcnt(0)\n"
"s_add_u32 m0, s24, 4096\n"
"v_mfma_f32_16x16x32_bf16 v[120:123], a[224:227], a[248:251], v[120:123]\n"
"s_cmp_le_u32 s65, 5\n"
"s_cselect_b32 s69, 6, 5\n"
"v_mfma_f32_16x16x32_bf16 v[124:127], a[228:231], a[248:251], v[124:127]\n"
"s_add_u32 s69, s69, s66\n"
"buffer_load_dwordx4 a[24:27], v5, s[44:47], s70 offen sc1\n"
"v_mfma_f32_16x16x32_bf16 v[124:127], a[96:99], a[64:67], v[124:127]\n"
"s_lshl_b32 s69, s69, 19\n"
"v_mfma_f32_16x16x32_bf16 v[140:143], a[96:99], a[68:71], v[140:143]\n"
"s_waitcnt vmcnt(15)\n"
"s_add_u32 s69, s69, s31\n"
"v_mfma_f32_16x16x32_bf16 v[140:143], a[228:231], a[252:255], v[140:143]\n"
"s_add_i32 s69, s69, 262144\n"
"v_mfma_f32_16x16x32_bf16 v[136:139], a[224:227], a[252:255], v[136:139]\n"
"buffer_load_dwordx4 v7, s[48:51], s69 offen lds\n"
"v_mfma_f32_16x16x32_bf16 v[136:139], a[44:47], a[68:71], v[136:139]\n"
"v_mfma_f32_16x16x32_bf16 v[132:135], a[40:43], a[68:71], v[132:135]\n"
"v_mfma_f32_16x16x32_bf16 v[132:135], a[220:223], a[252:255], v[132:135]\n"
"v_mfma_f32_16x16x32_bf16 v[128:131], a[216:219], a[252:255], v[128:131]\n"
"buffer_load_dwordx4 a[28:31], v5, s[44:47], s70 offen offset:1024 sc1\n"
"v_mfma_f32_16x16x32_bf16 v[128:131], a[36:39], a[68:71], v[128:131]\n"
"v_mfma_f32_16x16x32_bf16 v[144:147], a[36:39], a[72:75], v[144:147]\n"
"s_waitcnt vmcnt(12)\n"
"ds_read_b64_tr_b16 v[224:225], v11 offset:8192\n"
"v_mfma_f32_16x16x32_bf16 v[144:147], a[216:219], v[244:247], v[144:147]\n"
"ds_read_b64_tr_b16 v[226:227], v11 offset:10240\n"
"v_mfma_f32_16x16x32_bf16 v[148:151], a[220:223], v[244:247], v[148:151]\n"
"buffer_load_dwordx4 v8, s[48:51], s69 offen offset:1024 lds\n"
"v_mfma_f32_16x16x32_bf16 v[148:151], a[40:43], a[72:75], v[148:151]\n"
"ds_read_b64_tr_b16 v[228:229], v12 offset:8192\n"
"v_mfma_f32_16x16x32_bf16 v[152:155], a[44:47], a[72:75], v[152:155]\n"
"ds_read_b64_tr_b16 v[230:231], v12 offset:10240\n"
"v_mfma_f32_16x16x32_bf16 v[152:155], a[224:227], v[244:247], v[152:155]\n"
"ds_read_b64_tr_b16 v[232:233], v13 offset:8192\n"
"v_mfma_f32_16x16x32_bf16 v[156:159], a[228:231], v[244:247], v[156:159]\n"
"buffer_load_dwordx4 a[32:35], v6, s[44:47], s70 offen sc1\n"
"v_mfma_f32_16x16x32_bf16 v[156:159], a[96:99], a[72:75], v[156:159]\n"
"ds_read_b64_tr_b16 v[234:235], v13 offset:10240\n"
"v_mfma_f32_16x16x32_bf16 v[172:175], a[96:99], a[76:79], v[172:175]\n"
"ds_read_b64_tr_b16 v[236:237], v14 offset:8192\n"
"v_mfma_f32_16x16x32_bf16 v[172:175], a[228:231], v[248:251], v[172:175]\n"
"ds_read_b64_tr_b16 v[238:239], v14 offset:10240\n"
"v_mfma_f32_16x16x32_bf16 v[168:171], a[224:227], v[248:251], v[168:171]\n"
"buffer_load_dwordx4 v9, s[48:51], s69 offen offset:2048 lds\n"
"v_mfma_f32_16x16x32_bf16 v[168:171], a[44:47], a[76:79], v[168:171]\n"
"v_mfma_f32_16x16x32_bf16 v[164:167], a[40:43], a[76:79], v[164:167]\n"
"v_mfma_f32_16x16x32_bf16 v[164:167], a[220:223], v[248:251], v[164:167]\n"
"v_mfma_f32_16x16x32_bf16 v[160:163], a[216:219], v[248:251], v[160:163]\n"
"buffer_load_dwordx4 a[0:3], v2, s[44:47], s70 offen sc1\n"
"v_mfma_f32_16x16x32_bf16 v[160:163], a[36:39], a[76:79], v[160:163]\n"
"v_mfma_f32_16x16x32_bf16 v[176:179], a[36:39], a[80:83], v[176:179]\n"
"s_waitcnt vmcnt(15)\n"
"v_mfma_f32_16x16x32_bf16 v[176:179], a[216:219], v[252:255], v[176:179]\n"
"v_mfma_f32_16x16x32_bf16 v[180:183], a[220:223], v[252:255], v[180:183]\n"
"buffer_load_dwordx4 v10, s[48:51], s69 offen offset:3072 lds\n"
"v_mfma_f32_16x16x32_bf16 v[180:183], a[40:43], a[80:83], v[180:183]\n"
"v_mfma_f32_16x16x32_bf16 v[184:187], a[44:47], a[80:83], v[184:187]\n"
"v_mfma_f32_16x16x32_bf16 v[184:187], a[224:227], v[252:255], v[184:187]\n"
"v_mfma_f32_16x16x32_bf16 v[188:191], a[228:231], v[252:255], v[188:191]\n"
"buffer_load_dwordx4 a[4:7], v2, s[44:47], s70 offen offset:1024 sc1\n"
"v_mfma_f32_16x16x32_bf16 v[188:191], a[96:99], a[80:83], v[188:191]\n"
"v_mfma_f32_16x16x32_bf16 a[104:107], a[232:235], a[56:59], a[104:107]\n"
"s_waitcnt lgkmcnt(0)\n"
"s_add_u32 m0, s24, 8192\n"
"s_cmp_le_u32 s65, 6\n"
"s_cselect_b32 s69, 7, 6\n"
"s_add_u32 s69, s69, s66\n"
"s_lshl_b32 s69, s69, 19\n"
"s_add_u32 s69, s69, s31\n"
"v_mfma_f32_16x16x32_bf16 a[104:107], v[224:227], a[92:95], a[104:107]\n"
"s_add_i32 s69, s69, 262144\n"
"v_mfma_f32_16x16x32_bf16 a[108:111], v[228:231], a[92:95], a[108:111]\n"
"buffer_load_dwordx4 v7, s[48:51], s69 offen lds\n"
"v_mfma_f32_16x16x32_bf16 a[108:111], a[236:239], a[56:59], a[108:111]\n"
"s_cmp_le_u32 s65, 5\n"
"s_cselect_b32 s71, 6, 5\n"
"v_mfma_f32_16x16x32_bf16 a[112:115], a[240:243], a[56:59], a[112:115]\n"
"s_add_u32 s71, s71, s66\n"
"buffer_load_dwordx4 v8, s[48:51], s69 offen offset:1024 lds\n"
"v_mfma_f32_16x16x32_bf16 a[112:115], v[232:235], a[92:95], a[112:115]\n"
"s_mul_i32 s71, s71, 73728\n"
"v_mfma_f32_16x16x32_bf16 a[116:119], v[236:239], a[92:95], a[116:119]\n"
"s_add_u32 s71, s71, s25\n"
"buffer_load_dwordx4 v9, s[48:51], s69 offen offset:2048 lds\n"
"v_mfma_f32_16x16x32_bf16 a[116:119], a[244:247], a[56:59], a[116:119]\n"
"s_add_u32 s71, s71, 36864\n"
"s_cmp_le_u32 s65, 6\n"
"s_cselect_b32 s70, 7, 6\n"
"v_mfma_f32_16x16x32_bf16 a[132:135], a[244:247], a[60:63], a[132:135]\n"
"s_add_u32 s70, s70, s66\n"
"buffer_load_dwordx4 a[56:59], v3, s[44:47], s71 offen sc1\n"
"v_mfma_f32_16x16x32_bf16 a[132:135], v[236:239], a[100:103], a[132:135]\n"
"s_mul_i32 s70, s70, 73728\n"
"v_mfma_f32_16x16x32_bf16 a[128:131], v[232:235], a[100:103], a[128:131]\n"
"s_add_u32 s70, s70, s25\n"
"buffer_load_dwordx4 v10, s[48:51], s69 offen offset:3072 lds\n"
"v_mfma_f32_16x16x32_bf16 a[128:131], a[240:243], a[60:63], a[128:131]\n"
"s_add_u32 s70, s70, 36864\n"
"v_mfma_f32_16x16x32_bf16 a[124:127], a[236:239], a[60:63], a[124:127]\n"
"buffer_load_dwordx4 a[92:95], v3, s[44:47], s70 offen sc1\n"
"v_mfma_f32_16x16x32_bf16 a[124:127], v[228:231], a[100:103], a[124:127]\n"
"s_waitcnt vmcnt(15)\n"
"ds_read_b64_tr_b16 a[36:37], v11\n"
"v_mfma_f32_16x16x32_bf16 a[120:123], v[224:227], a[100:103], a[120:123]\n"
"ds_read_b64_tr_b16 a[38:39], v11 offset:2048\n"
"v_mfma_f32_16x16x32_bf16 a[120:123], a[232:235], a[60:63], a[120:123]\n"
"ds_read_b64_tr_b16 a[40:41], v12\n"
"v_mfma_f32_16x16x32_bf16 a[136:139], a[232:235], a[64:67], a[136:139]\n"
"buffer_load_dwordx4 a[60:63], v3, s[44:47], s71 offen offset:1024 sc1\n"
"v_mfma_f32_16x16x32_bf16 a[136:139], v[224:227], a[248:251], a[136:139]\n"
"ds_read_b64_tr_b16 a[42:43], v12 offset:2048\n"
"v_mfma_f32_16x16x32_bf16 a[140:143], v[228:231], a[248:251], a[140:143]\n"
"ds_read_b64_tr_b16 a[44:45], v13\n"
"v_mfma_f32_16x16x32_bf16 a[140:143], a[236:239], a[64:67], a[140:143]\n"
"ds_read_b64_tr_b16 a[46:47], v13 offset:2048\n"
"v_mfma_f32_16x16x32_bf16 a[144:147], a[240:243], a[64:67], a[144:147]\n"
"buffer_load_dwordx4 a[100:103], v3, s[44:47], s70 offen offset:1024 sc1\n"
"v_mfma_f32_16x16x32_bf16 a[144:147], v[232:235], a[248:251], a[144:147]\n"
"ds_read_b64_tr_b16 a[96:97], v14\n"
"v_mfma_f32_16x16x32_bf16 a[148:151], v[236:239], a[248:251], a[148:151]\n"
"ds_read_b64_tr_b16 a[98:99], v14 offset:2048\n"
"v_mfma_f32_16x16x32_bf16 a[148:151], a[244:247], a[64:67], a[148:151]\n"
"v_mfma_f32_16x16x32_bf16 a[164:167], a[244:247], a[68:71], a[164:167]\n"
"buffer_load_dwordx4 a[64:67], v4, s[44:47], s71 offen sc1\n"
"v_mfma_f32_16x16x32_bf16 a[164:167], v[236:239], a[252:255], a[164:167]\n"
"v_mfma_f32_16x16x32_bf16 a[160:163], v[232:235], a[252:255], a[160:163]\n"
"v_mfma_f32_16x16x32_bf16 a[160:163], a[240:243], a[68:71], a[160:163]\n"
"v_mfma_f32_16x16x32_bf16 a[156:159], a[236:239], a[68:71], a[156:159]\n"
"buffer_load_dwordx4 a[248:251], v4, s[44:47], s70 offen sc1\n"
"v_mfma_f32_16x16x32_bf16 a[156:159], v[228:231], a[252:255], a[156:159]\n"
"v_mfma_f32_16x16x32_bf16 a[152:155], v[224:227], a[252:255], a[152:155]\n"
"v_mfma_f32_16x16x32_bf16 a[152:155], a[232:235], a[68:71], a[152:155]\n"
"v_mfma_f32_16x16x32_bf16 a[168:171], a[232:235], a[72:75], a[168:171]\n"
"buffer_load_dwordx4 a[68:71], v4, s[44:47], s71 offen offset:1024 sc1\n"
"v_mfma_f32_16x16x32_bf16 a[168:171], v[224:227], v[244:247], a[168:171]\n"
"v_mfma_f32_16x16x32_bf16 a[172:175], v[228:231], v[244:247], a[172:175]\n"
"v_mfma_f32_16x16x32_bf16 a[172:175], a[236:239], a[72:75], a[172:175]\n"
"v_mfma_f32_16x16x32_bf16 a[176:179], a[240:243], a[72:75], a[176:179]\n"
"buffer_load_dwordx4 a[252:255], v4, s[44:47], s70 offen offset:1024 sc1\n"
"v_mfma_f32_16x16x32_bf16 a[176:179], v[232:235], v[244:247], a[176:179]\n"
"s_waitcnt vmcnt(13)\n"
"ds_read_b64_tr_b16 a[216:217], v11 offset:4096\n"
"v_mfma_f32_16x16x32_bf16 a[180:183], v[236:239], v[244:247], a[180:183]\n"
"ds_read_b64_tr_b16 a[218:219], v11 offset:6144\n"
"v_mfma_f32_16x16x32_bf16 a[180:183], a[244:247], a[72:75], a[180:183]\n"
"ds_read_b64_tr_b16 a[220:221], v12 offset:4096\n"
"v_mfma_f32_16x16x32_bf16 a[196:199], a[244:247], a[76:79], a[196:199]\n"
"buffer_load_dwordx4 a[72:75], v5, s[44:47], s71 offen sc1\n"
"v_mfma_f32_16x16x32_bf16 a[196:199], v[236:239], v[248:251], a[196:199]\n"
"ds_read_b64_tr_b16 a[222:223], v12 offset:6144\n"
"v_mfma_f32_16x16x32_bf16 a[192:195], v[232:235], v[248:251], a[192:195]\n"
"ds_read_b64_tr_b16 a[224:225], v13 offset:4096\n"
"v_mfma_f32_16x16x32_bf16 a[192:195], a[240:243], a[76:79], a[192:195]\n"
"ds_read_b64_tr_b16 a[226:227], v13 offset:6144\n"
"v_mfma_f32_16x16x32_bf16 a[188:191], a[236:239], a[76:79], a[188:191]\n"
"buffer_load_dwordx4 v[244:247], v5, s[44:47], s70 offen sc1\n"
"v_mfma_f32_16x16x32_bf16 a[188:191], v[228:231], v[248:251], a[188:191]\n"
"ds_read_b64_tr_b16 a[228:229], v14 offset:4096\n"
"v_mfma_f32_16x16x32_bf16 a[184:187], v[224:227], v[248:251], a[184:187]\n"
"ds_read_b64_tr_b16 a[230:231], v14 offset:6144\n"
"v_mfma_f32_16x16x32_bf16 a[184:187], a[232:235], a[76:79], a[184:187]\n"
"v_mfma_f32_16x16x32_bf16 a[200:203], a[232:235], a[80:83], a[200:203]\n"
"buffer_load_dwordx4 a[76:79], v5, s[44:47], s71 offen offset:1024 sc1\n"
"v_mfma_f32_16x16x32_bf16 a[200:203], v[224:227], v[252:255], a[200:203]\n"
"v_mfma_f32_16x16x32_bf16 a[204:207], v[228:231], v[252:255], a[204:207]\n"
"v_mfma_f32_16x16x32_bf16 a[204:207], a[236:239], a[80:83], a[204:207]\n"
"v_mfma_f32_16x16x32_bf16 a[208:211], a[240:243], a[80:83], a[208:211]\n"
"buffer_load_dwordx4 v[248:251], v5, s[44:47], s70 offen offset:1024 sc1\n"
"v_mfma_f32_16x16x32_bf16 a[208:211], v[232:235], v[252:255], a[208:211]\n"
"v_mfma_f32_16x16x32_bf16 a[212:215], v[236:239], v[252:255], a[212:215]\n"
"v_mfma_f32_16x16x32_bf16 a[212:215], a[244:247], a[80:83], a[212:215]\n"
"v_mfma_f32_16x16x32_bf16 v[204:207], a[244:247], a[48:51], v[204:207]\n"
"v_mfma_f32_16x16x32_bf16 v[204:207], v[236:239], a[84:87], v[204:207]\n"
"v_mfma_f32_16x16x32_bf16 v[200:203], v[232:235], a[84:87], v[200:203]\n"
"v_mfma_f32_16x16x32_bf16 v[200:203], a[240:243], a[48:51], v[200:203]\n"
"v_mfma_f32_16x16x32_bf16 v[196:199], a[236:239], a[48:51], v[196:199]\n"
"v_mfma_f32_16x16x32_bf16 v[196:199], v[228:231], a[84:87], v[196:199]\n"
"s_waitcnt vmcnt(11)\n"
"ds_read_b64_tr_b16 v[240:241], v11 offset:8192\n"
"v_mfma_f32_16x16x32_bf16 v[192:195], v[224:227], a[84:87], v[192:195]\n"
"ds_read_b64_tr_b16 v[242:243], v11 offset:10240\n"
"v_mfma_f32_16x16x32_bf16 v[192:195], a[232:235], a[48:51], v[192:195]\n"
"ds_read_b64_tr_b16 v[16:17], v12 offset:8192\n"
"v_mfma_f32_16x16x32_bf16 v[208:211], a[232:235], a[52:55], v[208:211]\n"
"ds_read_b64_tr_b16 v[18:19], v12 offset:10240\n"
"v_mfma_f32_16x16x32_bf16 v[208:211], v[224:227], a[88:91], v[208:211]\n"
"ds_read_b64_tr_b16 v[20:21], v13 offset:8192\n"
"v_mfma_f32_16x16x32_bf16 v[212:215], v[228:231], a[88:91], v[212:215]\n"
"ds_read_b64_tr_b16 v[22:23], v13 offset:10240\n"
"v_mfma_f32_16x16x32_bf16 v[212:215], a[236:239], a[52:55], v[212:215]\n"
"ds_read_b64_tr_b16 v[40:41], v14 offset:8192\n"
"v_mfma_f32_16x16x32_bf16 v[216:219], a[240:243], a[52:55], v[216:219]\n"
"ds_read_b64_tr_b16 v[42:43], v14 offset:10240\n"
"v_mfma_f32_16x16x32_bf16 v[216:219], v[232:235], a[88:91], v[216:219]\n"
"v_mfma_f32_16x16x32_bf16 v[220:223], v[236:239], a[88:91], v[220:223]\n"
"v_mfma_f32_16x16x32_bf16 v[220:223], a[244:247], a[52:55], v[220:223]\n"
"s_waitcnt lgkmcnt(8)\n"
"v_mfma_f32_16x16x32_bf16 a[104:107], a[36:39], a[8:11], a[104:107]\n"
"s_add_u32 m0, s24, 0\n"
"buffer_load_dwordx4 a[80:83], v6, s[44:47], s71 offen sc1\n"
"v_mfma_f32_16x16x32_bf16 a[108:111], a[40:43], a[8:11], a[108:111]\n"
"s_cmp_le_u32 s65, 4\n"
"s_cselect_b32 s69, 5, 4\n"
"v_mfma_f32_16x16x32_bf16 a[112:115], a[44:47], a[8:11], a[112:115]\n"
"s_add_u32 s69, s69, s66\n"
"buffer_load_dwordx4 v[252:255], v6, s[44:47], s70 offen sc1\n"
"v_mfma_f32_16x16x32_bf16 a[116:119], a[96:99], a[8:11], a[116:119]\n"
"s_lshl_b32 s69, s69, 19\n"
"v_mfma_f32_16x16x32_bf16 a[104:107], a[216:219], a[56:59], a[104:107]\n"
"s_add_u32 s69, s69, s26\n"
"v_mfma_f32_16x16x32_bf16 a[108:111], a[220:223], a[56:59], a[108:111]\n"
"s_add_i32 s69, s69, 262144\n"
"v_mfma_f32_16x16x32_bf16 a[112:115], a[224:227], a[56:59], a[112:115]\n"
"buffer_load_dwordx4 v7, s[48:51], s69 offen lds\n"
"v_mfma_f32_16x16x32_bf16 a[116:119], a[228:231], a[56:59], a[116:119]\n"
"s_waitcnt vmcnt(13) lgkmcnt(2)\n"
"buffer_load_dwordx4 v8, s[48:51], s69 offen offset:1024 lds\n"
"v_mfma_f32_16x16x32_bf16 a[104:107], v[240:243], a[92:95], a[104:107]\n"
"buffer_load_dwordx4 v9, s[48:51], s69 offen offset:2048 lds\n"
"v_mfma_f32_16x16x32_bf16 a[108:111], v[16:19], a[92:95], a[108:111]\n"
"buffer_load_dwordx4 v10, s[48:51], s69 offen offset:3072 lds\n"
"v_mfma_f32_16x16x32_bf16 a[112:115], v[20:23], a[92:95], a[112:115]\n"
"s_waitcnt lgkmcnt(0)\n"
"v_mfma_f32_16x16x32_bf16 a[116:119], v[40:43], a[92:95], a[116:119]\n"
"buffer_load_dwordx4 a[48:51], v2, s[44:47], s71 offen sc1\n"
"v_mfma_f32_16x16x32_bf16 a[132:135], a[96:99], a[12:15], a[132:135]\n"
"s_waitcnt vmcnt(16)\n"
"v_mfma_f32_16x16x32_bf16 a[132:135], a[228:231], a[60:63], a[132:135]\n"
"s_waitcnt vmcnt(15)\n"
"buffer_load_dwordx4 a[84:87], v2, s[44:47], s70 offen sc1\n"
"v_mfma_f32_16x16x32_bf16 a[132:135], v[40:43], a[100:103], a[132:135]\n"
"v_mfma_f32_16x16x32_bf16 a[128:131], v[20:23], a[100:103], a[128:131]\n"
"buffer_load_dwordx4 a[52:55], v2, s[44:47], s71 offen offset:1024 sc1\n"
"v_mfma_f32_16x16x32_bf16 a[128:131], a[224:227], a[60:63], a[128:131]\n"
"buffer_load_dwordx4 a[88:91], v2, s[44:47], s70 offen offset:1024 sc1\n"
"v_mfma_f32_16x16x32_bf16 a[128:131], a[44:47], a[12:15], a[128:131]\n"
"ds_write_b128 v26, a[104:107]\n"
"v_mfma_f32_16x16x32_bf16 a[124:127], a[40:43], a[12:15], a[124:127]\n"
"v_mfma_f32_16x16x32_bf16 a[124:127], a[220:223], a[60:63], a[124:127]\n"
"v_mfma_f32_16x16x32_bf16 a[124:127], v[16:19], a[100:103], a[124:127]\n"
"ds_write_b128 v26, a[108:111] offset:1024\n"
"v_mfma_f32_16x16x32_bf16 a[120:123], v[240:243], a[100:103], a[120:123]\n"
"v_mfma_f32_16x16x32_bf16 a[120:123], a[216:219], a[60:63], a[120:123]\n"
"v_mfma_f32_16x16x32_bf16 a[120:123], a[36:39], a[12:15], a[120:123]\n"
"ds_write_b128 v26, a[112:115] offset:2048\n"
"v_mfma_f32_16x16x32_bf16 a[136:139], a[36:39], a[16:19], a[136:139]\n"
"s_waitcnt vmcnt(17)\n"
"v_mfma_f32_16x16x32_bf16 a[136:139], a[216:219], a[64:67], a[136:139]\n"
"s_waitcnt vmcnt(16)\n"
"v_mfma_f32_16x16x32_bf16 a[136:139], v[240:243], a[248:251], a[136:139]\n"
"ds_write_b128 v26, a[116:119] offset:3072\n"
"v_mfma_f32_16x16x32_bf16 a[140:143], v[16:19], a[248:251], a[140:143]\n"
"v_mfma_f32_16x16x32_bf16 a[140:143], a[220:223], a[64:67], a[140:143]\n"
"v_mfma_f32_16x16x32_bf16 a[140:143], a[40:43], a[16:19], a[140:143]\n"
"ds_write_b128 v26, a[132:135] offset:7168\n"
"v_mfma_f32_16x16x32_bf16 a[144:147], a[44:47], a[16:19], a[144:147]\n"
"v_mfma_f32_16x16x32_bf16 a[144:147], a[224:227], a[64:67], a[144:147]\n"
"v_mfma_f32_16x16x32_bf16 a[144:147], v[20:23], a[248:251], a[144:147]\n"
"ds_write_b128 v26, a[128:131] offset:6144\n"
"v_mfma_f32_16x16x32_bf16 a[148:151], v[40:43], a[248:251], a[148:151]\n"
"v_mfma_f32_16x16x32_bf16 a[148:151], a[228:231], a[64:67], a[148:151]\n"
"v_mfma_f32_16x16x32_bf16 a[148:151], a[96:99], a[16:19], a[148:151]\n"
"ds_write_b128 v26, a[124:127] offset:5120\n"
"v_mfma_f32_16x16x32_bf16 a[164:167], a[96:99], a[20:23], a[164:167]\n"
"s_waitcnt vmcnt(15)\n"
"v_mfma_f32_16x16x32_bf16 a[164:167], a[228:231], a[68:71], a[164:167]\n"
"s_waitcnt vmcnt(14)\n"
"v_mfma_f32_16x16x32_bf16 a[164:167], v[40:43], a[252:255], a[164:167]\n"
"ds_write_b128 v26, a[120:123] offset:4096\n"
"v_mfma_f32_16x16x32_bf16 a[160:163], v[20:23], a[252:255], a[160:163]\n"
"v_mfma_f32_16x16x32_bf16 a[160:163], a[224:227], a[68:71], a[160:163]\n"
"v_mfma_f32_16x16x32_bf16 a[160:163], a[44:47], a[20:23], a[160:163]\n"
"ds_write_b128 v27, a[136:139]\n"
"v_mfma_f32_16x16x32_bf16 a[156:159], a[40:43], a[20:23], a[156:159]\n"
"v_mfma_f32_16x16x32_bf16 a[156:159], a[220:223], a[68:71], a[156:159]\n"
"v_mfma_f32_16x16x32_bf16 a[156:159], v[16:19], a[252:255], a[156:159]\n"
"ds_write_b128 v27, a[140:143] offset:1024\n"
"v_mfma_f32_16x16x32_bf16 a[152:155], v[240:243], a[252:255], a[152:155]\n"
"v_mfma_f32_16x16x32_bf16 a[152:155], a[216:219], a[68:71], a[152:155]\n"
"v_mfma_f32_16x16x32_bf16 a[152:155], a[36:39], a[20:23], a[152:155]\n"
"ds_write_b128 v27, a[144:147] offset:2048\n"
"v_mfma_f32_16x16x32_bf16 a[168:171], a[36:39], a[24:27], a[168:171]\n"
"s_waitcnt vmcnt(13)\n"
"v_mfma_f32_16x16x32_bf16 a[168:171], a[216:219], a[72:75], a[168:171]\n"
"s_waitcnt vmcnt(12)\n"
"v_mfma_f32_16x16x32_bf16 a[168:171], v[240:243], v[244:247], a[168:171]\n"
"ds_write_b128 v27, a[148:151] offset:3072\n"
"v_mfma_f32_16x16x32_bf16 a[172:175], v[16:19], v[244:247], a[172:175]\n"
"v_mfma_f32_16x16x32_bf16 a[172:175], a[220:223], a[72:75], a[172:175]\n"
"v_mfma_f32_16x16x32_bf16 a[172:175], a[40:43], a[24:27], a[172:175]\n"
"ds_write_b128 v27, a[164:167] offset:7168\n"
"v_mfma_f32_16x16x32_bf16 a[176:179], a[44:47], a[24:27], a[176:179]\n"
"v_mfma_f32_16x16x32_bf16 a[176:179], a[224:227], a[72:75], a[176:179]\n"
"v_mfma_f32_16x16x32_bf16 a[176:179], v[20:23], v[244:247], a[176:179]\n"
"ds_write_b128 v27, a[160:163] offset:6144\n"
"v_mfma_f32_16x16x32_bf16 a[180:183], v[40:43], v[244:247], a[180:183]\n"
"v_mfma_f32_16x16x32_bf16 a[180:183], a[228:231], a[72:75], a[180:183]\n"
"v_mfma_f32_16x16x32_bf16 a[180:183], a[96:99], a[24:27], a[180:183]\n"
"ds_write_b128 v27, a[156:159] offset:5120\n"
"v_mfma_f32_16x16x32_bf16 a[196:199], a[96:99], a[28:31], a[196:199]\n"
"s_waitcnt vmcnt(4)\n"
"ds_read_b64_tr_b16 a[232:233], v11\n"
"v_mfma_f32_16x16x32_bf16 a[196:199], a[228:231], a[76:79], a[196:199]\n"
"v_mfma_f32_16x16x32_bf16 a[196:199], v[40:43], v[248:251], a[196:199]\n"
"ds_write_b128 v27, a[152:155] offset:4096\n"
"v_mfma_f32_16x16x32_bf16 a[192:195], v[20:23], v[248:251], a[192:195]\n"
"ds_read_b64_tr_b16 a[234:235], v11 offset:2048\n"
"v_mfma_f32_16x16x32_bf16 a[192:195], a[224:227], a[76:79], a[192:195]\n"
"v_mfma_f32_16x16x32_bf16 a[192:195], a[44:47], a[28:31], a[192:195]\n"
"ds_write_b128 v28, a[168:171]\n"
"v_mfma_f32_16x16x32_bf16 a[188:191], a[40:43], a[28:31], a[188:191]\n"
"ds_read_b64_tr_b16 a[236:237], v12\n"
"v_mfma_f32_16x16x32_bf16 a[188:191], a[220:223], a[76:79], a[188:191]\n"
"v_mfma_f32_16x16x32_bf16 a[188:191], v[16:19], v[248:251], a[188:191]\n"
"ds_write_b128 v28, a[172:175] offset:1024\n"
"v_mfma_f32_16x16x32_bf16 a[184:187], v[240:243], v[248:251], a[184:187]\n"
"ds_read_b64_tr_b16 a[238:239], v12 offset:2048\n"
"v_mfma_f32_16x16x32_bf16 a[184:187], a[216:219], a[76:79], a[184:187]\n"
"v_mfma_f32_16x16x32_bf16 a[184:187], a[36:39], a[28:31], a[184:187]\n"
"ds_write_b128 v28, a[176:179] offset:2048\n"
"v_mfma_f32_16x16x32_bf16 a[200:203], a[36:39], a[32:35], a[200:203]\n"
"ds_read_b64_tr_b16 a[240:241], v13\n"
"v_mfma_f32_16x16x32_bf16 a[200:203], a[216:219], a[80:83], a[200:203]\n"
"v_mfma_f32_16x16x32_bf16 a[200:203], v[240:243], v[252:255], a[200:203]\n"
"ds_write_b128 v28, a[180:183] offset:3072\n"
"v_mfma_f32_16x16x32_bf16 a[204:207], v[16:19], v[252:255], a[204:207]\n"
"ds_read_b64_tr_b16 a[242:243], v13 offset:2048\n"
"v_mfma_f32_16x16x32_bf16 a[204:207], a[220:223], a[80:83], a[204:207]\n"
"v_mfma_f32_16x16x32_bf16 a[204:207], a[40:43], a[32:35], a[204:207]\n"
"ds_write_b128 v28, a[196:199] offset:7168\n"
"v_mfma_f32_16x16x32_bf16 a[208:211], a[44:47], a[32:35], a[208:211]\n"
"ds_read_b64_tr_b16 a[244:245], v14\n"
"v_mfma_f32_16x16x32_bf16 a[208:211], a[224:227], a[80:83], a[208:211]\n"
"v_mfma_f32_16x16x32_bf16 a[208:211], v[20:23], v[252:255], a[208:211]\n"
"ds_write_b128 v28, a[192:195] offset:6144\n"
"v_mfma_f32_16x16x32_bf16 a[212:215], v[40:43], v[252:255], a[212:215]\n"
"ds_read_b64_tr_b16 a[246:247], v14 offset:2048\n"
"v_mfma_f32_16x16x32_bf16 a[212:215], a[228:231], a[80:83], a[212:215]\n"
"v_mfma_f32_16x16x32_bf16 a[212:215], a[96:99], a[32:35], a[212:215]\n"
"ds_write_b128 v28, a[188:191] offset:5120\n"
"v_mfma_f32_16x16x32_bf16 v[204:207], a[96:99], a[0:3], v[204:207]\n"
"s_waitcnt vmcnt(3)\n"
"v_mfma_f32_16x16x32_bf16 v[204:207], a[228:231], a[48:51], v[204:207]\n"
"s_waitcnt vmcnt(2)\n"
"v_mfma_f32_16x16x32_bf16 v[204:207], v[40:43], a[84:87], v[204:207]\n"
"ds_write_b128 v28, a[184:187] offset:4096\n"
"v_mfma_f32_16x16x32_bf16 v[200:203], v[20:23], a[84:87], v[200:203]\n"
"v_mfma_f32_16x16x32_bf16 v[200:203], a[224:227], a[48:51], v[200:203]\n"
"v_mfma_f32_16x16x32_bf16 v[200:203], a[44:47], a[0:3], v[200:203]\n"
"ds_write_b128 v25, a[200:203]\n"
"v_mfma_f32_16x16x32_bf16 v[196:199], a[40:43], a[0:3], v[196:199]\n"
"v_mfma_f32_16x16x32_bf16 v[196:199], a[220:223], a[48:51], v[196:199]\n"
"v_mfma_f32_16x16x32_bf16 v[196:199], v[16:19], a[84:87], v[196:199]\n"
"ds_write_b128 v25, a[204:207] offset:4096\n"
"v_mfma_f32_16x16x32_bf16 v[192:195], v[240:243], a[84:87], v[192:195]\n"
"s_add_u32 m0, s24, 4096\n"
"v_mfma_f32_16x16x32_bf16 v[192:195], a[216:219], a[48:51], v[192:195]\n"
"s_cmp_le_u32 s65, 5\n"
"s_cselect_b32 s69, 6, 5\n"
"v_mfma_f32_16x16x32_bf16 v[192:195], a[36:39], a[0:3], v[192:195]\n"
"s_add_u32 s69, s69, s66\n"
"ds_write_b128 v25, a[208:211] offset:8192\n"
"v_mfma_f32_16x16x32_bf16 v[208:211], a[36:39], a[4:7], v[208:211]\n"
"s_waitcnt vmcnt(1)\n"
"s_lshl_b32 s69, s69, 19\n"
"v_mfma_f32_16x16x32_bf16 v[208:211], a[216:219], a[52:55], v[208:211]\n"
"s_waitcnt vmcnt(0)\n"
"s_add_u32 s69, s69, s26\n"
"v_mfma_f32_16x16x32_bf16 v[208:211], v[240:243], a[88:91], v[208:211]\n"
"s_add_i32 s69, s69, 262144\n"
"ds_write_b128 v25, a[212:215] offset:12288\n"
"v_mfma_f32_16x16x32_bf16 v[212:215], v[16:19], a[88:91], v[212:215]\n"
"buffer_load_dwordx4 v7, s[48:51], s69 offen lds\n"
"v_mfma_f32_16x16x32_bf16 v[212:215], a[220:223], a[52:55], v[212:215]\n"
"buffer_load_dwordx4 v8, s[48:51], s69 offen offset:1024 lds\n"
"v_mfma_f32_16x16x32_bf16 v[212:215], a[40:43], a[4:7], v[212:215]\n"
"buffer_load_dwordx4 v9, s[48:51], s69 offen offset:2048 lds\n"
"v_mfma_f32_16x16x32_bf16 v[216:219], a[44:47], a[4:7], v[216:219]\n"
"buffer_load_dwordx4 v10, s[48:51], s69 offen offset:3072 lds\n"
"v_mfma_f32_16x16x32_bf16 v[216:219], a[224:227], a[52:55], v[216:219]\n"
"s_add_u32 m0, s24, 8192\n"
"s_cmp_le_u32 s65, 6\n"
"s_cselect_b32 s69, 7, 6\n"
"s_add_u32 s69, s69, s66\n"
"s_lshl_b32 s69, s69, 19\n"
"s_add_u32 s69, s69, s26\n"
"s_add_i32 s69, s69, 262144\n"
"buffer_load_dwordx4 v7, s[48:51], s69 offen lds\n"
"v_mfma_f32_16x16x32_bf16 v[216:219], v[20:23], a[88:91], v[216:219]\n"
"buffer_load_dwordx4 v8, s[48:51], s69 offen offset:1024 lds\n"
"v_mfma_f32_16x16x32_bf16 v[220:223], v[40:43], a[88:91], v[220:223]\n"
"buffer_load_dwordx4 v9, s[48:51], s69 offen offset:2048 lds\n"
"v_mfma_f32_16x16x32_bf16 v[220:223], a[228:231], a[52:55], v[220:223]\n"
"buffer_load_dwordx4 v10, s[48:51], s69 offen offset:3072 lds\n"
"v_mfma_f32_16x16x32_bf16 v[220:223], a[96:99], a[4:7], v[220:223]\n"
"s_waitcnt lgkmcnt(0)\n"
"s_barrier\n"
"ds_read_b128 v[224:227], v29\n"
"ds_read_b128 v[228:231], v29 offset:8192\n"
"ds_read_b128 v[232:235], v29 offset:16384\n"
"ds_read_b128 v[236:239], v29 offset:1024\n"
"ds_read_b128 v[16:19], v29 offset:9216\n"
"ds_read_b128 v[20:23], v29 offset:17408\n"
"ds_read_b128 v[240:243], v30\n"
"s_mul_i32 s29, s67, 9216\n"
"s_add_u32 s29, s29, 1179648\n"
"s_waitcnt lgkmcnt(6)\n"
"v_pk_add_f32 v[192:193], v[192:193], v[224:225]\n"
"v_pk_add_f32 v[194:195], v[194:195], v[226:227]\n"
"s_waitcnt lgkmcnt(5)\n"
"v_pk_add_f32 v[192:193], v[192:193], v[228:229]\n"
"v_pk_add_f32 v[194:195], v[194:195], v[230:231]\n"
"s_waitcnt lgkmcnt(4)\n"
"v_pk_add_f32 v[192:193], v[192:193], v[232:233]\n"
"v_pk_add_f32 v[194:195], v[194:195], v[234:235]\n"
"ds_read_b128 v[224:227], v29 offset:2048\n"
"ds_read_b128 v[228:231], v29 offset:10240\n"
"ds_read_b128 v[232:235], v29 offset:18432\n"
"s_waitcnt lgkmcnt(6)\n"
"v_pk_add_f32 v[196:197], v[196:197], v[236:237]\n"
"v_pk_add_f32 v[198:199], v[198:199], v[238:239]\n"
"s_waitcnt lgkmcnt(5)\n"
"v_pk_add_f32 v[196:197], v[196:197], v[16:17]\n"
"v_pk_add_f32 v[198:199], v[198:199], v[18:19]\n"
"s_waitcnt lgkmcnt(4)\n"
"v_pk_add_f32 v[196:197], v[196:197], v[20:21]\n"
"v_pk_add_f32 v[198:199], v[198:199], v[22:23]\n"
"ds_read_b128 v[236:239], v29 offset:3072\n"
"ds_read_b128 v[16:19], v29 offset:11264\n"
"ds_read_b128 v[20:23], v29 offset:19456\n"
"v_cvt_pk_bf16_f32 v192, v192, v193\n"
"v_cvt_pk_bf16_f32 v193, v194, v195\n"
"v_cvt_pk_bf16_f32 v194, v196, v197\n"
"v_cvt_pk_bf16_f32 v195, v198, v199\n"
"buffer_store_dwordx4 v[192:195], v45, s[44:47], s29 offen sc1\n"
"s_waitcnt lgkmcnt(5)\n"
"v_pk_add_f32 v[200:201], v[200:201], v[224:225]\n"
"v_pk_add_f32 v[202:203], v[202:203], v[226:227]\n"
"s_waitcnt lgkmcnt(4)\n"
"v_pk_add_f32 v[200:201], v[200:201], v[228:229]\n"
"v_pk_add_f32 v[202:203], v[202:203], v[230:231]\n"
"s_waitcnt lgkmcnt(3)\n"
"v_pk_add_f32 v[200:201], v[200:201], v[232:233]\n"
"v_pk_add_f32 v[202:203], v[202:203], v[234:235]\n"
"ds_read_b128 v[224:227], v29 offset:4096\n"
"ds_read_b128 v[228:231], v29 offset:12288\n"
"ds_read_b128 v[232:235], v29 offset:20480\n"
"s_waitcnt lgkmcnt(5)\n"
"v_pk_add_f32 v[204:205], v[204:205], v[236:237]\n"
"v_pk_add_f32 v[206:207], v[206:207], v[238:239]\n"
"s_waitcnt lgkmcnt(4)\n"
"v_pk_add_f32 v[204:205], v[204:205], v[16:17]\n"
"v_pk_add_f32 v[206:207], v[206:207], v[18:19]\n"
"s_waitcnt lgkmcnt(3)\n"
"v_pk_add_f32 v[204:205], v[204:205], v[20:21]\n"
"v_pk_add_f32 v[206:207], v[206:207], v[22:23]\n"
"ds_read_b128 v[236:239], v29 offset:5120\n"
"ds_read_b128 v[16:19], v29 offset:13312\n"
"ds_read_b128 v[20:23], v29 offset:21504\n"
"v_cvt_pk_bf16_f32 v200, v200, v201\n"
"v_cvt_pk_bf16_f32 v201, v202, v203\n"
"v_cvt_pk_bf16_f32 v202, v204, v205\n"
"v_cvt_pk_bf16_f32 v203, v206, v207\n"
"buffer_store_dwordx4 v[200:203], v45, s[44:47], s29 offen offset:1024 sc1\n"
"s_waitcnt lgkmcnt(5)\n"
"v_pk_add_f32 v[208:209], v[208:209], v[224:225]\n"
"v_pk_add_f32 v[210:211], v[210:211], v[226:227]\n"
"s_waitcnt lgkmcnt(4)\n"
"v_pk_add_f32 v[208:209], v[208:209], v[228:229]\n"
"v_pk_add_f32 v[210:211], v[210:211], v[230:231]\n"
"s_waitcnt lgkmcnt(3)\n"
"v_pk_add_f32 v[208:209], v[208:209], v[232:233]\n"
"v_pk_add_f32 v[210:211], v[210:211], v[234:235]\n"
"ds_read_b128 v[224:227], v29 offset:6144\n"
"ds_read_b128 v[228:231], v29 offset:14336\n"
"ds_read_b128 v[232:235], v29 offset:22528\n"
"s_waitcnt lgkmcnt(5)\n"
"v_pk_add_f32 v[212:213], v[212:213], v[236:237]\n"
"v_pk_add_f32 v[214:215], v[214:215], v[238:239]\n"
"s_waitcnt lgkmcnt(4)\n"
"v_pk_add_f32 v[212:213], v[212:213], v[16:17]\n"
"v_pk_add_f32 v[214:215], v[214:215], v[18:19]\n"
"s_waitcnt lgkmcnt(3)\n"
"v_pk_add_f32 v[212:213], v[212:213], v[20:21]\n"
"v_pk_add_f32 v[214:215], v[214:215], v[22:23]\n"
"ds_read_b128 v[236:239], v29 offset:7168\n"
"ds_read_b128 v[16:19], v29 offset:15360\n"
"ds_read_b128 v[20:23], v29 offset:23552\n"
"v_cvt_pk_bf16_f32 v208, v208, v209\n"
"v_cvt_pk_bf16_f32 v209, v210, v211\n"
"v_cvt_pk_bf16_f32 v210, v212, v213\n"
"v_cvt_pk_bf16_f32 v211, v214, v215\n"
"buffer_store_dwordx4 v[208:211], v45, s[44:47], s29 offen offset:2048 sc1\n"
"s_waitcnt lgkmcnt(5)\n"
"v_pk_add_f32 v[216:217], v[216:217], v[224:225]\n"
"v_pk_add_f32 v[218:219], v[218:219], v[226:227]\n"
"s_waitcnt lgkmcnt(4)\n"
"v_pk_add_f32 v[216:217], v[216:217], v[228:229]\n"
"v_pk_add_f32 v[218:219], v[218:219], v[230:231]\n"
"s_waitcnt lgkmcnt(3)\n"
"v_pk_add_f32 v[216:217], v[216:217], v[232:233]\n"
"v_pk_add_f32 v[218:219], v[218:219], v[234:235]\n"
"ds_read_b128 v[224:227], v30 offset:1024\n"
"ds_read_b128 v[228:231], v30 offset:2048\n"
"ds_read_b128 v[232:235], v30 offset:3072\n"
"s_waitcnt lgkmcnt(5)\n"
"v_pk_add_f32 v[220:221], v[220:221], v[236:237]\n"
"v_pk_add_f32 v[222:223], v[222:223], v[238:239]\n"
"s_waitcnt lgkmcnt(4)\n"
"v_pk_add_f32 v[220:221], v[220:221], v[16:17]\n"
"v_pk_add_f32 v[222:223], v[222:223], v[18:19]\n"
"s_waitcnt lgkmcnt(3)\n"
"v_pk_add_f32 v[220:221], v[220:221], v[20:21]\n"
"v_pk_add_f32 v[222:223], v[222:223], v[22:23]\n"
"s_waitcnt vmcnt(7)\n"
"ds_read_b64_tr_b16 a[36:37], v11 offset:4096\n"
"ds_read_b64_tr_b16 a[38:39], v11 offset:6144\n"
"ds_read_b64_tr_b16 a[40:41], v12 offset:4096\n"
"ds_read_b64_tr_b16 a[42:43], v12 offset:6144\n"
"ds_read_b64_tr_b16 a[44:45], v13 offset:4096\n"
"ds_read_b64_tr_b16 a[46:47], v13 offset:6144\n"
"ds_read_b64_tr_b16 a[96:97], v14 offset:4096\n"
"ds_read_b64_tr_b16 a[98:99], v14 offset:6144\n"
"v_cvt_pk_bf16_f32 v216, v216, v217\n"
"v_cvt_pk_bf16_f32 v217, v218, v219\n"
"v_cvt_pk_bf16_f32 v218, v220, v221\n"
"v_cvt_pk_bf16_f32 v219, v222, v223\n"
"buffer_store_dwordx4 v[216:219], v45, s[44:47], s29 offen offset:3072 sc1\n"
"s_waitcnt lgkmcnt(9)\n"
"v_pk_add_f32 v[240:241], v[240:241], v[224:225]\n"
"v_pk_add_f32 v[242:243], v[242:243], v[226:227]\n"
"v_pk_add_f32 v[240:241], v[240:241], v[228:229]\n"
"v_pk_add_f32 v[242:243], v[242:243], v[230:231]\n"
"s_waitcnt lgkmcnt(8)\n"
"v_pk_add_f32 v[240:241], v[240:241], v[232:233]\n"
"v_pk_add_f32 v[242:243], v[242:243], v[234:235]\n"
"v_cvt_pk_bf16_f32 v240, v240, v241\n"
"v_cvt_pk_bf16_f32 v241, v242, v243\n"
"buffer_store_dwordx2 v[240:241], v33, s[44:47], s29 offen sc1\n"
"s_waitcnt vmcnt(5)\n"
"ds_read_b64_tr_b16 a[216:217], v11 offset:8192\n"
"ds_read_b64_tr_b16 a[218:219], v11 offset:10240\n"
"ds_read_b64_tr_b16 a[220:221], v12 offset:8192\n"
"ds_read_b64_tr_b16 a[222:223], v12 offset:10240\n"
"ds_read_b64_tr_b16 a[224:225], v13 offset:8192\n"
"ds_read_b64_tr_b16 a[226:227], v13 offset:10240\n"
"ds_read_b64_tr_b16 a[228:229], v14 offset:8192\n"
"ds_read_b64_tr_b16 a[230:231], v14 offset:10240\n"
"v_mfma_f32_16x16x32_bf16 v[80:83], a[232:235], a[8:11], v[80:83]\n"
"v_mfma_f32_16x16x32_bf16 v[84:87], a[236:239], a[8:11], v[84:87]\n"
"v_mfma_f32_16x16x32_bf16 v[88:91], a[240:243], a[8:11], v[88:91]\n"
"v_mfma_f32_16x16x32_bf16 v[92:95], a[244:247], a[8:11], v[92:95]\n"
"s_waitcnt lgkmcnt(8)\n"
"v_mfma_f32_16x16x32_bf16 v[80:83], a[36:39], a[56:59], v[80:83]\n"
"v_mfma_f32_16x16x32_bf16 v[84:87], a[40:43], a[56:59], v[84:87]\n"
"v_mfma_f32_16x16x32_bf16 v[88:91], a[44:47], a[56:59], v[88:91]\n"
"v_mfma_f32_16x16x32_bf16 v[92:95], a[96:99], a[56:59], v[92:95]\n"
"s_waitcnt lgkmcnt(6)\n"
"v_mfma_f32_16x16x32_bf16 v[80:83], a[216:219], a[92:95], v[80:83]\n"
"s_waitcnt lgkmcnt(4)\n"
"v_mfma_f32_16x16x32_bf16 v[84:87], a[220:223], a[92:95], v[84:87]\n"
"s_waitcnt lgkmcnt(2)\n"
"v_mfma_f32_16x16x32_bf16 v[88:91], a[224:227], a[92:95], v[88:91]\n"
"s_waitcnt lgkmcnt(0)\n"
"v_mfma_f32_16x16x32_bf16 v[92:95], a[228:231], a[92:95], v[92:95]\n"
"v_mfma_f32_16x16x32_bf16 v[108:111], a[244:247], a[12:15], v[108:111]\n"
"s_barrier\n"
"v_mfma_f32_16x16x32_bf16 v[108:111], a[96:99], a[60:63], v[108:111]\n"
"v_mfma_f32_16x16x32_bf16 v[108:111], a[228:231], a[100:103], v[108:111]\n"
"v_mfma_f32_16x16x32_bf16 v[104:107], a[224:227], a[100:103], v[104:107]\n"
"v_mfma_f32_16x16x32_bf16 v[104:107], a[44:47], a[60:63], v[104:107]\n"
"v_mfma_f32_16x16x32_bf16 v[104:107], a[240:243], a[12:15], v[104:107]\n"
"ds_write_b128 v26, v[80:83]\n"
"v_mfma_f32_16x16x32_bf16 v[100:103], a[236:239], a[12:15], v[100:103]\n"
"v_mfma_f32_16x16x32_bf16 v[100:103], a[40:43], a[60:63], v[100:103]\n"
"v_mfma_f32_16x16x32_bf16 v[100:103], a[220:223], a[100:103], v[100:103]\n"
"ds_write_b128 v26, v[84:87] offset:1024\n"
"v_mfma_f32_16x16x32_bf16 v[96:99], a[216:219], a[100:103], v[96:99]\n"
"v_mfma_f32_16x16x32_bf16 v[96:99], a[36:39], a[60:63], v[96:99]\n"
"v_mfma_f32_16x16x32_bf16 v[96:99], a[232:235], a[12:15], v[96:99]\n"
"ds_write_b128 v26, v[88:91] offset:2048\n"
"v_mfma_f32_16x16x32_bf16 v[112:115], a[232:235], a[16:19], v[112:115]\n"
"v_mfma_f32_16x16x32_bf16 v[112:115], a[36:39], a[64:67], v[112:115]\n"
"v_mfma_f32_16x16x32_bf16 v[112:115], a[216:219], a[248:251], v[112:115]\n"
"ds_write_b128 v26, v[92:95] offset:3072\n"
"v_mfma_f32_16x16x32_bf16 v[116:119], a[220:223], a[248:251], v[116:119]\n"
"v_mfma_f32_16x16x32_bf16 v[116:119], a[40:43], a[64:67], v[116:119]\n"
"v_mfma_f32_16x16x32_bf16 v[116:119], a[236:239], a[16:19], v[116:119]\n"
"ds_write_b128 v26, v[108:111] offset:7168\n"
"v_mfma_f32_16x16x32_bf16 v[120:123], a[240:243], a[16:19], v[120:123]\n"
"v_mfma_f32_16x16x32_bf16 v[120:123], a[44:47], a[64:67], v[120:123]\n"
"v_mfma_f32_16x16x32_bf16 v[120:123], a[224:227], a[248:251], v[120:123]\n"
"ds_write_b128 v26, v[104:107] offset:6144\n"
"v_mfma_f32_16x16x32_bf16 v[124:127], a[228:231], a[248:251], v[124:127]\n"
"v_mfma_f32_16x16x32_bf16 v[124:127], a[96:99], a[64:67], v[124:127]\n"
"v_mfma_f32_16x16x32_bf16 v[124:127], a[244:247], a[16:19], v[124:127]\n"
"ds_write_b128 v26, v[100:103] offset:5120\n"
"v_mfma_f32_16x16x32_bf16 v[140:143], a[244:247], a[20:23], v[140:143]\n"
"v_mfma_f32_16x16x32_bf16 v[140:143], a[96:99], a[68:71], v[140:143]\n"
"v_mfma_f32_16x16x32_bf16 v[140:143], a[228:231], a[252:255], v[140:143]\n"
"ds_write_b128 v26, v[96:99] offset:4096\n"
"v_mfma_f32_16x16x32_bf16 v[136:139], a[224:227], a[252:255], v[136:139]\n"
"v_mfma_f32_16x16x32_bf16 v[136:139], a[44:47], a[68:71], v[136:139]\n"
"v_mfma_f32_16x16x32_bf16 v[136:139], a[240:243], a[20:23], v[136:139]\n"
"ds_write_b128 v27, v[112:115]\n"
"v_mfma_f32_16x16x32_bf16 v[132:135], a[236:239], a[20:23], v[132:135]\n"
"v_mfma_f32_16x16x32_bf16 v[132:135], a[40:43], a[68:71], v[132:135]\n"
"v_mfma_f32_16x16x32_bf16 v[132:135], a[220:223], a[252:255], v[132:135]\n"
"ds_write_b128 v27, v[116:119] offset:1024\n"
"v_mfma_f32_16x16x32_bf16 v[128:131], a[216:219], a[252:255], v[128:131]\n"
"v_mfma_f32_16x16x32_bf16 v[128:131], a[36:39], a[68:71], v[128:131]\n"
"v_mfma_f32_16x16x32_bf16 v[128:131], a[232:235], a[20:23], v[128:131]\n"
"ds_write_b128 v27, v[120:123] offset:2048\n"
"v_mfma_f32_16x16x32_bf16 v[144:147], a[232:235], a[24:27], v[144:147]\n"
"v_mfma_f32_16x16x32_bf16 v[144:147], a[36:39], a[72:75], v[144:147]\n"
"v_mfma_f32_16x16x32_bf16 v[144:147], a[216:219], v[244:247], v[144:147]\n"
"ds_write_b128 v27, v[124:127] offset:3072\n"
"v_mfma_f32_16x16x32_bf16 v[148:151], a[220:223], v[244:247], v[148:151]\n"
"v_mfma_f32_16x16x32_bf16 v[148:151], a[40:43], a[72:75], v[148:151]\n"
"v_mfma_f32_16x16x32_bf16 v[148:151], a[236:239], a[24:27], v[148:151]\n"
"ds_write_b128 v27, v[140:143] offset:7168\n"
"v_mfma_f32_16x16x32_bf16 v[152:155], a[240:243], a[24:27], v[152:155]\n"
"v_mfma_f32_16x16x32_bf16 v[152:155], a[44:47], a[72:75], v[152:155]\n"
"v_mfma_f32_16x16x32_bf16 v[152:155], a[224:227], v[244:247], v[152:155]\n"
"ds_write_b128 v27, v[136:139] offset:6144\n"
"v_mfma_f32_16x16x32_bf16 v[156:159], a[228:231], v[244:247], v[156:159]\n"
"v_mfma_f32_16x16x32_bf16 v[156:159], a[96:99], a[72:75], v[156:159]\n"
"v_mfma_f32_16x16x32_bf16 v[156:159], a[244:247], a[24:27], v[156:159]\n"
"ds_write_b128 v27, v[132:135] offset:5120\n"
"v_mfma_f32_16x16x32_bf16 v[172:175], a[244:247], a[28:31], v[172:175]\n"
"v_mfma_f32_16x16x32_bf16 v[172:175], a[96:99], a[76:79], v[172:175]\n"
"v_mfma_f32_16x16x32_bf16 v[172:175], a[228:231], v[248:251], v[172:175]\n"
"ds_write_b128 v27, v[128:131] offset:4096\n"
"v_mfma_f32_16x16x32_bf16 v[168:171], a[224:227], v[248:251], v[168:171]\n"
"v_mfma_f32_16x16x32_bf16 v[168:171], a[44:47], a[76:79], v[168:171]\n"
"v_mfma_f32_16x16x32_bf16 v[168:171], a[240:243], a[28:31], v[168:171]\n"
"ds_write_b128 v28, v[144:147]\n"
"v_mfma_f32_16x16x32_bf16 v[164:167], a[236:239], a[28:31], v[164:167]\n"
"v_mfma_f32_16x16x32_bf16 v[164:167], a[40:43], a[76:79], v[164:167]\n"
"v_mfma_f32_16x16x32_bf16 v[164:167], a[220:223], v[248:251], v[164:167]\n"
"ds_write_b128 v28, v[148:151] offset:1024\n"
"v_mfma_f32_16x16x32_bf16 v[160:163], a[216:219], v[248:251], v[160:163]\n"
"v_mfma_f32_16x16x32_bf16 v[160:163], a[36:39], a[76:79], v[160:163]\n"
"v_mfma_f32_16x16x32_bf16 v[160:163], a[232:235], a[28:31], v[160:163]\n"
"ds_write_b128 v28, v[152:155] offset:2048\n"
"v_mfma_f32_16x16x32_bf16 v[176:179], a[232:235], a[32:35], v[176:179]\n"
"s_waitcnt vmcnt(0)\n"
"s_lshl_b32 s29, s67, 4\n"
"s_mov_b64 exec, 1\n"
"buffer_store_dwordx2 v[46:47], off, s[56:59], s29 offset:3072 sc1\n"
"s_mov_b64 exec, -1\n"
"v_mfma_f32_16x16x32_bf16 v[176:179], a[36:39], a[80:83], v[176:179]\n"
"v_mfma_f32_16x16x32_bf16 v[176:179], a[216:219], v[252:255], v[176:179]\n"
"ds_write_b128 v28, v[156:159] offset:3072\n"
"v_mfma_f32_16x16x32_bf16 v[180:183], a[220:223], v[252:255], v[180:183]\n"
"v_mfma_f32_16x16x32_bf16 v[180:183], a[40:43], a[80:83], v[180:183]\n"
"v_mfma_f32_16x16x32_bf16 v[180:183], a[236:239], a[32:35], v[180:183]\n"
"ds_write_b128 v28, v[172:175] offset:7168\n"
"v_mfma_f32_16x16x32_bf16 v[184:187], a[240:243], a[32:35], v[184:187]\n"
"v_mfma_f32_16x16x32_bf16 v[184:187], a[44:47], a[80:83], v[184:187]\n"
"v_mfma_f32_16x16x32_bf16 v[184:187], a[224:227], v[252:255], v[184:187]\n"
"ds_write_b128 v28, v[168:171] offset:6144\n"
"v_mfma_f32_16x16x32_bf16 v[188:191], a[228:231], v[252:255], v[188:191]\n"
"v_mfma_f32_16x16x32_bf16 v[188:191], a[96:99], a[80:83], v[188:191]\n"
"v_mfma_f32_16x16x32_bf16 v[188:191], a[244:247], a[32:35], v[188:191]\n"
"ds_write_b128 v28, v[164:167] offset:5120\n"
"v_mfma_f32_16x16x32_bf16 v[60:63], a[244:247], a[0:3], v[60:63]\n"
"v_mfma_f32_16x16x32_bf16 v[60:63], a[96:99], a[48:51], v[60:63]\n"
"v_mfma_f32_16x16x32_bf16 v[60:63], a[228:231], a[84:87], v[60:63]\n"
"ds_write_b128 v28, v[160:163] offset:4096\n"
"v_mfma_f32_16x16x32_bf16 v[56:59], a[224:227], a[84:87], v[56:59]\n"
"v_mfma_f32_16x16x32_bf16 v[56:59], a[44:47], a[48:51], v[56:59]\n"
"v_mfma_f32_16x16x32_bf16 v[56:59], a[240:243], a[0:3], v[56:59]\n"
"ds_write_b128 v25, v[176:179]\n"
"v_mfma_f32_16x16x32_bf16 v[52:55], a[236:239], a[0:3], v[52:55]\n"
"v_mfma_f32_16x16x32_bf16 v[52:55], a[40:43], a[48:51], v[52:55]\n"
"v_mfma_f32_16x16x32_bf16 v[52:55], a[220:223], a[84:87], v[52:55]\n"
"ds_write_b128 v25, v[180:183] offset:4096\n"
"v_mfma_f32_16x16x32_bf16 v[48:51], a[216:219], a[84:87], v[48:51]\n"
"v_mfma_f32_16x16x32_bf16 v[48:51], a[36:39], a[48:51], v[48:51]\n"
"v_mfma_f32_16x16x32_bf16 v[48:51], a[232:235], a[0:3], v[48:51]\n"
"ds_write_b128 v25, v[184:187] offset:8192\n"
"v_mfma_f32_16x16x32_bf16 v[64:67], a[232:235], a[4:7], v[64:67]\n"
"v_mfma_f32_16x16x32_bf16 v[64:67], a[36:39], a[52:55], v[64:67]\n"
"v_mfma_f32_16x16x32_bf16 v[64:67], a[216:219], a[88:91], v[64:67]\n"
"ds_write_b128 v25, v[188:191] offset:12288\n"
"v_mfma_f32_16x16x32_bf16 v[68:71], a[220:223], a[88:91], v[68:71]\n"
"v_mfma_f32_16x16x32_bf16 v[68:71], a[40:43], a[52:55], v[68:71]\n"
"v_mfma_f32_16x16x32_bf16 v[68:71], a[236:239], a[4:7], v[68:71]\n"
"v_mfma_f32_16x16x32_bf16 v[72:75], a[240:243], a[4:7], v[72:75]\n"
"s_lshl_b32 s29, s68, 4\n"
"buffer_load_dwordx2 v[34:35], off, s[56:59], s29 offset:3072 sc1\n"
"v_mfma_f32_16x16x32_bf16 v[72:75], a[44:47], a[52:55], v[72:75]\n"
"v_mfma_f32_16x16x32_bf16 v[72:75], a[224:227], a[88:91], v[72:75]\n"
"v_mfma_f32_16x16x32_bf16 v[76:79], a[228:231], a[88:91], v[76:79]\n"
"v_mfma_f32_16x16x32_bf16 v[76:79], a[96:99], a[52:55], v[76:79]\n"
"v_mfma_f32_16x16x32_bf16 v[76:79], a[244:247], a[4:7], v[76:79]\n"
"s_waitcnt lgkmcnt(0)\n"
"s_barrier\n"
"ds_read_b128 v[80:83], v29\n"
"ds_read_b128 v[84:87], v29 offset:8192\n"
"ds_read_b128 v[88:91], v29 offset:16384\n"
"ds_read_b128 v[92:95], v29 offset:1024\n"
"ds_read_b128 v[96:99], v29 offset:9216\n"
"ds_read_b128 v[100:103], v29 offset:17408\n"
"ds_read_b128 v[104:107], v29 offset:2048\n"
"ds_read_b128 v[108:111], v29 offset:10240\n"
"ds_read_b128 v[112:115], v29 offset:18432\n"
"ds_read_b128 v[116:119], v29 offset:3072\n"
"ds_read_b128 v[120:123], v29 offset:11264\n"
"ds_read_b128 v[124:127], v29 offset:19456\n"
"ds_read_b128 v[128:131], v29 offset:4096\n"
"ds_read_b128 v[132:135], v29 offset:12288\n"
"ds_read_b128 v[136:139], v29 offset:20480\n"
"ds_read_b128 v[140:143], v29 offset:5120\n"
"ds_read_b128 v[144:147], v29 offset:13312\n"
"ds_read_b128 v[148:151], v29 offset:21504\n"
"ds_read_b128 v[152:155], v29 offset:6144\n"
"ds_read_b128 v[156:159], v29 offset:14336\n"
"ds_read_b128 v[160:163], v29 offset:22528\n"
"ds_read_b128 v[164:167], v29 offset:7168\n"
"ds_read_b128 v[168:171], v29 offset:15360\n"
"ds_read_b128 v[172:175], v29 offset:23552\n"
"ds_read_b128 v[176:179], v30\n"
"ds_read_b128 v[180:183], v30 offset:1024\n"
"ds_read_b128 v[184:187], v30 offset:2048\n"
"ds_read_b128 v[188:191], v30 offset:3072\n"
"s_waitcnt vmcnt(0)\n"
"v_cmp_ne_u64 vcc, v[34:35], v[46:47]\n"
"s_nop 1\n"
"s_cbranch_vccnz L_spin3_%=\n"
"L_spinr3_%=:\n"
"s_mul_i32 s29, s68, 9216\n"
"s_add_u32 s29, s29, 1179648\n"
"buffer_load_dwordx4 v[192:195], v45, s[44:47], s29 offen sc1\n"
"buffer_load_dwordx4 v[200:203], v45, s[44:47], s29 offen offset:1024 sc1\n"
"buffer_load_dwordx4 v[208:211], v45, s[44:47], s29 offen offset:2048 sc1\n"
"buffer_load_dwordx4 v[216:219], v45, s[44:47], s29 offen offset:3072 sc1\n"
"buffer_load_dwordx2 v[240:241], v33, s[44:47], s29 offen sc1\n"
"s_waitcnt lgkmcnt(0)\n"
"v_pk_add_f32 v[48:49], v[48:49], v[80:81]\n"
"v_pk_add_f32 v[50:51], v[50:51], v[82:83]\n"
"v_pk_add_f32 v[84:85], v[84:85], v[88:89]\n"
"v_pk_add_f32 v[86:87], v[86:87], v[90:91]\n"
"v_pk_add_f32 v[48:49], v[48:49], v[84:85]\n"
"v_pk_add_f32 v[50:51], v[50:51], v[86:87]\n"
"v_pk_add_f32 v[52:53], v[52:53], v[92:93]\n"
"v_pk_add_f32 v[54:55], v[54:55], v[94:95]\n"
"v_pk_add_f32 v[96:97], v[96:97], v[100:101]\n"
"v_pk_add_f32 v[98:99], v[98:99], v[102:103]\n"
"v_pk_add_f32 v[52:53], v[52:53], v[96:97]\n"
"v_pk_add_f32 v[54:55], v[54:55], v[98:99]\n"
"v_pk_add_f32 v[56:57], v[56:57], v[104:105]\n"
"v_pk_add_f32 v[58:59], v[58:59], v[106:107]\n"
"v_pk_add_f32 v[108:109], v[108:109], v[112:113]\n"
"v_pk_add_f32 v[110:111], v[110:111], v[114:115]\n"
"v_pk_add_f32 v[56:57], v[56:57], v[108:109]\n"
"v_pk_add_f32 v[58:59], v[58:59], v[110:111]\n"
"v_pk_add_f32 v[60:61], v[60:61], v[116:117]\n"
"v_pk_add_f32 v[62:63], v[62:63], v[118:119]\n"
"v_pk_add_f32 v[120:121], v[120:121], v[124:125]\n"
"v_pk_add_f32 v[122:123], v[122:123], v[126:127]\n"
"v_pk_add_f32 v[60:61], v[60:61], v[120:121]\n"
"v_pk_add_f32 v[62:63], v[62:63], v[122:123]\n"
"v_pk_add_f32 v[64:65], v[64:65], v[128:129]\n"
"v_pk_add_f32 v[66:67], v[66:67], v[130:131]\n"
"v_pk_add_f32 v[132:133], v[132:133], v[136:137]\n"
"v_pk_add_f32 v[134:135], v[134:135], v[138:139]\n"
"v_pk_add_f32 v[64:65], v[64:65], v[132:133]\n"
"v_pk_add_f32 v[66:67], v[66:67], v[134:135]\n"
"v_pk_add_f32 v[68:69], v[68:69], v[140:141]\n"
"v_pk_add_f32 v[70:71], v[70:71], v[142:143]\n"
"v_pk_add_f32 v[144:145], v[144:145], v[148:149]\n"
"v_pk_add_f32 v[146:147], v[146:147], v[150:151]\n"
"v_pk_add_f32 v[68:69], v[68:69], v[144:145]\n"
"v_pk_add_f32 v[70:71], v[70:71], v[146:147]\n"
"v_pk_add_f32 v[72:73], v[72:73], v[152:153]\n"
"v_pk_add_f32 v[74:75], v[74:75], v[154:155]\n"
"v_pk_add_f32 v[156:157], v[156:157], v[160:161]\n"
"v_pk_add_f32 v[158:159], v[158:159], v[162:163]\n"
"v_pk_add_f32 v[72:73], v[72:73], v[156:157]\n"
"v_pk_add_f32 v[74:75], v[74:75], v[158:159]\n"
"v_pk_add_f32 v[76:77], v[76:77], v[164:165]\n"
"v_pk_add_f32 v[78:79], v[78:79], v[166:167]\n"
"v_pk_add_f32 v[168:169], v[168:169], v[172:173]\n"
"v_pk_add_f32 v[170:171], v[170:171], v[174:175]\n"
"v_pk_add_f32 v[76:77], v[76:77], v[168:169]\n"
"v_pk_add_f32 v[78:79], v[78:79], v[170:171]\n"
"v_pk_add_f32 v[176:177], v[176:177], v[180:181]\n"
"v_pk_add_f32 v[178:179], v[178:179], v[182:183]\n"
"v_pk_add_f32 v[184:185], v[184:185], v[188:189]\n"
"v_pk_add_f32 v[186:187], v[186:187], v[190:191]\n"
"v_pk_add_f32 v[176:177], v[176:177], v[184:185]\n"
"v_pk_add_f32 v[178:179], v[178:179], v[186:187]\n"
"s_waitcnt vmcnt(4)\n"
"v_lshlrev_b32 v40, 16, v192\n"
"v_and_b32 v41, 0xffff0000, v192\n"
"v_lshlrev_b32 v42, 16, v193\n"
"v_and_b32 v43, 0xffff0000, v193\n"
"v_pk_add_f32 v[48:49], v[48:49], v[40:41]\n"
"v_pk_add_f32 v[50:51], v[50:51], v[42:43]\n"
"v_cvt_pk_bf16_f32 v48, v48, v49\n"
"v_cvt_pk_bf16_f32 v49, v50, v51\n"
"v_lshlrev_b32 v40, 16, v194\n"
"v_and_b32 v41, 0xffff0000, v194\n"
"v_lshlrev_b32 v42, 16, v195\n"
"v_and_b32 v43, 0xffff0000, v195\n"
"v_pk_add_f32 v[52:53], v[52:53], v[40:41]\n"
"v_pk_add_f32 v[54:55], v[54:55], v[42:43]\n"
"v_cvt_pk_bf16_f32 v52, v52, v53\n"
"v_cvt_pk_bf16_f32 v53, v54, v55\n"
"ds_write_b64 v15, v[48:49]\n"
"ds_write_b64 v15, v[52:53] offset:32\n"
"s_waitcnt vmcnt(3)\n"
"v_lshlrev_b32 v40, 16, v200\n"
"v_and_b32 v41, 0xffff0000, v200\n"
"v_lshlrev_b32 v42, 16, v201\n"
"v_and_b32 v43, 0xffff0000, v201\n"
"v_pk_add_f32 v[56:57], v[56:57], v[40:41]\n"
"v_pk_add_f32 v[58:59], v[58:59], v[42:43]\n"
"v_cvt_pk_bf16_f32 v56, v56, v57\n"
"v_cvt_pk_bf16_f32 v57, v58, v59\n"
"v_lshlrev_b32 v40, 16, v202\n"
"v_and_b32 v41, 0xffff0000, v202\n"
"v_lshlrev_b32 v42, 16, v203\n"
"v_and_b32 v43, 0xffff0000, v203\n"
"v_pk_add_f32 v[60:61], v[60:61], v[40:41]\n"
"v_pk_add_f32 v[62:63], v[62:63], v[42:43]\n"
"v_cvt_pk_bf16_f32 v60, v60, v61\n"
"v_cvt_pk_bf16_f32 v61, v62, v63\n"
"ds_write_b64 v15, v[56:57] offset:64\n"
"ds_write_b64 v15, v[60:61] offset:96\n"
"ds_read_b128 v[80:83], v37\n"
"ds_read_b128 v[84:87], v37 offset:1152\n"
"s_waitcnt vmcnt(2)\n"
"v_lshlrev_b32 v40, 16, v208\n"
"v_and_b32 v41, 0xffff0000, v208\n"
"v_lshlrev_b32 v42, 16, v209\n"
"v_and_b32 v43, 0xffff0000, v209\n"
"v_pk_add_f32 v[64:65], v[64:65], v[40:41]\n"
"v_pk_add_f32 v[66:67], v[66:67], v[42:43]\n"
"v_cvt_pk_bf16_f32 v64, v64, v65\n"
"v_cvt_pk_bf16_f32 v65, v66, v67\n"
"v_lshlrev_b32 v40, 16, v210\n"
"v_and_b32 v41, 0xffff0000, v210\n"
"v_lshlrev_b32 v42, 16, v211\n"
"v_and_b32 v43, 0xffff0000, v211\n"
"v_pk_add_f32 v[68:69], v[68:69], v[40:41]\n"
"v_pk_add_f32 v[70:71], v[70:71], v[42:43]\n"
"v_cvt_pk_bf16_f32 v68, v68, v69\n"
"v_cvt_pk_bf16_f32 v69, v70, v71\n"
"ds_write_b64 v15, v[64:65] offset:2304\n"
"ds_write_b64 v15, v[68:69] offset:2336\n"
"s_mov_b32 s69, 0\n"
"s_waitcnt lgkmcnt(2)\n"
"buffer_store_dwordx4 v[80:83], v31, s[52:55], s69 offen\n"
"s_mov_b32 s69, 16384\n"
"buffer_store_dwordx4 v[84:87], v31, s[52:55], s69 offen\n"
"s_waitcnt vmcnt(3)\n"
"v_lshlrev_b32 v40, 16, v216\n"
"v_and_b32 v41, 0xffff0000, v216\n"
"v_lshlrev_b32 v42, 16, v217\n"
"v_and_b32 v43, 0xffff0000, v217\n"
"v_pk_add_f32 v[72:73], v[72:73], v[40:41]\n"
"v_pk_add_f32 v[74:75], v[74:75], v[42:43]\n"
"v_cvt_pk_bf16_f32 v72, v72, v73\n"
"v_cvt_pk_bf16_f32 v73, v74, v75\n"
"v_lshlrev_b32 v40, 16, v218\n"
"v_and_b32 v41, 0xffff0000, v218\n"
"v_lshlrev_b32 v42, 16, v219\n"
"v_and_b32 v43, 0xffff0000, v219\n"
"v_pk_add_f32 v[76:77], v[76:77], v[40:41]\n"
"v_pk_add_f32 v[78:79], v[78:79], v[42:43]\n"
"v_cvt_pk_bf16_f32 v76, v76, v77\n"
"v_cvt_pk_bf16_f32 v77, v78, v79\n"
"ds_write_b64 v15, v[72:73] offset:2368\n"
"ds_write_b64 v15, v[76:77] offset:2400\n"
"ds_read_b128 v[88:91], v37 offset:2304\n"
"ds_read_b128 v[92:95], v37 offset:3456\n"
"s_waitcnt vmcnt(2)\n"
"v_lshlrev_b32 v40, 16, v240\n"
"v_and_b32 v41, 0xffff0000, v240\n"
"v_lshlrev_b32 v42, 16, v241\n"
"v_and_b32 v43, 0xffff0000, v241\n"
"v_pk_add_f32 v[176:177], v[176:177], v[40:41]\n"
"v_pk_add_f32 v[178:179], v[178:179], v[42:43]\n"
"v_cvt_pk_bf16_f32 v176, v176, v177\n"
"v_cvt_pk_bf16_f32 v177, v178, v179\n"
"ds_write_b64 v15, v[176:177] offset:4608\n"
"ds_read_b128 v[96:99], v39 offset:4608\n"
"s_mov_b32 s69, 32768\n"
"s_waitcnt lgkmcnt(2)\n"
"buffer_store_dwordx4 v[88:91], v31, s[52:55], s69 offen\n"
"s_mov_b32 s69, 49152\n"
"buffer_store_dwordx4 v[92:95], v31, s[52:55], s69 offen\n"
"s_waitcnt lgkmcnt(0)\n"
"buffer_store_dwordx4 v[96:99], v32, s[52:55], 0 offen\n"
"s_mov_b64 exec, 1\n"
"buffer_store_dwordx2 v[46:47], off, s[56:59], s35 offset:0 sc1\n"
"s_mov_b64 exec, -1\n"
"s_branch L_end_%=\n"
"L_helper_%=:\n"
"s_memrealtime s[60:61]\n"
"s_waitcnt lgkmcnt(0)\n"
"s_mov_b32 s63, s60\n"
"s_mov_b32 m0, 49152\n"
"s_mul_i32 s71, s23, 12\n"
"s_add_u32 s71, s76, s71\n"
"s_and_b32 s71, s71, 0x1ffff\n"
"buffer_load_dwordx4 v17, s[36:39], s71 offen nt lds\n"
"buffer_load_dwordx4 v18, s[36:39], s71 offen offset:1024 nt lds\n"
"buffer_load_dwordx4 v19, s[36:39], s71 offen offset:2048 nt lds\n"
"buffer_load_dwordx4 v20, s[36:39], s71 offen offset:3072 nt lds\n"
"s_mov_b32 m0, 53248\n"
"s_mul_i32 s71, s23, 13\n"
"s_add_u32 s71, s76, s71\n"
"s_and_b32 s71, s71, 0x1ffff\n"
"buffer_load_dwordx4 v17, s[36:39], s71 offen nt lds\n"
"buffer_load_dwordx4 v18, s[36:39], s71 offen offset:1024 nt lds\n"
"buffer_load_dwordx4 v19, s[36:39], s71 offen offset:2048 nt lds\n"
"buffer_load_dwordx4 v20, s[36:39], s71 offen offset:3072 nt lds\n"
"s_mov_b32 m0, 57344\n"
"s_mul_i32 s71, s23, 14\n"
"s_add_u32 s71, s76, s71\n"
"s_and_b32 s71, s71, 0x1ffff\n"
"buffer_load_dwordx4 v17, s[36:39], s71 offen nt lds\n"
"buffer_load_dwordx4 v18, s[36:39], s71 offen offset:1024 nt lds\n"
"buffer_load_dwordx4 v19, s[36:39], s71 offen offset:2048 nt lds\n"
"buffer_load_dwordx4 v20, s[36:39], s71 offen offset:3072 nt lds\n"
"s_mov_b32 m0, 61440\n"
"s_mul_i32 s71, s23, 15\n"
"s_add_u32 s71, s76, s71\n"
"s_and_b32 s71, s71, 0x1ffff\n"
"buffer_load_dwordx4 v17, s[36:39], s71 offen nt lds\n"
"buffer_load_dwordx4 v18, s[36:39], s71 offen offset:1024 nt lds\n"
"buffer_load_dwordx4 v19, s[36:39], s71 offen offset:2048 nt lds\n"
"buffer_load_dwordx4 v20, s[36:39], s71 offen offset:3072 nt lds\n"
"s_mov_b32 m0, 0\n"
"s_mul_i32 s71, s23, 16\n"
"s_add_u32 s71, s76, s71\n"
"s_and_b32 s71, s71, 0x1ffff\n"
"buffer_load_dwordx4 v17, s[36:39], s71 offen nt lds\n"
"buffer_load_dwordx4 v18, s[36:39], s71 offen offset:1024 nt lds\n"
"buffer_load_dwordx4 v19, s[36:39], s71 offen offset:2048 nt lds\n"
"buffer_load_dwordx4 v20, s[36:39], s71 offen offset:3072 nt lds\n"
"s_mov_b32 m0, 4096\n"
"s_mul_i32 s71, s23, 17\n"
"s_add_u32 s71, s76, s71\n"
"s_and_b32 s71, s71, 0x1ffff\n"
"buffer_load_dwordx4 v17, s[36:39], s71 offen nt lds\n"
"buffer_load_dwordx4 v18, s[36:39], s71 offen offset:1024 nt lds\n"
"buffer_load_dwordx4 v19, s[36:39], s71 offen offset:2048 nt lds\n"
"buffer_load_dwordx4 v20, s[36:39], s71 offen offset:3072 nt lds\n"
"s_mov_b32 m0, 139264\n"
"s_mul_i32 s71, s23, 16\n"
"s_add_u32 s71, s76, s71\n"
"s_and_b32 s71, s71, 0x1ffff\n"
"buffer_load_dwordx4 v23, s[40:43], s71 offen nt lds\n"
"buffer_load_dwordx4 v24, s[40:43], s71 offen offset:1024 nt lds\n"
"buffer_load_dwordx4 v23, s[40:43], s71 offen offset:2048 nt lds\n"
"buffer_load_dwordx4 v24, s[40:43], s71 offen offset:3072 nt lds\n"
"s_mov_b32 m0, 143360\n"
"s_mul_i32 s71, s23, 17\n"
"s_add_u32 s71, s76, s71\n"
"s_and_b32 s71, s71, 0x1ffff\n"
"buffer_load_dwordx4 v23, s[40:43], s71 offen nt lds\n"
"buffer_load_dwordx4 v24, s[40:43], s71 offen offset:1024 nt lds\n"
"buffer_load_dwordx4 v23, s[40:43], s71 offen offset:2048 nt lds\n"
"buffer_load_dwordx4 v24, s[40:43], s71 offen offset:3072 nt lds\n"
"s_barrier\n"
"s_mul_i32 s71, s23, 16\n"
"s_add_u32 s71, s76, s71\n"
"s_and_b32 s71, s71, 0x1ffff\n"
"s_or_b32 s71, s71, 0x20000\n"
"buffer_load_dwordx4 v[48:51], v17, s[36:39], s71 offen nt\n"
"buffer_load_dwordx4 v[52:55], v18, s[36:39], s71 offen offset:1024 nt\n"
"buffer_load_dwordx4 v[56:59], v19, s[36:39], s71 offen offset:2048 nt\n"
"buffer_load_dwordx4 v[60:63], v20, s[36:39], s71 offen offset:3072 nt\n"
"s_mul_i32 s71, s23, 17\n"
"s_add_u32 s71, s76, s71\n"
"s_and_b32 s71, s71, 0x1ffff\n"
"s_or_b32 s71, s71, 0x20000\n"
"buffer_load_dwordx4 v[64:67], v17, s[36:39], s71 offen nt\n"
"buffer_load_dwordx4 v[68:71], v18, s[36:39], s71 offen offset:1024 nt\n"
"buffer_load_dwordx4 v[72:75], v19, s[36:39], s71 offen offset:2048 nt\n"
"buffer_load_dwordx4 v[76:79], v20, s[36:39], s71 offen offset:3072 nt\n"
"ds_write_b128 v196, v[220:223]\n"
"ds_write_b128 v196, v[224:227] offset:1024\n"
"ds_write_b128 v196, v[228:231] offset:8192\n"
"ds_write_b128 v196, v[232:235] offset:9216\n"
"ds_write_b128 v196, v[236:239] offset:16384\n"
"ds_write_b128 v196, v[240:243] offset:17408\n"
"ds_write_b128 v196, v[244:247] offset:24576\n"
"ds_write_b128 v196, v[248:251] offset:25600\n"
"ds_write_b128 v196, v[252:255] offset:32768\n"
"ds_write_b128 v196, v[200:203] offset:33792\n"
"s_add_u32 s24, s64, 1\n"
"s_and_b32 s24, s24, 3\n"
"s_mul_i32 s24, s24, 12288\n"
"s_mul_i32 s25, s64, 9216\n"
"s_lshl_b32 s26, s64, 16\n"
"s_lshl_b32 s69, s79, 7\n"
"s_add_u32 s26, s26, s69\n"
"s_mov_b32 s27, 0x4b800000\n"
"v_lshlrev_b32 v45, 4, v1\n"
"s_add_u32 s69, s64, 0\n"
"s_and_b32 s69, s69, 3\n"
"s_lshl_b32 s69, s69, 11\n"
"v_add_u32 v2, s69, v45\n"
"s_add_u32 s69, s64, 1\n"
"s_and_b32 s69, s69, 3\n"
"s_lshl_b32 s69, s69, 11\n"
"v_add_u32 v3, s69, v45\n"
"s_add_u32 s69, s64, 2\n"
"s_and_b32 s69, s69, 3\n"
"s_lshl_b32 s69, s69, 11\n"
"v_add_u32 v4, s69, v45\n"
"s_add_u32 s69, s64, 3\n"
"s_and_b32 s69, s69, 3\n"
"s_lshl_b32 s69, s69, 11\n"
"v_add_u32 v5, s69, v45\n"
"v_add_u32 v6, 8192, v45\n"
"s_add_u32 s69, s25, 65536\n"
"v_add_u32 v15, s69, v2\n"
"v_add_u32 v16, s69, v3\n"
"v_add_u32 v21, s69, v4\n"
"v_add_u32 v22, s69, v5\n"
"v_add_u32 v37, s69, v6\n"
"v_lshrrev_b32 v40, 3, v1\n"
"v_and_b32 v41, 7, v1\n"
"v_lshrrev_b32 v42, 1, v40\n"
"v_lshlrev_b32 v42, 1, v42\n"
"v_xor_b32 v42, v42, v41\n"
"v_lshlrev_b32 v42, 4, v42\n"
"v_lshlrev_b32 v43, 11, v40\n"
"v_add_u32 v7, v42, v43\n"
"v_add_u32 v8, 15360, v7\n"
"v_add_u32 v9, 30720, v7\n"
"v_add_u32 v10, 46080, v7\n"
"s_sub_u32 s69, s24, s65\n"
"v_add_u32 v11, s69, v11\n"
"v_add_u32 v12, s69, v12\n"
"v_add_u32 v13, s69, v13\n"
"v_add_u32 v14, s69, v14\n"
"s_xor_b32 s31, s26, 1792\n"
"s_and_b32 s65, s79, 7\n"
"s_and_b32 s66, s79, 8\n"
"s_lshl_b32 s68, s79, 2\n"
"s_add_u32 s68, s68, s64\n"
"s_xor_b32 s67, s68, 56\n"
"s_lshl_b32 s34, s79, 6\n"
"s_cmp_eq_u32 s64, 3\n"
"s_cselect_b32 s34, s34, 0x10000\n"
"s_cmp_eq_u32 s68, 3\n"
"s_cselect_b32 s35, 0, 0x10000\n"
"v_and_b32 v40, 7, v1\n"
"v_add_u32 v40, s66, v40\n"
"v_lshlrev_b32 v38, 6, v40\n"
"v_add_u32 v25, 65536, v45\n"
"v_add_u32 v33, 4096, v45\n"
"v_add_u32 v36, 8192, v45\n"
"s_lshr_b32 s69, s48, 7\n"
"s_sub_u32 s69, 3, s69\n"
"s_and_b32 s69, s69, 7\n"
"s_lshl_b32 s69, s69, 7\n"
"s_lshr_b32 s70, %1, 7\n"
"s_lshl_b32 s70, s70, 10\n"
"s_add_u32 s69, s69, s70\n"
"s_lshr_b32 s70, %1, 6\n"
"s_and_b32 s70, s70, 1\n"
"s_add_u32 s70, s70, s66\n"
"s_lshl_b32 s70, s70, 19\n"
"s_add_u32 s69, s69, s70\n"
"s_lshr_b32 s70, %1, 3\n"
"s_and_b32 s70, s70, 7\n"
"s_lshl_b32 s70, s70, 15\n"
"s_add_u32 s69, s69, s70\n"
"v_lshrrev_b32 v39, 2, v1\n"
"v_lshlrev_b32 v39, 11, v39\n"
"v_add_u32 v39, s69, v39\n"
"L_ch_%=:\n"
"s_getpc_b64 s[4:5]\n"
"s_sub_u32 s4, s4, L_ch_%=-L_join_%=+6144\n"
"s_subb_u32 s5, s5, 0\n"
"s_and_b32 s5, s5, 0xffff\n"
"s_mov_b32 s6, 32768\n"
"s_mov_b32 s7, 0x20000\n"
"s_lshr_b32 s69, %1, 3\n"
"s_and_b32 s69, s69, 7\n"
"s_lshl_b32 s69, s69, 12\n"
"v_lshrrev_b32 v27, 1, v1\n"
"v_lshlrev_b32 v27, 7, v27\n"
"v_add_u32 v27, s69, v27\n"
"v_add_u32 v26, 98304, v45\n"
"L_pace1_%=:\n"
"s_memrealtime s[60:61]\n"
"s_waitcnt lgkmcnt(0)\n"
"s_sub_u32 s60, s60, s63\n"
"s_cmp_lt_u32 s60, 108\n"
"s_cbranch_scc1 L_pace1_%=\n"
"s_barrier\n"
"s_mov_b32 m0, 8192\n"
"s_mul_i32 s71, s23, 18\n"
"s_add_u32 s71, s76, s71\n"
"s_and_b32 s71, s71, 0x1ffff\n"
"buffer_load_dwordx4 v17, s[36:39], s71 offen nt lds\n"
"buffer_load_dwordx4 v18, s[36:39], s71 offen offset:1024 nt lds\n"
"buffer_load_dwordx4 v19, s[36:39], s71 offen offset:2048 nt lds\n"
"buffer_load_dwordx4 v20, s[36:39], s71 offen offset:3072 nt lds\n"
"s_mov_b32 m0, 12288\n"
"s_mul_i32 s71, s23, 19\n"
"s_add_u32 s71, s76, s71\n"
"s_and_b32 s71, s71, 0x1ffff\n"
"buffer_load_dwordx4 v17, s[36:39], s71 offen nt lds\n"
"buffer_load_dwordx4 v18, s[36:39], s71 offen offset:1024 nt lds\n"
"buffer_load_dwordx4 v19, s[36:39], s71 offen offset:2048 nt lds\n"
"buffer_load_dwordx4 v20, s[36:39], s71 offen offset:3072 nt lds\n"
"s_mul_i32 s71, s23, 16\n"
"s_add_u32 s71, s76, s71\n"
"s_and_b32 s71, s71, 0x1ffff\n"
"s_or_b32 s71, s71, 0x20000\n"
"buffer_load_dwordx4 v[80:83], v23, s[40:43], s71 offen nt\n"
"buffer_load_dwordx4 v[84:87], v24, s[40:43], s71 offen offset:1024 nt\n"
"buffer_load_dwordx4 v[88:91], v23, s[40:43], s71 offen offset:2048 nt\n"
"buffer_load_dwordx4 v[92:95], v24, s[40:43], s71 offen offset:3072 nt\n"
"s_mul_i32 s71, s23, 17\n"
"s_add_u32 s71, s76, s71\n"
"s_and_b32 s71, s71, 0x1ffff\n"
"s_or_b32 s71, s71, 0x20000\n"
"buffer_load_dwordx4 v[96:99], v23, s[40:43], s71 offen nt\n"
"buffer_load_dwordx4 v[100:103], v24, s[40:43], s71 offen offset:1024 nt\n"
"buffer_load_dwordx4 v[104:107], v23, s[40:43], s71 offen offset:2048 nt\n"
"buffer_load_dwordx4 v[108:111], v24, s[40:43], s71 offen offset:3072 nt\n"
"L_pace2_%=:\n"
"s_memrealtime s[60:61]\n"
"s_waitcnt lgkmcnt(0)\n"
"s_sub_u32 s60, s60, s63\n"
"s_cmp_lt_u32 s60, 166\n"
"s_cbranch_scc1 L_pace2_%=\n"
"s_barrier\n"
"s_mov_b32 m0, 147456\n"
"s_mul_i32 s71, s23, 18\n"
"s_add_u32 s71, s76, s71\n"
"s_and_b32 s71, s71, 0x1ffff\n"
"buffer_load_dwordx4 v23, s[40:43], s71 offen nt lds\n"
"buffer_load_dwordx4 v24, s[40:43], s71 offen offset:1024 nt lds\n"
"buffer_load_dwordx4 v23, s[40:43], s71 offen offset:2048 nt lds\n"
"buffer_load_dwordx4 v24, s[40:43], s71 offen offset:3072 nt lds\n"
"s_mov_b32 m0, 151552\n"
"s_mul_i32 s71, s23, 19\n"
"s_add_u32 s71, s76, s71\n"
"s_and_b32 s71, s71, 0x1ffff\n"
"buffer_load_dwordx4 v23, s[40:43], s71 offen nt lds\n"
"buffer_load_dwordx4 v24, s[40:43], s71 offen offset:1024 nt lds\n"
"buffer_load_dwordx4 v23, s[40:43], s71 offen offset:2048 nt lds\n"
"buffer_load_dwordx4 v24, s[40:43], s71 offen offset:3072 nt lds\n"
"s_mov_b32 m0, 16384\n"
"s_mul_i32 s71, s23, 20\n"
"s_add_u32 s71, s76, s71\n"
"s_and_b32 s71, s71, 0x1ffff\n"
"buffer_load_dwordx4 v17, s[36:39], s71 offen nt lds\n"
"buffer_load_dwordx4 v18, s[36:39], s71 offen offset:1024 nt lds\n"
"buffer_load_dwordx4 v19, s[36:39], s71 offen offset:2048 nt lds\n"
"buffer_load_dwordx4 v20, s[36:39], s71 offen offset:3072 nt lds\n"
"s_mov_b32 m0, 20480\n"
"s_mul_i32 s71, s23, 21\n"
"s_add_u32 s71, s76, s71\n"
"s_and_b32 s71, s71, 0x1ffff\n"
"buffer_load_dwordx4 v17, s[36:39], s71 offen nt lds\n"
"buffer_load_dwordx4 v18, s[36:39], s71 offen offset:1024 nt lds\n"
"buffer_load_dwordx4 v19, s[36:39], s71 offen offset:2048 nt lds\n"
"buffer_load_dwordx4 v20, s[36:39], s71 offen offset:3072 nt lds\n"
"s_mul_i32 s71, s23, 18\n"
"s_add_u32 s71, s76, s71\n"
"s_and_b32 s71, s71, 0x1ffff\n"
"s_or_b32 s71, s71, 0x20000\n"
"buffer_load_dwordx4 v[112:115], v17, s[36:39], s71 offen nt\n"
"buffer_load_dwordx4 v[116:119], v18, s[36:39], s71 offen offset:1024 nt\n"
"buffer_load_dwordx4 v[120:123], v19, s[36:39], s71 offen offset:2048 nt\n"
"buffer_load_dwordx4 v[124:127], v20, s[36:39], s71 offen offset:3072 nt\n"
"L_pace3_%=:\n"
"s_memrealtime s[60:61]\n"
"s_waitcnt lgkmcnt(0)\n"
"s_sub_u32 s60, s60, s63\n"
"s_cmp_lt_u32 s60, 224\n"
"s_cbranch_scc1 L_pace3_%=\n"
"s_waitcnt vmcnt(62)\n"
"s_barrier\n"
"s_mov_b32 m0, 155648\n"
"s_mul_i32 s71, s23, 20\n"
"s_add_u32 s71, s76, s71\n"
"s_and_b32 s71, s71, 0x1ffff\n"
"buffer_load_dwordx4 v23, s[40:43], s71 offen nt lds\n"
"buffer_load_dwordx4 v24, s[40:43], s71 offen offset:1024 nt lds\n"
"buffer_load_dwordx4 v23, s[40:43], s71 offen offset:2048 nt lds\n"
"buffer_load_dwordx4 v24, s[40:43], s71 offen offset:3072 nt lds\n"
"s_mov_b32 m0, 159744\n"
"s_mul_i32 s71, s23, 21\n"
"s_add_u32 s71, s76, s71\n"
"s_and_b32 s71, s71, 0x1ffff\n"
"buffer_load_dwordx4 v23, s[40:43], s71 offen nt lds\n"
"buffer_load_dwordx4 v24, s[40:43], s71 offen offset:1024 nt lds\n"
"buffer_load_dwordx4 v23, s[40:43], s71 offen offset:2048 nt lds\n"
"buffer_load_dwordx4 v24, s[40:43], s71 offen offset:3072 nt lds\n"
"s_mov_b32 m0, 24576\n"
"s_mul_i32 s71, s23, 22\n"
"s_add_u32 s71, s76, s71\n"
"s_and_b32 s71, s71, 0x1ffff\n"
"buffer_load_dwordx4 v17, s[36:39], s71 offen nt lds\n"
"buffer_load_dwordx4 v18, s[36:39], s71 offen offset:1024 nt lds\n"
"buffer_load_dwordx4 v19, s[36:39], s71 offen offset:2048 nt lds\n"
"buffer_load_dwordx4 v20, s[36:39], s71 offen offset:3072 nt lds\n"
"s_mov_b32 m0, 28672\n"
"s_mul_i32 s71, s23, 23\n"
"s_add_u32 s71, s76, s71\n"
"s_and_b32 s71, s71, 0x1ffff\n"
"buffer_load_dwordx4 v17, s[36:39], s71 offen nt lds\n"
"buffer_load_dwordx4 v18, s[36:39], s71 offen offset:1024 nt lds\n"
"buffer_load_dwordx4 v19, s[36:39], s71 offen offset:2048 nt lds\n"
"buffer_load_dwordx4 v20, s[36:39], s71 offen offset:3072 nt lds\n"
"s_mul_i32 s71, s23, 19\n"
"s_add_u32 s71, s76, s71\n"
"s_and_b32 s71, s71, 0x1ffff\n"
"s_or_b32 s71, s71, 0x20000\n"
"buffer_load_dwordx4 v[128:131], v17, s[36:39], s71 offen nt\n"
"buffer_load_dwordx4 v[132:135], v18, s[36:39], s71 offen offset:1024 nt\n"
"buffer_load_dwordx4 v[136:139], v19, s[36:39], s71 offen offset:2048 nt\n"
"buffer_load_dwordx4 v[140:143], v20, s[36:39], s71 offen offset:3072 nt\n"
"L_pace4_%=:\n"
"s_memrealtime s[60:61]\n"
"s_waitcnt lgkmcnt(0)\n"
"s_sub_u32 s60, s60, s63\n"
"s_cmp_lt_u32 s60, 282\n"
"s_cbranch_scc1 L_pace4_%=\n"
"s_barrier\n"
"s_mov_b32 m0, 98304\n"
"s_mul_i32 s71, s23, 22\n"
"s_add_u32 s71, s76, s71\n"
"s_and_b32 s71, s71, 0x1ffff\n"
"buffer_load_dwordx4 v23, s[40:43], s71 offen nt lds\n"
"buffer_load_dwordx4 v24, s[40:43], s71 offen offset:1024 nt lds\n"
"buffer_load_dwordx4 v23, s[40:43], s71 offen offset:2048 nt lds\n"
"buffer_load_dwordx4 v24, s[40:43], s71 offen offset:3072 nt lds\n"
"s_mov_b32 m0, 102400\n"
"s_mul_i32 s71, s23, 23\n"
"s_add_u32 s71, s76, s71\n"
"s_and_b32 s71, s71, 0x1ffff\n"
"buffer_load_dwordx4 v23, s[40:43], s71 offen nt lds\n"
"buffer_load_dwordx4 v24, s[40:43], s71 offen offset:1024 nt lds\n"
"buffer_load_dwordx4 v23, s[40:43], s71 offen offset:2048 nt lds\n"
"buffer_load_dwordx4 v24, s[40:43], s71 offen offset:3072 nt lds\n"
"s_mov_b32 m0, 32768\n"
"s_mul_i32 s71, s23, 24\n"
"s_add_u32 s71, s76, s71\n"
"s_and_b32 s71, s71, 0x1ffff\n"
"buffer_load_dwordx4 v17, s[36:39], s71 offen nt lds\n"
"buffer_load_dwordx4 v18, s[36:39], s71 offen offset:1024 nt lds\n"
"buffer_load_dwordx4 v19, s[36:39], s71 offen offset:2048 nt lds\n"
"buffer_load_dwordx4 v20, s[36:39], s71 offen offset:3072 nt lds\n"
"s_mov_b32 m0, 36864\n"
"s_mul_i32 s71, s23, 25\n"
"s_add_u32 s71, s76, s71\n"
"s_and_b32 s71, s71, 0x1ffff\n"
"buffer_load_dwordx4 v17, s[36:39], s71 offen nt lds\n"
"buffer_load_dwordx4 v18, s[36:39], s71 offen offset:1024 nt lds\n"
"buffer_load_dwordx4 v19, s[36:39], s71 offen offset:2048 nt lds\n"
"buffer_load_dwordx4 v20, s[36:39], s71 offen offset:3072 nt lds\n"
"L_pace5_%=:\n"
"s_memrealtime s[60:61]\n"
"s_waitcnt lgkmcnt(0)\n"
"s_sub_u32 s60, s60, s63\n"
"s_cmp_lt_u32 s60, 340\n"
"s_cbranch_scc1 L_pace5_%=\n"
"s_barrier\n"
"s_mov_b32 m0, 106496\n"
"s_mul_i32 s71, s23, 24\n"
"s_add_u32 s71, s76, s71\n"
"s_and_b32 s71, s71, 0x1ffff\n"
"buffer_load_dwordx4 v23, s[40:43], s71 offen nt lds\n"
"buffer_load_dwordx4 v24, s[40:43], s71 offen offset:1024 nt lds\n"
"buffer_load_dwordx4 v23, s[40:43], s71 offen offset:2048 nt lds\n"
"buffer_load_dwordx4 v24, s[40:43], s71 offen offset:3072 nt lds\n"
"s_mov_b32 m0, 110592\n"
"s_mul_i32 s71, s23, 25\n"
"s_add_u32 s71, s76, s71\n"
"s_and_b32 s71, s71, 0x1ffff\n"
"buffer_load_dwordx4 v23, s[40:43], s71 offen nt lds\n"
"buffer_load_dwordx4 v24, s[40:43], s71 offen offset:1024 nt lds\n"
"buffer_load_dwordx4 v23, s[40:43], s71 offen offset:2048 nt lds\n"
"buffer_load_dwordx4 v24, s[40:43], s71 offen offset:3072 nt lds\n"
"s_mov_b32 m0, 40960\n"
"s_mul_i32 s71, s23, 26\n"
"s_add_u32 s71, s76, s71\n"
"s_and_b32 s71, s71, 0x1ffff\n"
"buffer_load_dwordx4 v17, s[36:39], s71 offen nt lds\n"
"buffer_load_dwordx4 v18, s[36:39], s71 offen offset:1024 nt lds\n"
"buffer_load_dwordx4 v19, s[36:39], s71 offen offset:2048 nt lds\n"
"buffer_load_dwordx4 v20, s[36:39], s71 offen offset:3072 nt lds\n"
"s_mov_b32 m0, 45056\n"
"s_mul_i32 s71, s23, 27\n"
"s_add_u32 s71, s76, s71\n"
"s_and_b32 s71, s71, 0x1ffff\n"
"buffer_load_dwordx4 v17, s[36:39], s71 offen nt lds\n"
"buffer_load_dwordx4 v18, s[36:39], s71 offen offset:1024 nt lds\n"
"buffer_load_dwordx4 v19, s[36:39], s71 offen offset:2048 nt lds\n"
"buffer_load_dwordx4 v20, s[36:39], s71 offen offset:3072 nt lds\n"
"L_pace6_%=:\n"
"s_memrealtime s[60:61]\n"
"s_waitcnt lgkmcnt(0)\n"
"s_sub_u32 s60, s60, s63\n"
"s_cmp_lt_u32 s60, 398\n"
"s_cbranch_scc1 L_pace6_%=\n"
"s_waitcnt vmcnt(62)\n"
"s_barrier\n"
"s_mov_b32 m0, 114688\n"
"s_mul_i32 s71, s23, 26\n"
"s_add_u32 s71, s76, s71\n"
"s_and_b32 s71, s71, 0x1ffff\n"
"buffer_load_dwordx4 v23, s[40:43], s71 offen nt lds\n"
"buffer_load_dwordx4 v24, s[40:43], s71 offen offset:1024 nt lds\n"
"buffer_load_dwordx4 v23, s[40:43], s71 offen offset:2048 nt lds\n"
"buffer_load_dwordx4 v24, s[40:43], s71 offen offset:3072 nt lds\n"
"s_mov_b32 m0, 118784\n"
"s_mul_i32 s71, s23, 27\n"
"s_add_u32 s71, s76, s71\n"
"s_and_b32 s71, s71, 0x1ffff\n"
"buffer_load_dwordx4 v23, s[40:43], s71 offen nt lds\n"
"buffer_load_dwordx4 v24, s[40:43], s71 offen offset:1024 nt lds\n"
"buffer_load_dwordx4 v23, s[40:43], s71 offen offset:2048 nt lds\n"
"buffer_load_dwordx4 v24, s[40:43], s71 offen offset:3072 nt lds\n"
"s_mov_b32 m0, 49152\n"
"s_mul_i32 s71, s23, 28\n"
"s_add_u32 s71, s76, s71\n"
"s_and_b32 s71, s71, 0x1ffff\n"
"buffer_load_dwordx4 v17, s[36:39], s71 offen nt lds\n"
"buffer_load_dwordx4 v18, s[36:39], s71 offen offset:1024 nt lds\n"
"buffer_load_dwordx4 v19, s[36:39], s71 offen offset:2048 nt lds\n"
"buffer_load_dwordx4 v20, s[36:39], s71 offen offset:3072 nt lds\n"
"s_mov_b32 m0, 53248\n"
"s_mul_i32 s71, s23, 29\n"
"s_add_u32 s71, s76, s71\n"
"s_and_b32 s71, s71, 0x1ffff\n"
"buffer_load_dwordx4 v17, s[36:39], s71 offen nt lds\n"
"buffer_load_dwordx4 v18, s[36:39], s71 offen offset:1024 nt lds\n"
"buffer_load_dwordx4 v19, s[36:39], s71 offen offset:2048 nt lds\n"
"buffer_load_dwordx4 v20, s[36:39], s71 offen offset:3072 nt lds\n"
"L_pace7_%=:\n"
"s_memrealtime s[60:61]\n"
"s_waitcnt lgkmcnt(0)\n"
"s_sub_u32 s60, s60, s63\n"
"s_cmp_lt_u32 s60, 456\n"
"s_cbranch_scc1 L_pace7_%=\n"
"s_barrier\n"
"s_mov_b32 m0, 122880\n"
"s_mul_i32 s71, s23, 28\n"
"s_add_u32 s71, s76, s71\n"
"s_and_b32 s71, s71, 0x1ffff\n"
"buffer_load_dwordx4 v23, s[40:43], s71 offen nt lds\n"
"buffer_load_dwordx4 v24, s[40:43], s71 offen offset:1024 nt lds\n"
"buffer_load_dwordx4 v23, s[40:43], s71 offen offset:2048 nt lds\n"
"buffer_load_dwordx4 v24, s[40:43], s71 offen offset:3072 nt lds\n"
"s_mov_b32 m0, 126976\n"
"s_mul_i32 s71, s23, 29\n"
"s_add_u32 s71, s76, s71\n"
"s_and_b32 s71, s71, 0x1ffff\n"
"buffer_load_dwordx4 v23, s[40:43], s71 offen nt lds\n"
"buffer_load_dwordx4 v24, s[40:43], s71 offen offset:1024 nt lds\n"
"buffer_load_dwordx4 v23, s[40:43], s71 offen offset:2048 nt lds\n"
"buffer_load_dwordx4 v24, s[40:43], s71 offen offset:3072 nt lds\n"
"s_mov_b32 m0, 57344\n"
"s_mul_i32 s71, s23, 30\n"
"s_add_u32 s71, s76, s71\n"
"s_and_b32 s71, s71, 0x1ffff\n"
"buffer_load_dwordx4 v17, s[36:39], s71 offen nt lds\n"
"buffer_load_dwordx4 v18, s[36:39], s71 offen offset:1024 nt lds\n"
"buffer_load_dwordx4 v19, s[36:39], s71 offen offset:2048 nt lds\n"
"buffer_load_dwordx4 v20, s[36:39], s71 offen offset:3072 nt lds\n"
"s_mov_b32 m0, 61440\n"
"s_mul_i32 s71, s23, 31\n"
"s_add_u32 s71, s76, s71\n"
"s_and_b32 s71, s71, 0x1ffff\n"
"buffer_load_dwordx4 v17, s[36:39], s71 offen nt lds\n"
"buffer_load_dwordx4 v18, s[36:39], s71 offen offset:1024 nt lds\n"
"buffer_load_dwordx4 v19, s[36:39], s71 offen offset:2048 nt lds\n"
"buffer_load_dwordx4 v20, s[36:39], s71 offen offset:3072 nt lds\n"
"L_pace8_%=:\n"
"s_memrealtime s[60:61]\n"
"s_waitcnt lgkmcnt(0)\n"
"s_sub_u32 s60, s60, s63\n"
"s_cmp_lt_u32 s60, 514\n"
"s_cbranch_scc1 L_pace8_%=\n"
"s_barrier\n"
"s_mov_b32 m0, 131072\n"
"s_mul_i32 s71, s23, 30\n"
"s_add_u32 s71, s76, s71\n"
"s_and_b32 s71, s71, 0x1ffff\n"
"buffer_load_dwordx4 v23, s[40:43], s71 offen nt lds\n"
"buffer_load_dwordx4 v24, s[40:43], s71 offen offset:1024 nt lds\n"
"buffer_load_dwordx4 v23, s[40:43], s71 offen offset:2048 nt lds\n"
"buffer_load_dwordx4 v24, s[40:43], s71 offen offset:3072 nt lds\n"
"s_mov_b32 m0, 135168\n"
"s_mul_i32 s71, s23, 31\n"
"s_add_u32 s71, s76, s71\n"
"s_and_b32 s71, s71, 0x1ffff\n"
"buffer_load_dwordx4 v23, s[40:43], s71 offen nt lds\n"
"buffer_load_dwordx4 v24, s[40:43], s71 offen offset:1024 nt lds\n"
"buffer_load_dwordx4 v23, s[40:43], s71 offen offset:2048 nt lds\n"
"buffer_load_dwordx4 v24, s[40:43], s71 offen offset:3072 nt lds\n"
"s_mov_b32 m0, 0\n"
"s_mul_i32 s71, s23, 0\n"
"s_add_u32 s71, s76, s71\n"
"s_and_b32 s71, s71, 0x1ffff\n"
"s_or_b32 s71, s71, 0x20000\n"
"buffer_load_dwordx4 v17, s[36:39], s71 offen nt lds\n"
"buffer_load_dwordx4 v18, s[36:39], s71 offen offset:1024 nt lds\n"
"buffer_load_dwordx4 v19, s[36:39], s71 offen offset:2048 nt lds\n"
"buffer_load_dwordx4 v20, s[36:39], s71 offen offset:3072 nt lds\n"
"s_mov_b32 m0, 4096\n"
"s_mul_i32 s71, s23, 1\n"
"s_add_u32 s71, s76, s71\n"
"s_and_b32 s71, s71, 0x1ffff\n"
"s_or_b32 s71, s71, 0x20000\n"
"buffer_load_dwordx4 v17, s[36:39], s71 offen nt lds\n"
"buffer_load_dwordx4 v18, s[36:39], s71 offen offset:1024 nt lds\n"
"buffer_load_dwordx4 v19, s[36:39], s71 offen offset:2048 nt lds\n"
"buffer_load_dwordx4 v20, s[36:39], s71 offen offset:3072 nt lds\n"
"L_pace9_%=:\n"
"s_memrealtime s[60:61]\n"
"s_waitcnt lgkmcnt(0)\n"
"s_sub_u32 s60, s60, s63\n"
"s_cmp_lt_u32 s60, 572\n"
"s_cbranch_scc1 L_pace9_%=\n"
"s_waitcnt vmcnt(62)\n"
"s_barrier\n"
"s_mov_b32 m0, 139264\n"
"s_mul_i32 s71, s23, 0\n"
"s_add_u32 s71, s76, s71\n"
"s_and_b32 s71, s71, 0x1ffff\n"
"s_or_b32 s71, s71, 0x20000\n"
"buffer_load_dwordx4 v23, s[40:43], s71 offen nt lds\n"
"buffer_load_dwordx4 v24, s[40:43], s71 offen offset:1024 nt lds\n"
"buffer_load_dwordx4 v23, s[40:43], s71 offen offset:2048 nt lds\n"
"buffer_load_dwordx4 v24, s[40:43], s71 offen offset:3072 nt lds\n"
"s_mov_b32 m0, 143360\n"
"s_mul_i32 s71, s23, 1\n"
"s_add_u32 s71, s76, s71\n"
"s_and_b32 s71, s71, 0x1ffff\n"
"s_or_b32 s71, s71, 0x20000\n"
"buffer_load_dwordx4 v23, s[40:43], s71 offen nt lds\n"
"buffer_load_dwordx4 v24, s[40:43], s71 offen offset:1024 nt lds\n"
"buffer_load_dwordx4 v23, s[40:43], s71 offen offset:2048 nt lds\n"
"buffer_load_dwordx4 v24, s[40:43], s71 offen offset:3072 nt lds\n"
"s_mov_b32 m0, 8192\n"
"s_mul_i32 s71, s23, 2\n"
"s_add_u32 s71, s76, s71\n"
"s_and_b32 s71, s71, 0x1ffff\n"
"s_or_b32 s71, s71, 0x20000\n"
"buffer_load_dwordx4 v17, s[36:39], s71 offen nt lds\n"
"buffer_load_dwordx4 v18, s[36:39], s71 offen offset:1024 nt lds\n"
"buffer_load_dwordx4 v19, s[36:39], s71 offen offset:2048 nt lds\n"
"buffer_load_dwordx4 v20, s[36:39], s71 offen offset:3072 nt lds\n"
"s_mov_b32 m0, 12288\n"
"s_mul_i32 s71, s23, 3\n"
"s_add_u32 s71, s76, s71\n"
"s_and_b32 s71, s71, 0x1ffff\n"
"s_or_b32 s71, s71, 0x20000\n"
"buffer_load_dwordx4 v17, s[36:39], s71 offen nt lds\n"
"buffer_load_dwordx4 v18, s[36:39], s71 offen offset:1024 nt lds\n"
"buffer_load_dwordx4 v19, s[36:39], s71 offen offset:2048 nt lds\n"
"buffer_load_dwordx4 v20, s[36:39], s71 offen offset:3072 nt lds\n"
"L_pace10_%=:\n"
"s_memrealtime s[60:61]\n"
"s_waitcnt lgkmcnt(0)\n"
"s_sub_u32 s60, s60, s63\n"
"s_cmp_lt_u32 s60, 622\n"
"s_cbranch_scc1 L_pace10_%=\n"
"s_barrier\n"
"s_mov_b32 m0, 147456\n"
"s_mul_i32 s71, s23, 2\n"
"s_add_u32 s71, s76, s71\n"
"s_and_b32 s71, s71, 0x1ffff\n"
"s_or_b32 s71, s71, 0x20000\n"
"buffer_load_dwordx4 v23, s[40:43], s71 offen nt lds\n"
"buffer_load_dwordx4 v24, s[40:43], s71 offen offset:1024 nt lds\n"
"buffer_load_dwordx4 v23, s[40:43], s71 offen offset:2048 nt lds\n"
"buffer_load_dwordx4 v24, s[40:43], s71 offen offset:3072 nt lds\n"
"s_mov_b32 m0, 151552\n"
"s_mul_i32 s71, s23, 3\n"
"s_add_u32 s71, s76, s71\n"
"s_and_b32 s71, s71, 0x1ffff\n"
"s_or_b32 s71, s71, 0x20000\n"
"buffer_load_dwordx4 v23, s[40:43], s71 offen nt lds\n"
"buffer_load_dwordx4 v24, s[40:43], s71 offen offset:1024 nt lds\n"
"buffer_load_dwordx4 v23, s[40:43], s71 offen offset:2048 nt lds\n"
"buffer_load_dwordx4 v24, s[40:43], s71 offen offset:3072 nt lds\n"
"s_mov_b32 m0, 16384\n"
"s_mul_i32 s71, s23, 4\n"
"s_add_u32 s71, s76, s71\n"
"s_and_b32 s71, s71, 0x1ffff\n"
"s_or_b32 s71, s71, 0x20000\n"
"buffer_load_dwordx4 v17, s[36:39], s71 offen nt lds\n"
"buffer_load_dwordx4 v18, s[36:39], s71 offen offset:1024 nt lds\n"
"buffer_load_dwordx4 v19, s[36:39], s71 offen offset:2048 nt lds\n"
"buffer_load_dwordx4 v20, s[36:39], s71 offen offset:3072 nt lds\n"
"s_mov_b32 m0, 20480\n"
"s_mul_i32 s71, s23, 5\n"
"s_add_u32 s71, s76, s71\n"
"s_and_b32 s71, s71, 0x1ffff\n"
"s_or_b32 s71, s71, 0x20000\n"
"buffer_load_dwordx4 v17, s[36:39], s71 offen nt lds\n"
"buffer_load_dwordx4 v18, s[36:39], s71 offen offset:1024 nt lds\n"
"buffer_load_dwordx4 v19, s[36:39], s71 offen offset:2048 nt lds\n"
"buffer_load_dwordx4 v20, s[36:39], s71 offen offset:3072 nt lds\n"
"L_pace11_%=:\n"
"s_memrealtime s[60:61]\n"
"s_waitcnt lgkmcnt(0)\n"
"s_sub_u32 s60, s60, s63\n"
"s_cmp_lt_u32 s60, 672\n"
"s_cbranch_scc1 L_pace11_%=\n"
"s_waitcnt vmcnt(62)\n"
"s_barrier\n"
"s_mov_b32 m0, 155648\n"
"s_mul_i32 s71, s23, 4\n"
"s_add_u32 s71, s76, s71\n"
"s_and_b32 s71, s71, 0x1ffff\n"
"s_or_b32 s71, s71, 0x20000\n"
"buffer_load_dwordx4 v23, s[40:43], s71 offen nt lds\n"
"buffer_load_dwordx4 v24, s[40:43], s71 offen offset:1024 nt lds\n"
"buffer_load_dwordx4 v23, s[40:43], s71 offen offset:2048 nt lds\n"
"buffer_load_dwordx4 v24, s[40:43], s71 offen offset:3072 nt lds\n"
"s_mov_b32 m0, 159744\n"
"s_mul_i32 s71, s23, 5\n"
"s_add_u32 s71, s76, s71\n"
"s_and_b32 s71, s71, 0x1ffff\n"
"s_or_b32 s71, s71, 0x20000\n"
"buffer_load_dwordx4 v23, s[40:43], s71 offen nt lds\n"
"buffer_load_dwordx4 v24, s[40:43], s71 offen offset:1024 nt lds\n"
"buffer_load_dwordx4 v23, s[40:43], s71 offen offset:2048 nt lds\n"
"buffer_load_dwordx4 v24, s[40:43], s71 offen offset:3072 nt lds\n"
"s_mov_b32 m0, 24576\n"
"s_mul_i32 s71, s23, 6\n"
"s_add_u32 s71, s76, s71\n"
"s_and_b32 s71, s71, 0x1ffff\n"
"s_or_b32 s71, s71, 0x20000\n"
"buffer_load_dwordx4 v17, s[36:39], s71 offen nt lds\n"
"buffer_load_dwordx4 v18, s[36:39], s71 offen offset:1024 nt lds\n"
"buffer_load_dwordx4 v19, s[36:39], s71 offen offset:2048 nt lds\n"
"buffer_load_dwordx4 v20, s[36:39], s71 offen offset:3072 nt lds\n"
"s_mov_b32 m0, 28672\n"
"s_mul_i32 s71, s23, 7\n"
"s_add_u32 s71, s76, s71\n"
"s_and_b32 s71, s71, 0x1ffff\n"
"s_or_b32 s71, s71, 0x20000\n"
"buffer_load_dwordx4 v17, s[36:39], s71 offen nt lds\n"
"buffer_load_dwordx4 v18, s[36:39], s71 offen offset:1024 nt lds\n"
"buffer_load_dwordx4 v19, s[36:39], s71 offen offset:2048 nt lds\n"
"buffer_load_dwordx4 v20, s[36:39], s71 offen offset:3072 nt lds\n"
"L_pace12_%=:\n"
"s_memrealtime s[60:61]\n"
"s_waitcnt lgkmcnt(0)\n"
"s_sub_u32 s60, s60, s63\n"
"s_cmp_lt_u32 s60, 722\n"
"s_cbranch_scc1 L_pace12_%=\n"
"s_barrier\n"
"s_mov_b32 m0, 98304\n"
"s_mul_i32 s71, s23, 6\n"
"s_add_u32 s71, s76, s71\n"
"s_and_b32 s71, s71, 0x1ffff\n"
"s_or_b32 s71, s71, 0x20000\n"
"buffer_load_dwordx4 v23, s[40:43], s71 offen nt lds\n"
"buffer_load_dwordx4 v24, s[40:43], s71 offen offset:1024 nt lds\n"
"buffer_load_dwordx4 v23, s[40:43], s71 offen offset:2048 nt lds\n"
"buffer_load_dwordx4 v24, s[40:43], s71 offen offset:3072 nt lds\n"
"s_mov_b32 m0, 102400\n"
"s_mul_i32 s71, s23, 7\n"
"s_add_u32 s71, s76, s71\n"
"s_and_b32 s71, s71, 0x1ffff\n"
"s_or_b32 s71, s71, 0x20000\n"
"buffer_load_dwordx4 v23, s[40:43], s71 offen nt lds\n"
"buffer_load_dwordx4 v24, s[40:43], s71 offen offset:1024 nt lds\n"
"buffer_load_dwordx4 v23, s[40:43], s71 offen offset:2048 nt lds\n"
"buffer_load_dwordx4 v24, s[40:43], s71 offen offset:3072 nt lds\n"
"s_mov_b32 m0, 32768\n"
"s_mul_i32 s71, s23, 8\n"
"s_add_u32 s71, s76, s71\n"
"s_and_b32 s71, s71, 0x1ffff\n"
"s_or_b32 s71, s71, 0x20000\n"
"buffer_load_dwordx4 v17, s[36:39], s71 offen nt lds\n"
"buffer_load_dwordx4 v18, s[36:39], s71 offen offset:1024 nt lds\n"
"buffer_load_dwordx4 v19, s[36:39], s71 offen offset:2048 nt lds\n"
"buffer_load_dwordx4 v20, s[36:39], s71 offen offset:3072 nt lds\n"
"s_mov_b32 m0, 36864\n"
"s_mul_i32 s71, s23, 9\n"
"s_add_u32 s71, s76, s71\n"
"s_and_b32 s71, s71, 0x1ffff\n"
"s_or_b32 s71, s71, 0x20000\n"
"buffer_load_dwordx4 v17, s[36:39], s71 offen nt lds\n"
"buffer_load_dwordx4 v18, s[36:39], s71 offen offset:1024 nt lds\n"
"buffer_load_dwordx4 v19, s[36:39], s71 offen offset:2048 nt lds\n"
"buffer_load_dwordx4 v20, s[36:39], s71 offen offset:3072 nt lds\n"
"L_pace13_%=:\n"
"s_memrealtime s[60:61]\n"
"s_waitcnt lgkmcnt(0)\n"
"s_sub_u32 s60, s60, s63\n"
"s_cmp_lt_u32 s60, 772\n"
"s_cbranch_scc1 L_pace13_%=\n"
"s_waitcnt vmcnt(62)\n"
"s_barrier\n"
"s_mov_b32 m0, 106496\n"
"s_mul_i32 s71, s23, 8\n"
"s_add_u32 s71, s76, s71\n"
"s_and_b32 s71, s71, 0x1ffff\n"
"s_or_b32 s71, s71, 0x20000\n"
"buffer_load_dwordx4 v23, s[40:43], s71 offen nt lds\n"
"buffer_load_dwordx4 v24, s[40:43], s71 offen offset:1024 nt lds\n"
"buffer_load_dwordx4 v23, s[40:43], s71 offen offset:2048 nt lds\n"
"buffer_load_dwordx4 v24, s[40:43], s71 offen offset:3072 nt lds\n"
"s_mov_b32 m0, 110592\n"
"s_mul_i32 s71, s23, 9\n"
"s_add_u32 s71, s76, s71\n"
"s_and_b32 s71, s71, 0x1ffff\n"
"s_or_b32 s71, s71, 0x20000\n"
"buffer_load_dwordx4 v23, s[40:43], s71 offen nt lds\n"
"buffer_load_dwordx4 v24, s[40:43], s71 offen offset:1024 nt lds\n"
"buffer_load_dwordx4 v23, s[40:43], s71 offen offset:2048 nt lds\n"
"buffer_load_dwordx4 v24, s[40:43], s71 offen offset:3072 nt lds\n"
"s_mov_b32 m0, 40960\n"
"s_mul_i32 s71, s23, 10\n"
"s_add_u32 s71, s76, s71\n"
"s_and_b32 s71, s71, 0x1ffff\n"
"s_or_b32 s71, s71, 0x20000\n"
"buffer_load_dwordx4 v17, s[36:39], s71 offen nt lds\n"
"buffer_load_dwordx4 v18, s[36:39], s71 offen offset:1024 nt lds\n"
"buffer_load_dwordx4 v19, s[36:39], s71 offen offset:2048 nt lds\n"
"buffer_load_dwordx4 v20, s[36:39], s71 offen offset:3072 nt lds\n"
"s_mov_b32 m0, 45056\n"
"s_mul_i32 s71, s23, 11\n"
"s_add_u32 s71, s76, s71\n"
"s_and_b32 s71, s71, 0x1ffff\n"
"s_or_b32 s71, s71, 0x20000\n"
"buffer_load_dwordx4 v17, s[36:39], s71 offen nt lds\n"
"buffer_load_dwordx4 v18, s[36:39], s71 offen offset:1024 nt lds\n"
"buffer_load_dwordx4 v19, s[36:39], s71 offen offset:2048 nt lds\n"
"buffer_load_dwordx4 v20, s[36:39], s71 offen offset:3072 nt lds\n"
"L_pace14_%=:\n"
"s_memrealtime s[60:61]\n"
"s_waitcnt lgkmcnt(0)\n"
"s_sub_u32 s60, s60, s63\n"
"s_cmp_lt_u32 s60, 822\n"
"s_cbranch_scc1 L_pace14_%=\n"
"s_barrier\n"
"s_mov_b32 m0, 114688\n"
"s_mul_i32 s71, s23, 10\n"
"s_add_u32 s71, s76, s71\n"
"s_and_b32 s71, s71, 0x1ffff\n"
"s_or_b32 s71, s71, 0x20000\n"
"buffer_load_dwordx4 v23, s[40:43], s71 offen nt lds\n"
"buffer_load_dwordx4 v24, s[40:43], s71 offen offset:1024 nt lds\n"
"buffer_load_dwordx4 v23, s[40:43], s71 offen offset:2048 nt lds\n"
"buffer_load_dwordx4 v24, s[40:43], s71 offen offset:3072 nt lds\n"
"s_mov_b32 m0, 118784\n"
"s_mul_i32 s71, s23, 11\n"
"s_add_u32 s71, s76, s71\n"
"s_and_b32 s71, s71, 0x1ffff\n"
"s_or_b32 s71, s71, 0x20000\n"
"buffer_load_dwordx4 v23, s[40:43], s71 offen nt lds\n"
"buffer_load_dwordx4 v24, s[40:43], s71 offen offset:1024 nt lds\n"
"buffer_load_dwordx4 v23, s[40:43], s71 offen offset:2048 nt lds\n"
"buffer_load_dwordx4 v24, s[40:43], s71 offen offset:3072 nt lds\n"
"s_mov_b32 m0, 49152\n"
"s_mul_i32 s71, s23, 12\n"
"s_add_u32 s71, s76, s71\n"
"s_and_b32 s71, s71, 0x1ffff\n"
"s_or_b32 s71, s71, 0x20000\n"
"buffer_load_dwordx4 v17, s[36:39], s71 offen nt lds\n"
"buffer_load_dwordx4 v18, s[36:39], s71 offen offset:1024 nt lds\n"
"buffer_load_dwordx4 v19, s[36:39], s71 offen offset:2048 nt lds\n"
"buffer_load_dwordx4 v20, s[36:39], s71 offen offset:3072 nt lds\n"
"s_mov_b32 m0, 53248\n"
"s_mul_i32 s71, s23, 13\n"
"s_add_u32 s71, s76, s71\n"
"s_and_b32 s71, s71, 0x1ffff\n"
"s_or_b32 s71, s71, 0x20000\n"
"buffer_load_dwordx4 v17, s[36:39], s71 offen nt lds\n"
"buffer_load_dwordx4 v18, s[36:39], s71 offen offset:1024 nt lds\n"
"buffer_load_dwordx4 v19, s[36:39], s71 offen offset:2048 nt lds\n"
"buffer_load_dwordx4 v20, s[36:39], s71 offen offset:3072 nt lds\n"
"L_pace15_%=:\n"
"s_memrealtime s[60:61]\n"
"s_waitcnt lgkmcnt(0)\n"
"s_sub_u32 s60, s60, s63\n"
"s_cmp_lt_u32 s60, 872\n"
"s_cbranch_scc1 L_pace15_%=\n"
"s_waitcnt vmcnt(62)\n"
"s_barrier\n"
"s_mov_b32 m0, 122880\n"
"s_mul_i32 s71, s23, 12\n"
"s_add_u32 s71, s76, s71\n"
"s_and_b32 s71, s71, 0x1ffff\n"
"s_or_b32 s71, s71, 0x20000\n"
"buffer_load_dwordx4 v23, s[40:43], s71 offen nt lds\n"
"buffer_load_dwordx4 v24, s[40:43], s71 offen offset:1024 nt lds\n"
"buffer_load_dwordx4 v23, s[40:43], s71 offen offset:2048 nt lds\n"
"buffer_load_dwordx4 v24, s[40:43], s71 offen offset:3072 nt lds\n"
"s_mov_b32 m0, 126976\n"
"s_mul_i32 s71, s23, 13\n"
"s_add_u32 s71, s76, s71\n"
"s_and_b32 s71, s71, 0x1ffff\n"
"s_or_b32 s71, s71, 0x20000\n"
"buffer_load_dwordx4 v23, s[40:43], s71 offen nt lds\n"
"buffer_load_dwordx4 v24, s[40:43], s71 offen offset:1024 nt lds\n"
"buffer_load_dwordx4 v23, s[40:43], s71 offen offset:2048 nt lds\n"
"buffer_load_dwordx4 v24, s[40:43], s71 offen offset:3072 nt lds\n"
"s_mov_b32 m0, 57344\n"
"s_mul_i32 s71, s23, 14\n"
"s_add_u32 s71, s76, s71\n"
"s_and_b32 s71, s71, 0x1ffff\n"
"s_or_b32 s71, s71, 0x20000\n"
"buffer_load_dwordx4 v17, s[36:39], s71 offen nt lds\n"
"buffer_load_dwordx4 v18, s[36:39], s71 offen offset:1024 nt lds\n"
"buffer_load_dwordx4 v19, s[36:39], s71 offen offset:2048 nt lds\n"
"buffer_load_dwordx4 v20, s[36:39], s71 offen offset:3072 nt lds\n"
"s_mov_b32 m0, 61440\n"
"s_mul_i32 s71, s23, 15\n"
"s_add_u32 s71, s76, s71\n"
"s_and_b32 s71, s71, 0x1ffff\n"
"s_or_b32 s71, s71, 0x20000\n"
"buffer_load_dwordx4 v17, s[36:39], s71 offen nt lds\n"
"buffer_load_dwordx4 v18, s[36:39], s71 offen offset:1024 nt lds\n"
"buffer_load_dwordx4 v19, s[36:39], s71 offen offset:2048 nt lds\n"
"buffer_load_dwordx4 v20, s[36:39], s71 offen offset:3072 nt lds\n"
"L_pace16_%=:\n"
"s_memrealtime s[60:61]\n"
"s_waitcnt lgkmcnt(0)\n"
"s_sub_u32 s60, s60, s63\n"
"s_cmp_lt_u32 s60, 922\n"
"s_cbranch_scc1 L_pace16_%=\n"
"s_barrier\n"
"ds_read_b128 a[0:3], v25\n"
"ds_read_b128 a[4:7], v25 offset:12288\n"
"ds_read_b128 a[12:15], v25 offset:1024\n"
"ds_read_b128 a[16:19], v25 offset:13312\n"
"ds_read_b128 a[24:27], v25 offset:2048\n"
"ds_read_b128 a[28:31], v25 offset:14336\n"
"ds_read_b128 a[48:51], v25 offset:3072\n"
"ds_read_b128 a[52:55], v25 offset:15360\n"
"ds_read_b128 a[60:63], v25 offset:4096\n"
"ds_read_b128 a[64:67], v25 offset:16384\n"
"ds_read_b128 a[72:75], v25 offset:5120\n"
"ds_read_b128 a[76:79], v25 offset:17408\n"
"ds_read_b128 a[84:87], v25 offset:6144\n"
"ds_read_b128 a[88:91], v25 offset:18432\n"
"ds_read_b128 a[100:103], v25 offset:7168\n"
"ds_read_b128 a[248:251], v25 offset:19456\n"
"ds_read_b128 v[244:247], v25 offset:8192\n"
"ds_read_b128 v[248:251], v25 offset:20480\n"
"ds_read_b128 a[232:235], v25 offset:9216\n"
"ds_read_b128 a[236:239], v25 offset:21504\n"
"ds_read_b128 a[244:247], v25 offset:10240\n"
"ds_read_b128 v[224:227], v25 offset:22528\n"
"ds_read_b128 v[232:235], v25 offset:11264\n"
"ds_read_b128 v[236:239], v25 offset:23552\n"
"s_mov_b32 m0, 131072\n"
"s_mul_i32 s71, s23, 14\n"
"s_add_u32 s71, s76, s71\n"
"s_and_b32 s71, s71, 0x1ffff\n"
"s_or_b32 s71, s71, 0x20000\n"
"buffer_load_dwordx4 v23, s[40:43], s71 offen nt lds\n"
"buffer_load_dwordx4 v24, s[40:43], s71 offen offset:1024 nt lds\n"
"buffer_load_dwordx4 v23, s[40:43], s71 offen offset:2048 nt lds\n"
"buffer_load_dwordx4 v24, s[40:43], s71 offen offset:3072 nt lds\n"
"s_mov_b32 m0, 135168\n"
"s_mul_i32 s71, s23, 15\n"
"s_add_u32 s71, s76, s71\n"
"s_and_b32 s71, s71, 0x1ffff\n"
"s_or_b32 s71, s71, 0x20000\n"
"buffer_load_dwordx4 v23, s[40:43], s71 offen nt lds\n"
"buffer_load_dwordx4 v24, s[40:43], s71 offen offset:1024 nt lds\n"
"buffer_load_dwordx4 v23, s[40:43], s71 offen offset:2048 nt lds\n"
"buffer_load_dwordx4 v24, s[40:43], s71 offen offset:3072 nt lds\n"
"s_mul_i32 s70, s79, 73728\n"
"s_add_u32 s70, s70, 0\n"
"s_waitcnt lgkmcnt(4)\n"
"buffer_store_dwordx4 a[0:3], v45, s[44:47], s70 offen sc1\n"
"s_mul_i32 s70, s79, 73728\n"
"s_add_u32 s70, s70, 0\n"
"buffer_store_dwordx4 a[4:7], v45, s[44:47], s70 offen offset:1024 sc1\n"
"s_mul_i32 s70, s79, 73728\n"
"s_add_u32 s70, s70, 0\n"
"buffer_store_dwordx4 a[12:15], v45, s[44:47], s70 offen offset:3072 sc1\n"
"s_mul_i32 s70, s79, 73728\n"
"s_add_u32 s70, s70, 0\n"
"buffer_store_dwordx4 a[16:19], v33, s[44:47], s70 offen sc1\n"
"s_mul_i32 s70, s79, 73728\n"
"s_add_u32 s70, s70, 0\n"
"buffer_store_dwordx4 a[24:27], v33, s[44:47], s70 offen offset:2048 sc1\n"
"s_mul_i32 s70, s79, 73728\n"
"s_add_u32 s70, s70, 0\n"
"buffer_store_dwordx4 a[28:31], v33, s[44:47], s70 offen offset:3072 sc1\n"
"s_mul_i32 s70, s79, 73728\n"
"s_add_u32 s70, s70, 9216\n"
"buffer_store_dwordx4 a[48:51], v45, s[44:47], s70 offen sc1\n"
"s_mul_i32 s70, s79, 73728\n"
"s_add_u32 s70, s70, 9216\n"
"buffer_store_dwordx4 a[52:55], v45, s[44:47], s70 offen offset:1024 sc1\n"
"s_mul_i32 s70, s79, 73728\n"
"s_add_u32 s70, s70, 9216\n"
"buffer_store_dwordx4 a[60:63], v45, s[44:47], s70 offen offset:3072 sc1\n"
"s_mul_i32 s70, s79, 73728\n"
"s_add_u32 s70, s70, 9216\n"
"buffer_store_dwordx4 a[64:67], v33, s[44:47], s70 offen sc1\n"
"s_mul_i32 s70, s79, 73728\n"
"s_add_u32 s70, s70, 9216\n"
"buffer_store_dwordx4 a[72:75], v33, s[44:47], s70 offen offset:2048 sc1\n"
"s_mul_i32 s70, s79, 73728\n"
"s_add_u32 s70, s70, 9216\n"
"buffer_store_dwordx4 a[76:79], v33, s[44:47], s70 offen offset:3072 sc1\n"
"L_pace17_%=:\n"
"s_memrealtime s[60:61]\n"
"s_waitcnt lgkmcnt(0)\n"
"s_sub_u32 s60, s60, s63\n"
"s_cmp_lt_u32 s60, 1008\n"
"s_cbranch_scc1 L_pace17_%=\n"
"s_waitcnt vmcnt(62)\n"
"s_barrier\n"
"ds_read_b128 a[8:11], v25 offset:24576\n"
"ds_read_b128 a[20:23], v25 offset:25600\n"
"ds_read_b128 a[32:35], v25 offset:26624\n"
"ds_read_b128 a[56:59], v25 offset:27648\n"
"ds_read_b128 a[68:71], v25 offset:28672\n"
"ds_read_b128 a[80:83], v25 offset:29696\n"
"ds_read_b128 a[92:95], v45\n"
"ds_read_b128 a[252:255], v45 offset:1024\n"
"ds_read_b128 v[252:255], v45 offset:2048\n"
"ds_read_b128 a[240:243], v45 offset:3072\n"
"ds_read_b128 v[228:231], v45 offset:4096\n"
"ds_read_b128 v[240:243], v45 offset:5120\n"
"s_mul_i32 s70, s79, 73728\n"
"s_add_u32 s70, s70, 18432\n"
"buffer_store_dwordx4 a[84:87], v45, s[44:47], s70 offen sc1\n"
"s_mul_i32 s70, s79, 73728\n"
"s_add_u32 s70, s70, 18432\n"
"buffer_store_dwordx4 a[88:91], v45, s[44:47], s70 offen offset:1024 sc1\n"
"s_mul_i32 s70, s79, 73728\n"
"s_add_u32 s70, s70, 18432\n"
"buffer_store_dwordx4 a[100:103], v45, s[44:47], s70 offen offset:3072 sc1\n"
"s_mul_i32 s70, s79, 73728\n"
"s_add_u32 s70, s70, 18432\n"
"buffer_store_dwordx4 a[248:251], v33, s[44:47], s70 offen sc1\n"
"s_mul_i32 s70, s79, 73728\n"
"s_add_u32 s70, s70, 18432\n"
"buffer_store_dwordx4 v[244:247], v33, s[44:47], s70 offen offset:2048 sc1\n"
"s_mul_i32 s70, s79, 73728\n"
"s_add_u32 s70, s70, 18432\n"
"buffer_store_dwordx4 v[248:251], v33, s[44:47], s70 offen offset:3072 sc1\n"
"s_mul_i32 s70, s79, 73728\n"
"s_add_u32 s70, s70, 27648\n"
"buffer_store_dwordx4 a[232:235], v45, s[44:47], s70 offen sc1\n"
"s_mul_i32 s70, s79, 73728\n"
"s_add_u32 s70, s70, 27648\n"
"buffer_store_dwordx4 a[236:239], v45, s[44:47], s70 offen offset:1024 sc1\n"
"s_mul_i32 s70, s79, 73728\n"
"s_add_u32 s70, s70, 27648\n"
"buffer_store_dwordx4 a[244:247], v45, s[44:47], s70 offen offset:3072 sc1\n"
"s_mul_i32 s70, s79, 73728\n"
"s_add_u32 s70, s70, 27648\n"
"buffer_store_dwordx4 v[224:227], v33, s[44:47], s70 offen sc1\n"
"s_mul_i32 s70, s79, 73728\n"
"s_add_u32 s70, s70, 27648\n"
"buffer_store_dwordx4 v[232:235], v33, s[44:47], s70 offen offset:2048 sc1\n"
"s_mul_i32 s70, s79, 73728\n"
"s_add_u32 s70, s70, 27648\n"
"buffer_store_dwordx4 v[236:239], v33, s[44:47], s70 offen offset:3072 sc1\n"
"L_pace18_%=:\n"
"s_memrealtime s[60:61]\n"
"s_waitcnt lgkmcnt(0)\n"
"s_sub_u32 s60, s60, s63\n"
"s_cmp_lt_u32 s60, 1058\n"
"s_cbranch_scc1 L_pace18_%=\n"
"s_barrier\n"
"s_mul_i32 s70, s79, 73728\n"
"s_add_u32 s70, s70, 0\n"
"buffer_store_dwordx4 a[8:11], v45, s[44:47], s70 offen offset:2048 sc1\n"
"s_mul_i32 s70, s79, 73728\n"
"s_add_u32 s70, s70, 0\n"
"buffer_store_dwordx4 a[20:23], v33, s[44:47], s70 offen offset:1024 sc1\n"
"s_mul_i32 s70, s79, 73728\n"
"s_add_u32 s70, s70, 0\n"
"buffer_store_dwordx4 a[32:35], v36, s[44:47], s70 offen sc1\n"
"s_mul_i32 s70, s79, 73728\n"
"s_add_u32 s70, s70, 9216\n"
"buffer_store_dwordx4 a[56:59], v45, s[44:47], s70 offen offset:2048 sc1\n"
"s_mul_i32 s70, s79, 73728\n"
"s_add_u32 s70, s70, 9216\n"
"buffer_store_dwordx4 a[68:71], v33, s[44:47], s70 offen offset:1024 sc1\n"
"s_mul_i32 s70, s79, 73728\n"
"s_add_u32 s70, s70, 9216\n"
"buffer_store_dwordx4 a[80:83], v36, s[44:47], s70 offen sc1\n"
"s_mul_i32 s70, s79, 73728\n"
"s_add_u32 s70, s70, 18432\n"
"buffer_store_dwordx4 a[92:95], v45, s[44:47], s70 offen offset:2048 sc1\n"
"s_mul_i32 s70, s79, 73728\n"
"s_add_u32 s70, s70, 18432\n"
"buffer_store_dwordx4 a[252:255], v33, s[44:47], s70 offen offset:1024 sc1\n"
"s_mul_i32 s70, s79, 73728\n"
"s_add_u32 s70, s70, 18432\n"
"buffer_store_dwordx4 v[252:255], v36, s[44:47], s70 offen sc1\n"
"s_mul_i32 s70, s79, 73728\n"
"s_add_u32 s70, s70, 27648\n"
"buffer_store_dwordx4 a[240:243], v45, s[44:47], s70 offen offset:2048 sc1\n"
"s_mul_i32 s70, s79, 73728\n"
"s_add_u32 s70, s70, 27648\n"
"buffer_store_dwordx4 v[228:231], v33, s[44:47], s70 offen offset:1024 sc1\n"
"s_mul_i32 s70, s79, 73728\n"
"s_add_u32 s70, s70, 27648\n"
"buffer_store_dwordx4 v[240:243], v36, s[44:47], s70 offen sc1\n"
"ds_write_b128 v45, v[48:51]\n"
"ds_write_b128 v45, v[52:55] offset:1024\n"
"ds_write_b128 v45, v[56:59] offset:2048\n"
"ds_write_b128 v45, v[60:63] offset:3072\n"
"ds_write_b128 v45, v[64:67] offset:4096\n"
"ds_write_b128 v45, v[68:71] offset:5120\n"
"ds_write_b128 v45, v[72:75] offset:6144\n"
"ds_write_b128 v45, v[76:79] offset:7168\n"
"ds_write_b128 v26, v[80:83] offset:40960\n"
"ds_write_b128 v26, v[84:87] offset:41984\n"
"ds_write_b128 v26, v[88:91] offset:43008\n"
"ds_write_b128 v26, v[92:95] offset:44032\n"
"ds_write_b128 v26, v[96:99] offset:45056\n"
"ds_write_b128 v26, v[100:103] offset:46080\n"
"ds_write_b128 v26, v[104:107] offset:47104\n"
"ds_write_b128 v26, v[108:111] offset:48128\n"
"ds_write_b128 v45, v[112:115] offset:8192\n"
"ds_write_b128 v45, v[116:119] offset:9216\n"
"ds_write_b128 v45, v[120:123] offset:10240\n"
"ds_write_b128 v45, v[124:127] offset:11264\n"
"ds_write_b128 v45, v[128:131] offset:12288\n"
"ds_write_b128 v45, v[132:135] offset:13312\n"
"ds_write_b128 v45, v[136:139] offset:14336\n"
"ds_write_b128 v45, v[140:143] offset:15360\n"
"L_pace19_%=:\n"
"s_memrealtime s[60:61]\n"
"s_waitcnt lgkmcnt(0)\n"
"s_sub_u32 s60, s60, s63\n"
"s_cmp_lt_u32 s60, 1108\n"
"s_cbranch_scc1 L_pace19_%=\n"
"s_waitcnt vmcnt(62)\n"
"s_barrier\n"
"s_waitcnt vmcnt(0)\n"
"s_mov_b64 exec, 1\n"
"buffer_store_dwordx2 v[46:47], off, s[56:59], s34 offset:1024 sc1\n"
"s_mov_b64 exec, -1\n"
"s_mov_b32 m0, 147456\n"
"s_mul_i32 s71, s23, 18\n"
"s_add_u32 s71, s76, s71\n"
"s_and_b32 s71, s71, 0x1ffff\n"
"s_or_b32 s71, s71, 0x20000\n"
"buffer_load_dwordx4 v23, s[40:43], s71 offen nt lds\n"
"buffer_load_dwordx4 v24, s[40:43], s71 offen offset:1024 nt lds\n"
"buffer_load_dwordx4 v23, s[40:43], s71 offen offset:2048 nt lds\n"
"buffer_load_dwordx4 v24, s[40:43], s71 offen offset:3072 nt lds\n"
"s_mov_b32 m0, 151552\n"
"s_mul_i32 s71, s23, 19\n"
"s_add_u32 s71, s76, s71\n"
"s_and_b32 s71, s71, 0x1ffff\n"
"s_or_b32 s71, s71, 0x20000\n"
"buffer_load_dwordx4 v23, s[40:43], s71 offen nt lds\n"
"buffer_load_dwordx4 v24, s[40:43], s71 offen offset:1024 nt lds\n"
"buffer_load_dwordx4 v23, s[40:43], s71 offen offset:2048 nt lds\n"
"buffer_load_dwordx4 v24, s[40:43], s71 offen offset:3072 nt lds\n"
"L_pace20_%=:\n"
"s_memrealtime s[60:61]\n"
"s_waitcnt lgkmcnt(0)\n"
"s_sub_u32 s60, s60, s63\n"
"s_cmp_lt_u32 s60, 1158\n"
"s_cbranch_scc1 L_pace20_%=\n"
"s_barrier\n"
"s_mov_b32 m0, 16384\n"
"s_mul_i32 s71, s23, 20\n"
"s_add_u32 s71, s76, s71\n"
"s_and_b32 s71, s71, 0x1ffff\n"
"s_or_b32 s71, s71, 0x20000\n"
"buffer_load_dwordx4 v17, s[36:39], s71 offen nt lds\n"
"buffer_load_dwordx4 v18, s[36:39], s71 offen offset:1024 nt lds\n"
"buffer_load_dwordx4 v19, s[36:39], s71 offen offset:2048 nt lds\n"
"buffer_load_dwordx4 v20, s[36:39], s71 offen offset:3072 nt lds\n"
"s_mov_b32 m0, 20480\n"
"s_mul_i32 s71, s23, 21\n"
"s_add_u32 s71, s76, s71\n"
"s_and_b32 s71, s71, 0x1ffff\n"
"s_or_b32 s71, s71, 0x20000\n"
"buffer_load_dwordx4 v17, s[36:39], s71 offen nt lds\n"
"buffer_load_dwordx4 v18, s[36:39], s71 offen offset:1024 nt lds\n"
"buffer_load_dwordx4 v19, s[36:39], s71 offen offset:2048 nt lds\n"
"buffer_load_dwordx4 v20, s[36:39], s71 offen offset:3072 nt lds\n"
"s_mov_b32 m0, 155648\n"
"s_mul_i32 s71, s23, 20\n"
"s_add_u32 s71, s76, s71\n"
"s_and_b32 s71, s71, 0x1ffff\n"
"s_or_b32 s71, s71, 0x20000\n"
"buffer_load_dwordx4 v23, s[40:43], s71 offen nt lds\n"
"buffer_load_dwordx4 v24, s[40:43], s71 offen offset:1024 nt lds\n"
"buffer_load_dwordx4 v23, s[40:43], s71 offen offset:2048 nt lds\n"
"buffer_load_dwordx4 v24, s[40:43], s71 offen offset:3072 nt lds\n"
"s_mov_b32 m0, 159744\n"
"s_mul_i32 s71, s23, 21\n"
"s_add_u32 s71, s76, s71\n"
"s_and_b32 s71, s71, 0x1ffff\n"
"s_or_b32 s71, s71, 0x20000\n"
"buffer_load_dwordx4 v23, s[40:43], s71 offen nt lds\n"
"buffer_load_dwordx4 v24, s[40:43], s71 offen offset:1024 nt lds\n"
"buffer_load_dwordx4 v23, s[40:43], s71 offen offset:2048 nt lds\n"
"buffer_load_dwordx4 v24, s[40:43], s71 offen offset:3072 nt lds\n"
"L_pace21_%=:\n"
"s_memrealtime s[60:61]\n"
"s_waitcnt lgkmcnt(0)\n"
"s_sub_u32 s60, s60, s63\n"
"s_cmp_lt_u32 s60, 1208\n"
"s_cbranch_scc1 L_pace21_%=\n"
"s_barrier\n"
"s_mov_b32 m0, 24576\n"
"s_mul_i32 s71, s23, 22\n"
"s_add_u32 s71, s76, s71\n"
"s_and_b32 s71, s71, 0x1ffff\n"
"s_or_b32 s71, s71, 0x20000\n"
"buffer_load_dwordx4 v17, s[36:39], s71 offen nt lds\n"
"buffer_load_dwordx4 v18, s[36:39], s71 offen offset:1024 nt lds\n"
"buffer_load_dwordx4 v19, s[36:39], s71 offen offset:2048 nt lds\n"
"buffer_load_dwordx4 v20, s[36:39], s71 offen offset:3072 nt lds\n"
"s_mov_b32 m0, 28672\n"
"s_mul_i32 s71, s23, 23\n"
"s_add_u32 s71, s76, s71\n"
"s_and_b32 s71, s71, 0x1ffff\n"
"s_or_b32 s71, s71, 0x20000\n"
"buffer_load_dwordx4 v17, s[36:39], s71 offen nt lds\n"
"buffer_load_dwordx4 v18, s[36:39], s71 offen offset:1024 nt lds\n"
"buffer_load_dwordx4 v19, s[36:39], s71 offen offset:2048 nt lds\n"
"buffer_load_dwordx4 v20, s[36:39], s71 offen offset:3072 nt lds\n"
"L_pace22_%=:\n"
"s_memrealtime s[60:61]\n"
"s_waitcnt lgkmcnt(0)\n"
"s_sub_u32 s60, s60, s63\n"
"s_cmp_lt_u32 s60, 1258\n"
"s_cbranch_scc1 L_pace22_%=\n"
"s_barrier\n"
"s_mov_b32 m0, 98304\n"
"s_mul_i32 s71, s23, 22\n"
"s_add_u32 s71, s76, s71\n"
"s_and_b32 s71, s71, 0x1ffff\n"
"s_or_b32 s71, s71, 0x20000\n"
"buffer_load_dwordx4 v23, s[40:43], s71 offen nt lds\n"
"buffer_load_dwordx4 v24, s[40:43], s71 offen offset:1024 nt lds\n"
"buffer_load_dwordx4 v23, s[40:43], s71 offen offset:2048 nt lds\n"
"buffer_load_dwordx4 v24, s[40:43], s71 offen offset:3072 nt lds\n"
"s_mov_b32 m0, 102400\n"
"s_mul_i32 s71, s23, 23\n"
"s_add_u32 s71, s76, s71\n"
"s_and_b32 s71, s71, 0x1ffff\n"
"s_or_b32 s71, s71, 0x20000\n"
"buffer_load_dwordx4 v23, s[40:43], s71 offen nt lds\n"
"buffer_load_dwordx4 v24, s[40:43], s71 offen offset:1024 nt lds\n"
"buffer_load_dwordx4 v23, s[40:43], s71 offen offset:2048 nt lds\n"
"buffer_load_dwordx4 v24, s[40:43], s71 offen offset:3072 nt lds\n"
"s_mov_b32 m0, 32768\n"
"s_mul_i32 s71, s23, 24\n"
"s_add_u32 s71, s76, s71\n"
"s_and_b32 s71, s71, 0x1ffff\n"
"s_or_b32 s71, s71, 0x20000\n"
"buffer_load_dwordx4 v17, s[36:39], s71 offen nt lds\n"
"buffer_load_dwordx4 v18, s[36:39], s71 offen offset:1024 nt lds\n"
"buffer_load_dwordx4 v19, s[36:39], s71 offen offset:2048 nt lds\n"
"buffer_load_dwordx4 v20, s[36:39], s71 offen offset:3072 nt lds\n"
"s_mov_b32 m0, 36864\n"
"s_mul_i32 s71, s23, 25\n"
"s_add_u32 s71, s76, s71\n"
"s_and_b32 s71, s71, 0x1ffff\n"
"s_or_b32 s71, s71, 0x20000\n"
"buffer_load_dwordx4 v17, s[36:39], s71 offen nt lds\n"
"buffer_load_dwordx4 v18, s[36:39], s71 offen offset:1024 nt lds\n"
"buffer_load_dwordx4 v19, s[36:39], s71 offen offset:2048 nt lds\n"
"buffer_load_dwordx4 v20, s[36:39], s71 offen offset:3072 nt lds\n"
"L_pace23_%=:\n"
"s_memrealtime s[60:61]\n"
"s_waitcnt lgkmcnt(0)\n"
"s_sub_u32 s60, s60, s63\n"
"s_cmp_lt_u32 s60, 1308\n"
"s_cbranch_scc1 L_pace23_%=\n"
"s_barrier\n"
"s_mov_b32 m0, 106496\n"
"s_mul_i32 s71, s23, 24\n"
"s_add_u32 s71, s76, s71\n"
"s_and_b32 s71, s71, 0x1ffff\n"
"s_or_b32 s71, s71, 0x20000\n"
"buffer_load_dwordx4 v23, s[40:43], s71 offen nt lds\n"
"buffer_load_dwordx4 v24, s[40:43], s71 offen offset:1024 nt lds\n"
"buffer_load_dwordx4 v23, s[40:43], s71 offen offset:2048 nt lds\n"
"buffer_load_dwordx4 v24, s[40:43], s71 offen offset:3072 nt lds\n"
"s_mov_b32 m0, 110592\n"
"s_mul_i32 s71, s23, 25\n"
"s_add_u32 s71, s76, s71\n"
"s_and_b32 s71, s71, 0x1ffff\n"
"s_or_b32 s71, s71, 0x20000\n"
"buffer_load_dwordx4 v23, s[40:43], s71 offen nt lds\n"
"buffer_load_dwordx4 v24, s[40:43], s71 offen offset:1024 nt lds\n"
"buffer_load_dwordx4 v23, s[40:43], s71 offen offset:2048 nt lds\n"
"buffer_load_dwordx4 v24, s[40:43], s71 offen offset:3072 nt lds\n"
"L_pace24_%=:\n"
"s_memrealtime s[60:61]\n"
"s_waitcnt lgkmcnt(0)\n"
"s_sub_u32 s60, s60, s63\n"
"s_cmp_lt_u32 s60, 1358\n"
"s_cbranch_scc1 L_pace24_%=\n"
"s_waitcnt vmcnt(48)\n"
"s_barrier\n"
"s_mov_b32 m0, 40960\n"
"s_mul_i32 s71, s23, 26\n"
"s_add_u32 s71, s76, s71\n"
"s_and_b32 s71, s71, 0x1ffff\n"
"s_or_b32 s71, s71, 0x20000\n"
"buffer_load_dwordx4 v17, s[36:39], s71 offen nt lds\n"
"buffer_load_dwordx4 v18, s[36:39], s71 offen offset:1024 nt lds\n"
"buffer_load_dwordx4 v19, s[36:39], s71 offen offset:2048 nt lds\n"
"buffer_load_dwordx4 v20, s[36:39], s71 offen offset:3072 nt lds\n"
"s_mov_b32 m0, 45056\n"
"s_mul_i32 s71, s23, 27\n"
"s_add_u32 s71, s76, s71\n"
"s_and_b32 s71, s71, 0x1ffff\n"
"s_or_b32 s71, s71, 0x20000\n"
"buffer_load_dwordx4 v17, s[36:39], s71 offen nt lds\n"
"buffer_load_dwordx4 v18, s[36:39], s71 offen offset:1024 nt lds\n"
"buffer_load_dwordx4 v19, s[36:39], s71 offen offset:2048 nt lds\n"
"buffer_load_dwordx4 v20, s[36:39], s71 offen offset:3072 nt lds\n"
"s_mov_b32 m0, 114688\n"
"s_mul_i32 s71, s23, 26\n"
"s_add_u32 s71, s76, s71\n"
"s_and_b32 s71, s71, 0x1ffff\n"
"s_or_b32 s71, s71, 0x20000\n"
"buffer_load_dwordx4 v23, s[40:43], s71 offen nt lds\n"
"buffer_load_dwordx4 v24, s[40:43], s71 offen offset:1024 nt lds\n"
"buffer_load_dwordx4 v23, s[40:43], s71 offen offset:2048 nt lds\n"
"buffer_load_dwordx4 v24, s[40:43], s71 offen offset:3072 nt lds\n"
"s_mov_b32 m0, 118784\n"
"s_mul_i32 s71, s23, 27\n"
"s_add_u32 s71, s76, s71\n"
"s_and_b32 s71, s71, 0x1ffff\n"
"s_or_b32 s71, s71, 0x20000\n"
"buffer_load_dwordx4 v23, s[40:43], s71 offen nt lds\n"
"buffer_load_dwordx4 v24, s[40:43], s71 offen offset:1024 nt lds\n"
"buffer_load_dwordx4 v23, s[40:43], s71 offen offset:2048 nt lds\n"
"buffer_load_dwordx4 v24, s[40:43], s71 offen offset:3072 nt lds\n"
"buffer_load_dword v44, v27, s[4:7], 0 offen\n"
"L_pace25_%=:\n"
"s_memrealtime s[60:61]\n"
"s_waitcnt lgkmcnt(0)\n"
"s_sub_u32 s60, s60, s63\n"
"s_cmp_lt_u32 s60, 1408\n"
"s_cbranch_scc1 L_pace25_%=\n"
"s_waitcnt vmcnt(49)\n"
"s_barrier\n"
"s_mov_b32 m0, 49152\n"
"s_mul_i32 s71, s23, 28\n"
"s_add_u32 s71, s76, s71\n"
"s_and_b32 s71, s71, 0x1ffff\n"
"s_or_b32 s71, s71, 0x20000\n"
"buffer_load_dwordx4 v17, s[36:39], s71 offen nt lds\n"
"buffer_load_dwordx4 v18, s[36:39], s71 offen offset:1024 nt lds\n"
"buffer_load_dwordx4 v19, s[36:39], s71 offen offset:2048 nt lds\n"
"buffer_load_dwordx4 v20, s[36:39], s71 offen offset:3072 nt lds\n"
"s_mov_b32 m0, 53248\n"
"s_mul_i32 s71, s23, 29\n"
"s_add_u32 s71, s76, s71\n"
"s_and_b32 s71, s71, 0x1ffff\n"
"s_or_b32 s71, s71, 0x20000\n"
"buffer_load_dwordx4 v17, s[36:39], s71 offen nt lds\n"
"buffer_load_dwordx4 v18, s[36:39], s71 offen offset:1024 nt lds\n"
"buffer_load_dwordx4 v19, s[36:39], s71 offen offset:2048 nt lds\n"
"buffer_load_dwordx4 v20, s[36:39], s71 offen offset:3072 nt lds\n"
"s_mov_b32 s30, 0\n"
"buffer_load_dword v44, v39, s[48:51], s30 offen\n"
"s_mov_b32 s30, 1048576\n"
"buffer_load_dword v44, v39, s[48:51], s30 offen\n"
"s_add_u32 m0, s24, 4096\n"
"s_mov_b32 s69, s79\n"
"s_lshl_b32 s69, s69, 19\n"
"s_add_u32 s69, s69, s26\n"
"s_add_i32 s69, s69, -196608\n"
"buffer_load_dwordx4 v7, s[48:51], s69 offen lds\n"
"buffer_load_dwordx4 v8, s[48:51], s69 offen offset:1024 lds\n"
"buffer_load_dwordx4 v9, s[48:51], s69 offen offset:2048 lds\n"
"buffer_load_dwordx4 v10, s[48:51], s69 offen offset:3072 lds\n"
"s_add_u32 m0, s24, 8192\n"
"s_mov_b32 s69, s79\n"
"s_lshl_b32 s69, s69, 19\n"
"s_add_u32 s69, s69, s31\n"
"s_add_i32 s69, s69, -196608\n"
"buffer_load_dwordx4 v7, s[48:51], s69 offen lds\n"
"buffer_load_dwordx4 v8, s[48:51], s69 offen offset:1024 lds\n"
"buffer_load_dwordx4 v9, s[48:51], s69 offen offset:2048 lds\n"
"buffer_load_dwordx4 v10, s[48:51], s69 offen offset:3072 lds\n"
"s_add_u32 m0, s24, 0\n"
"s_mov_b32 s69, s79\n"
"s_lshl_b32 s69, s69, 19\n"
"s_add_u32 s69, s69, s26\n"
"s_add_i32 s69, s69, -131072\n"
"buffer_load_dwordx4 v7, s[48:51], s69 offen lds\n"
"buffer_load_dwordx4 v8, s[48:51], s69 offen offset:1024 lds\n"
"buffer_load_dwordx4 v9, s[48:51], s69 offen offset:2048 lds\n"
"buffer_load_dwordx4 v10, s[48:51], s69 offen offset:3072 lds\n"
"L_pace26_%=:\n"
"s_memrealtime s[60:61]\n"
"s_waitcnt lgkmcnt(0)\n"
"s_sub_u32 s60, s60, s63\n"
"s_cmp_lt_u32 s60, 1458\n"
"s_cbranch_scc1 L_pace26_%=\n"
"s_waitcnt vmcnt(55)\n"
"s_barrier\n"
"s_mov_b32 m0, 122880\n"
"s_mul_i32 s71, s23, 28\n"
"s_add_u32 s71, s76, s71\n"
"s_and_b32 s71, s71, 0x1ffff\n"
"s_or_b32 s71, s71, 0x20000\n"
"buffer_load_dwordx4 v23, s[40:43], s71 offen nt lds\n"
"buffer_load_dwordx4 v24, s[40:43], s71 offen offset:1024 nt lds\n"
"buffer_load_dwordx4 v23, s[40:43], s71 offen offset:2048 nt lds\n"
"buffer_load_dwordx4 v24, s[40:43], s71 offen offset:3072 nt lds\n"
"s_mov_b32 m0, 126976\n"
"s_mul_i32 s71, s23, 29\n"
"s_add_u32 s71, s76, s71\n"
"s_and_b32 s71, s71, 0x1ffff\n"
"s_or_b32 s71, s71, 0x20000\n"
"buffer_load_dwordx4 v23, s[40:43], s71 offen nt lds\n"
"buffer_load_dwordx4 v24, s[40:43], s71 offen offset:1024 nt lds\n"
"buffer_load_dwordx4 v23, s[40:43], s71 offen offset:2048 nt lds\n"
"buffer_load_dwordx4 v24, s[40:43], s71 offen offset:3072 nt lds\n"
"s_mov_b32 m0, 57344\n"
"s_mul_i32 s71, s23, 30\n"
"s_add_u32 s71, s76, s71\n"
"s_and_b32 s71, s71, 0x1ffff\n"
"s_or_b32 s71, s71, 0x20000\n"
"buffer_load_dwordx4 v17, s[36:39], s71 offen nt lds\n"
"buffer_load_dwordx4 v18, s[36:39], s71 offen offset:1024 nt lds\n"
"buffer_load_dwordx4 v19, s[36:39], s71 offen offset:2048 nt lds\n"
"buffer_load_dwordx4 v20, s[36:39], s71 offen offset:3072 nt lds\n"
"s_mov_b32 m0, 61440\n"
"s_mul_i32 s71, s23, 31\n"
"s_add_u32 s71, s76, s71\n"
"s_and_b32 s71, s71, 0x1ffff\n"
"s_or_b32 s71, s71, 0x20000\n"
"buffer_load_dwordx4 v17, s[36:39], s71 offen nt lds\n"
"buffer_load_dwordx4 v18, s[36:39], s71 offen offset:1024 nt lds\n"
"buffer_load_dwordx4 v19, s[36:39], s71 offen offset:2048 nt lds\n"
"buffer_load_dwordx4 v20, s[36:39], s71 offen offset:3072 nt lds\n"
"L_pace27_%=:\n"
"s_memrealtime s[60:61]\n"
"s_waitcnt lgkmcnt(0)\n"
"s_sub_u32 s60, s60, s63\n"
"s_cmp_lt_u32 s60, 1508\n"
"s_cbranch_scc1 L_pace27_%=\n"
"s_waitcnt vmcnt(55)\n"
"s_barrier\n"
"buffer_load_dwordx2 v[34:35], v38, s[56:59], 0 offen offset:1024 sc1\n"
"s_mov_b32 m0, 131072\n"
"s_mul_i32 s71, s23, 30\n"
"s_add_u32 s71, s76, s71\n"
"s_and_b32 s71, s71, 0x1ffff\n"
"s_or_b32 s71, s71, 0x20000\n"
"buffer_load_dwordx4 v23, s[40:43], s71 offen nt lds\n"
"buffer_load_dwordx4 v24, s[40:43], s71 offen offset:1024 nt lds\n"
"buffer_load_dwordx4 v23, s[40:43], s71 offen offset:2048 nt lds\n"
"buffer_load_dwordx4 v24, s[40:43], s71 offen offset:3072 nt lds\n"
"s_mov_b32 m0, 135168\n"
"s_mul_i32 s71, s23, 31\n"
"s_add_u32 s71, s76, s71\n"
"s_and_b32 s71, s71, 0x1ffff\n"
"s_or_b32 s71, s71, 0x20000\n"
"buffer_load_dwordx4 v23, s[40:43], s71 offen nt lds\n"
"buffer_load_dwordx4 v24, s[40:43], s71 offen offset:1024 nt lds\n"
"buffer_load_dwordx4 v23, s[40:43], s71 offen offset:2048 nt lds\n"
"buffer_load_dwordx4 v24, s[40:43], s71 offen offset:3072 nt lds\n"
"v_mov_b32 v24, v39\n"
"s_waitcnt vmcnt(33)\n"
"ds_read_b64_tr_b16 a[36:37], v11 offset:4096\n"
"ds_read_b64_tr_b16 a[38:39], v11 offset:6144\n"
"ds_read_b64_tr_b16 a[40:41], v12 offset:4096\n"
"ds_read_b64_tr_b16 a[42:43], v12 offset:6144\n"
"ds_read_b64_tr_b16 a[44:45], v13 offset:4096\n"
"ds_read_b64_tr_b16 a[46:47], v13 offset:6144\n"
"ds_read_b64_tr_b16 a[96:97], v14 offset:4096\n"
"ds_read_b64_tr_b16 a[98:99], v14 offset:6144\n"
"s_waitcnt lgkmcnt(6)\n"
"v_mfma_f32_16x16x32_bf16 v[48:51], a[36:39], a[24:27], 0\n"
"s_waitcnt lgkmcnt(0)\n"
"s_add_u32 m0, s24, 4096\n"
"s_mov_b32 s69, s79\n"
"s_lshl_b32 s69, s69, 19\n"
"v_mfma_f32_16x16x32_bf16 v[52:55], a[40:43], a[24:27], 0\n"
"s_add_u32 s69, s69, s31\n"
"v_mfma_f32_16x16x32_bf16 v[56:59], a[44:47], a[24:27], 0\n"
"s_add_i32 s69, s69, -131072\n"
"v_mfma_f32_16x16x32_bf16 v[60:63], a[96:99], a[24:27], 0\n"
"buffer_load_dwordx4 v7, s[48:51], s69 offen lds\n"
"v_mfma_f32_16x16x32_bf16 v[76:79], a[96:99], a[28:31], 0\n"
"v_mfma_f32_16x16x32_bf16 v[72:75], a[44:47], a[28:31], 0\n"
"v_mfma_f32_16x16x32_bf16 v[68:71], a[40:43], a[28:31], 0\n"
"v_mfma_f32_16x16x32_bf16 v[64:67], a[36:39], a[28:31], 0\n"
"v_mfma_f32_16x16x32_bf16 v[80:83], a[36:39], a[0:3], 0\n"
"v_mfma_f32_16x16x32_bf16 v[84:87], a[40:43], a[0:3], 0\n"
"v_mfma_f32_16x16x32_bf16 v[88:91], a[44:47], a[0:3], 0\n"
"s_waitcnt vmcnt(30)\n"
"ds_read_b64_tr_b16 a[216:217], v11 offset:8192\n"
"v_mfma_f32_16x16x32_bf16 v[92:95], a[96:99], a[0:3], 0\n"
"buffer_load_dwordx4 v8, s[48:51], s69 offen offset:1024 lds\n"
"v_mfma_f32_16x16x32_bf16 v[108:111], a[96:99], a[4:7], 0\n"
"ds_read_b64_tr_b16 a[218:219], v11 offset:10240\n"
"v_mfma_f32_16x16x32_bf16 v[104:107], a[44:47], a[4:7], 0\n"
"ds_read_b64_tr_b16 a[220:221], v12 offset:8192\n"
"v_mfma_f32_16x16x32_bf16 v[100:103], a[40:43], a[4:7], 0\n"
"ds_read_b64_tr_b16 a[222:223], v12 offset:10240\n"
"v_mfma_f32_16x16x32_bf16 v[96:99], a[36:39], a[4:7], 0\n"
"ds_read_b64_tr_b16 a[224:225], v13 offset:8192\n"
"v_mfma_f32_16x16x32_bf16 v[112:115], a[36:39], a[8:11], 0\n"
"ds_read_b64_tr_b16 a[226:227], v13 offset:10240\n"
"v_mfma_f32_16x16x32_bf16 v[116:119], a[40:43], a[8:11], 0\n"
"ds_read_b64_tr_b16 a[228:229], v14 offset:8192\n"
"v_mfma_f32_16x16x32_bf16 v[120:123], a[44:47], a[8:11], 0\n"
"ds_read_b64_tr_b16 a[230:231], v14 offset:10240\n"
"v_mfma_f32_16x16x32_bf16 v[124:127], a[96:99], a[8:11], 0\n"
"buffer_load_dwordx4 v9, s[48:51], s69 offen offset:2048 lds\n"
"v_mfma_f32_16x16x32_bf16 v[140:143], a[96:99], a[12:15], 0\n"
"v_mfma_f32_16x16x32_bf16 v[136:139], a[44:47], a[12:15], 0\n"
"v_mfma_f32_16x16x32_bf16 v[132:135], a[40:43], a[12:15], 0\n"
"v_mfma_f32_16x16x32_bf16 v[128:131], a[36:39], a[12:15], 0\n"
"v_mfma_f32_16x16x32_bf16 v[144:147], a[36:39], a[16:19], 0\n"
"v_mfma_f32_16x16x32_bf16 v[148:151], a[40:43], a[16:19], 0\n"
"v_mfma_f32_16x16x32_bf16 v[152:155], a[44:47], a[16:19], 0\n"
"v_mfma_f32_16x16x32_bf16 v[156:159], a[96:99], a[16:19], 0\n"
"buffer_load_dwordx4 v10, s[48:51], s69 offen offset:3072 lds\n"
"v_mfma_f32_16x16x32_bf16 v[172:175], a[96:99], a[20:23], 0\n"
"v_mfma_f32_16x16x32_bf16 v[168:171], a[44:47], a[20:23], 0\n"
"v_mfma_f32_16x16x32_bf16 v[164:167], a[40:43], a[20:23], 0\n"
"v_mfma_f32_16x16x32_bf16 v[160:163], a[36:39], a[20:23], 0\n"
"v_mfma_f32_16x16x32_bf16 v[176:179], a[36:39], a[32:35], 0\n"
"v_mfma_f32_16x16x32_bf16 v[180:183], a[40:43], a[32:35], 0\n"
"v_mfma_f32_16x16x32_bf16 v[184:187], a[44:47], a[32:35], 0\n"
"v_mfma_f32_16x16x32_bf16 v[188:191], a[96:99], a[32:35], 0\n"
"L_pace28_%=:\n"
"s_memrealtime s[60:61]\n"
"s_waitcnt lgkmcnt(0)\n"
"s_sub_u32 s60, s60, s63\n"
"s_cmp_lt_u32 s60, 1558\n"
"s_cbranch_scc1 L_pace28_%=\n"
"s_barrier\n"
"v_mfma_f32_16x16x32_bf16 v[192:195], a[216:219], a[24:27], 0\n"
"s_add_u32 m0, s24, 8192\n"
"s_mov_b32 s69, s79\n"
"s_lshl_b32 s69, s69, 19\n"
"v_mfma_f32_16x16x32_bf16 v[196:199], a[220:223], a[24:27], 0\n"
"s_add_u32 s69, s69, s26\n"
"v_mfma_f32_16x16x32_bf16 v[200:203], a[224:227], a[24:27], 0\n"
"s_add_i32 s69, s69, -65536\n"
"v_mfma_f32_16x16x32_bf16 v[204:207], a[228:231], a[24:27], 0\n"
"buffer_load_dwordx4 v7, s[48:51], s69 offen lds\n"
"v_mfma_f32_16x16x32_bf16 v[220:223], a[228:231], a[28:31], 0\n"
"v_mfma_f32_16x16x32_bf16 v[216:219], a[224:227], a[28:31], 0\n"
"buffer_load_dwordx4 v8, s[48:51], s69 offen offset:1024 lds\n"
"v_mfma_f32_16x16x32_bf16 v[212:215], a[220:223], a[28:31], 0\n"
"v_mfma_f32_16x16x32_bf16 v[208:211], a[216:219], a[28:31], 0\n"
"buffer_load_dwordx4 v9, s[48:51], s69 offen offset:2048 lds\n"
"v_mfma_f32_16x16x32_bf16 a[104:107], a[216:219], a[0:3], 0\n"
"v_mfma_f32_16x16x32_bf16 a[108:111], a[220:223], a[0:3], 0\n"
"buffer_load_dwordx4 v10, s[48:51], s69 offen offset:3072 lds\n"
"v_mfma_f32_16x16x32_bf16 a[112:115], a[224:227], a[0:3], 0\n"
"s_mov_b32 m0, 12288\n"
"s_cmp_le_u32 s65, 0\n"
"s_cselect_b32 s69, 1, 0\n"
"s_add_u32 s69, s69, s66\n"
"s_lshl_b32 s69, s69, 19\n"
"s_add_u32 s69, s69, s26\n"
"s_add_i32 s69, s69, -196608\n"
"s_waitcnt vmcnt(33)\n"
"ds_read_b64_tr_b16 a[36:37], v11\n"
"v_mfma_f32_16x16x32_bf16 a[116:119], a[228:231], a[0:3], 0\n"
"buffer_load_dwordx4 v7, s[48:51], s69 offen lds\n"
"v_mfma_f32_16x16x32_bf16 a[132:135], a[228:231], a[4:7], 0\n"
"ds_read_b64_tr_b16 a[38:39], v11 offset:2048\n"
"v_mfma_f32_16x16x32_bf16 a[128:131], a[224:227], a[4:7], 0\n"
"buffer_load_dwordx4 v8, s[48:51], s69 offen offset:1024 lds\n"
"v_mfma_f32_16x16x32_bf16 a[124:127], a[220:223], a[4:7], 0\n"
"ds_read_b64_tr_b16 a[40:41], v12\n"
"v_mfma_f32_16x16x32_bf16 a[120:123], a[216:219], a[4:7], 0\n"
"buffer_load_dwordx4 v9, s[48:51], s69 offen offset:2048 lds\n"
"v_mfma_f32_16x16x32_bf16 a[136:139], a[216:219], a[8:11], 0\n"
"ds_read_b64_tr_b16 a[42:43], v12 offset:2048\n"
"v_mfma_f32_16x16x32_bf16 a[140:143], a[220:223], a[8:11], 0\n"
"buffer_load_dwordx4 v10, s[48:51], s69 offen offset:3072 lds\n"
"v_mfma_f32_16x16x32_bf16 a[144:147], a[224:227], a[8:11], 0\n"
"s_mov_b32 m0, 24576\n"
"s_cmp_le_u32 s65, 0\n"
"s_cselect_b32 s69, 1, 0\n"
"s_add_u32 s69, s69, s66\n"
"s_lshl_b32 s69, s69, 19\n"
"s_add_u32 s69, s69, s26\n"
"s_add_i32 s69, s69, -131072\n"
"ds_read_b64_tr_b16 a[44:45], v13\n"
"v_mfma_f32_16x16x32_bf16 a[148:151], a[228:231], a[8:11], 0\n"
"buffer_load_dwordx4 v7, s[48:51], s69 offen lds\n"
"v_mfma_f32_16x16x32_bf16 a[164:167], a[228:231], a[12:15], 0\n"
"ds_read_b64_tr_b16 a[46:47], v13 offset:2048\n"
"v_mfma_f32_16x16x32_bf16 a[160:163], a[224:227], a[12:15], 0\n"
"buffer_load_dwordx4 v8, s[48:51], s69 offen offset:1024 lds\n"
"v_mfma_f32_16x16x32_bf16 a[156:159], a[220:223], a[12:15], 0\n"
"ds_read_b64_tr_b16 a[96:97], v14\n"
"v_mfma_f32_16x16x32_bf16 a[152:155], a[216:219], a[12:15], 0\n"
"buffer_load_dwordx4 v9, s[48:51], s69 offen offset:2048 lds\n"
"v_mfma_f32_16x16x32_bf16 a[168:171], a[216:219], a[16:19], 0\n"
"ds_read_b64_tr_b16 a[98:99], v14 offset:2048\n"
"v_mfma_f32_16x16x32_bf16 a[172:175], a[220:223], a[16:19], 0\n"
"buffer_load_dwordx4 v10, s[48:51], s69 offen offset:3072 lds\n"
"v_mfma_f32_16x16x32_bf16 a[176:179], a[224:227], a[16:19], 0\n"
"s_mov_b32 m0, 36864\n"
"s_cmp_le_u32 s65, 0\n"
"s_cselect_b32 s69, 1, 0\n"
"s_add_u32 s69, s69, s66\n"
"s_lshl_b32 s69, s69, 19\n"
"s_add_u32 s69, s69, s26\n"
"s_add_i32 s69, s69, -65536\n"
"v_mfma_f32_16x16x32_bf16 a[180:183], a[228:231], a[16:19], 0\n"
"buffer_load_dwordx4 v7, s[48:51], s69 offen lds\n"
"v_mfma_f32_16x16x32_bf16 a[196:199], a[228:231], a[20:23], 0\n"
"v_mfma_f32_16x16x32_bf16 a[192:195], a[224:227], a[20:23], 0\n"
"buffer_load_dwordx4 v8, s[48:51], s69 offen offset:1024 lds\n"
"v_mfma_f32_16x16x32_bf16 a[188:191], a[220:223], a[20:23], 0\n"
"v_mfma_f32_16x16x32_bf16 a[184:187], a[216:219], a[20:23], 0\n"
"buffer_load_dwordx4 v9, s[48:51], s69 offen offset:2048 lds\n"
"v_mfma_f32_16x16x32_bf16 a[200:203], a[216:219], a[32:35], 0\n"
"v_mfma_f32_16x16x32_bf16 a[204:207], a[220:223], a[32:35], 0\n"
"buffer_load_dwordx4 v10, s[48:51], s69 offen offset:3072 lds\n"
"v_mfma_f32_16x16x32_bf16 a[208:211], a[224:227], a[32:35], 0\n"
"v_mfma_f32_16x16x32_bf16 a[212:215], a[228:231], a[32:35], 0\n"
"s_waitcnt lgkmcnt(1)\n"
"v_mfma_f32_16x16x32_bf16 v[48:51], a[36:39], a[72:75], v[48:51]\n"
"s_waitcnt lgkmcnt(0)\n"
"s_add_u32 m0, s24, 0\n"
"s_mov_b32 s69, s79\n"
"s_lshl_b32 s69, s69, 19\n"
"v_mfma_f32_16x16x32_bf16 v[52:55], a[40:43], a[72:75], v[52:55]\n"
"s_add_u32 s69, s69, s31\n"
"v_mfma_f32_16x16x32_bf16 v[56:59], a[44:47], a[72:75], v[56:59]\n"
"s_add_i32 s69, s69, -65536\n"
"v_mfma_f32_16x16x32_bf16 v[60:63], a[96:99], a[72:75], v[60:63]\n"
"buffer_load_dwordx4 v7, s[48:51], s69 offen lds\n"
"v_mfma_f32_16x16x32_bf16 v[76:79], a[96:99], a[76:79], v[76:79]\n"
"v_mfma_f32_16x16x32_bf16 v[72:75], a[44:47], a[76:79], v[72:75]\n"
"v_mfma_f32_16x16x32_bf16 v[68:71], a[40:43], a[76:79], v[68:71]\n"
"v_mfma_f32_16x16x32_bf16 v[64:67], a[36:39], a[76:79], v[64:67]\n"
"v_mfma_f32_16x16x32_bf16 v[80:83], a[36:39], a[48:51], v[80:83]\n"
"v_mfma_f32_16x16x32_bf16 v[84:87], a[40:43], a[48:51], v[84:87]\n"
"v_mfma_f32_16x16x32_bf16 v[88:91], a[44:47], a[48:51], v[88:91]\n"
"s_waitcnt vmcnt(17)\n"
"ds_read_b64_tr_b16 a[216:217], v11 offset:4096\n"
"v_cmp_ne_u64 vcc, v[34:35], v[46:47]\n"
"v_mfma_f32_16x16x32_bf16 v[92:95], a[96:99], a[48:51], v[92:95]\n"
"buffer_load_dwordx4 v8, s[48:51], s69 offen offset:1024 lds\n"
"v_mfma_f32_16x16x32_bf16 v[108:111], a[96:99], a[52:55], v[108:111]\n"
"s_cbranch_vccnz L_spin2_%=\n"
"L_spinr2_%=:\n"
"s_barrier\n"
"ds_read_b64_tr_b16 a[218:219], v11 offset:6144\n"
"v_mfma_f32_16x16x32_bf16 v[104:107], a[44:47], a[52:55], v[104:107]\n"
"s_mov_b32 s30, 2097152\n"
"ds_read_b64_tr_b16 a[220:221], v12 offset:4096\n"
"v_mfma_f32_16x16x32_bf16 v[100:103], a[40:43], a[52:55], v[100:103]\n"
"ds_read_b64_tr_b16 a[222:223], v12 offset:6144\n"
"buffer_load_dword v44, v39, s[48:51], s30 offen\n"
"v_mfma_f32_16x16x32_bf16 v[96:99], a[36:39], a[52:55], v[96:99]\n"
"ds_read_b64_tr_b16 a[224:225], v13 offset:4096\n"
"v_mfma_f32_16x16x32_bf16 v[112:115], a[36:39], a[56:59], v[112:115]\n"
"ds_read_b64_tr_b16 a[226:227], v13 offset:6144\n"
"v_mfma_f32_16x16x32_bf16 v[116:119], a[40:43], a[56:59], v[116:119]\n"
"ds_read_b64_tr_b16 a[228:229], v14 offset:4096\n"
"v_mfma_f32_16x16x32_bf16 v[120:123], a[44:47], a[56:59], v[120:123]\n"
"ds_read_b64_tr_b16 a[230:231], v14 offset:6144\n"
"v_mfma_f32_16x16x32_bf16 v[124:127], a[96:99], a[56:59], v[124:127]\n"
"buffer_load_dwordx4 v9, s[48:51], s69 offen offset:2048 lds\n"
"v_mfma_f32_16x16x32_bf16 v[140:143], a[96:99], a[60:63], v[140:143]\n"
"v_mfma_f32_16x16x32_bf16 v[136:139], a[44:47], a[60:63], v[136:139]\n"
"v_mfma_f32_16x16x32_bf16 v[132:135], a[40:43], a[60:63], v[132:135]\n"
"v_mfma_f32_16x16x32_bf16 v[128:131], a[36:39], a[60:63], v[128:131]\n"
"v_mfma_f32_16x16x32_bf16 v[144:147], a[36:39], a[64:67], v[144:147]\n"
"v_mfma_f32_16x16x32_bf16 v[148:151], a[40:43], a[64:67], v[148:151]\n"
"v_mfma_f32_16x16x32_bf16 v[152:155], a[44:47], a[64:67], v[152:155]\n"
"v_mfma_f32_16x16x32_bf16 v[156:159], a[96:99], a[64:67], v[156:159]\n"
"buffer_load_dwordx4 v10, s[48:51], s69 offen offset:3072 lds\n"
"v_mfma_f32_16x16x32_bf16 v[172:175], a[96:99], a[68:71], v[172:175]\n"
"v_mfma_f32_16x16x32_bf16 v[168:171], a[44:47], a[68:71], v[168:171]\n"
"v_mfma_f32_16x16x32_bf16 v[164:167], a[40:43], a[68:71], v[164:167]\n"
"v_mfma_f32_16x16x32_bf16 v[160:163], a[36:39], a[68:71], v[160:163]\n"
"v_mfma_f32_16x16x32_bf16 v[176:179], a[36:39], a[80:83], v[176:179]\n"
"v_mfma_f32_16x16x32_bf16 v[180:183], a[40:43], a[80:83], v[180:183]\n"
"v_mfma_f32_16x16x32_bf16 v[184:187], a[44:47], a[80:83], v[184:187]\n"
"v_mfma_f32_16x16x32_bf16 v[188:191], a[96:99], a[80:83], v[188:191]\n"
"s_waitcnt lgkmcnt(3)\n"
"v_mfma_f32_16x16x32_bf16 v[192:195], a[216:219], a[72:75], v[192:195]\n"
"s_waitcnt lgkmcnt(0)\n"
"s_add_u32 m0, s24, 4096\n"
"s_mov_b32 s69, s79\n"
"v_mfma_f32_16x16x32_bf16 v[196:199], a[220:223], a[72:75], v[196:199]\n"
"s_lshl_b32 s69, s69, 19\n"
"v_mfma_f32_16x16x32_bf16 v[200:203], a[224:227], a[72:75], v[200:203]\n"
"s_add_u32 s69, s69, s26\n"
"v_mfma_f32_16x16x32_bf16 v[204:207], a[228:231], a[72:75], v[204:207]\n"
"buffer_load_dwordx4 v7, s[48:51], s69 offen lds\n"
"v_mfma_f32_16x16x32_bf16 v[220:223], a[228:231], a[76:79], v[220:223]\n"
"v_mfma_f32_16x16x32_bf16 v[216:219], a[224:227], a[76:79], v[216:219]\n"
"buffer_load_dwordx4 v8, s[48:51], s69 offen offset:1024 lds\n"
"v_mfma_f32_16x16x32_bf16 v[212:215], a[220:223], a[76:79], v[212:215]\n"
"v_mfma_f32_16x16x32_bf16 v[208:211], a[216:219], a[76:79], v[208:211]\n"
"buffer_load_dwordx4 v9, s[48:51], s69 offen offset:2048 lds\n"
"v_mfma_f32_16x16x32_bf16 a[104:107], a[216:219], a[48:51], a[104:107]\n"
"v_mfma_f32_16x16x32_bf16 a[108:111], a[220:223], a[48:51], a[108:111]\n"
"buffer_load_dwordx4 v10, s[48:51], s69 offen offset:3072 lds\n"
"v_mfma_f32_16x16x32_bf16 a[112:115], a[224:227], a[48:51], a[112:115]\n"
"s_mov_b32 m0, 16384\n"
"s_mov_b32 s69, s79\n"
"s_lshl_b32 s69, s69, 19\n"
"s_add_u32 s69, s69, s26\n"
"s_add_i32 s69, s69, 65536\n"
"s_waitcnt vmcnt(21)\n"
"ds_read_b64_tr_b16 a[36:37], v11 offset:8192\n"
"v_mfma_f32_16x16x32_bf16 a[116:119], a[228:231], a[48:51], a[116:119]\n"
"buffer_load_dwordx4 v7, s[48:51], s69 offen lds\n"
"v_mfma_f32_16x16x32_bf16 a[132:135], a[228:231], a[52:55], a[132:135]\n"
"ds_read_b64_tr_b16 a[38:39], v11 offset:10240\n"
"v_mfma_f32_16x16x32_bf16 a[128:131], a[224:227], a[52:55], a[128:131]\n"
"buffer_load_dwordx4 v8, s[48:51], s69 offen offset:1024 lds\n"
"v_mfma_f32_16x16x32_bf16 a[124:127], a[220:223], a[52:55], a[124:127]\n"
"ds_read_b64_tr_b16 a[40:41], v12 offset:8192\n"
"v_mfma_f32_16x16x32_bf16 a[120:123], a[216:219], a[52:55], a[120:123]\n"
"buffer_load_dwordx4 v9, s[48:51], s69 offen offset:2048 lds\n"
"v_mfma_f32_16x16x32_bf16 a[136:139], a[216:219], a[56:59], a[136:139]\n"
"ds_read_b64_tr_b16 a[42:43], v12 offset:10240\n"
"v_mfma_f32_16x16x32_bf16 a[140:143], a[220:223], a[56:59], a[140:143]\n"
"buffer_load_dwordx4 v10, s[48:51], s69 offen offset:3072 lds\n"
"v_mfma_f32_16x16x32_bf16 a[144:147], a[224:227], a[56:59], a[144:147]\n"
"s_mov_b32 m0, 28672\n"
"s_mov_b32 s69, s79\n"
"s_lshl_b32 s69, s69, 19\n"
"s_add_u32 s69, s69, s26\n"
"s_add_i32 s69, s69, 131072\n"
"ds_read_b64_tr_b16 a[44:45], v13 offset:8192\n"
"v_mfma_f32_16x16x32_bf16 a[148:151], a[228:231], a[56:59], a[148:151]\n"
"buffer_load_dwordx4 v7, s[48:51], s69 offen lds\n"
"v_mfma_f32_16x16x32_bf16 a[164:167], a[228:231], a[60:63], a[164:167]\n"
"ds_read_b64_tr_b16 a[46:47], v13 offset:10240\n"
"v_mfma_f32_16x16x32_bf16 a[160:163], a[224:227], a[60:63], a[160:163]\n"
"buffer_load_dwordx4 v8, s[48:51], s69 offen offset:1024 lds\n"
"v_mfma_f32_16x16x32_bf16 a[156:159], a[220:223], a[60:63], a[156:159]\n"
"ds_read_b64_tr_b16 a[96:97], v14 offset:8192\n"
"v_mfma_f32_16x16x32_bf16 a[152:155], a[216:219], a[60:63], a[152:155]\n"
"buffer_load_dwordx4 v9, s[48:51], s69 offen offset:2048 lds\n"
"v_mfma_f32_16x16x32_bf16 a[168:171], a[216:219], a[64:67], a[168:171]\n"
"s_barrier\n"
"ds_read_b64_tr_b16 a[98:99], v14 offset:10240\n"
"v_mfma_f32_16x16x32_bf16 a[172:175], a[220:223], a[64:67], a[172:175]\n"
"buffer_load_dwordx4 v10, s[48:51], s69 offen offset:3072 lds\n"
"v_mfma_f32_16x16x32_bf16 a[176:179], a[224:227], a[64:67], a[176:179]\n"
"s_mov_b32 m0, 40960\n"
"s_mov_b32 s69, s79\n"
"s_lshl_b32 s69, s69, 19\n"
"s_add_u32 s69, s69, s26\n"
"s_add_i32 s69, s69, 196608\n"
"v_mfma_f32_16x16x32_bf16 a[180:183], a[228:231], a[64:67], a[180:183]\n"
"buffer_load_dwordx4 v7, s[48:51], s69 offen lds\n"
"v_mfma_f32_16x16x32_bf16 a[196:199], a[228:231], a[68:71], a[196:199]\n"
"v_mfma_f32_16x16x32_bf16 a[192:195], a[224:227], a[68:71], a[192:195]\n"
"buffer_load_dwordx4 v8, s[48:51], s69 offen offset:1024 lds\n"
"v_mfma_f32_16x16x32_bf16 a[188:191], a[220:223], a[68:71], a[188:191]\n"
"v_mfma_f32_16x16x32_bf16 a[184:187], a[216:219], a[68:71], a[184:187]\n"
"buffer_load_dwordx4 v9, s[48:51], s69 offen offset:2048 lds\n"
"v_mfma_f32_16x16x32_bf16 a[200:203], a[216:219], a[80:83], a[200:203]\n"
"v_mfma_f32_16x16x32_bf16 a[204:207], a[220:223], a[80:83], a[204:207]\n"
"buffer_load_dwordx4 v10, s[48:51], s69 offen offset:3072 lds\n"
"v_mfma_f32_16x16x32_bf16 a[208:211], a[224:227], a[80:83], a[208:211]\n"
"v_mfma_f32_16x16x32_bf16 a[212:215], a[228:231], a[80:83], a[212:215]\n"
"s_waitcnt lgkmcnt(1)\n"
"v_mfma_f32_16x16x32_bf16 v[48:51], a[36:39], v[244:247], v[48:51]\n"
"s_waitcnt lgkmcnt(0)\n"
"s_add_u32 m0, s24, 8192\n"
"s_mov_b32 s69, s79\n"
"v_mfma_f32_16x16x32_bf16 v[52:55], a[40:43], v[244:247], v[52:55]\n"
"s_lshl_b32 s69, s69, 19\n"
"v_mfma_f32_16x16x32_bf16 v[56:59], a[44:47], v[244:247], v[56:59]\n"
"s_add_u32 s69, s69, s31\n"
"v_mfma_f32_16x16x32_bf16 v[60:63], a[96:99], v[244:247], v[60:63]\n"
"buffer_load_dwordx4 v7, s[48:51], s69 offen lds\n"
"v_mfma_f32_16x16x32_bf16 v[76:79], a[96:99], v[248:251], v[76:79]\n"
"v_mfma_f32_16x16x32_bf16 v[72:75], a[44:47], v[248:251], v[72:75]\n"
"buffer_load_dwordx4 v8, s[48:51], s69 offen offset:1024 lds\n"
"v_mfma_f32_16x16x32_bf16 v[68:71], a[40:43], v[248:251], v[68:71]\n"
"v_mfma_f32_16x16x32_bf16 v[64:67], a[36:39], v[248:251], v[64:67]\n"
"buffer_load_dwordx4 v9, s[48:51], s69 offen offset:2048 lds\n"
"v_mfma_f32_16x16x32_bf16 v[80:83], a[36:39], a[84:87], v[80:83]\n"
"v_mfma_f32_16x16x32_bf16 v[84:87], a[40:43], a[84:87], v[84:87]\n"
"buffer_load_dwordx4 v10, s[48:51], s69 offen offset:3072 lds\n"
"v_mfma_f32_16x16x32_bf16 v[88:91], a[44:47], a[84:87], v[88:91]\n"
"s_mov_b32 m0, 20480\n"
"s_cmp_le_u32 s65, 0\n"
"s_cselect_b32 s69, 1, 0\n"
"s_add_u32 s69, s69, s66\n"
"s_lshl_b32 s69, s69, 19\n"
"s_add_u32 s69, s69, s31\n"
"s_add_i32 s69, s69, -196608\n"
"s_waitcnt vmcnt(20)\n"
"ds_read_b64_tr_b16 a[216:217], v11\n"
"v_mfma_f32_16x16x32_bf16 v[92:95], a[96:99], a[84:87], v[92:95]\n"
"buffer_load_dwordx4 v7, s[48:51], s69 offen lds\n"
"v_mfma_f32_16x16x32_bf16 v[108:111], a[96:99], a[88:91], v[108:111]\n"
"ds_read_b64_tr_b16 a[218:219], v11 offset:2048\n"
"v_mfma_f32_16x16x32_bf16 v[104:107], a[44:47], a[88:91], v[104:107]\n"
"buffer_load_dwordx4 v8, s[48:51], s69 offen offset:1024 lds\n"
"v_mfma_f32_16x16x32_bf16 v[100:103], a[40:43], a[88:91], v[100:103]\n"
"ds_read_b64_tr_b16 a[220:221], v12\n"
"v_mfma_f32_16x16x32_bf16 v[96:99], a[36:39], a[88:91], v[96:99]\n"
"buffer_load_dwordx4 v9, s[48:51], s69 offen offset:2048 lds\n"
"v_mfma_f32_16x16x32_bf16 v[112:115], a[36:39], a[92:95], v[112:115]\n"
"ds_read_b64_tr_b16 a[222:223], v12 offset:2048\n"
"v_mfma_f32_16x16x32_bf16 v[116:119], a[40:43], a[92:95], v[116:119]\n"
"buffer_load_dwordx4 v10, s[48:51], s69 offen offset:3072 lds\n"
"v_mfma_f32_16x16x32_bf16 v[120:123], a[44:47], a[92:95], v[120:123]\n"
"s_mov_b32 m0, 32768\n"
"s_cmp_le_u32 s65, 0\n"
"s_cselect_b32 s69, 1, 0\n"
"s_add_u32 s69, s69, s66\n"
"s_lshl_b32 s69, s69, 19\n"
"s_add_u32 s69, s69, s31\n"
"s_add_i32 s69, s69, -131072\n"
"ds_read_b64_tr_b16 a[224:225], v13\n"
"v_mfma_f32_16x16x32_bf16 v[124:127], a[96:99], a[92:95], v[124:127]\n"
"buffer_load_dwordx4 v7, s[48:51], s69 offen lds\n"
"v_mfma_f32_16x16x32_bf16 v[140:143], a[96:99], a[100:103], v[140:143]\n"
"ds_read_b64_tr_b16 a[226:227], v13 offset:2048\n"
"v_mfma_f32_16x16x32_bf16 v[136:139], a[44:47], a[100:103], v[136:139]\n"
"buffer_load_dwordx4 v8, s[48:51], s69 offen offset:1024 lds\n"
"v_mfma_f32_16x16x32_bf16 v[132:135], a[40:43], a[100:103], v[132:135]\n"
"ds_read_b64_tr_b16 a[228:229], v14\n"
"v_mfma_f32_16x16x32_bf16 v[128:131], a[36:39], a[100:103], v[128:131]\n"
"buffer_load_dwordx4 v9, s[48:51], s69 offen offset:2048 lds\n"
"v_mfma_f32_16x16x32_bf16 v[144:147], a[36:39], a[248:251], v[144:147]\n"
"ds_read_b64_tr_b16 a[230:231], v14 offset:2048\n"
"v_mfma_f32_16x16x32_bf16 v[148:151], a[40:43], a[248:251], v[148:151]\n"
"buffer_load_dwordx4 v10, s[48:51], s69 offen offset:3072 lds\n"
"v_mfma_f32_16x16x32_bf16 v[152:155], a[44:47], a[248:251], v[152:155]\n"
"s_mov_b32 m0, 45056\n"
"s_cmp_le_u32 s65, 0\n"
"s_cselect_b32 s69, 1, 0\n"
"s_add_u32 s69, s69, s66\n"
"s_lshl_b32 s69, s69, 19\n"
"s_add_u32 s69, s69, s31\n"
"s_add_i32 s69, s69, -65536\n"
"v_mfma_f32_16x16x32_bf16 v[156:159], a[96:99], a[248:251], v[156:159]\n"
"buffer_load_dwordx4 v7, s[48:51], s69 offen lds\n"
"v_mfma_f32_16x16x32_bf16 v[172:175], a[96:99], a[252:255], v[172:175]\n"
"v_mfma_f32_16x16x32_bf16 v[168:171], a[44:47], a[252:255], v[168:171]\n"
"buffer_load_dwordx4 v8, s[48:51], s69 offen offset:1024 lds\n"
"v_mfma_f32_16x16x32_bf16 v[164:167], a[40:43], a[252:255], v[164:167]\n"
"v_mfma_f32_16x16x32_bf16 v[160:163], a[36:39], a[252:255], v[160:163]\n"
"buffer_load_dwordx4 v9, s[48:51], s69 offen offset:2048 lds\n"
"v_mfma_f32_16x16x32_bf16 v[176:179], a[36:39], v[252:255], v[176:179]\n"
"v_mfma_f32_16x16x32_bf16 v[180:183], a[40:43], v[252:255], v[180:183]\n"
"buffer_load_dwordx4 v10, s[48:51], s69 offen offset:3072 lds\n"
"v_mfma_f32_16x16x32_bf16 v[184:187], a[44:47], v[252:255], v[184:187]\n"
"v_mfma_f32_16x16x32_bf16 v[188:191], a[96:99], v[252:255], v[188:191]\n"
"s_waitcnt lgkmcnt(1)\n"
"v_mfma_f32_16x16x32_bf16 v[192:195], a[216:219], v[244:247], v[192:195]\n"
"s_waitcnt lgkmcnt(0)\n"
"s_add_u32 m0, s24, 0\n"
"s_cmp_le_u32 s65, 0\n"
"s_cselect_b32 s69, 1, 0\n"
"s_add_u32 s69, s69, s66\n"
"s_barrier\n"
"v_mfma_f32_16x16x32_bf16 v[196:199], a[220:223], v[244:247], v[196:199]\n"
"s_lshl_b32 s69, s69, 19\n"
"v_mfma_f32_16x16x32_bf16 v[200:203], a[224:227], v[244:247], v[200:203]\n"
"s_add_u32 s69, s69, s26\n"
"v_mfma_f32_16x16x32_bf16 v[204:207], a[228:231], v[244:247], v[204:207]\n"
"buffer_load_dwordx4 v7, s[48:51], s69 offen lds\n"
"v_mfma_f32_16x16x32_bf16 v[220:223], a[228:231], v[248:251], v[220:223]\n"
"v_mfma_f32_16x16x32_bf16 v[216:219], a[224:227], v[248:251], v[216:219]\n"
"v_mfma_f32_16x16x32_bf16 v[212:215], a[220:223], v[248:251], v[212:215]\n"
"v_mfma_f32_16x16x32_bf16 v[208:211], a[216:219], v[248:251], v[208:211]\n"
"v_mfma_f32_16x16x32_bf16 a[104:107], a[216:219], a[84:87], a[104:107]\n"
"v_mfma_f32_16x16x32_bf16 a[108:111], a[220:223], a[84:87], a[108:111]\n"
"v_mfma_f32_16x16x32_bf16 a[112:115], a[224:227], a[84:87], a[112:115]\n"
"s_waitcnt vmcnt(29)\n"
"ds_read_b64_tr_b16 a[36:37], v11 offset:4096\n"
"v_mfma_f32_16x16x32_bf16 a[116:119], a[228:231], a[84:87], a[116:119]\n"
"buffer_load_dwordx4 v8, s[48:51], s69 offen offset:1024 lds\n"
"v_mfma_f32_16x16x32_bf16 a[132:135], a[228:231], a[88:91], a[132:135]\n"
"ds_read_b64_tr_b16 a[38:39], v11 offset:6144\n"
"v_mfma_f32_16x16x32_bf16 a[128:131], a[224:227], a[88:91], a[128:131]\n"
"ds_read_b64_tr_b16 a[40:41], v12 offset:4096\n"
"v_mfma_f32_16x16x32_bf16 a[124:127], a[220:223], a[88:91], a[124:127]\n"
"ds_read_b64_tr_b16 a[42:43], v12 offset:6144\n"
"v_mfma_f32_16x16x32_bf16 a[120:123], a[216:219], a[88:91], a[120:123]\n"
"ds_read_b64_tr_b16 a[44:45], v13 offset:4096\n"
"v_mfma_f32_16x16x32_bf16 a[136:139], a[216:219], a[92:95], a[136:139]\n"
"ds_read_b64_tr_b16 a[46:47], v13 offset:6144\n"
"v_mfma_f32_16x16x32_bf16 a[140:143], a[220:223], a[92:95], a[140:143]\n"
"ds_read_b64_tr_b16 a[96:97], v14 offset:4096\n"
"v_mfma_f32_16x16x32_bf16 a[144:147], a[224:227], a[92:95], a[144:147]\n"
"ds_read_b64_tr_b16 a[98:99], v14 offset:6144\n"
"v_mfma_f32_16x16x32_bf16 a[148:151], a[228:231], a[92:95], a[148:151]\n"
"buffer_load_dwordx4 v9, s[48:51], s69 offen offset:2048 lds\n"
"v_mfma_f32_16x16x32_bf16 a[164:167], a[228:231], a[100:103], a[164:167]\n"
"v_mfma_f32_16x16x32_bf16 a[160:163], a[224:227], a[100:103], a[160:163]\n"
"v_mfma_f32_16x16x32_bf16 a[156:159], a[220:223], a[100:103], a[156:159]\n"
"v_mfma_f32_16x16x32_bf16 a[152:155], a[216:219], a[100:103], a[152:155]\n"
"v_mfma_f32_16x16x32_bf16 a[168:171], a[216:219], a[248:251], a[168:171]\n"
"v_mfma_f32_16x16x32_bf16 a[172:175], a[220:223], a[248:251], a[172:175]\n"
"v_mfma_f32_16x16x32_bf16 a[176:179], a[224:227], a[248:251], a[176:179]\n"
"v_mfma_f32_16x16x32_bf16 a[180:183], a[228:231], a[248:251], a[180:183]\n"
"buffer_load_dwordx4 v10, s[48:51], s69 offen offset:3072 lds\n"
"v_mfma_f32_16x16x32_bf16 a[196:199], a[228:231], a[252:255], a[196:199]\n"
"v_mfma_f32_16x16x32_bf16 a[192:195], a[224:227], a[252:255], a[192:195]\n"
"v_mfma_f32_16x16x32_bf16 a[188:191], a[220:223], a[252:255], a[188:191]\n"
"v_mfma_f32_16x16x32_bf16 a[184:187], a[216:219], a[252:255], a[184:187]\n"
"v_mfma_f32_16x16x32_bf16 a[200:203], a[216:219], v[252:255], a[200:203]\n"
"v_mfma_f32_16x16x32_bf16 a[204:207], a[220:223], v[252:255], a[204:207]\n"
"v_mfma_f32_16x16x32_bf16 a[208:211], a[224:227], v[252:255], a[208:211]\n"
"v_mfma_f32_16x16x32_bf16 a[212:215], a[228:231], v[252:255], a[212:215]\n"
"s_waitcnt lgkmcnt(3)\n"
"v_mfma_f32_16x16x32_bf16 v[48:51], a[36:39], v[232:235], v[48:51]\n"
"s_waitcnt lgkmcnt(0)\n"
"s_add_u32 m0, s24, 4096\n"
"s_mov_b32 s69, s79\n"
"s_lshl_b32 s69, s69, 19\n"
"v_mfma_f32_16x16x32_bf16 v[52:55], a[40:43], v[232:235], v[52:55]\n"
"s_add_u32 s69, s69, s26\n"
"v_mfma_f32_16x16x32_bf16 v[56:59], a[44:47], v[232:235], v[56:59]\n"
"s_add_i32 s69, s69, 262144\n"
"v_mfma_f32_16x16x32_bf16 v[60:63], a[96:99], v[232:235], v[60:63]\n"
"buffer_load_dwordx4 v7, s[48:51], s69 offen lds\n"
"v_mfma_f32_16x16x32_bf16 v[76:79], a[96:99], v[236:239], v[76:79]\n"
"v_mfma_f32_16x16x32_bf16 v[72:75], a[44:47], v[236:239], v[72:75]\n"
"v_mfma_f32_16x16x32_bf16 v[68:71], a[40:43], v[236:239], v[68:71]\n"
"v_mfma_f32_16x16x32_bf16 v[64:67], a[36:39], v[236:239], v[64:67]\n"
"v_mfma_f32_16x16x32_bf16 v[80:83], a[36:39], a[232:235], v[80:83]\n"
"v_mfma_f32_16x16x32_bf16 v[84:87], a[40:43], a[232:235], v[84:87]\n"
"v_mfma_f32_16x16x32_bf16 v[88:91], a[44:47], a[232:235], v[88:91]\n"
"s_waitcnt vmcnt(17)\n"
"ds_read_b64_tr_b16 a[216:217], v11 offset:8192\n"
"v_mfma_f32_16x16x32_bf16 v[92:95], a[96:99], a[232:235], v[92:95]\n"
"buffer_load_dwordx4 v8, s[48:51], s69 offen offset:1024 lds\n"
"v_mfma_f32_16x16x32_bf16 v[108:111], a[96:99], a[236:239], v[108:111]\n"
"ds_read_b64_tr_b16 a[218:219], v11 offset:10240\n"
"v_mfma_f32_16x16x32_bf16 v[104:107], a[44:47], a[236:239], v[104:107]\n"
"ds_read_b64_tr_b16 a[220:221], v12 offset:8192\n"
"v_mfma_f32_16x16x32_bf16 v[100:103], a[40:43], a[236:239], v[100:103]\n"
"ds_read_b64_tr_b16 a[222:223], v12 offset:10240\n"
"v_mfma_f32_16x16x32_bf16 v[96:99], a[36:39], a[236:239], v[96:99]\n"
"ds_read_b64_tr_b16 a[224:225], v13 offset:8192\n"
"v_mfma_f32_16x16x32_bf16 v[112:115], a[36:39], a[240:243], v[112:115]\n"
"ds_read_b64_tr_b16 a[226:227], v13 offset:10240\n"
"v_mfma_f32_16x16x32_bf16 v[116:119], a[40:43], a[240:243], v[116:119]\n"
"ds_read_b64_tr_b16 a[228:229], v14 offset:8192\n"
"v_mfma_f32_16x16x32_bf16 v[120:123], a[44:47], a[240:243], v[120:123]\n"
"ds_read_b64_tr_b16 a[230:231], v14 offset:10240\n"
"v_mfma_f32_16x16x32_bf16 v[124:127], a[96:99], a[240:243], v[124:127]\n"
"buffer_load_dwordx4 v9, s[48:51], s69 offen offset:2048 lds\n"
"v_mfma_f32_16x16x32_bf16 v[140:143], a[96:99], a[244:247], v[140:143]\n"
"v_mfma_f32_16x16x32_bf16 v[136:139], a[44:47], a[244:247], v[136:139]\n"
"v_mfma_f32_16x16x32_bf16 v[132:135], a[40:43], a[244:247], v[132:135]\n"
"v_mfma_f32_16x16x32_bf16 v[128:131], a[36:39], a[244:247], v[128:131]\n"
"v_mfma_f32_16x16x32_bf16 v[144:147], a[36:39], v[224:227], v[144:147]\n"
"v_mfma_f32_16x16x32_bf16 v[148:151], a[40:43], v[224:227], v[148:151]\n"
"v_mfma_f32_16x16x32_bf16 v[152:155], a[44:47], v[224:227], v[152:155]\n"
"v_mfma_f32_16x16x32_bf16 v[156:159], a[96:99], v[224:227], v[156:159]\n"
"buffer_load_dwordx4 v10, s[48:51], s69 offen offset:3072 lds\n"
"v_mfma_f32_16x16x32_bf16 v[172:175], a[96:99], v[228:231], v[172:175]\n"
"v_mfma_f32_16x16x32_bf16 v[168:171], a[44:47], v[228:231], v[168:171]\n"
"v_mfma_f32_16x16x32_bf16 v[164:167], a[40:43], v[228:231], v[164:167]\n"
"v_mfma_f32_16x16x32_bf16 v[160:163], a[36:39], v[228:231], v[160:163]\n"
"v_mfma_f32_16x16x32_bf16 v[176:179], a[36:39], v[240:243], v[176:179]\n"
"v_mfma_f32_16x16x32_bf16 v[180:183], a[40:43], v[240:243], v[180:183]\n"
"v_mfma_f32_16x16x32_bf16 v[184:187], a[44:47], v[240:243], v[184:187]\n"
"v_mfma_f32_16x16x32_bf16 v[188:191], a[96:99], v[240:243], v[188:191]\n"
"s_waitcnt lgkmcnt(3)\n"
"v_mfma_f32_16x16x32_bf16 v[192:195], a[216:219], v[232:235], v[192:195]\n"
"s_waitcnt lgkmcnt(0)\n"
"s_add_u32 m0, s24, 8192\n"
"s_cmp_le_u32 s65, 0\n"
"s_cselect_b32 s69, 1, 0\n"
"s_add_u32 s69, s69, s66\n"
"v_mfma_f32_16x16x32_bf16 v[196:199], a[220:223], v[232:235], v[196:199]\n"
"s_lshl_b32 s69, s69, 19\n"
"v_mfma_f32_16x16x32_bf16 v[200:203], a[224:227], v[232:235], v[200:203]\n"
"s_add_u32 s69, s69, s31\n"
"v_mfma_f32_16x16x32_bf16 v[204:207], a[228:231], v[232:235], v[204:207]\n"
"buffer_load_dwordx4 v7, s[48:51], s69 offen lds\n"
"v_mfma_f32_16x16x32_bf16 v[220:223], a[228:231], v[236:239], v[220:223]\n"
"v_mfma_f32_16x16x32_bf16 v[216:219], a[224:227], v[236:239], v[216:219]\n"
"buffer_load_dwordx4 v8, s[48:51], s69 offen offset:1024 lds\n"
"v_mfma_f32_16x16x32_bf16 v[212:215], a[220:223], v[236:239], v[212:215]\n"
"v_mfma_f32_16x16x32_bf16 v[208:211], a[216:219], v[236:239], v[208:211]\n"
"v_mfma_f32_16x16x32_bf16 a[104:107], a[216:219], a[232:235], a[104:107]\n"
"buffer_load_dwordx4 v9, s[48:51], s69 offen offset:2048 lds\n"
"v_mfma_f32_16x16x32_bf16 a[108:111], a[220:223], a[232:235], a[108:111]\n"
"s_cmp_le_u32 s65, 0\n"
"s_cselect_b32 s70, 1, 0\n"
"v_mfma_f32_16x16x32_bf16 a[112:115], a[224:227], a[232:235], a[112:115]\n"
"s_add_u32 s70, s70, s66\n"
"buffer_load_dwordx4 v10, s[48:51], s69 offen offset:3072 lds\n"
"v_mfma_f32_16x16x32_bf16 a[116:119], a[228:231], a[232:235], a[116:119]\n"
"s_mul_i32 s70, s70, 73728\n"
"s_waitcnt vmcnt(8)\n"
"ds_read_b64_tr_b16 a[36:37], v11\n"
"v_mfma_f32_16x16x32_bf16 a[132:135], a[228:231], a[236:239], a[132:135]\n"
"s_add_u32 s70, s70, s25\n"
"ds_read_b64_tr_b16 a[38:39], v11 offset:2048\n"
"v_mfma_f32_16x16x32_bf16 a[128:131], a[224:227], a[236:239], a[128:131]\n"
"buffer_load_dwordx4 a[0:3], v2, s[44:47], s70 offen sc1\n"
"v_mfma_f32_16x16x32_bf16 a[124:127], a[220:223], a[236:239], a[124:127]\n"
"ds_read_b64_tr_b16 a[40:41], v12\n"
"v_mfma_f32_16x16x32_bf16 a[120:123], a[216:219], a[236:239], a[120:123]\n"
"buffer_load_dwordx4 a[4:7], v2, s[44:47], s70 offen offset:1024 sc1\n"
"v_mfma_f32_16x16x32_bf16 a[136:139], a[216:219], a[240:243], a[136:139]\n"
"ds_read_b64_tr_b16 a[42:43], v12 offset:2048\n"
"v_mfma_f32_16x16x32_bf16 a[140:143], a[220:223], a[240:243], a[140:143]\n"
"ds_read_b64_tr_b16 a[44:45], v13\n"
"v_mfma_f32_16x16x32_bf16 a[144:147], a[224:227], a[240:243], a[144:147]\n"
"buffer_load_dwordx4 a[8:11], v3, s[44:47], s70 offen sc1\n"
"v_mfma_f32_16x16x32_bf16 a[148:151], a[228:231], a[240:243], a[148:151]\n"
"ds_read_b64_tr_b16 a[46:47], v13 offset:2048\n"
"v_mfma_f32_16x16x32_bf16 a[164:167], a[228:231], a[244:247], a[164:167]\n"
"buffer_load_dwordx4 a[12:15], v3, s[44:47], s70 offen offset:1024 sc1\n"
"v_mfma_f32_16x16x32_bf16 a[160:163], a[224:227], a[244:247], a[160:163]\n"
"ds_read_b64_tr_b16 a[96:97], v14\n"
"v_mfma_f32_16x16x32_bf16 a[156:159], a[220:223], a[244:247], a[156:159]\n"
"ds_read_b64_tr_b16 a[98:99], v14 offset:2048\n"
"v_mfma_f32_16x16x32_bf16 a[152:155], a[216:219], a[244:247], a[152:155]\n"
"buffer_load_dwordx4 a[16:19], v4, s[44:47], s70 offen sc1\n"
"v_mfma_f32_16x16x32_bf16 a[168:171], a[216:219], v[224:227], a[168:171]\n"
"v_mfma_f32_16x16x32_bf16 a[172:175], a[220:223], v[224:227], a[172:175]\n"
"buffer_load_dwordx4 a[20:23], v4, s[44:47], s70 offen offset:1024 sc1\n"
"v_mfma_f32_16x16x32_bf16 a[176:179], a[224:227], v[224:227], a[176:179]\n"
"v_mfma_f32_16x16x32_bf16 a[180:183], a[228:231], v[224:227], a[180:183]\n"
"v_mfma_f32_16x16x32_bf16 a[196:199], a[228:231], v[228:231], a[196:199]\n"
"buffer_load_dwordx4 a[24:27], v5, s[44:47], s70 offen sc1\n"
"v_mfma_f32_16x16x32_bf16 a[192:195], a[224:227], v[228:231], a[192:195]\n"
"v_mfma_f32_16x16x32_bf16 a[188:191], a[220:223], v[228:231], a[188:191]\n"
"buffer_load_dwordx4 a[28:31], v5, s[44:47], s70 offen offset:1024 sc1\n"
"v_mfma_f32_16x16x32_bf16 a[184:187], a[216:219], v[228:231], a[184:187]\n"
"v_mfma_f32_16x16x32_bf16 a[200:203], a[216:219], v[240:243], a[200:203]\n"
"v_mfma_f32_16x16x32_bf16 a[204:207], a[220:223], v[240:243], a[204:207]\n"
"buffer_load_dwordx4 a[32:35], v6, s[44:47], s70 offen sc1\n"
"v_mfma_f32_16x16x32_bf16 a[208:211], a[224:227], v[240:243], a[208:211]\n"
"v_mfma_f32_16x16x32_bf16 a[212:215], a[228:231], v[240:243], a[212:215]\n"
"s_waitcnt lgkmcnt(0)\n"
"s_add_u32 m0, s24, 0\n"
"s_mov_b32 s69, s79\n"
"s_lshl_b32 s69, s69, 19\n"
"s_add_u32 s69, s69, s31\n"
"s_add_i32 s69, s69, 262144\n"
"buffer_load_dwordx4 v7, s[48:51], s69 offen lds\n"
"buffer_load_dwordx4 v8, s[48:51], s69 offen offset:1024 lds\n"
"buffer_load_dwordx4 v9, s[48:51], s69 offen offset:2048 lds\n"
"buffer_load_dwordx4 v10, s[48:51], s69 offen offset:3072 lds\n"
"s_barrier\n"
"s_waitcnt vmcnt(12)\n"
"v_mfma_f32_16x16x32_bf16 v[48:51], a[36:39], a[0:3], v[48:51]\n"
"ds_read_b64_tr_b16 a[216:217], v11 offset:4096\n"
"v_mfma_f32_16x16x32_bf16 v[52:55], a[40:43], a[0:3], v[52:55]\n"
"ds_read_b64_tr_b16 a[218:219], v11 offset:6144\n"
"v_mfma_f32_16x16x32_bf16 v[56:59], a[44:47], a[0:3], v[56:59]\n"
"ds_read_b64_tr_b16 a[220:221], v12 offset:4096\n"
"v_mfma_f32_16x16x32_bf16 v[60:63], a[96:99], a[0:3], v[60:63]\n"
"s_waitcnt vmcnt(11)\n"
"ds_read_b64_tr_b16 a[222:223], v12 offset:6144\n"
"v_mfma_f32_16x16x32_bf16 v[76:79], a[96:99], a[4:7], v[76:79]\n"
"ds_read_b64_tr_b16 a[224:225], v13 offset:4096\n"
"v_mfma_f32_16x16x32_bf16 v[72:75], a[44:47], a[4:7], v[72:75]\n"
"ds_read_b64_tr_b16 a[226:227], v13 offset:6144\n"
"v_mfma_f32_16x16x32_bf16 v[68:71], a[40:43], a[4:7], v[68:71]\n"
"ds_read_b64_tr_b16 a[228:229], v14 offset:4096\n"
"v_mfma_f32_16x16x32_bf16 v[64:67], a[36:39], a[4:7], v[64:67]\n"
"s_waitcnt vmcnt(10)\n"
"ds_read_b64_tr_b16 a[230:231], v14 offset:6144\n"
"v_mfma_f32_16x16x32_bf16 v[80:83], a[36:39], a[8:11], v[80:83]\n"
"ds_read_b64_tr_b16 a[232:233], v11 offset:8192\n"
"v_mfma_f32_16x16x32_bf16 v[84:87], a[40:43], a[8:11], v[84:87]\n"
"ds_read_b64_tr_b16 a[234:235], v11 offset:10240\n"
"v_mfma_f32_16x16x32_bf16 v[88:91], a[44:47], a[8:11], v[88:91]\n"
"s_cmp_le_u32 s65, 1\n"
"s_cselect_b32 s70, 2, 1\n"
"ds_read_b64_tr_b16 a[236:237], v12 offset:8192\n"
"v_mfma_f32_16x16x32_bf16 v[92:95], a[96:99], a[8:11], v[92:95]\n"
"s_waitcnt vmcnt(9)\n"
"s_add_u32 s70, s70, s66\n"
"ds_read_b64_tr_b16 a[238:239], v12 offset:10240\n"
"v_mfma_f32_16x16x32_bf16 v[108:111], a[96:99], a[12:15], v[108:111]\n"
"s_mul_i32 s70, s70, 73728\n"
"ds_read_b64_tr_b16 a[240:241], v13 offset:8192\n"
"v_mfma_f32_16x16x32_bf16 v[104:107], a[44:47], a[12:15], v[104:107]\n"
"s_add_u32 s70, s70, s25\n"
"ds_read_b64_tr_b16 a[242:243], v13 offset:10240\n"
"v_mfma_f32_16x16x32_bf16 v[100:103], a[40:43], a[12:15], v[100:103]\n"
"s_waitcnt lgkmcnt(6)\n"
"ds_read_b64_tr_b16 a[244:245], v14 offset:8192\n"
"buffer_load_dwordx4 a[84:87], v2, s[44:47], s70 offen sc1\n"
"v_mfma_f32_16x16x32_bf16 v[96:99], a[36:39], a[12:15], v[96:99]\n"
"s_waitcnt vmcnt(9)\n"
"s_add_u32 m0, s24, 4096\n"
"ds_read_b64_tr_b16 a[246:247], v14 offset:10240\n"
"v_mfma_f32_16x16x32_bf16 v[112:115], a[36:39], a[16:19], v[112:115]\n"
"s_cmp_le_u32 s65, 1\n"
"s_cselect_b32 s69, 2, 1\n"
"ds_read_b128 a[48:51], v15\n"
"v_mfma_f32_16x16x32_bf16 v[116:119], a[40:43], a[16:19], v[116:119]\n"
"s_add_u32 s69, s69, s66\n"
"buffer_load_dwordx4 a[88:91], v2, s[44:47], s70 offen offset:1024 sc1\n"
"v_mfma_f32_16x16x32_bf16 v[120:123], a[44:47], a[16:19], v[120:123]\n"
"s_lshl_b32 s69, s69, 19\n"
"ds_read_b128 a[52:55], v15 offset:1024\n"
"v_mfma_f32_16x16x32_bf16 v[124:127], a[96:99], a[16:19], v[124:127]\n"
"s_waitcnt vmcnt(9)\n"
"s_add_u32 s69, s69, s26\n"
"ds_read_b128 a[56:59], v16\n"
"v_mfma_f32_16x16x32_bf16 v[140:143], a[96:99], a[20:23], v[140:143]\n"
"buffer_load_dwordx4 v7, s[48:51], s69 offen lds\n"
"v_mfma_f32_16x16x32_bf16 v[136:139], a[44:47], a[20:23], v[136:139]\n"
"ds_read_b128 a[60:63], v16 offset:1024\n"
"v_mfma_f32_16x16x32_bf16 v[132:135], a[40:43], a[20:23], v[132:135]\n"
"ds_read_b128 a[64:67], v21\n"
"v_mfma_f32_16x16x32_bf16 v[128:131], a[36:39], a[20:23], v[128:131]\n"
"s_waitcnt vmcnt(9)\n"
"ds_read_b128 a[68:71], v21 offset:1024\n"
"v_mfma_f32_16x16x32_bf16 v[144:147], a[36:39], a[24:27], v[144:147]\n"
"buffer_load_dwordx4 v8, s[48:51], s69 offen offset:1024 lds\n"
"v_mfma_f32_16x16x32_bf16 v[148:151], a[40:43], a[24:27], v[148:151]\n"
"ds_read_b128 a[72:75], v22\n"
"v_mfma_f32_16x16x32_bf16 v[152:155], a[44:47], a[24:27], v[152:155]\n"
"ds_read_b128 a[76:79], v22 offset:1024\n"
"v_mfma_f32_16x16x32_bf16 v[156:159], a[96:99], a[24:27], v[156:159]\n"
"s_waitcnt vmcnt(9)\n"
"buffer_load_dwordx4 v9, s[48:51], s69 offen offset:2048 lds\n"
"v_mfma_f32_16x16x32_bf16 v[172:175], a[96:99], a[28:31], v[172:175]\n"
"ds_read_b128 a[80:83], v37\n"
"v_mfma_f32_16x16x32_bf16 v[168:171], a[44:47], a[28:31], v[168:171]\n"
"v_mfma_f32_16x16x32_bf16 v[164:167], a[40:43], a[28:31], v[164:167]\n"
"v_mfma_f32_16x16x32_bf16 v[160:163], a[36:39], a[28:31], v[160:163]\n"
"s_waitcnt vmcnt(9)\n"
"buffer_load_dwordx4 v10, s[48:51], s69 offen offset:3072 lds\n"
"v_mfma_f32_16x16x32_bf16 v[176:179], a[36:39], a[32:35], v[176:179]\n"
"v_mfma_f32_16x16x32_bf16 v[180:183], a[40:43], a[32:35], v[180:183]\n"
"s_mul_i32 s29, s79, 73728\n"
"v_mfma_f32_16x16x32_bf16 v[184:187], a[44:47], a[32:35], v[184:187]\n"
"s_add_u32 s29, s29, s25\n"
"buffer_load_dwordx4 a[92:95], v3, s[44:47], s70 offen sc1\n"
"v_mfma_f32_16x16x32_bf16 v[188:191], a[96:99], a[32:35], v[188:191]\n"
"s_waitcnt lgkmcnt(3)\n"
"s_add_u32 s29, s29, 36864\n"
"v_mfma_f32_16x16x32_bf16 v[48:51], a[216:219], a[48:51], v[48:51]\n"
"s_add_u32 m0, s24, 8192\n"
"v_mfma_f32_16x16x32_bf16 v[52:55], a[220:223], a[48:51], v[52:55]\n"
"s_cmp_le_u32 s65, 2\n"
"s_cselect_b32 s69, 3, 2\n"
"buffer_load_dwordx4 a[100:103], v3, s[44:47], s70 offen offset:1024 sc1\n"
"v_mfma_f32_16x16x32_bf16 v[56:59], a[224:227], a[48:51], v[56:59]\n"
"s_add_u32 s69, s69, s66\n"
"v_mfma_f32_16x16x32_bf16 v[60:63], a[228:231], a[48:51], v[60:63]\n"
"s_lshl_b32 s69, s69, 19\n"
"v_mfma_f32_16x16x32_bf16 v[76:79], a[228:231], a[52:55], v[76:79]\n"
"s_add_u32 s69, s69, s26\n"
"v_mfma_f32_16x16x32_bf16 v[72:75], a[224:227], a[52:55], v[72:75]\n"
"buffer_load_dwordx4 v7, s[48:51], s69 offen lds\n"
"v_mfma_f32_16x16x32_bf16 v[68:71], a[220:223], a[52:55], v[68:71]\n"
"v_mfma_f32_16x16x32_bf16 v[64:67], a[216:219], a[52:55], v[64:67]\n"
"v_mfma_f32_16x16x32_bf16 v[80:83], a[216:219], a[56:59], v[80:83]\n"
"buffer_load_dwordx4 v8, s[48:51], s69 offen offset:1024 lds\n"
"v_mfma_f32_16x16x32_bf16 v[84:87], a[220:223], a[56:59], v[84:87]\n"
"v_mfma_f32_16x16x32_bf16 v[88:91], a[224:227], a[56:59], v[88:91]\n"
"v_mfma_f32_16x16x32_bf16 v[92:95], a[228:231], a[56:59], v[92:95]\n"
"v_mfma_f32_16x16x32_bf16 v[108:111], a[228:231], a[60:63], v[108:111]\n"
"buffer_load_dwordx4 v9, s[48:51], s69 offen offset:2048 lds\n"
"v_mfma_f32_16x16x32_bf16 v[104:107], a[224:227], a[60:63], v[104:107]\n"
"s_waitcnt vmcnt(11)\n"
"ds_read_b64_tr_b16 v[224:225], v11\n"
"v_mfma_f32_16x16x32_bf16 v[100:103], a[220:223], a[60:63], v[100:103]\n"
"ds_read_b64_tr_b16 v[226:227], v11 offset:2048\n"
"v_mfma_f32_16x16x32_bf16 v[96:99], a[216:219], a[60:63], v[96:99]\n"
"buffer_load_dwordx4 v10, s[48:51], s69 offen offset:3072 lds\n"
"v_mfma_f32_16x16x32_bf16 v[112:115], a[216:219], a[64:67], v[112:115]\n"
"ds_read_b64_tr_b16 v[228:229], v12\n"
"v_mfma_f32_16x16x32_bf16 v[116:119], a[220:223], a[64:67], v[116:119]\n"
"ds_read_b64_tr_b16 v[230:231], v12 offset:2048\n"
"v_mfma_f32_16x16x32_bf16 v[120:123], a[224:227], a[64:67], v[120:123]\n"
"buffer_load_dwordx4 a[248:251], v4, s[44:47], s70 offen sc1\n"
"v_mfma_f32_16x16x32_bf16 v[124:127], a[228:231], a[64:67], v[124:127]\n"
"ds_read_b64_tr_b16 v[232:233], v13\n"
"v_mfma_f32_16x16x32_bf16 v[140:143], a[228:231], a[68:71], v[140:143]\n"
"ds_read_b64_tr_b16 v[234:235], v13 offset:2048\n"
"v_mfma_f32_16x16x32_bf16 v[136:139], a[224:227], a[68:71], v[136:139]\n"
"ds_read_b64_tr_b16 v[236:237], v14\n"
"v_mfma_f32_16x16x32_bf16 v[132:135], a[220:223], a[68:71], v[132:135]\n"
"buffer_load_dwordx4 a[252:255], v4, s[44:47], s70 offen offset:1024 sc1\n"
"v_mfma_f32_16x16x32_bf16 v[128:131], a[216:219], a[68:71], v[128:131]\n"
"s_waitcnt lgkmcnt(7)\n"
"ds_read_b64_tr_b16 v[238:239], v14 offset:2048\n"
"v_mfma_f32_16x16x32_bf16 v[144:147], a[216:219], a[72:75], v[144:147]\n"
"v_mfma_f32_16x16x32_bf16 v[148:151], a[220:223], a[72:75], v[148:151]\n"
"buffer_load_dwordx4 v[244:247], v5, s[44:47], s70 offen sc1\n"
"v_mfma_f32_16x16x32_bf16 v[152:155], a[224:227], a[72:75], v[152:155]\n"
"v_mfma_f32_16x16x32_bf16 v[156:159], a[228:231], a[72:75], v[156:159]\n"
"v_mfma_f32_16x16x32_bf16 v[172:175], a[228:231], a[76:79], v[172:175]\n"
"v_mfma_f32_16x16x32_bf16 v[168:171], a[224:227], a[76:79], v[168:171]\n"
"buffer_load_dwordx4 v[248:251], v5, s[44:47], s70 offen offset:1024 sc1\n"
"v_mfma_f32_16x16x32_bf16 v[164:167], a[220:223], a[76:79], v[164:167]\n"
"v_mfma_f32_16x16x32_bf16 v[160:163], a[216:219], a[76:79], v[160:163]\n"
"v_mfma_f32_16x16x32_bf16 v[176:179], a[216:219], a[80:83], v[176:179]\n"
"buffer_load_dwordx4 v[252:255], v6, s[44:47], s70 offen sc1\n"
"v_mfma_f32_16x16x32_bf16 v[180:183], a[220:223], a[80:83], v[180:183]\n"
"v_mfma_f32_16x16x32_bf16 v[184:187], a[224:227], a[80:83], v[184:187]\n"
"v_mfma_f32_16x16x32_bf16 v[188:191], a[228:231], a[80:83], v[188:191]\n"
"v_mfma_f32_16x16x32_bf16 v[192:195], a[232:235], a[0:3], v[192:195]\n"
"s_waitcnt lgkmcnt(0)\n"
"s_add_u32 m0, s24, 0\n"
"s_cmp_le_u32 s65, 1\n"
"s_cselect_b32 s69, 2, 1\n"
"v_mfma_f32_16x16x32_bf16 v[192:195], v[224:227], a[48:51], v[192:195]\n"
"s_add_u32 s69, s69, s66\n"
"v_mfma_f32_16x16x32_bf16 v[196:199], v[228:231], a[48:51], v[196:199]\n"
"s_mov_b32 s30, 3145728\n"
"s_lshl_b32 s69, s69, 19\n"
"v_mfma_f32_16x16x32_bf16 v[196:199], a[236:239], a[0:3], v[196:199]\n"
"s_add_u32 s69, s69, s31\n"
"buffer_load_dword v44, v24, s[48:51], s30 offen\n"
"v_mfma_f32_16x16x32_bf16 v[200:203], a[240:243], a[0:3], v[200:203]\n"
"s_cmp_le_u32 s65, 2\n"
"s_cselect_b32 s71, 3, 2\n"
"buffer_load_dwordx4 v7, s[48:51], s69 offen lds\n"
"v_mfma_f32_16x16x32_bf16 v[200:203], v[232:235], a[48:51], v[200:203]\n"
"s_add_u32 s71, s71, s66\n"
"v_mfma_f32_16x16x32_bf16 v[204:207], v[236:239], a[48:51], v[204:207]\n"
"s_mul_i32 s71, s71, 73728\n"
"v_mfma_f32_16x16x32_bf16 v[204:207], a[244:247], a[0:3], v[204:207]\n"
"s_add_u32 s71, s71, s25\n"
"v_mfma_f32_16x16x32_bf16 v[220:223], a[244:247], a[4:7], v[220:223]\n"
"buffer_load_dwordx4 a[0:3], v2, s[44:47], s71 offen sc1\n"
"v_mfma_f32_16x16x32_bf16 v[220:223], v[236:239], a[52:55], v[220:223]\n"
"v_mfma_f32_16x16x32_bf16 v[216:219], v[232:235], a[52:55], v[216:219]\n"
"v_mfma_f32_16x16x32_bf16 v[216:219], a[240:243], a[4:7], v[216:219]\n"
"v_mfma_f32_16x16x32_bf16 v[212:215], a[236:239], a[4:7], v[212:215]\n"
"buffer_load_dwordx4 v8, s[48:51], s69 offen offset:1024 lds\n"
"v_mfma_f32_16x16x32_bf16 v[212:215], v[228:231], a[52:55], v[212:215]\n"
"s_waitcnt vmcnt(15)\n"
"ds_read_b64_tr_b16 a[36:37], v11 offset:4096\n"
"v_mfma_f32_16x16x32_bf16 v[208:211], v[224:227], a[52:55], v[208:211]\n"
"ds_read_b64_tr_b16 a[38:39], v11 offset:6144\n"
"v_mfma_f32_16x16x32_bf16 v[208:211], a[232:235], a[4:7], v[208:211]\n"
"ds_read_b64_tr_b16 a[40:41], v12 offset:4096\n"
"v_mfma_f32_16x16x32_bf16 a[104:107], a[232:235], a[8:11], a[104:107]\n"
"buffer_load_dwordx4 a[4:7], v2, s[44:47], s71 offen offset:1024 sc1\n"
"v_mfma_f32_16x16x32_bf16 a[104:107], v[224:227], a[56:59], a[104:107]\n"
"ds_read_b64_tr_b16 a[42:43], v12 offset:6144\n"
"v_mfma_f32_16x16x32_bf16 a[108:111], v[228:231], a[56:59], a[108:111]\n"
"ds_read_b64_tr_b16 a[44:45], v13 offset:4096\n"
"v_mfma_f32_16x16x32_bf16 a[108:111], a[236:239], a[8:11], a[108:111]\n"
"ds_read_b64_tr_b16 a[46:47], v13 offset:6144\n"
"v_mfma_f32_16x16x32_bf16 a[112:115], a[240:243], a[8:11], a[112:115]\n"
"buffer_load_dwordx4 v9, s[48:51], s69 offen offset:2048 lds\n"
"v_mfma_f32_16x16x32_bf16 a[112:115], v[232:235], a[56:59], a[112:115]\n"
"ds_read_b64_tr_b16 a[96:97], v14 offset:4096\n"
"v_mfma_f32_16x16x32_bf16 a[116:119], v[236:239], a[56:59], a[116:119]\n"
"ds_read_b64_tr_b16 a[98:99], v14 offset:6144\n"
"v_mfma_f32_16x16x32_bf16 a[116:119], a[244:247], a[8:11], a[116:119]\n"
"v_mfma_f32_16x16x32_bf16 a[132:135], a[244:247], a[12:15], a[132:135]\n"
"buffer_load_dwordx4 a[8:11], v3, s[44:47], s71 offen sc1\n"
"v_mfma_f32_16x16x32_bf16 a[132:135], v[236:239], a[60:63], a[132:135]\n"
"v_mfma_f32_16x16x32_bf16 a[128:131], v[232:235], a[60:63], a[128:131]\n"
"v_mfma_f32_16x16x32_bf16 a[128:131], a[240:243], a[12:15], a[128:131]\n"
"v_mfma_f32_16x16x32_bf16 a[124:127], a[236:239], a[12:15], a[124:127]\n"
"buffer_load_dwordx4 v10, s[48:51], s69 offen offset:3072 lds\n"
"v_mfma_f32_16x16x32_bf16 a[124:127], v[228:231], a[60:63], a[124:127]\n"
"v_mfma_f32_16x16x32_bf16 a[120:123], v[224:227], a[60:63], a[120:123]\n"
"v_mfma_f32_16x16x32_bf16 a[120:123], a[232:235], a[12:15], a[120:123]\n"
"v_mfma_f32_16x16x32_bf16 a[136:139], a[232:235], a[16:19], a[136:139]\n"
"buffer_load_dwordx4 a[12:15], v3, s[44:47], s71 offen offset:1024 sc1\n"
"v_mfma_f32_16x16x32_bf16 a[136:139], v[224:227], a[64:67], a[136:139]\n"
"v_mfma_f32_16x16x32_bf16 a[140:143], v[228:231], a[64:67], a[140:143]\n"
"v_mfma_f32_16x16x32_bf16 a[140:143], a[236:239], a[16:19], a[140:143]\n"
"v_mfma_f32_16x16x32_bf16 a[144:147], a[240:243], a[16:19], a[144:147]\n"
"v_mfma_f32_16x16x32_bf16 a[144:147], v[232:235], a[64:67], a[144:147]\n"
"s_barrier\n"
"s_mov_b64 exec, 1\n"
"buffer_store_dwordx2 v[46:47], off, s[56:59], s34 offset:2048 sc1\n"
"s_mov_b64 exec, -1\n"
"v_mfma_f32_16x16x32_bf16 a[148:151], v[236:239], a[64:67], a[148:151]\n"
"s_waitcnt lgkmcnt(0)\n"
"v_mfma_f32_16x16x32_bf16 a[148:151], a[244:247], a[16:19], a[148:151]\n"
"s_add_u32 m0, s24, 4096\n"
"v_mfma_f32_16x16x32_bf16 a[164:167], a[244:247], a[20:23], a[164:167]\n"
"s_cmp_le_u32 s65, 2\n"
"s_cselect_b32 s69, 3, 2\n"
"buffer_load_dwordx4 a[16:19], v4, s[44:47], s71 offen sc1\n"
"v_mfma_f32_16x16x32_bf16 a[164:167], v[236:239], a[68:71], a[164:167]\n"
"s_add_u32 s69, s69, s66\n"
"v_mfma_f32_16x16x32_bf16 a[160:163], v[232:235], a[68:71], a[160:163]\n"
"s_lshl_b32 s69, s69, 19\n"
"v_mfma_f32_16x16x32_bf16 a[160:163], a[240:243], a[20:23], a[160:163]\n"
"s_add_u32 s69, s69, s31\n"
"v_mfma_f32_16x16x32_bf16 a[156:159], a[236:239], a[20:23], a[156:159]\n"
"buffer_load_dwordx4 v7, s[48:51], s69 offen lds\n"
"v_mfma_f32_16x16x32_bf16 a[156:159], v[228:231], a[68:71], a[156:159]\n"
"v_mfma_f32_16x16x32_bf16 a[152:155], v[224:227], a[68:71], a[152:155]\n"
"v_mfma_f32_16x16x32_bf16 a[152:155], a[232:235], a[20:23], a[152:155]\n"
"v_mfma_f32_16x16x32_bf16 a[168:171], a[232:235], a[24:27], a[168:171]\n"
"buffer_load_dwordx4 a[20:23], v4, s[44:47], s71 offen offset:1024 sc1\n"
"v_mfma_f32_16x16x32_bf16 a[168:171], v[224:227], a[72:75], a[168:171]\n"
"s_waitcnt vmcnt(18)\n"
"ds_read_b64_tr_b16 a[216:217], v11 offset:8192\n"
"v_mfma_f32_16x16x32_bf16 a[172:175], v[228:231], a[72:75], a[172:175]\n"
"ds_read_b64_tr_b16 a[218:219], v11 offset:10240\n"
"v_mfma_f32_16x16x32_bf16 a[172:175], a[236:239], a[24:27], a[172:175]\n"
"ds_read_b64_tr_b16 a[220:221], v12 offset:8192\n"
"v_mfma_f32_16x16x32_bf16 a[176:179], a[240:243], a[24:27], a[176:179]\n"
"buffer_load_dwordx4 v8, s[48:51], s69 offen offset:1024 lds\n"
"v_mfma_f32_16x16x32_bf16 a[176:179], v[232:235], a[72:75], a[176:179]\n"
"ds_read_b64_tr_b16 a[222:223], v12 offset:10240\n"
"v_mfma_f32_16x16x32_bf16 a[180:183], v[236:239], a[72:75], a[180:183]\n"
"ds_read_b64_tr_b16 a[224:225], v13 offset:8192\n"
"v_mfma_f32_16x16x32_bf16 a[180:183], a[244:247], a[24:27], a[180:183]\n"
"ds_read_b64_tr_b16 a[226:227], v13 offset:10240\n"
"v_mfma_f32_16x16x32_bf16 a[196:199], a[244:247], a[28:31], a[196:199]\n"
"buffer_load_dwordx4 a[24:27], v5, s[44:47], s71 offen sc1\n"
"v_mfma_f32_16x16x32_bf16 a[196:199], v[236:239], a[76:79], a[196:199]\n"
"ds_read_b64_tr_b16 a[228:229], v14 offset:8192\n"
"v_mfma_f32_16x16x32_bf16 a[192:195], v[232:235], a[76:79], a[192:195]\n"
"ds_read_b64_tr_b16 a[230:231], v14 offset:10240\n"
"v_mfma_f32_16x16x32_bf16 a[192:195], a[240:243], a[28:31], a[192:195]\n"
"v_mfma_f32_16x16x32_bf16 a[188:191], a[236:239], a[28:31], a[188:191]\n"
"buffer_load_dwordx4 v9, s[48:51], s69 offen offset:2048 lds\n"
"v_mfma_f32_16x16x32_bf16 a[188:191], v[228:231], a[76:79], a[188:191]\n"
"v_mfma_f32_16x16x32_bf16 a[184:187], v[224:227], a[76:79], a[184:187]\n"
"v_mfma_f32_16x16x32_bf16 a[184:187], a[232:235], a[28:31], a[184:187]\n"
"v_mfma_f32_16x16x32_bf16 a[200:203], a[232:235], a[32:35], a[200:203]\n"
"buffer_load_dwordx4 a[28:31], v5, s[44:47], s71 offen offset:1024 sc1\n"
"v_mfma_f32_16x16x32_bf16 a[200:203], v[224:227], a[80:83], a[200:203]\n"
"v_mfma_f32_16x16x32_bf16 a[204:207], v[228:231], a[80:83], a[204:207]\n"
"v_mfma_f32_16x16x32_bf16 a[204:207], a[236:239], a[32:35], a[204:207]\n"
"v_mfma_f32_16x16x32_bf16 a[208:211], a[240:243], a[32:35], a[208:211]\n"
"buffer_load_dwordx4 v10, s[48:51], s69 offen offset:3072 lds\n"
"v_mfma_f32_16x16x32_bf16 a[208:211], v[232:235], a[80:83], a[208:211]\n"
"v_mfma_f32_16x16x32_bf16 a[212:215], v[236:239], a[80:83], a[212:215]\n"
"v_mfma_f32_16x16x32_bf16 a[212:215], a[244:247], a[32:35], a[212:215]\n"
"s_waitcnt vmcnt(17)\n"
"s_waitcnt lgkmcnt(0)\n"
"s_branch L_join_%=\n"
"L_slow00_%=:\n"
"s_waitcnt vmcnt(0) lgkmcnt(0)\n"
"s_nop 7\n"
"v_and_b32 v247, 15, v1\n"
"v_lshrrev_b32 v215, 4, v1\n"
"v_lshlrev_b32 v244, 8, v247\n"
"v_lshlrev_b32 v245, 4, v215\n"
"v_add_u32 v244, v244, v245\n"
"v_lshlrev_b32 v245, 10, v215\n"
"v_lshlrev_b32 v247, 1, v247\n"
"v_add_u32 v245, v245, v247\n"
"v_add_u32 v246, 4096, v245\n"
"v_mov_b32 v247, 0xff800000\n"
"s_mov_b32 s28, 0\n"
"L_slow00a_%=:\n"
"buffer_load_dwordx4 v[216:219], v244, s[36:39], s28 offen\n"
"buffer_load_dwordx4 v[220:223], v244, s[36:39], s28 offen offset:64\n"
"buffer_load_dwordx4 v[224:227], v244, s[36:39], s28 offen offset:128\n"
"buffer_load_dwordx4 v[228:231], v244, s[36:39], s28 offen offset:192\n"
"s_waitcnt vmcnt(3)\n"
"v_mfma_f32_16x16x32_bf16 v[232:235], v[216:219], a[0:3], 0\n"
"s_waitcnt vmcnt(2)\n"
"v_mfma_f32_16x16x32_bf16 v[232:235], v[220:223], a[4:7], v[232:235]\n"
"s_waitcnt vmcnt(1)\n"
"v_mfma_f32_16x16x32_bf16 v[232:235], v[224:227], a[8:11], v[232:235]\n"
"s_waitcnt vmcnt(0)\n"
"v_mfma_f32_16x16x32_bf16 v[232:235], v[228:231], a[12:15], v[232:235]\n"
"s_add_u32 s28, s28, 4096\n"
"s_nop 6\n"
"v_max3_f32 v247, v247, v232, v233\n"
"v_max3_f32 v247, v247, v234, v235\n"
"s_cmp_lg_u32 s28, 131072\n"
"s_cbranch_scc1 L_slow00a_%=\n"
"v_mov_b32 v215, v247\n"
"s_nop 1\n"
"v_permlane32_swap_b32 v247, v215\n"
"v_max_f32 v247, v247, v215\n"
"v_mov_b32 v215, v247\n"
"s_nop 1\n"
"v_permlane16_swap_b32 v247, v215\n"
"v_max_f32 v247, v247, v215\n"
"v_sub_f32 v156, 0, v247\n"
"v_mov_b32 v144, 0\n"
"v_sub_f32 v157, 0, v247\n"
"v_mov_b32 v145, 0\n"
"v_sub_f32 v158, 0, v247\n"
"v_mov_b32 v146, 0\n"
"v_sub_f32 v159, 0, v247\n"
"v_mov_b32 v147, 0\n"
"v_mov_b32 v48, 0\n"
"v_mov_b32 v49, 0\n"
"v_mov_b32 v50, 0\n"
"v_mov_b32 v51, 0\n"
"v_mov_b32 v52, 0\n"
"v_mov_b32 v53, 0\n"
"v_mov_b32 v54, 0\n"
"v_mov_b32 v55, 0\n"
"v_mov_b32 v56, 0\n"
"v_mov_b32 v57, 0\n"
"v_mov_b32 v58, 0\n"
"v_mov_b32 v59, 0\n"
"v_mov_b32 v60, 0\n"
"v_mov_b32 v61, 0\n"
"v_mov_b32 v62, 0\n"
"v_mov_b32 v63, 0\n"
"v_mov_b32 v64, 0\n"
"v_mov_b32 v65, 0\n"
"v_mov_b32 v66, 0\n"
"v_mov_b32 v67, 0\n"
"v_mov_b32 v68, 0\n"
"v_mov_b32 v69, 0\n"
"v_mov_b32 v70, 0\n"
"v_mov_b32 v71, 0\n"
"v_mov_b32 v72, 0\n"
"v_mov_b32 v73, 0\n"
"v_mov_b32 v74, 0\n"
"v_mov_b32 v75, 0\n"
"v_mov_b32 v76, 0\n"
"v_mov_b32 v77, 0\n"
"v_mov_b32 v78, 0\n"
"v_mov_b32 v79, 0\n"
"s_mov_b32 s28, 0\n"
"L_slow00b_%=:\n"
"buffer_load_dwordx4 v[216:219], v244, s[36:39], s28 offen\n"
"buffer_load_dwordx4 v[220:223], v244, s[36:39], s28 offen offset:64\n"
"buffer_load_dwordx4 v[224:227], v244, s[36:39], s28 offen offset:128\n"
"buffer_load_dwordx4 v[228:231], v244, s[36:39], s28 offen offset:192\n"
"s_waitcnt vmcnt(3)\n"
"v_mfma_f32_16x16x32_bf16 v[232:235], v[216:219], a[0:3], v[156:159]\n"
"s_waitcnt vmcnt(2)\n"
"v_mfma_f32_16x16x32_bf16 v[232:235], v[220:223], a[4:7], v[232:235]\n"
"s_waitcnt vmcnt(1)\n"
"v_mfma_f32_16x16x32_bf16 v[232:235], v[224:227], a[8:11], v[232:235]\n"
"s_waitcnt vmcnt(0)\n"
"v_mfma_f32_16x16x32_bf16 v[232:235], v[228:231], a[12:15], v[232:235]\n"
"s_add_u32 s28, s28, 4096\n"
"buffer_load_dwordx4 v[216:219], v244, s[36:39], s28 offen\n"
"buffer_load_dwordx4 v[220:223], v244, s[36:39], s28 offen offset:64\n"
"buffer_load_dwordx4 v[224:227], v244, s[36:39], s28 offen offset:128\n"
"buffer_load_dwordx4 v[228:231], v244, s[36:39], s28 offen offset:192\n"
"s_waitcnt vmcnt(3)\n"
"v_mfma_f32_16x16x32_bf16 v[236:239], v[216:219], a[0:3], v[156:159]\n"
"s_waitcnt vmcnt(2)\n"
"v_mfma_f32_16x16x32_bf16 v[236:239], v[220:223], a[4:7], v[236:239]\n"
"s_waitcnt vmcnt(1)\n"
"v_mfma_f32_16x16x32_bf16 v[236:239], v[224:227], a[8:11], v[236:239]\n"
"s_waitcnt vmcnt(0)\n"
"v_mfma_f32_16x16x32_bf16 v[236:239], v[228:231], a[12:15], v[236:239]\n"
"s_add_u32 s28, s28, 4096\n"
"v_exp_f32 v232, v232\n"
"v_exp_f32 v233, v233\n"
"v_exp_f32 v234, v234\n"
"v_exp_f32 v235, v235\n"
"s_nop 2\n"
"v_exp_f32 v236, v236\n"
"v_exp_f32 v237, v237\n"
"v_exp_f32 v238, v238\n"
"v_exp_f32 v239, v239\n"
"v_cvt_pk_bf16_f32 v240, v232, v233\n"
"v_cvt_pk_bf16_f32 v241, v234, v235\n"
"v_cvt_pk_bf16_f32 v242, v236, v237\n"
"v_cvt_pk_bf16_f32 v243, v238, v239\n"
"s_nop 1\n"
"v_mfma_f32_16x16x32_bf16 v[144:147], v[192:195], v[240:243], v[144:147]\n"
"s_sub_u32 s28, s28, 8192\n"
"buffer_load_ushort v216, v245, s[40:43], s28 offen\n"
"buffer_load_ushort v217, v245, s[40:43], s28 offen offset:256\n"
"buffer_load_ushort v218, v245, s[40:43], s28 offen offset:512\n"
"buffer_load_ushort v219, v245, s[40:43], s28 offen offset:768\n"
"buffer_load_ushort v220, v246, s[40:43], s28 offen\n"
"buffer_load_ushort v221, v246, s[40:43], s28 offen offset:256\n"
"buffer_load_ushort v222, v246, s[40:43], s28 offen offset:512\n"
"buffer_load_ushort v223, v246, s[40:43], s28 offen offset:768\n"
"s_waitcnt vmcnt(6)\n"
"v_lshl_or_b32 v224, v217, 16, v216\n"
"s_waitcnt vmcnt(4)\n"
"v_lshl_or_b32 v225, v219, 16, v218\n"
"s_waitcnt vmcnt(2)\n"
"v_lshl_or_b32 v226, v221, 16, v220\n"
"s_waitcnt vmcnt(0)\n"
"v_lshl_or_b32 v227, v223, 16, v222\n"
"s_nop 1\n"
"v_mfma_f32_16x16x32_bf16 v[48:51], v[224:227], v[240:243], v[48:51]\n"
"buffer_load_ushort v216, v245, s[40:43], s28 offen offset:32\n"
"buffer_load_ushort v217, v245, s[40:43], s28 offen offset:288\n"
"buffer_load_ushort v218, v245, s[40:43], s28 offen offset:544\n"
"buffer_load_ushort v219, v245, s[40:43], s28 offen offset:800\n"
"buffer_load_ushort v220, v246, s[40:43], s28 offen offset:32\n"
"buffer_load_ushort v221, v246, s[40:43], s28 offen offset:288\n"
"buffer_load_ushort v222, v246, s[40:43], s28 offen offset:544\n"
"buffer_load_ushort v223, v246, s[40:43], s28 offen offset:800\n"
"s_waitcnt vmcnt(6)\n"
"v_lshl_or_b32 v224, v217, 16, v216\n"
"s_waitcnt vmcnt(4)\n"
"v_lshl_or_b32 v225, v219, 16, v218\n"
"s_waitcnt vmcnt(2)\n"
"v_lshl_or_b32 v226, v221, 16, v220\n"
"s_waitcnt vmcnt(0)\n"
"v_lshl_or_b32 v227, v223, 16, v222\n"
"s_nop 1\n"
"v_mfma_f32_16x16x32_bf16 v[52:55], v[224:227], v[240:243], v[52:55]\n"
"buffer_load_ushort v216, v245, s[40:43], s28 offen offset:64\n"
"buffer_load_ushort v217, v245, s[40:43], s28 offen offset:320\n"
"buffer_load_ushort v218, v245, s[40:43], s28 offen offset:576\n"
"buffer_load_ushort v219, v245, s[40:43], s28 offen offset:832\n"
"buffer_load_ushort v220, v246, s[40:43], s28 offen offset:64\n"
"buffer_load_ushort v221, v246, s[40:43], s28 offen offset:320\n"
"buffer_load_ushort v222, v246, s[40:43], s28 offen offset:576\n"
"buffer_load_ushort v223, v246, s[40:43], s28 offen offset:832\n"
"s_waitcnt vmcnt(6)\n"
"v_lshl_or_b32 v224, v217, 16, v216\n"
"s_waitcnt vmcnt(4)\n"
"v_lshl_or_b32 v225, v219, 16, v218\n"
"s_waitcnt vmcnt(2)\n"
"v_lshl_or_b32 v226, v221, 16, v220\n"
"s_waitcnt vmcnt(0)\n"
"v_lshl_or_b32 v227, v223, 16, v222\n"
"s_nop 1\n"
"v_mfma_f32_16x16x32_bf16 v[56:59], v[224:227], v[240:243], v[56:59]\n"
"buffer_load_ushort v216, v245, s[40:43], s28 offen offset:96\n"
"buffer_load_ushort v217, v245, s[40:43], s28 offen offset:352\n"
"buffer_load_ushort v218, v245, s[40:43], s28 offen offset:608\n"
"buffer_load_ushort v219, v245, s[40:43], s28 offen offset:864\n"
"buffer_load_ushort v220, v246, s[40:43], s28 offen offset:96\n"
"buffer_load_ushort v221, v246, s[40:43], s28 offen offset:352\n"
"buffer_load_ushort v222, v246, s[40:43], s28 offen offset:608\n"
"buffer_load_ushort v223, v246, s[40:43], s28 offen offset:864\n"
"s_waitcnt vmcnt(6)\n"
"v_lshl_or_b32 v224, v217, 16, v216\n"
"s_waitcnt vmcnt(4)\n"
"v_lshl_or_b32 v225, v219, 16, v218\n"
"s_waitcnt vmcnt(2)\n"
"v_lshl_or_b32 v226, v221, 16, v220\n"
"s_waitcnt vmcnt(0)\n"
"v_lshl_or_b32 v227, v223, 16, v222\n"
"s_nop 1\n"
"v_mfma_f32_16x16x32_bf16 v[60:63], v[224:227], v[240:243], v[60:63]\n"
"buffer_load_ushort v216, v245, s[40:43], s28 offen offset:128\n"
"buffer_load_ushort v217, v245, s[40:43], s28 offen offset:384\n"
"buffer_load_ushort v218, v245, s[40:43], s28 offen offset:640\n"
"buffer_load_ushort v219, v245, s[40:43], s28 offen offset:896\n"
"buffer_load_ushort v220, v246, s[40:43], s28 offen offset:128\n"
"buffer_load_ushort v221, v246, s[40:43], s28 offen offset:384\n"
"buffer_load_ushort v222, v246, s[40:43], s28 offen offset:640\n"
"buffer_load_ushort v223, v246, s[40:43], s28 offen offset:896\n"
"s_waitcnt vmcnt(6)\n"
"v_lshl_or_b32 v224, v217, 16, v216\n"
"s_waitcnt vmcnt(4)\n"
"v_lshl_or_b32 v225, v219, 16, v218\n"
"s_waitcnt vmcnt(2)\n"
"v_lshl_or_b32 v226, v221, 16, v220\n"
"s_waitcnt vmcnt(0)\n"
"v_lshl_or_b32 v227, v223, 16, v222\n"
"s_nop 1\n"
"v_mfma_f32_16x16x32_bf16 v[64:67], v[224:227], v[240:243], v[64:67]\n"
"buffer_load_ushort v216, v245, s[40:43], s28 offen offset:160\n"
"buffer_load_ushort v217, v245, s[40:43], s28 offen offset:416\n"
"buffer_load_ushort v218, v245, s[40:43], s28 offen offset:672\n"
"buffer_load_ushort v219, v245, s[40:43], s28 offen offset:928\n"
"buffer_load_ushort v220, v246, s[40:43], s28 offen offset:160\n"
"buffer_load_ushort v221, v246, s[40:43], s28 offen offset:416\n"
"buffer_load_ushort v222, v246, s[40:43], s28 offen offset:672\n"
"buffer_load_ushort v223, v246, s[40:43], s28 offen offset:928\n"
"s_waitcnt vmcnt(6)\n"
"v_lshl_or_b32 v224, v217, 16, v216\n"
"s_waitcnt vmcnt(4)\n"
"v_lshl_or_b32 v225, v219, 16, v218\n"
"s_waitcnt vmcnt(2)\n"
"v_lshl_or_b32 v226, v221, 16, v220\n"
"s_waitcnt vmcnt(0)\n"
"v_lshl_or_b32 v227, v223, 16, v222\n"
"s_nop 1\n"
"v_mfma_f32_16x16x32_bf16 v[68:71], v[224:227], v[240:243], v[68:71]\n"
"buffer_load_ushort v216, v245, s[40:43], s28 offen offset:192\n"
"buffer_load_ushort v217, v245, s[40:43], s28 offen offset:448\n"
"buffer_load_ushort v218, v245, s[40:43], s28 offen offset:704\n"
"buffer_load_ushort v219, v245, s[40:43], s28 offen offset:960\n"
"buffer_load_ushort v220, v246, s[40:43], s28 offen offset:192\n"
"buffer_load_ushort v221, v246, s[40:43], s28 offen offset:448\n"
"buffer_load_ushort v222, v246, s[40:43], s28 offen offset:704\n"
"buffer_load_ushort v223, v246, s[40:43], s28 offen offset:960\n"
"s_waitcnt vmcnt(6)\n"
"v_lshl_or_b32 v224, v217, 16, v216\n"
"s_waitcnt vmcnt(4)\n"
"v_lshl_or_b32 v225, v219, 16, v218\n"
"s_waitcnt vmcnt(2)\n"
"v_lshl_or_b32 v226, v221, 16, v220\n"
"s_waitcnt vmcnt(0)\n"
"v_lshl_or_b32 v227, v223, 16, v222\n"
"s_nop 1\n"
"v_mfma_f32_16x16x32_bf16 v[72:75], v[224:227], v[240:243], v[72:75]\n"
"buffer_load_ushort v216, v245, s[40:43], s28 offen offset:224\n"
"buffer_load_ushort v217, v245, s[40:43], s28 offen offset:480\n"
"buffer_load_ushort v218, v245, s[40:43], s28 offen offset:736\n"
"buffer_load_ushort v219, v245, s[40:43], s28 offen offset:992\n"
"buffer_load_ushort v220, v246, s[40:43], s28 offen offset:224\n"
"buffer_load_ushort v221, v246, s[40:43], s28 offen offset:480\n"
"buffer_load_ushort v222, v246, s[40:43], s28 offen offset:736\n"
"buffer_load_ushort v223, v246, s[40:43], s28 offen offset:992\n"
"s_waitcnt vmcnt(6)\n"
"v_lshl_or_b32 v224, v217, 16, v216\n"
"s_waitcnt vmcnt(4)\n"
"v_lshl_or_b32 v225, v219, 16, v218\n"
"s_waitcnt vmcnt(2)\n"
"v_lshl_or_b32 v226, v221, 16, v220\n"
"s_waitcnt vmcnt(0)\n"
"v_lshl_or_b32 v227, v223, 16, v222\n"
"s_nop 1\n"
"v_mfma_f32_16x16x32_bf16 v[76:79], v[224:227], v[240:243], v[76:79]\n"
"s_add_u32 s28, s28, 8192\n"
"s_cmp_lg_u32 s28, 131072\n"
"s_nop 7\n"
"s_cbranch_scc1 L_slow00b_%=\n"
"s_branch L_slow00r_%=\n"
"L_slow01_%=:\n"
"s_waitcnt vmcnt(0) lgkmcnt(0)\n"
"s_nop 7\n"
"v_and_b32 v247, 15, v1\n"
"v_lshrrev_b32 v215, 4, v1\n"
"v_lshlrev_b32 v244, 8, v247\n"
"v_lshlrev_b32 v245, 4, v215\n"
"v_add_u32 v244, v244, v245\n"
"v_lshlrev_b32 v245, 10, v215\n"
"v_lshlrev_b32 v247, 1, v247\n"
"v_add_u32 v245, v245, v247\n"
"v_add_u32 v246, 4096, v245\n"
"v_mov_b32 v247, 0xff800000\n"
"s_mov_b32 s28, 0\n"
"L_slow01a_%=:\n"
"buffer_load_dwordx4 v[216:219], v244, s[36:39], s28 offen\n"
"buffer_load_dwordx4 v[220:223], v244, s[36:39], s28 offen offset:64\n"
"buffer_load_dwordx4 v[224:227], v244, s[36:39], s28 offen offset:128\n"
"buffer_load_dwordx4 v[228:231], v244, s[36:39], s28 offen offset:192\n"
"s_waitcnt vmcnt(3)\n"
"v_mfma_f32_16x16x32_bf16 v[232:235], v[216:219], a[16:19], 0\n"
"s_waitcnt vmcnt(2)\n"
"v_mfma_f32_16x16x32_bf16 v[232:235], v[220:223], a[20:23], v[232:235]\n"
"s_waitcnt vmcnt(1)\n"
"v_mfma_f32_16x16x32_bf16 v[232:235], v[224:227], a[24:27], v[232:235]\n"
"s_waitcnt vmcnt(0)\n"
"v_mfma_f32_16x16x32_bf16 v[232:235], v[228:231], a[28:31], v[232:235]\n"
"s_add_u32 s28, s28, 4096\n"
"s_nop 6\n"
"v_max3_f32 v247, v247, v232, v233\n"
"v_max3_f32 v247, v247, v234, v235\n"
"s_cmp_lg_u32 s28, 131072\n"
"s_cbranch_scc1 L_slow01a_%=\n"
"v_mov_b32 v215, v247\n"
"s_nop 1\n"
"v_permlane32_swap_b32 v247, v215\n"
"v_max_f32 v247, v247, v215\n"
"v_mov_b32 v215, v247\n"
"s_nop 1\n"
"v_permlane16_swap_b32 v247, v215\n"
"v_max_f32 v247, v247, v215\n"
"v_sub_f32 v160, 0, v247\n"
"v_mov_b32 v148, 0\n"
"v_sub_f32 v161, 0, v247\n"
"v_mov_b32 v149, 0\n"
"v_sub_f32 v162, 0, v247\n"
"v_mov_b32 v150, 0\n"
"v_sub_f32 v163, 0, v247\n"
"v_mov_b32 v151, 0\n"
"v_mov_b32 v80, 0\n"
"v_mov_b32 v81, 0\n"
"v_mov_b32 v82, 0\n"
"v_mov_b32 v83, 0\n"
"v_mov_b32 v84, 0\n"
"v_mov_b32 v85, 0\n"
"v_mov_b32 v86, 0\n"
"v_mov_b32 v87, 0\n"
"v_mov_b32 v88, 0\n"
"v_mov_b32 v89, 0\n"
"v_mov_b32 v90, 0\n"
"v_mov_b32 v91, 0\n"
"v_mov_b32 v92, 0\n"
"v_mov_b32 v93, 0\n"
"v_mov_b32 v94, 0\n"
"v_mov_b32 v95, 0\n"
"v_mov_b32 v96, 0\n"
"v_mov_b32 v97, 0\n"
"v_mov_b32 v98, 0\n"
"v_mov_b32 v99, 0\n"
"v_mov_b32 v100, 0\n"
"v_mov_b32 v101, 0\n"
"v_mov_b32 v102, 0\n"
"v_mov_b32 v103, 0\n"
"v_mov_b32 v104, 0\n"
"v_mov_b32 v105, 0\n"
"v_mov_b32 v106, 0\n"
"v_mov_b32 v107, 0\n"
"v_mov_b32 v108, 0\n"
"v_mov_b32 v109, 0\n"
"v_mov_b32 v110, 0\n"
"v_mov_b32 v111, 0\n"
"s_mov_b32 s28, 0\n"
"L_slow01b_%=:\n"
"buffer_load_dwordx4 v[216:219], v244, s[36:39], s28 offen\n"
"buffer_load_dwordx4 v[220:223], v244, s[36:39], s28 offen offset:64\n"
"buffer_load_dwordx4 v[224:227], v244, s[36:39], s28 offen offset:128\n"
"buffer_load_dwordx4 v[228:231], v244, s[36:39], s28 offen offset:192\n"
"s_waitcnt vmcnt(3)\n"
"v_mfma_f32_16x16x32_bf16 v[232:235], v[216:219], a[16:19], v[160:163]\n"
"s_waitcnt vmcnt(2)\n"
"v_mfma_f32_16x16x32_bf16 v[232:235], v[220:223], a[20:23], v[232:235]\n"
"s_waitcnt vmcnt(1)\n"
"v_mfma_f32_16x16x32_bf16 v[232:235], v[224:227], a[24:27], v[232:235]\n"
"s_waitcnt vmcnt(0)\n"
"v_mfma_f32_16x16x32_bf16 v[232:235], v[228:231], a[28:31], v[232:235]\n"
"s_add_u32 s28, s28, 4096\n"
"buffer_load_dwordx4 v[216:219], v244, s[36:39], s28 offen\n"
"buffer_load_dwordx4 v[220:223], v244, s[36:39], s28 offen offset:64\n"
"buffer_load_dwordx4 v[224:227], v244, s[36:39], s28 offen offset:128\n"
"buffer_load_dwordx4 v[228:231], v244, s[36:39], s28 offen offset:192\n"
"s_waitcnt vmcnt(3)\n"
"v_mfma_f32_16x16x32_bf16 v[236:239], v[216:219], a[16:19], v[160:163]\n"
"s_waitcnt vmcnt(2)\n"
"v_mfma_f32_16x16x32_bf16 v[236:239], v[220:223], a[20:23], v[236:239]\n"
"s_waitcnt vmcnt(1)\n"
"v_mfma_f32_16x16x32_bf16 v[236:239], v[224:227], a[24:27], v[236:239]\n"
"s_waitcnt vmcnt(0)\n"
"v_mfma_f32_16x16x32_bf16 v[236:239], v[228:231], a[28:31], v[236:239]\n"
"s_add_u32 s28, s28, 4096\n"
"v_exp_f32 v232, v232\n"
"v_exp_f32 v233, v233\n"
"v_exp_f32 v234, v234\n"
"v_exp_f32 v235, v235\n"
"s_nop 2\n"
"v_exp_f32 v236, v236\n"
"v_exp_f32 v237, v237\n"
"v_exp_f32 v238, v238\n"
"v_exp_f32 v239, v239\n"
"v_cvt_pk_bf16_f32 v240, v232, v233\n"
"v_cvt_pk_bf16_f32 v241, v234, v235\n"
"v_cvt_pk_bf16_f32 v242, v236, v237\n"
"v_cvt_pk_bf16_f32 v243, v238, v239\n"
"s_nop 1\n"
"v_mfma_f32_16x16x32_bf16 v[148:151], v[192:195], v[240:243], v[148:151]\n"
"s_sub_u32 s28, s28, 8192\n"
"buffer_load_ushort v216, v245, s[40:43], s28 offen\n"
"buffer_load_ushort v217, v245, s[40:43], s28 offen offset:256\n"
"buffer_load_ushort v218, v245, s[40:43], s28 offen offset:512\n"
"buffer_load_ushort v219, v245, s[40:43], s28 offen offset:768\n"
"buffer_load_ushort v220, v246, s[40:43], s28 offen\n"
"buffer_load_ushort v221, v246, s[40:43], s28 offen offset:256\n"
"buffer_load_ushort v222, v246, s[40:43], s28 offen offset:512\n"
"buffer_load_ushort v223, v246, s[40:43], s28 offen offset:768\n"
"s_waitcnt vmcnt(6)\n"
"v_lshl_or_b32 v224, v217, 16, v216\n"
"s_waitcnt vmcnt(4)\n"
"v_lshl_or_b32 v225, v219, 16, v218\n"
"s_waitcnt vmcnt(2)\n"
"v_lshl_or_b32 v226, v221, 16, v220\n"
"s_waitcnt vmcnt(0)\n"
"v_lshl_or_b32 v227, v223, 16, v222\n"
"s_nop 1\n"
"v_mfma_f32_16x16x32_bf16 v[80:83], v[224:227], v[240:243], v[80:83]\n"
"buffer_load_ushort v216, v245, s[40:43], s28 offen offset:32\n"
"buffer_load_ushort v217, v245, s[40:43], s28 offen offset:288\n"
"buffer_load_ushort v218, v245, s[40:43], s28 offen offset:544\n"
"buffer_load_ushort v219, v245, s[40:43], s28 offen offset:800\n"
"buffer_load_ushort v220, v246, s[40:43], s28 offen offset:32\n"
"buffer_load_ushort v221, v246, s[40:43], s28 offen offset:288\n"
"buffer_load_ushort v222, v246, s[40:43], s28 offen offset:544\n"
"buffer_load_ushort v223, v246, s[40:43], s28 offen offset:800\n"
"s_waitcnt vmcnt(6)\n"
"v_lshl_or_b32 v224, v217, 16, v216\n"
"s_waitcnt vmcnt(4)\n"
"v_lshl_or_b32 v225, v219, 16, v218\n"
"s_waitcnt vmcnt(2)\n"
"v_lshl_or_b32 v226, v221, 16, v220\n"
"s_waitcnt vmcnt(0)\n"
"v_lshl_or_b32 v227, v223, 16, v222\n"
"s_nop 1\n"
"v_mfma_f32_16x16x32_bf16 v[84:87], v[224:227], v[240:243], v[84:87]\n"
"buffer_load_ushort v216, v245, s[40:43], s28 offen offset:64\n"
"buffer_load_ushort v217, v245, s[40:43], s28 offen offset:320\n"
"buffer_load_ushort v218, v245, s[40:43], s28 offen offset:576\n"
"buffer_load_ushort v219, v245, s[40:43], s28 offen offset:832\n"
"buffer_load_ushort v220, v246, s[40:43], s28 offen offset:64\n"
"buffer_load_ushort v221, v246, s[40:43], s28 offen offset:320\n"
"buffer_load_ushort v222, v246, s[40:43], s28 offen offset:576\n"
"buffer_load_ushort v223, v246, s[40:43], s28 offen offset:832\n"
"s_waitcnt vmcnt(6)\n"
"v_lshl_or_b32 v224, v217, 16, v216\n"
"s_waitcnt vmcnt(4)\n"
"v_lshl_or_b32 v225, v219, 16, v218\n"
"s_waitcnt vmcnt(2)\n"
"v_lshl_or_b32 v226, v221, 16, v220\n"
"s_waitcnt vmcnt(0)\n"
"v_lshl_or_b32 v227, v223, 16, v222\n"
"s_nop 1\n"
"v_mfma_f32_16x16x32_bf16 v[88:91], v[224:227], v[240:243], v[88:91]\n"
"buffer_load_ushort v216, v245, s[40:43], s28 offen offset:96\n"
"buffer_load_ushort v217, v245, s[40:43], s28 offen offset:352\n"
"buffer_load_ushort v218, v245, s[40:43], s28 offen offset:608\n"
"buffer_load_ushort v219, v245, s[40:43], s28 offen offset:864\n"
"buffer_load_ushort v220, v246, s[40:43], s28 offen offset:96\n"
"buffer_load_ushort v221, v246, s[40:43], s28 offen offset:352\n"
"buffer_load_ushort v222, v246, s[40:43], s28 offen offset:608\n"
"buffer_load_ushort v223, v246, s[40:43], s28 offen offset:864\n"
"s_waitcnt vmcnt(6)\n"
"v_lshl_or_b32 v224, v217, 16, v216\n"
"s_waitcnt vmcnt(4)\n"
"v_lshl_or_b32 v225, v219, 16, v218\n"
"s_waitcnt vmcnt(2)\n"
"v_lshl_or_b32 v226, v221, 16, v220\n"
"s_waitcnt vmcnt(0)\n"
"v_lshl_or_b32 v227, v223, 16, v222\n"
"s_nop 1\n"
"v_mfma_f32_16x16x32_bf16 v[92:95], v[224:227], v[240:243], v[92:95]\n"
"buffer_load_ushort v216, v245, s[40:43], s28 offen offset:128\n"
"buffer_load_ushort v217, v245, s[40:43], s28 offen offset:384\n"
"buffer_load_ushort v218, v245, s[40:43], s28 offen offset:640\n"
"buffer_load_ushort v219, v245, s[40:43], s28 offen offset:896\n"
"buffer_load_ushort v220, v246, s[40:43], s28 offen offset:128\n"
"buffer_load_ushort v221, v246, s[40:43], s28 offen offset:384\n"
"buffer_load_ushort v222, v246, s[40:43], s28 offen offset:640\n"
"buffer_load_ushort v223, v246, s[40:43], s28 offen offset:896\n"
"s_waitcnt vmcnt(6)\n"
"v_lshl_or_b32 v224, v217, 16, v216\n"
"s_waitcnt vmcnt(4)\n"
"v_lshl_or_b32 v225, v219, 16, v218\n"
"s_waitcnt vmcnt(2)\n"
"v_lshl_or_b32 v226, v221, 16, v220\n"
"s_waitcnt vmcnt(0)\n"
"v_lshl_or_b32 v227, v223, 16, v222\n"
"s_nop 1\n"
"v_mfma_f32_16x16x32_bf16 v[96:99], v[224:227], v[240:243], v[96:99]\n"
"buffer_load_ushort v216, v245, s[40:43], s28 offen offset:160\n"
"buffer_load_ushort v217, v245, s[40:43], s28 offen offset:416\n"
"buffer_load_ushort v218, v245, s[40:43], s28 offen offset:672\n"
"buffer_load_ushort v219, v245, s[40:43], s28 offen offset:928\n"
"buffer_load_ushort v220, v246, s[40:43], s28 offen offset:160\n"
"buffer_load_ushort v221, v246, s[40:43], s28 offen offset:416\n"
"buffer_load_ushort v222, v246, s[40:43], s28 offen offset:672\n"
"buffer_load_ushort v223, v246, s[40:43], s28 offen offset:928\n"
"s_waitcnt vmcnt(6)\n"
"v_lshl_or_b32 v224, v217, 16, v216\n"
"s_waitcnt vmcnt(4)\n"
"v_lshl_or_b32 v225, v219, 16, v218\n"
"s_waitcnt vmcnt(2)\n"
"v_lshl_or_b32 v226, v221, 16, v220\n"
"s_waitcnt vmcnt(0)\n"
"v_lshl_or_b32 v227, v223, 16, v222\n"
"s_nop 1\n"
"v_mfma_f32_16x16x32_bf16 v[100:103], v[224:227], v[240:243], v[100:103]\n"
"buffer_load_ushort v216, v245, s[40:43], s28 offen offset:192\n"
"buffer_load_ushort v217, v245, s[40:43], s28 offen offset:448\n"
"buffer_load_ushort v218, v245, s[40:43], s28 offen offset:704\n"
"buffer_load_ushort v219, v245, s[40:43], s28 offen offset:960\n"
"buffer_load_ushort v220, v246, s[40:43], s28 offen offset:192\n"
"buffer_load_ushort v221, v246, s[40:43], s28 offen offset:448\n"
"buffer_load_ushort v222, v246, s[40:43], s28 offen offset:704\n"
"buffer_load_ushort v223, v246, s[40:43], s28 offen offset:960\n"
"s_waitcnt vmcnt(6)\n"
"v_lshl_or_b32 v224, v217, 16, v216\n"
"s_waitcnt vmcnt(4)\n"
"v_lshl_or_b32 v225, v219, 16, v218\n"
"s_waitcnt vmcnt(2)\n"
"v_lshl_or_b32 v226, v221, 16, v220\n"
"s_waitcnt vmcnt(0)\n"
"v_lshl_or_b32 v227, v223, 16, v222\n"
"s_nop 1\n"
"v_mfma_f32_16x16x32_bf16 v[104:107], v[224:227], v[240:243], v[104:107]\n"
"buffer_load_ushort v216, v245, s[40:43], s28 offen offset:224\n"
"buffer_load_ushort v217, v245, s[40:43], s28 offen offset:480\n"
"buffer_load_ushort v218, v245, s[40:43], s28 offen offset:736\n"
"buffer_load_ushort v219, v245, s[40:43], s28 offen offset:992\n"
"buffer_load_ushort v220, v246, s[40:43], s28 offen offset:224\n"
"buffer_load_ushort v221, v246, s[40:43], s28 offen offset:480\n"
"buffer_load_ushort v222, v246, s[40:43], s28 offen offset:736\n"
"buffer_load_ushort v223, v246, s[40:43], s28 offen offset:992\n"
"s_waitcnt vmcnt(6)\n"
"v_lshl_or_b32 v224, v217, 16, v216\n"
"s_waitcnt vmcnt(4)\n"
"v_lshl_or_b32 v225, v219, 16, v218\n"
"s_waitcnt vmcnt(2)\n"
"v_lshl_or_b32 v226, v221, 16, v220\n"
"s_waitcnt vmcnt(0)\n"
"v_lshl_or_b32 v227, v223, 16, v222\n"
"s_nop 1\n"
"v_mfma_f32_16x16x32_bf16 v[108:111], v[224:227], v[240:243], v[108:111]\n"
"s_add_u32 s28, s28, 8192\n"
"s_cmp_lg_u32 s28, 131072\n"
"s_nop 7\n"
"s_cbranch_scc1 L_slow01b_%=\n"
"s_branch L_slow01r_%=\n"
"L_slow02_%=:\n"
"s_waitcnt vmcnt(0) lgkmcnt(0)\n"
"s_nop 7\n"
"v_and_b32 v247, 15, v1\n"
"v_lshrrev_b32 v215, 4, v1\n"
"v_lshlrev_b32 v244, 8, v247\n"
"v_lshlrev_b32 v245, 4, v215\n"
"v_add_u32 v244, v244, v245\n"
"v_lshlrev_b32 v245, 10, v215\n"
"v_lshlrev_b32 v247, 1, v247\n"
"v_add_u32 v245, v245, v247\n"
"v_add_u32 v246, 4096, v245\n"
"v_mov_b32 v247, 0xff800000\n"
"s_mov_b32 s28, 0\n"
"L_slow02a_%=:\n"
"buffer_load_dwordx4 v[216:219], v244, s[36:39], s28 offen\n"
"buffer_load_dwordx4 v[220:223], v244, s[36:39], s28 offen offset:64\n"
"buffer_load_dwordx4 v[224:227], v244, s[36:39], s28 offen offset:128\n"
"buffer_load_dwordx4 v[228:231], v244, s[36:39], s28 offen offset:192\n"
"s_waitcnt vmcnt(3)\n"
"v_mfma_f32_16x16x32_bf16 v[232:235], v[216:219], a[32:35], 0\n"
"s_waitcnt vmcnt(2)\n"
"v_mfma_f32_16x16x32_bf16 v[232:235], v[220:223], a[36:39], v[232:235]\n"
"s_waitcnt vmcnt(1)\n"
"v_mfma_f32_16x16x32_bf16 v[232:235], v[224:227], a[40:43], v[232:235]\n"
"s_waitcnt vmcnt(0)\n"
"v_mfma_f32_16x16x32_bf16 v[232:235], v[228:231], a[44:47], v[232:235]\n"
"s_add_u32 s28, s28, 4096\n"
"s_nop 6\n"
"v_max3_f32 v247, v247, v232, v233\n"
"v_max3_f32 v247, v247, v234, v235\n"
"s_cmp_lg_u32 s28, 131072\n"
"s_cbranch_scc1 L_slow02a_%=\n"
"v_mov_b32 v215, v247\n"
"s_nop 1\n"
"v_permlane32_swap_b32 v247, v215\n"
"v_max_f32 v247, v247, v215\n"
"v_mov_b32 v215, v247\n"
"s_nop 1\n"
"v_permlane16_swap_b32 v247, v215\n"
"v_max_f32 v247, v247, v215\n"
"v_sub_f32 v164, 0, v247\n"
"v_mov_b32 v152, 0\n"
"v_sub_f32 v165, 0, v247\n"
"v_mov_b32 v153, 0\n"
"v_sub_f32 v166, 0, v247\n"
"v_mov_b32 v154, 0\n"
"v_sub_f32 v167, 0, v247\n"
"v_mov_b32 v155, 0\n"
"v_mov_b32 v112, 0\n"
"v_mov_b32 v113, 0\n"
"v_mov_b32 v114, 0\n"
"v_mov_b32 v115, 0\n"
"v_mov_b32 v116, 0\n"
"v_mov_b32 v117, 0\n"
"v_mov_b32 v118, 0\n"
"v_mov_b32 v119, 0\n"
"v_mov_b32 v120, 0\n"
"v_mov_b32 v121, 0\n"
"v_mov_b32 v122, 0\n"
"v_mov_b32 v123, 0\n"
"v_mov_b32 v124, 0\n"
"v_mov_b32 v125, 0\n"
"v_mov_b32 v126, 0\n"
"v_mov_b32 v127, 0\n"
"v_mov_b32 v128, 0\n"
"v_mov_b32 v129, 0\n"
"v_mov_b32 v130, 0\n"
"v_mov_b32 v131, 0\n"
"v_mov_b32 v132, 0\n"
"v_mov_b32 v133, 0\n"
"v_mov_b32 v134, 0\n"
"v_mov_b32 v135, 0\n"
"v_mov_b32 v136, 0\n"
"v_mov_b32 v137, 0\n"
"v_mov_b32 v138, 0\n"
"v_mov_b32 v139, 0\n"
"v_mov_b32 v140, 0\n"
"v_mov_b32 v141, 0\n"
"v_mov_b32 v142, 0\n"
"v_mov_b32 v143, 0\n"
"s_mov_b32 s28, 0\n"
"L_slow02b_%=:\n"
"buffer_load_dwordx4 v[216:219], v244, s[36:39], s28 offen\n"
"buffer_load_dwordx4 v[220:223], v244, s[36:39], s28 offen offset:64\n"
"buffer_load_dwordx4 v[224:227], v244, s[36:39], s28 offen offset:128\n"
"buffer_load_dwordx4 v[228:231], v244, s[36:39], s28 offen offset:192\n"
"s_waitcnt vmcnt(3)\n"
"v_mfma_f32_16x16x32_bf16 v[232:235], v[216:219], a[32:35], v[164:167]\n"
"s_waitcnt vmcnt(2)\n"
"v_mfma_f32_16x16x32_bf16 v[232:235], v[220:223], a[36:39], v[232:235]\n"
"s_waitcnt vmcnt(1)\n"
"v_mfma_f32_16x16x32_bf16 v[232:235], v[224:227], a[40:43], v[232:235]\n"
"s_waitcnt vmcnt(0)\n"
"v_mfma_f32_16x16x32_bf16 v[232:235], v[228:231], a[44:47], v[232:235]\n"
"s_add_u32 s28, s28, 4096\n"
"buffer_load_dwordx4 v[216:219], v244, s[36:39], s28 offen\n"
"buffer_load_dwordx4 v[220:223], v244, s[36:39], s28 offen offset:64\n"
"buffer_load_dwordx4 v[224:227], v244, s[36:39], s28 offen offset:128\n"
"buffer_load_dwordx4 v[228:231], v244, s[36:39], s28 offen offset:192\n"
"s_waitcnt vmcnt(3)\n"
"v_mfma_f32_16x16x32_bf16 v[236:239], v[216:219], a[32:35], v[164:167]\n"
"s_waitcnt vmcnt(2)\n"
"v_mfma_f32_16x16x32_bf16 v[236:239], v[220:223], a[36:39], v[236:239]\n"
"s_waitcnt vmcnt(1)\n"
"v_mfma_f32_16x16x32_bf16 v[236:239], v[224:227], a[40:43], v[236:239]\n"
"s_waitcnt vmcnt(0)\n"
"v_mfma_f32_16x16x32_bf16 v[236:239], v[228:231], a[44:47], v[236:239]\n"
"s_add_u32 s28, s28, 4096\n"
"v_exp_f32 v232, v232\n"
"v_exp_f32 v233, v233\n"
"v_exp_f32 v234, v234\n"
"v_exp_f32 v235, v235\n"
"s_nop 2\n"
"v_exp_f32 v236, v236\n"
"v_exp_f32 v237, v237\n"
"v_exp_f32 v238, v238\n"
"v_exp_f32 v239, v239\n"
"v_cvt_pk_bf16_f32 v240, v232, v233\n"
"v_cvt_pk_bf16_f32 v241, v234, v235\n"
"v_cvt_pk_bf16_f32 v242, v236, v237\n"
"v_cvt_pk_bf16_f32 v243, v238, v239\n"
"s_nop 1\n"
"v_mfma_f32_16x16x32_bf16 v[152:155], v[192:195], v[240:243], v[152:155]\n"
"s_sub_u32 s28, s28, 8192\n"
"buffer_load_ushort v216, v245, s[40:43], s28 offen\n"
"buffer_load_ushort v217, v245, s[40:43], s28 offen offset:256\n"
"buffer_load_ushort v218, v245, s[40:43], s28 offen offset:512\n"
"buffer_load_ushort v219, v245, s[40:43], s28 offen offset:768\n"
"buffer_load_ushort v220, v246, s[40:43], s28 offen\n"
"buffer_load_ushort v221, v246, s[40:43], s28 offen offset:256\n"
"buffer_load_ushort v222, v246, s[40:43], s28 offen offset:512\n"
"buffer_load_ushort v223, v246, s[40:43], s28 offen offset:768\n"
"s_waitcnt vmcnt(6)\n"
"v_lshl_or_b32 v224, v217, 16, v216\n"
"s_waitcnt vmcnt(4)\n"
"v_lshl_or_b32 v225, v219, 16, v218\n"
"s_waitcnt vmcnt(2)\n"
"v_lshl_or_b32 v226, v221, 16, v220\n"
"s_waitcnt vmcnt(0)\n"
"v_lshl_or_b32 v227, v223, 16, v222\n"
"s_nop 1\n"
"v_mfma_f32_16x16x32_bf16 v[112:115], v[224:227], v[240:243], v[112:115]\n"
"buffer_load_ushort v216, v245, s[40:43], s28 offen offset:32\n"
"buffer_load_ushort v217, v245, s[40:43], s28 offen offset:288\n"
"buffer_load_ushort v218, v245, s[40:43], s28 offen offset:544\n"
"buffer_load_ushort v219, v245, s[40:43], s28 offen offset:800\n"
"buffer_load_ushort v220, v246, s[40:43], s28 offen offset:32\n"
"buffer_load_ushort v221, v246, s[40:43], s28 offen offset:288\n"
"buffer_load_ushort v222, v246, s[40:43], s28 offen offset:544\n"
"buffer_load_ushort v223, v246, s[40:43], s28 offen offset:800\n"
"s_waitcnt vmcnt(6)\n"
"v_lshl_or_b32 v224, v217, 16, v216\n"
"s_waitcnt vmcnt(4)\n"
"v_lshl_or_b32 v225, v219, 16, v218\n"
"s_waitcnt vmcnt(2)\n"
"v_lshl_or_b32 v226, v221, 16, v220\n"
"s_waitcnt vmcnt(0)\n"
"v_lshl_or_b32 v227, v223, 16, v222\n"
"s_nop 1\n"
"v_mfma_f32_16x16x32_bf16 v[116:119], v[224:227], v[240:243], v[116:119]\n"
"buffer_load_ushort v216, v245, s[40:43], s28 offen offset:64\n"
"buffer_load_ushort v217, v245, s[40:43], s28 offen offset:320\n"
"buffer_load_ushort v218, v245, s[40:43], s28 offen offset:576\n"
"buffer_load_ushort v219, v245, s[40:43], s28 offen offset:832\n"
"buffer_load_ushort v220, v246, s[40:43], s28 offen offset:64\n"
"buffer_load_ushort v221, v246, s[40:43], s28 offen offset:320\n"
"buffer_load_ushort v222, v246, s[40:43], s28 offen offset:576\n"
"buffer_load_ushort v223, v246, s[40:43], s28 offen offset:832\n"
"s_waitcnt vmcnt(6)\n"
"v_lshl_or_b32 v224, v217, 16, v216\n"
"s_waitcnt vmcnt(4)\n"
"v_lshl_or_b32 v225, v219, 16, v218\n"
"s_waitcnt vmcnt(2)\n"
"v_lshl_or_b32 v226, v221, 16, v220\n"
"s_waitcnt vmcnt(0)\n"
"v_lshl_or_b32 v227, v223, 16, v222\n"
"s_nop 1\n"
"v_mfma_f32_16x16x32_bf16 v[120:123], v[224:227], v[240:243], v[120:123]\n"
"buffer_load_ushort v216, v245, s[40:43], s28 offen offset:96\n"
"buffer_load_ushort v217, v245, s[40:43], s28 offen offset:352\n"
"buffer_load_ushort v218, v245, s[40:43], s28 offen offset:608\n"
"buffer_load_ushort v219, v245, s[40:43], s28 offen offset:864\n"
"buffer_load_ushort v220, v246, s[40:43], s28 offen offset:96\n"
"buffer_load_ushort v221, v246, s[40:43], s28 offen offset:352\n"
"buffer_load_ushort v222, v246, s[40:43], s28 offen offset:608\n"
"buffer_load_ushort v223, v246, s[40:43], s28 offen offset:864\n"
"s_waitcnt vmcnt(6)\n"
"v_lshl_or_b32 v224, v217, 16, v216\n"
"s_waitcnt vmcnt(4)\n"
"v_lshl_or_b32 v225, v219, 16, v218\n"
"s_waitcnt vmcnt(2)\n"
"v_lshl_or_b32 v226, v221, 16, v220\n"
"s_waitcnt vmcnt(0)\n"
"v_lshl_or_b32 v227, v223, 16, v222\n"
"s_nop 1\n"
"v_mfma_f32_16x16x32_bf16 v[124:127], v[224:227], v[240:243], v[124:127]\n"
"buffer_load_ushort v216, v245, s[40:43], s28 offen offset:128\n"
"buffer_load_ushort v217, v245, s[40:43], s28 offen offset:384\n"
"buffer_load_ushort v218, v245, s[40:43], s28 offen offset:640\n"
"buffer_load_ushort v219, v245, s[40:43], s28 offen offset:896\n"
"buffer_load_ushort v220, v246, s[40:43], s28 offen offset:128\n"
"buffer_load_ushort v221, v246, s[40:43], s28 offen offset:384\n"
"buffer_load_ushort v222, v246, s[40:43], s28 offen offset:640\n"
"buffer_load_ushort v223, v246, s[40:43], s28 offen offset:896\n"
"s_waitcnt vmcnt(6)\n"
"v_lshl_or_b32 v224, v217, 16, v216\n"
"s_waitcnt vmcnt(4)\n"
"v_lshl_or_b32 v225, v219, 16, v218\n"
"s_waitcnt vmcnt(2)\n"
"v_lshl_or_b32 v226, v221, 16, v220\n"
"s_waitcnt vmcnt(0)\n"
"v_lshl_or_b32 v227, v223, 16, v222\n"
"s_nop 1\n"
"v_mfma_f32_16x16x32_bf16 v[128:131], v[224:227], v[240:243], v[128:131]\n"
"buffer_load_ushort v216, v245, s[40:43], s28 offen offset:160\n"
"buffer_load_ushort v217, v245, s[40:43], s28 offen offset:416\n"
"buffer_load_ushort v218, v245, s[40:43], s28 offen offset:672\n"
"buffer_load_ushort v219, v245, s[40:43], s28 offen offset:928\n"
"buffer_load_ushort v220, v246, s[40:43], s28 offen offset:160\n"
"buffer_load_ushort v221, v246, s[40:43], s28 offen offset:416\n"
"buffer_load_ushort v222, v246, s[40:43], s28 offen offset:672\n"
"buffer_load_ushort v223, v246, s[40:43], s28 offen offset:928\n"
"s_waitcnt vmcnt(6)\n"
"v_lshl_or_b32 v224, v217, 16, v216\n"
"s_waitcnt vmcnt(4)\n"
"v_lshl_or_b32 v225, v219, 16, v218\n"
"s_waitcnt vmcnt(2)\n"
"v_lshl_or_b32 v226, v221, 16, v220\n"
"s_waitcnt vmcnt(0)\n"
"v_lshl_or_b32 v227, v223, 16, v222\n"
"s_nop 1\n"
"v_mfma_f32_16x16x32_bf16 v[132:135], v[224:227], v[240:243], v[132:135]\n"
"buffer_load_ushort v216, v245, s[40:43], s28 offen offset:192\n"
"buffer_load_ushort v217, v245, s[40:43], s28 offen offset:448\n"
"buffer_load_ushort v218, v245, s[40:43], s28 offen offset:704\n"
"buffer_load_ushort v219, v245, s[40:43], s28 offen offset:960\n"
"buffer_load_ushort v220, v246, s[40:43], s28 offen offset:192\n"
"buffer_load_ushort v221, v246, s[40:43], s28 offen offset:448\n"
"buffer_load_ushort v222, v246, s[40:43], s28 offen offset:704\n"
"buffer_load_ushort v223, v246, s[40:43], s28 offen offset:960\n"
"s_waitcnt vmcnt(6)\n"
"v_lshl_or_b32 v224, v217, 16, v216\n"
"s_waitcnt vmcnt(4)\n"
"v_lshl_or_b32 v225, v219, 16, v218\n"
"s_waitcnt vmcnt(2)\n"
"v_lshl_or_b32 v226, v221, 16, v220\n"
"s_waitcnt vmcnt(0)\n"
"v_lshl_or_b32 v227, v223, 16, v222\n"
"s_nop 1\n"
"v_mfma_f32_16x16x32_bf16 v[136:139], v[224:227], v[240:243], v[136:139]\n"
"buffer_load_ushort v216, v245, s[40:43], s28 offen offset:224\n"
"buffer_load_ushort v217, v245, s[40:43], s28 offen offset:480\n"
"buffer_load_ushort v218, v245, s[40:43], s28 offen offset:736\n"
"buffer_load_ushort v219, v245, s[40:43], s28 offen offset:992\n"
"buffer_load_ushort v220, v246, s[40:43], s28 offen offset:224\n"
"buffer_load_ushort v221, v246, s[40:43], s28 offen offset:480\n"
"buffer_load_ushort v222, v246, s[40:43], s28 offen offset:736\n"
"buffer_load_ushort v223, v246, s[40:43], s28 offen offset:992\n"
"s_waitcnt vmcnt(6)\n"
"v_lshl_or_b32 v224, v217, 16, v216\n"
"s_waitcnt vmcnt(4)\n"
"v_lshl_or_b32 v225, v219, 16, v218\n"
"s_waitcnt vmcnt(2)\n"
"v_lshl_or_b32 v226, v221, 16, v220\n"
"s_waitcnt vmcnt(0)\n"
"v_lshl_or_b32 v227, v223, 16, v222\n"
"s_nop 1\n"
"v_mfma_f32_16x16x32_bf16 v[140:143], v[224:227], v[240:243], v[140:143]\n"
"s_add_u32 s28, s28, 8192\n"
"s_cmp_lg_u32 s28, 131072\n"
"s_nop 7\n"
"s_cbranch_scc1 L_slow02b_%=\n"
"s_branch L_slow02r_%=\n"
"L_slow10_%=:\n"
"s_waitcnt vmcnt(0) lgkmcnt(0)\n"
"s_nop 7\n"
"v_and_b32 v247, 15, v1\n"
"v_lshrrev_b32 v215, 4, v1\n"
"v_lshlrev_b32 v244, 8, v247\n"
"v_lshlrev_b32 v245, 4, v215\n"
"v_add_u32 v244, v244, v245\n"
"v_lshlrev_b32 v245, 10, v215\n"
"v_lshlrev_b32 v247, 1, v247\n"
"v_add_u32 v245, v245, v247\n"
"v_add_u32 v246, 4096, v245\n"
"v_mov_b32 v247, 0xff800000\n"
"s_mov_b32 s28, 131072\n"
"L_slow10a_%=:\n"
"buffer_load_dwordx4 v[216:219], v244, s[36:39], s28 offen\n"
"buffer_load_dwordx4 v[220:223], v244, s[36:39], s28 offen offset:64\n"
"buffer_load_dwordx4 v[224:227], v244, s[36:39], s28 offen offset:128\n"
"buffer_load_dwordx4 v[228:231], v244, s[36:39], s28 offen offset:192\n"
"s_waitcnt vmcnt(3)\n"
"v_mfma_f32_16x16x32_bf16 v[232:235], v[216:219], a[48:51], 0\n"
"s_waitcnt vmcnt(2)\n"
"v_mfma_f32_16x16x32_bf16 v[232:235], v[220:223], a[52:55], v[232:235]\n"
"s_waitcnt vmcnt(1)\n"
"v_mfma_f32_16x16x32_bf16 v[232:235], v[224:227], a[56:59], v[232:235]\n"
"s_waitcnt vmcnt(0)\n"
"v_mfma_f32_16x16x32_bf16 v[232:235], v[228:231], a[60:63], v[232:235]\n"
"s_add_u32 s28, s28, 4096\n"
"s_nop 6\n"
"v_max3_f32 v247, v247, v232, v233\n"
"v_max3_f32 v247, v247, v234, v235\n"
"s_cmp_lg_u32 s28, 262144\n"
"s_cbranch_scc1 L_slow10a_%=\n"
"v_mov_b32 v215, v247\n"
"s_nop 1\n"
"v_permlane32_swap_b32 v247, v215\n"
"v_max_f32 v247, v247, v215\n"
"v_mov_b32 v215, v247\n"
"s_nop 1\n"
"v_permlane16_swap_b32 v247, v215\n"
"v_max_f32 v247, v247, v215\n"
"v_sub_f32 v156, 0, v247\n"
"v_mov_b32 v144, 0\n"
"v_sub_f32 v157, 0, v247\n"
"v_mov_b32 v145, 0\n"
"v_sub_f32 v158, 0, v247\n"
"v_mov_b32 v146, 0\n"
"v_sub_f32 v159, 0, v247\n"
"v_mov_b32 v147, 0\n"
"v_mov_b32 v48, 0\n"
"v_mov_b32 v49, 0\n"
"v_mov_b32 v50, 0\n"
"v_mov_b32 v51, 0\n"
"v_mov_b32 v52, 0\n"
"v_mov_b32 v53, 0\n"
"v_mov_b32 v54, 0\n"
"v_mov_b32 v55, 0\n"
"v_mov_b32 v56, 0\n"
"v_mov_b32 v57, 0\n"
"v_mov_b32 v58, 0\n"
"v_mov_b32 v59, 0\n"
"v_mov_b32 v60, 0\n"
"v_mov_b32 v61, 0\n"
"v_mov_b32 v62, 0\n"
"v_mov_b32 v63, 0\n"
"v_mov_b32 v64, 0\n"
"v_mov_b32 v65, 0\n"
"v_mov_b32 v66, 0\n"
"v_mov_b32 v67, 0\n"
"v_mov_b32 v68, 0\n"
"v_mov_b32 v69, 0\n"
"v_mov_b32 v70, 0\n"
"v_mov_b32 v71, 0\n"
"v_mov_b32 v72, 0\n"
"v_mov_b32 v73, 0\n"
"v_mov_b32 v74, 0\n"
"v_mov_b32 v75, 0\n"
"v_mov_b32 v76, 0\n"
"v_mov_b32 v77, 0\n"
"v_mov_b32 v78, 0\n"
"v_mov_b32 v79, 0\n"
"s_mov_b32 s28, 131072\n"
"L_slow10b_%=:\n"
"buffer_load_dwordx4 v[216:219], v244, s[36:39], s28 offen\n"
"buffer_load_dwordx4 v[220:223], v244, s[36:39], s28 offen offset:64\n"
"buffer_load_dwordx4 v[224:227], v244, s[36:39], s28 offen offset:128\n"
"buffer_load_dwordx4 v[228:231], v244, s[36:39], s28 offen offset:192\n"
"s_waitcnt vmcnt(3)\n"
"v_mfma_f32_16x16x32_bf16 v[232:235], v[216:219], a[48:51], v[156:159]\n"
"s_waitcnt vmcnt(2)\n"
"v_mfma_f32_16x16x32_bf16 v[232:235], v[220:223], a[52:55], v[232:235]\n"
"s_waitcnt vmcnt(1)\n"
"v_mfma_f32_16x16x32_bf16 v[232:235], v[224:227], a[56:59], v[232:235]\n"
"s_waitcnt vmcnt(0)\n"
"v_mfma_f32_16x16x32_bf16 v[232:235], v[228:231], a[60:63], v[232:235]\n"
"s_add_u32 s28, s28, 4096\n"
"buffer_load_dwordx4 v[216:219], v244, s[36:39], s28 offen\n"
"buffer_load_dwordx4 v[220:223], v244, s[36:39], s28 offen offset:64\n"
"buffer_load_dwordx4 v[224:227], v244, s[36:39], s28 offen offset:128\n"
"buffer_load_dwordx4 v[228:231], v244, s[36:39], s28 offen offset:192\n"
"s_waitcnt vmcnt(3)\n"
"v_mfma_f32_16x16x32_bf16 v[236:239], v[216:219], a[48:51], v[156:159]\n"
"s_waitcnt vmcnt(2)\n"
"v_mfma_f32_16x16x32_bf16 v[236:239], v[220:223], a[52:55], v[236:239]\n"
"s_waitcnt vmcnt(1)\n"
"v_mfma_f32_16x16x32_bf16 v[236:239], v[224:227], a[56:59], v[236:239]\n"
"s_waitcnt vmcnt(0)\n"
"v_mfma_f32_16x16x32_bf16 v[236:239], v[228:231], a[60:63], v[236:239]\n"
"s_add_u32 s28, s28, 4096\n"
"v_exp_f32 v232, v232\n"
"v_exp_f32 v233, v233\n"
"v_exp_f32 v234, v234\n"
"v_exp_f32 v235, v235\n"
"s_nop 2\n"
"v_exp_f32 v236, v236\n"
"v_exp_f32 v237, v237\n"
"v_exp_f32 v238, v238\n"
"v_exp_f32 v239, v239\n"
"v_cvt_pk_bf16_f32 v240, v232, v233\n"
"v_cvt_pk_bf16_f32 v241, v234, v235\n"
"v_cvt_pk_bf16_f32 v242, v236, v237\n"
"v_cvt_pk_bf16_f32 v243, v238, v239\n"
"s_nop 1\n"
"v_mfma_f32_16x16x32_bf16 v[144:147], v[192:195], v[240:243], v[144:147]\n"
"s_sub_u32 s28, s28, 8192\n"
"buffer_load_ushort v216, v245, s[40:43], s28 offen\n"
"buffer_load_ushort v217, v245, s[40:43], s28 offen offset:256\n"
"buffer_load_ushort v218, v245, s[40:43], s28 offen offset:512\n"
"buffer_load_ushort v219, v245, s[40:43], s28 offen offset:768\n"
"buffer_load_ushort v220, v246, s[40:43], s28 offen\n"
"buffer_load_ushort v221, v246, s[40:43], s28 offen offset:256\n"
"buffer_load_ushort v222, v246, s[40:43], s28 offen offset:512\n"
"buffer_load_ushort v223, v246, s[40:43], s28 offen offset:768\n"
"s_waitcnt vmcnt(6)\n"
"v_lshl_or_b32 v224, v217, 16, v216\n"
"s_waitcnt vmcnt(4)\n"
"v_lshl_or_b32 v225, v219, 16, v218\n"
"s_waitcnt vmcnt(2)\n"
"v_lshl_or_b32 v226, v221, 16, v220\n"
"s_waitcnt vmcnt(0)\n"
"v_lshl_or_b32 v227, v223, 16, v222\n"
"s_nop 1\n"
"v_mfma_f32_16x16x32_bf16 v[48:51], v[224:227], v[240:243], v[48:51]\n"
"buffer_load_ushort v216, v245, s[40:43], s28 offen offset:32\n"
"buffer_load_ushort v217, v245, s[40:43], s28 offen offset:288\n"
"buffer_load_ushort v218, v245, s[40:43], s28 offen offset:544\n"
"buffer_load_ushort v219, v245, s[40:43], s28 offen offset:800\n"
"buffer_load_ushort v220, v246, s[40:43], s28 offen offset:32\n"
"buffer_load_ushort v221, v246, s[40:43], s28 offen offset:288\n"
"buffer_load_ushort v222, v246, s[40:43], s28 offen offset:544\n"
"buffer_load_ushort v223, v246, s[40:43], s28 offen offset:800\n"
"s_waitcnt vmcnt(6)\n"
"v_lshl_or_b32 v224, v217, 16, v216\n"
"s_waitcnt vmcnt(4)\n"
"v_lshl_or_b32 v225, v219, 16, v218\n"
"s_waitcnt vmcnt(2)\n"
"v_lshl_or_b32 v226, v221, 16, v220\n"
"s_waitcnt vmcnt(0)\n"
"v_lshl_or_b32 v227, v223, 16, v222\n"
"s_nop 1\n"
"v_mfma_f32_16x16x32_bf16 v[52:55], v[224:227], v[240:243], v[52:55]\n"
"buffer_load_ushort v216, v245, s[40:43], s28 offen offset:64\n"
"buffer_load_ushort v217, v245, s[40:43], s28 offen offset:320\n"
"buffer_load_ushort v218, v245, s[40:43], s28 offen offset:576\n"
"buffer_load_ushort v219, v245, s[40:43], s28 offen offset:832\n"
"buffer_load_ushort v220, v246, s[40:43], s28 offen offset:64\n"
"buffer_load_ushort v221, v246, s[40:43], s28 offen offset:320\n"
"buffer_load_ushort v222, v246, s[40:43], s28 offen offset:576\n"
"buffer_load_ushort v223, v246, s[40:43], s28 offen offset:832\n"
"s_waitcnt vmcnt(6)\n"
"v_lshl_or_b32 v224, v217, 16, v216\n"
"s_waitcnt vmcnt(4)\n"
"v_lshl_or_b32 v225, v219, 16, v218\n"
"s_waitcnt vmcnt(2)\n"
"v_lshl_or_b32 v226, v221, 16, v220\n"
"s_waitcnt vmcnt(0)\n"
"v_lshl_or_b32 v227, v223, 16, v222\n"
"s_nop 1\n"
"v_mfma_f32_16x16x32_bf16 v[56:59], v[224:227], v[240:243], v[56:59]\n"
"buffer_load_ushort v216, v245, s[40:43], s28 offen offset:96\n"
"buffer_load_ushort v217, v245, s[40:43], s28 offen offset:352\n"
"buffer_load_ushort v218, v245, s[40:43], s28 offen offset:608\n"
"buffer_load_ushort v219, v245, s[40:43], s28 offen offset:864\n"
"buffer_load_ushort v220, v246, s[40:43], s28 offen offset:96\n"
"buffer_load_ushort v221, v246, s[40:43], s28 offen offset:352\n"
"buffer_load_ushort v222, v246, s[40:43], s28 offen offset:608\n"
"buffer_load_ushort v223, v246, s[40:43], s28 offen offset:864\n"
"s_waitcnt vmcnt(6)\n"
"v_lshl_or_b32 v224, v217, 16, v216\n"
"s_waitcnt vmcnt(4)\n"
"v_lshl_or_b32 v225, v219, 16, v218\n"
"s_waitcnt vmcnt(2)\n"
"v_lshl_or_b32 v226, v221, 16, v220\n"
"s_waitcnt vmcnt(0)\n"
"v_lshl_or_b32 v227, v223, 16, v222\n"
"s_nop 1\n"
"v_mfma_f32_16x16x32_bf16 v[60:63], v[224:227], v[240:243], v[60:63]\n"
"buffer_load_ushort v216, v245, s[40:43], s28 offen offset:128\n"
"buffer_load_ushort v217, v245, s[40:43], s28 offen offset:384\n"
"buffer_load_ushort v218, v245, s[40:43], s28 offen offset:640\n"
"buffer_load_ushort v219, v245, s[40:43], s28 offen offset:896\n"
"buffer_load_ushort v220, v246, s[40:43], s28 offen offset:128\n"
"buffer_load_ushort v221, v246, s[40:43], s28 offen offset:384\n"
"buffer_load_ushort v222, v246, s[40:43], s28 offen offset:640\n"
"buffer_load_ushort v223, v246, s[40:43], s28 offen offset:896\n"
"s_waitcnt vmcnt(6)\n"
"v_lshl_or_b32 v224, v217, 16, v216\n"
"s_waitcnt vmcnt(4)\n"
"v_lshl_or_b32 v225, v219, 16, v218\n"
"s_waitcnt vmcnt(2)\n"
"v_lshl_or_b32 v226, v221, 16, v220\n"
"s_waitcnt vmcnt(0)\n"
"v_lshl_or_b32 v227, v223, 16, v222\n"
"s_nop 1\n"
"v_mfma_f32_16x16x32_bf16 v[64:67], v[224:227], v[240:243], v[64:67]\n"
"buffer_load_ushort v216, v245, s[40:43], s28 offen offset:160\n"
"buffer_load_ushort v217, v245, s[40:43], s28 offen offset:416\n"
"buffer_load_ushort v218, v245, s[40:43], s28 offen offset:672\n"
"buffer_load_ushort v219, v245, s[40:43], s28 offen offset:928\n"
"buffer_load_ushort v220, v246, s[40:43], s28 offen offset:160\n"
"buffer_load_ushort v221, v246, s[40:43], s28 offen offset:416\n"
"buffer_load_ushort v222, v246, s[40:43], s28 offen offset:672\n"
"buffer_load_ushort v223, v246, s[40:43], s28 offen offset:928\n"
"s_waitcnt vmcnt(6)\n"
"v_lshl_or_b32 v224, v217, 16, v216\n"
"s_waitcnt vmcnt(4)\n"
"v_lshl_or_b32 v225, v219, 16, v218\n"
"s_waitcnt vmcnt(2)\n"
"v_lshl_or_b32 v226, v221, 16, v220\n"
"s_waitcnt vmcnt(0)\n"
"v_lshl_or_b32 v227, v223, 16, v222\n"
"s_nop 1\n"
"v_mfma_f32_16x16x32_bf16 v[68:71], v[224:227], v[240:243], v[68:71]\n"
"buffer_load_ushort v216, v245, s[40:43], s28 offen offset:192\n"
"buffer_load_ushort v217, v245, s[40:43], s28 offen offset:448\n"
"buffer_load_ushort v218, v245, s[40:43], s28 offen offset:704\n"
"buffer_load_ushort v219, v245, s[40:43], s28 offen offset:960\n"
"buffer_load_ushort v220, v246, s[40:43], s28 offen offset:192\n"
"buffer_load_ushort v221, v246, s[40:43], s28 offen offset:448\n"
"buffer_load_ushort v222, v246, s[40:43], s28 offen offset:704\n"
"buffer_load_ushort v223, v246, s[40:43], s28 offen offset:960\n"
"s_waitcnt vmcnt(6)\n"
"v_lshl_or_b32 v224, v217, 16, v216\n"
"s_waitcnt vmcnt(4)\n"
"v_lshl_or_b32 v225, v219, 16, v218\n"
"s_waitcnt vmcnt(2)\n"
"v_lshl_or_b32 v226, v221, 16, v220\n"
"s_waitcnt vmcnt(0)\n"
"v_lshl_or_b32 v227, v223, 16, v222\n"
"s_nop 1\n"
"v_mfma_f32_16x16x32_bf16 v[72:75], v[224:227], v[240:243], v[72:75]\n"
"buffer_load_ushort v216, v245, s[40:43], s28 offen offset:224\n"
"buffer_load_ushort v217, v245, s[40:43], s28 offen offset:480\n"
"buffer_load_ushort v218, v245, s[40:43], s28 offen offset:736\n"
"buffer_load_ushort v219, v245, s[40:43], s28 offen offset:992\n"
"buffer_load_ushort v220, v246, s[40:43], s28 offen offset:224\n"
"buffer_load_ushort v221, v246, s[40:43], s28 offen offset:480\n"
"buffer_load_ushort v222, v246, s[40:43], s28 offen offset:736\n"
"buffer_load_ushort v223, v246, s[40:43], s28 offen offset:992\n"
"s_waitcnt vmcnt(6)\n"
"v_lshl_or_b32 v224, v217, 16, v216\n"
"s_waitcnt vmcnt(4)\n"
"v_lshl_or_b32 v225, v219, 16, v218\n"
"s_waitcnt vmcnt(2)\n"
"v_lshl_or_b32 v226, v221, 16, v220\n"
"s_waitcnt vmcnt(0)\n"
"v_lshl_or_b32 v227, v223, 16, v222\n"
"s_nop 1\n"
"v_mfma_f32_16x16x32_bf16 v[76:79], v[224:227], v[240:243], v[76:79]\n"
"s_add_u32 s28, s28, 8192\n"
"s_cmp_lg_u32 s28, 262144\n"
"s_nop 7\n"
"s_cbranch_scc1 L_slow10b_%=\n"
"s_branch L_slow10r_%=\n"
"L_slow11_%=:\n"
"s_waitcnt vmcnt(0) lgkmcnt(0)\n"
"s_nop 7\n"
"v_and_b32 v247, 15, v1\n"
"v_lshrrev_b32 v215, 4, v1\n"
"v_lshlrev_b32 v244, 8, v247\n"
"v_lshlrev_b32 v245, 4, v215\n"
"v_add_u32 v244, v244, v245\n"
"v_lshlrev_b32 v245, 10, v215\n"
"v_lshlrev_b32 v247, 1, v247\n"
"v_add_u32 v245, v245, v247\n"
"v_add_u32 v246, 4096, v245\n"
"v_mov_b32 v247, 0xff800000\n"
"s_mov_b32 s28, 131072\n"
"L_slow11a_%=:\n"
"buffer_load_dwordx4 v[216:219], v244, s[36:39], s28 offen\n"
"buffer_load_dwordx4 v[220:223], v244, s[36:39], s28 offen offset:64\n"
"buffer_load_dwordx4 v[224:227], v244, s[36:39], s28 offen offset:128\n"
"buffer_load_dwordx4 v[228:231], v244, s[36:39], s28 offen offset:192\n"
"s_waitcnt vmcnt(3)\n"
"v_mfma_f32_16x16x32_bf16 v[232:235], v[216:219], a[64:67], 0\n"
"s_waitcnt vmcnt(2)\n"
"v_mfma_f32_16x16x32_bf16 v[232:235], v[220:223], a[68:71], v[232:235]\n"
"s_waitcnt vmcnt(1)\n"
"v_mfma_f32_16x16x32_bf16 v[232:235], v[224:227], a[72:75], v[232:235]\n"
"s_waitcnt vmcnt(0)\n"
"v_mfma_f32_16x16x32_bf16 v[232:235], v[228:231], a[76:79], v[232:235]\n"
"s_add_u32 s28, s28, 4096\n"
"s_nop 6\n"
"v_max3_f32 v247, v247, v232, v233\n"
"v_max3_f32 v247, v247, v234, v235\n"
"s_cmp_lg_u32 s28, 262144\n"
"s_cbranch_scc1 L_slow11a_%=\n"
"v_mov_b32 v215, v247\n"
"s_nop 1\n"
"v_permlane32_swap_b32 v247, v215\n"
"v_max_f32 v247, v247, v215\n"
"v_mov_b32 v215, v247\n"
"s_nop 1\n"
"v_permlane16_swap_b32 v247, v215\n"
"v_max_f32 v247, v247, v215\n"
"v_sub_f32 v160, 0, v247\n"
"v_mov_b32 v148, 0\n"
"v_sub_f32 v161, 0, v247\n"
"v_mov_b32 v149, 0\n"
"v_sub_f32 v162, 0, v247\n"
"v_mov_b32 v150, 0\n"
"v_sub_f32 v163, 0, v247\n"
"v_mov_b32 v151, 0\n"
"v_mov_b32 v80, 0\n"
"v_mov_b32 v81, 0\n"
"v_mov_b32 v82, 0\n"
"v_mov_b32 v83, 0\n"
"v_mov_b32 v84, 0\n"
"v_mov_b32 v85, 0\n"
"v_mov_b32 v86, 0\n"
"v_mov_b32 v87, 0\n"
"v_mov_b32 v88, 0\n"
"v_mov_b32 v89, 0\n"
"v_mov_b32 v90, 0\n"
"v_mov_b32 v91, 0\n"
"v_mov_b32 v92, 0\n"
"v_mov_b32 v93, 0\n"
"v_mov_b32 v94, 0\n"
"v_mov_b32 v95, 0\n"
"v_mov_b32 v96, 0\n"
"v_mov_b32 v97, 0\n"
"v_mov_b32 v98, 0\n"
"v_mov_b32 v99, 0\n"
"v_mov_b32 v100, 0\n"
"v_mov_b32 v101, 0\n"
"v_mov_b32 v102, 0\n"
"v_mov_b32 v103, 0\n"
"v_mov_b32 v104, 0\n"
"v_mov_b32 v105, 0\n"
"v_mov_b32 v106, 0\n"
"v_mov_b32 v107, 0\n"
"v_mov_b32 v108, 0\n"
"v_mov_b32 v109, 0\n"
"v_mov_b32 v110, 0\n"
"v_mov_b32 v111, 0\n"
"s_mov_b32 s28, 131072\n"
"L_slow11b_%=:\n"
"buffer_load_dwordx4 v[216:219], v244, s[36:39], s28 offen\n"
"buffer_load_dwordx4 v[220:223], v244, s[36:39], s28 offen offset:64\n"
"buffer_load_dwordx4 v[224:227], v244, s[36:39], s28 offen offset:128\n"
"buffer_load_dwordx4 v[228:231], v244, s[36:39], s28 offen offset:192\n"
"s_waitcnt vmcnt(3)\n"
"v_mfma_f32_16x16x32_bf16 v[232:235], v[216:219], a[64:67], v[160:163]\n"
"s_waitcnt vmcnt(2)\n"
"v_mfma_f32_16x16x32_bf16 v[232:235], v[220:223], a[68:71], v[232:235]\n"
"s_waitcnt vmcnt(1)\n"
"v_mfma_f32_16x16x32_bf16 v[232:235], v[224:227], a[72:75], v[232:235]\n"
"s_waitcnt vmcnt(0)\n"
"v_mfma_f32_16x16x32_bf16 v[232:235], v[228:231], a[76:79], v[232:235]\n"
"s_add_u32 s28, s28, 4096\n"
"buffer_load_dwordx4 v[216:219], v244, s[36:39], s28 offen\n"
"buffer_load_dwordx4 v[220:223], v244, s[36:39], s28 offen offset:64\n"
"buffer_load_dwordx4 v[224:227], v244, s[36:39], s28 offen offset:128\n"
"buffer_load_dwordx4 v[228:231], v244, s[36:39], s28 offen offset:192\n"
"s_waitcnt vmcnt(3)\n"
"v_mfma_f32_16x16x32_bf16 v[236:239], v[216:219], a[64:67], v[160:163]\n"
"s_waitcnt vmcnt(2)\n"
"v_mfma_f32_16x16x32_bf16 v[236:239], v[220:223], a[68:71], v[236:239]\n"
"s_waitcnt vmcnt(1)\n"
"v_mfma_f32_16x16x32_bf16 v[236:239], v[224:227], a[72:75], v[236:239]\n"
"s_waitcnt vmcnt(0)\n"
"v_mfma_f32_16x16x32_bf16 v[236:239], v[228:231], a[76:79], v[236:239]\n"
"s_add_u32 s28, s28, 4096\n"
"v_exp_f32 v232, v232\n"
"v_exp_f32 v233, v233\n"
"v_exp_f32 v234, v234\n"
"v_exp_f32 v235, v235\n"
"s_nop 2\n"
"v_exp_f32 v236, v236\n"
"v_exp_f32 v237, v237\n"
"v_exp_f32 v238, v238\n"
"v_exp_f32 v239, v239\n"
"v_cvt_pk_bf16_f32 v240, v232, v233\n"
"v_cvt_pk_bf16_f32 v241, v234, v235\n"
"v_cvt_pk_bf16_f32 v242, v236, v237\n"
"v_cvt_pk_bf16_f32 v243, v238, v239\n"
"s_nop 1\n"
"v_mfma_f32_16x16x32_bf16 v[148:151], v[192:195], v[240:243], v[148:151]\n"
"s_sub_u32 s28, s28, 8192\n"
"buffer_load_ushort v216, v245, s[40:43], s28 offen\n"
"buffer_load_ushort v217, v245, s[40:43], s28 offen offset:256\n"
"buffer_load_ushort v218, v245, s[40:43], s28 offen offset:512\n"
"buffer_load_ushort v219, v245, s[40:43], s28 offen offset:768\n"
"buffer_load_ushort v220, v246, s[40:43], s28 offen\n"
"buffer_load_ushort v221, v246, s[40:43], s28 offen offset:256\n"
"buffer_load_ushort v222, v246, s[40:43], s28 offen offset:512\n"
"buffer_load_ushort v223, v246, s[40:43], s28 offen offset:768\n"
"s_waitcnt vmcnt(6)\n"
"v_lshl_or_b32 v224, v217, 16, v216\n"
"s_waitcnt vmcnt(4)\n"
"v_lshl_or_b32 v225, v219, 16, v218\n"
"s_waitcnt vmcnt(2)\n"
"v_lshl_or_b32 v226, v221, 16, v220\n"
"s_waitcnt vmcnt(0)\n"
"v_lshl_or_b32 v227, v223, 16, v222\n"
"s_nop 1\n"
"v_mfma_f32_16x16x32_bf16 v[80:83], v[224:227], v[240:243], v[80:83]\n"
"buffer_load_ushort v216, v245, s[40:43], s28 offen offset:32\n"
"buffer_load_ushort v217, v245, s[40:43], s28 offen offset:288\n"
"buffer_load_ushort v218, v245, s[40:43], s28 offen offset:544\n"
"buffer_load_ushort v219, v245, s[40:43], s28 offen offset:800\n"
"buffer_load_ushort v220, v246, s[40:43], s28 offen offset:32\n"
"buffer_load_ushort v221, v246, s[40:43], s28 offen offset:288\n"
"buffer_load_ushort v222, v246, s[40:43], s28 offen offset:544\n"
"buffer_load_ushort v223, v246, s[40:43], s28 offen offset:800\n"
"s_waitcnt vmcnt(6)\n"
"v_lshl_or_b32 v224, v217, 16, v216\n"
"s_waitcnt vmcnt(4)\n"
"v_lshl_or_b32 v225, v219, 16, v218\n"
"s_waitcnt vmcnt(2)\n"
"v_lshl_or_b32 v226, v221, 16, v220\n"
"s_waitcnt vmcnt(0)\n"
"v_lshl_or_b32 v227, v223, 16, v222\n"
"s_nop 1\n"
"v_mfma_f32_16x16x32_bf16 v[84:87], v[224:227], v[240:243], v[84:87]\n"
"buffer_load_ushort v216, v245, s[40:43], s28 offen offset:64\n"
"buffer_load_ushort v217, v245, s[40:43], s28 offen offset:320\n"
"buffer_load_ushort v218, v245, s[40:43], s28 offen offset:576\n"
"buffer_load_ushort v219, v245, s[40:43], s28 offen offset:832\n"
"buffer_load_ushort v220, v246, s[40:43], s28 offen offset:64\n"
"buffer_load_ushort v221, v246, s[40:43], s28 offen offset:320\n"
"buffer_load_ushort v222, v246, s[40:43], s28 offen offset:576\n"
"buffer_load_ushort v223, v246, s[40:43], s28 offen offset:832\n"
"s_waitcnt vmcnt(6)\n"
"v_lshl_or_b32 v224, v217, 16, v216\n"
"s_waitcnt vmcnt(4)\n"
"v_lshl_or_b32 v225, v219, 16, v218\n"
"s_waitcnt vmcnt(2)\n"
"v_lshl_or_b32 v226, v221, 16, v220\n"
"s_waitcnt vmcnt(0)\n"
"v_lshl_or_b32 v227, v223, 16, v222\n"
"s_nop 1\n"
"v_mfma_f32_16x16x32_bf16 v[88:91], v[224:227], v[240:243], v[88:91]\n"
"buffer_load_ushort v216, v245, s[40:43], s28 offen offset:96\n"
"buffer_load_ushort v217, v245, s[40:43], s28 offen offset:352\n"
"buffer_load_ushort v218, v245, s[40:43], s28 offen offset:608\n"
"buffer_load_ushort v219, v245, s[40:43], s28 offen offset:864\n"
"buffer_load_ushort v220, v246, s[40:43], s28 offen offset:96\n"
"buffer_load_ushort v221, v246, s[40:43], s28 offen offset:352\n"
"buffer_load_ushort v222, v246, s[40:43], s28 offen offset:608\n"
"buffer_load_ushort v223, v246, s[40:43], s28 offen offset:864\n"
"s_waitcnt vmcnt(6)\n"
"v_lshl_or_b32 v224, v217, 16, v216\n"
"s_waitcnt vmcnt(4)\n"
"v_lshl_or_b32 v225, v219, 16, v218\n"
"s_waitcnt vmcnt(2)\n"
"v_lshl_or_b32 v226, v221, 16, v220\n"
"s_waitcnt vmcnt(0)\n"
"v_lshl_or_b32 v227, v223, 16, v222\n"
"s_nop 1\n"
"v_mfma_f32_16x16x32_bf16 v[92:95], v[224:227], v[240:243], v[92:95]\n"
"buffer_load_ushort v216, v245, s[40:43], s28 offen offset:128\n"
"buffer_load_ushort v217, v245, s[40:43], s28 offen offset:384\n"
"buffer_load_ushort v218, v245, s[40:43], s28 offen offset:640\n"
"buffer_load_ushort v219, v245, s[40:43], s28 offen offset:896\n"
"buffer_load_ushort v220, v246, s[40:43], s28 offen offset:128\n"
"buffer_load_ushort v221, v246, s[40:43], s28 offen offset:384\n"
"buffer_load_ushort v222, v246, s[40:43], s28 offen offset:640\n"
"buffer_load_ushort v223, v246, s[40:43], s28 offen offset:896\n"
"s_waitcnt vmcnt(6)\n"
"v_lshl_or_b32 v224, v217, 16, v216\n"
"s_waitcnt vmcnt(4)\n"
"v_lshl_or_b32 v225, v219, 16, v218\n"
"s_waitcnt vmcnt(2)\n"
"v_lshl_or_b32 v226, v221, 16, v220\n"
"s_waitcnt vmcnt(0)\n"
"v_lshl_or_b32 v227, v223, 16, v222\n"
"s_nop 1\n"
"v_mfma_f32_16x16x32_bf16 v[96:99], v[224:227], v[240:243], v[96:99]\n"
"buffer_load_ushort v216, v245, s[40:43], s28 offen offset:160\n"
"buffer_load_ushort v217, v245, s[40:43], s28 offen offset:416\n"
"buffer_load_ushort v218, v245, s[40:43], s28 offen offset:672\n"
"buffer_load_ushort v219, v245, s[40:43], s28 offen offset:928\n"
"buffer_load_ushort v220, v246, s[40:43], s28 offen offset:160\n"
"buffer_load_ushort v221, v246, s[40:43], s28 offen offset:416\n"
"buffer_load_ushort v222, v246, s[40:43], s28 offen offset:672\n"
"buffer_load_ushort v223, v246, s[40:43], s28 offen offset:928\n"
"s_waitcnt vmcnt(6)\n"
"v_lshl_or_b32 v224, v217, 16, v216\n"
"s_waitcnt vmcnt(4)\n"
"v_lshl_or_b32 v225, v219, 16, v218\n"
"s_waitcnt vmcnt(2)\n"
"v_lshl_or_b32 v226, v221, 16, v220\n"
"s_waitcnt vmcnt(0)\n"
"v_lshl_or_b32 v227, v223, 16, v222\n"
"s_nop 1\n"
"v_mfma_f32_16x16x32_bf16 v[100:103], v[224:227], v[240:243], v[100:103]\n"
"buffer_load_ushort v216, v245, s[40:43], s28 offen offset:192\n"
"buffer_load_ushort v217, v245, s[40:43], s28 offen offset:448\n"
"buffer_load_ushort v218, v245, s[40:43], s28 offen offset:704\n"
"buffer_load_ushort v219, v245, s[40:43], s28 offen offset:960\n"
"buffer_load_ushort v220, v246, s[40:43], s28 offen offset:192\n"
"buffer_load_ushort v221, v246, s[40:43], s28 offen offset:448\n"
"buffer_load_ushort v222, v246, s[40:43], s28 offen offset:704\n"
"buffer_load_ushort v223, v246, s[40:43], s28 offen offset:960\n"
"s_waitcnt vmcnt(6)\n"
"v_lshl_or_b32 v224, v217, 16, v216\n"
"s_waitcnt vmcnt(4)\n"
"v_lshl_or_b32 v225, v219, 16, v218\n"
"s_waitcnt vmcnt(2)\n"
"v_lshl_or_b32 v226, v221, 16, v220\n"
"s_waitcnt vmcnt(0)\n"
"v_lshl_or_b32 v227, v223, 16, v222\n"
"s_nop 1\n"
"v_mfma_f32_16x16x32_bf16 v[104:107], v[224:227], v[240:243], v[104:107]\n"
"buffer_load_ushort v216, v245, s[40:43], s28 offen offset:224\n"
"buffer_load_ushort v217, v245, s[40:43], s28 offen offset:480\n"
"buffer_load_ushort v218, v245, s[40:43], s28 offen offset:736\n"
"buffer_load_ushort v219, v245, s[40:43], s28 offen offset:992\n"
"buffer_load_ushort v220, v246, s[40:43], s28 offen offset:224\n"
"buffer_load_ushort v221, v246, s[40:43], s28 offen offset:480\n"
"buffer_load_ushort v222, v246, s[40:43], s28 offen offset:736\n"
"buffer_load_ushort v223, v246, s[40:43], s28 offen offset:992\n"
"s_waitcnt vmcnt(6)\n"
"v_lshl_or_b32 v224, v217, 16, v216\n"
"s_waitcnt vmcnt(4)\n"
"v_lshl_or_b32 v225, v219, 16, v218\n"
"s_waitcnt vmcnt(2)\n"
"v_lshl_or_b32 v226, v221, 16, v220\n"
"s_waitcnt vmcnt(0)\n"
"v_lshl_or_b32 v227, v223, 16, v222\n"
"s_nop 1\n"
"v_mfma_f32_16x16x32_bf16 v[108:111], v[224:227], v[240:243], v[108:111]\n"
"s_add_u32 s28, s28, 8192\n"
"s_cmp_lg_u32 s28, 262144\n"
"s_nop 7\n"
"s_cbranch_scc1 L_slow11b_%=\n"
"s_branch L_slow11r_%=\n"
"L_slow12_%=:\n"
"s_waitcnt vmcnt(0) lgkmcnt(0)\n"
"s_nop 7\n"
"v_and_b32 v247, 15, v1\n"
"v_lshrrev_b32 v215, 4, v1\n"
"v_lshlrev_b32 v244, 8, v247\n"
"v_lshlrev_b32 v245, 4, v215\n"
"v_add_u32 v244, v244, v245\n"
"v_lshlrev_b32 v245, 10, v215\n"
"v_lshlrev_b32 v247, 1, v247\n"
"v_add_u32 v245, v245, v247\n"
"v_add_u32 v246, 4096, v245\n"
"v_mov_b32 v247, 0xff800000\n"
"s_mov_b32 s28, 131072\n"
"L_slow12a_%=:\n"
"buffer_load_dwordx4 v[216:219], v244, s[36:39], s28 offen\n"
"buffer_load_dwordx4 v[220:223], v244, s[36:39], s28 offen offset:64\n"
"buffer_load_dwordx4 v[224:227], v244, s[36:39], s28 offen offset:128\n"
"buffer_load_dwordx4 v[228:231], v244, s[36:39], s28 offen offset:192\n"
"s_waitcnt vmcnt(3)\n"
"v_mfma_f32_16x16x32_bf16 v[232:235], v[216:219], a[80:83], 0\n"
"s_waitcnt vmcnt(2)\n"
"v_mfma_f32_16x16x32_bf16 v[232:235], v[220:223], a[84:87], v[232:235]\n"
"s_waitcnt vmcnt(1)\n"
"v_mfma_f32_16x16x32_bf16 v[232:235], v[224:227], a[88:91], v[232:235]\n"
"s_waitcnt vmcnt(0)\n"
"v_mfma_f32_16x16x32_bf16 v[232:235], v[228:231], a[92:95], v[232:235]\n"
"s_add_u32 s28, s28, 4096\n"
"s_nop 6\n"
"v_max3_f32 v247, v247, v232, v233\n"
"v_max3_f32 v247, v247, v234, v235\n"
"s_cmp_lg_u32 s28, 262144\n"
"s_cbranch_scc1 L_slow12a_%=\n"
"v_mov_b32 v215, v247\n"
"s_nop 1\n"
"v_permlane32_swap_b32 v247, v215\n"
"v_max_f32 v247, v247, v215\n"
"v_mov_b32 v215, v247\n"
"s_nop 1\n"
"v_permlane16_swap_b32 v247, v215\n"
"v_max_f32 v247, v247, v215\n"
"v_sub_f32 v164, 0, v247\n"
"v_mov_b32 v152, 0\n"
"v_sub_f32 v165, 0, v247\n"
"v_mov_b32 v153, 0\n"
"v_sub_f32 v166, 0, v247\n"
"v_mov_b32 v154, 0\n"
"v_sub_f32 v167, 0, v247\n"
"v_mov_b32 v155, 0\n"
"v_mov_b32 v112, 0\n"
"v_mov_b32 v113, 0\n"
"v_mov_b32 v114, 0\n"
"v_mov_b32 v115, 0\n"
"v_mov_b32 v116, 0\n"
"v_mov_b32 v117, 0\n"
"v_mov_b32 v118, 0\n"
"v_mov_b32 v119, 0\n"
"v_mov_b32 v120, 0\n"
"v_mov_b32 v121, 0\n"
"v_mov_b32 v122, 0\n"
"v_mov_b32 v123, 0\n"
"v_mov_b32 v124, 0\n"
"v_mov_b32 v125, 0\n"
"v_mov_b32 v126, 0\n"
"v_mov_b32 v127, 0\n"
"v_mov_b32 v128, 0\n"
"v_mov_b32 v129, 0\n"
"v_mov_b32 v130, 0\n"
"v_mov_b32 v131, 0\n"
"v_mov_b32 v132, 0\n"
"v_mov_b32 v133, 0\n"
"v_mov_b32 v134, 0\n"
"v_mov_b32 v135, 0\n"
"v_mov_b32 v136, 0\n"
"v_mov_b32 v137, 0\n"
"v_mov_b32 v138, 0\n"
"v_mov_b32 v139, 0\n"
"v_mov_b32 v140, 0\n"
"v_mov_b32 v141, 0\n"
"v_mov_b32 v142, 0\n"
"v_mov_b32 v143, 0\n"
"s_mov_b32 s28, 131072\n"
"L_slow12b_%=:\n"
"buffer_load_dwordx4 v[216:219], v244, s[36:39], s28 offen\n"
"buffer_load_dwordx4 v[220:223], v244, s[36:39], s28 offen offset:64\n"
"buffer_load_dwordx4 v[224:227], v244, s[36:39], s28 offen offset:128\n"
"buffer_load_dwordx4 v[228:231], v244, s[36:39], s28 offen offset:192\n"
"s_waitcnt vmcnt(3)\n"
"v_mfma_f32_16x16x32_bf16 v[232:235], v[216:219], a[80:83], v[164:167]\n"
"s_waitcnt vmcnt(2)\n"
"v_mfma_f32_16x16x32_bf16 v[232:235], v[220:223], a[84:87], v[232:235]\n"
"s_waitcnt vmcnt(1)\n"
"v_mfma_f32_16x16x32_bf16 v[232:235], v[224:227], a[88:91], v[232:235]\n"
"s_waitcnt vmcnt(0)\n"
"v_mfma_f32_16x16x32_bf16 v[232:235], v[228:231], a[92:95], v[232:235]\n"
"s_add_u32 s28, s28, 4096\n"
"buffer_load_dwordx4 v[216:219], v244, s[36:39], s28 offen\n"
"buffer_load_dwordx4 v[220:223], v244, s[36:39], s28 offen offset:64\n"
"buffer_load_dwordx4 v[224:227], v244, s[36:39], s28 offen offset:128\n"
"buffer_load_dwordx4 v[228:231], v244, s[36:39], s28 offen offset:192\n"
"s_waitcnt vmcnt(3)\n"
"v_mfma_f32_16x16x32_bf16 v[236:239], v[216:219], a[80:83], v[164:167]\n"
"s_waitcnt vmcnt(2)\n"
"v_mfma_f32_16x16x32_bf16 v[236:239], v[220:223], a[84:87], v[236:239]\n"
"s_waitcnt vmcnt(1)\n"
"v_mfma_f32_16x16x32_bf16 v[236:239], v[224:227], a[88:91], v[236:239]\n"
"s_waitcnt vmcnt(0)\n"
"v_mfma_f32_16x16x32_bf16 v[236:239], v[228:231], a[92:95], v[236:239]\n"
"s_add_u32 s28, s28, 4096\n"
"v_exp_f32 v232, v232\n"
"v_exp_f32 v233, v233\n"
"v_exp_f32 v234, v234\n"
"v_exp_f32 v235, v235\n"
"s_nop 2\n"
"v_exp_f32 v236, v236\n"
"v_exp_f32 v237, v237\n"
"v_exp_f32 v238, v238\n"
"v_exp_f32 v239, v239\n"
"v_cvt_pk_bf16_f32 v240, v232, v233\n"
"v_cvt_pk_bf16_f32 v241, v234, v235\n"
"v_cvt_pk_bf16_f32 v242, v236, v237\n"
"v_cvt_pk_bf16_f32 v243, v238, v239\n"
"s_nop 1\n"
"v_mfma_f32_16x16x32_bf16 v[152:155], v[192:195], v[240:243], v[152:155]\n"
"s_sub_u32 s28, s28, 8192\n"
"buffer_load_ushort v216, v245, s[40:43], s28 offen\n"
"buffer_load_ushort v217, v245, s[40:43], s28 offen offset:256\n"
"buffer_load_ushort v218, v245, s[40:43], s28 offen offset:512\n"
"buffer_load_ushort v219, v245, s[40:43], s28 offen offset:768\n"
"buffer_load_ushort v220, v246, s[40:43], s28 offen\n"
"buffer_load_ushort v221, v246, s[40:43], s28 offen offset:256\n"
"buffer_load_ushort v222, v246, s[40:43], s28 offen offset:512\n"
"buffer_load_ushort v223, v246, s[40:43], s28 offen offset:768\n"
"s_waitcnt vmcnt(6)\n"
"v_lshl_or_b32 v224, v217, 16, v216\n"
"s_waitcnt vmcnt(4)\n"
"v_lshl_or_b32 v225, v219, 16, v218\n"
"s_waitcnt vmcnt(2)\n"
"v_lshl_or_b32 v226, v221, 16, v220\n"
"s_waitcnt vmcnt(0)\n"
"v_lshl_or_b32 v227, v223, 16, v222\n"
"s_nop 1\n"
"v_mfma_f32_16x16x32_bf16 v[112:115], v[224:227], v[240:243], v[112:115]\n"
"buffer_load_ushort v216, v245, s[40:43], s28 offen offset:32\n"
"buffer_load_ushort v217, v245, s[40:43], s28 offen offset:288\n"
"buffer_load_ushort v218, v245, s[40:43], s28 offen offset:544\n"
"buffer_load_ushort v219, v245, s[40:43], s28 offen offset:800\n"
"buffer_load_ushort v220, v246, s[40:43], s28 offen offset:32\n"
"buffer_load_ushort v221, v246, s[40:43], s28 offen offset:288\n"
"buffer_load_ushort v222, v246, s[40:43], s28 offen offset:544\n"
"buffer_load_ushort v223, v246, s[40:43], s28 offen offset:800\n"
"s_waitcnt vmcnt(6)\n"
"v_lshl_or_b32 v224, v217, 16, v216\n"
"s_waitcnt vmcnt(4)\n"
"v_lshl_or_b32 v225, v219, 16, v218\n"
"s_waitcnt vmcnt(2)\n"
"v_lshl_or_b32 v226, v221, 16, v220\n"
"s_waitcnt vmcnt(0)\n"
"v_lshl_or_b32 v227, v223, 16, v222\n"
"s_nop 1\n"
"v_mfma_f32_16x16x32_bf16 v[116:119], v[224:227], v[240:243], v[116:119]\n"
"buffer_load_ushort v216, v245, s[40:43], s28 offen offset:64\n"
"buffer_load_ushort v217, v245, s[40:43], s28 offen offset:320\n"
"buffer_load_ushort v218, v245, s[40:43], s28 offen offset:576\n"
"buffer_load_ushort v219, v245, s[40:43], s28 offen offset:832\n"
"buffer_load_ushort v220, v246, s[40:43], s28 offen offset:64\n"
"buffer_load_ushort v221, v246, s[40:43], s28 offen offset:320\n"
"buffer_load_ushort v222, v246, s[40:43], s28 offen offset:576\n"
"buffer_load_ushort v223, v246, s[40:43], s28 offen offset:832\n"
"s_waitcnt vmcnt(6)\n"
"v_lshl_or_b32 v224, v217, 16, v216\n"
"s_waitcnt vmcnt(4)\n"
"v_lshl_or_b32 v225, v219, 16, v218\n"
"s_waitcnt vmcnt(2)\n"
"v_lshl_or_b32 v226, v221, 16, v220\n"
"s_waitcnt vmcnt(0)\n"
"v_lshl_or_b32 v227, v223, 16, v222\n"
"s_nop 1\n"
"v_mfma_f32_16x16x32_bf16 v[120:123], v[224:227], v[240:243], v[120:123]\n"
"buffer_load_ushort v216, v245, s[40:43], s28 offen offset:96\n"
"buffer_load_ushort v217, v245, s[40:43], s28 offen offset:352\n"
"buffer_load_ushort v218, v245, s[40:43], s28 offen offset:608\n"
"buffer_load_ushort v219, v245, s[40:43], s28 offen offset:864\n"
"buffer_load_ushort v220, v246, s[40:43], s28 offen offset:96\n"
"buffer_load_ushort v221, v246, s[40:43], s28 offen offset:352\n"
"buffer_load_ushort v222, v246, s[40:43], s28 offen offset:608\n"
"buffer_load_ushort v223, v246, s[40:43], s28 offen offset:864\n"
"s_waitcnt vmcnt(6)\n"
"v_lshl_or_b32 v224, v217, 16, v216\n"
"s_waitcnt vmcnt(4)\n"
"v_lshl_or_b32 v225, v219, 16, v218\n"
"s_waitcnt vmcnt(2)\n"
"v_lshl_or_b32 v226, v221, 16, v220\n"
"s_waitcnt vmcnt(0)\n"
"v_lshl_or_b32 v227, v223, 16, v222\n"
"s_nop 1\n"
"v_mfma_f32_16x16x32_bf16 v[124:127], v[224:227], v[240:243], v[124:127]\n"
"buffer_load_ushort v216, v245, s[40:43], s28 offen offset:128\n"
"buffer_load_ushort v217, v245, s[40:43], s28 offen offset:384\n"
"buffer_load_ushort v218, v245, s[40:43], s28 offen offset:640\n"
"buffer_load_ushort v219, v245, s[40:43], s28 offen offset:896\n"
"buffer_load_ushort v220, v246, s[40:43], s28 offen offset:128\n"
"buffer_load_ushort v221, v246, s[40:43], s28 offen offset:384\n"
"buffer_load_ushort v222, v246, s[40:43], s28 offen offset:640\n"
"buffer_load_ushort v223, v246, s[40:43], s28 offen offset:896\n"
"s_waitcnt vmcnt(6)\n"
"v_lshl_or_b32 v224, v217, 16, v216\n"
"s_waitcnt vmcnt(4)\n"
"v_lshl_or_b32 v225, v219, 16, v218\n"
"s_waitcnt vmcnt(2)\n"
"v_lshl_or_b32 v226, v221, 16, v220\n"
"s_waitcnt vmcnt(0)\n"
"v_lshl_or_b32 v227, v223, 16, v222\n"
"s_nop 1\n"
"v_mfma_f32_16x16x32_bf16 v[128:131], v[224:227], v[240:243], v[128:131]\n"
"buffer_load_ushort v216, v245, s[40:43], s28 offen offset:160\n"
"buffer_load_ushort v217, v245, s[40:43], s28 offen offset:416\n"
"buffer_load_ushort v218, v245, s[40:43], s28 offen offset:672\n"
"buffer_load_ushort v219, v245, s[40:43], s28 offen offset:928\n"
"buffer_load_ushort v220, v246, s[40:43], s28 offen offset:160\n"
"buffer_load_ushort v221, v246, s[40:43], s28 offen offset:416\n"
"buffer_load_ushort v222, v246, s[40:43], s28 offen offset:672\n"
"buffer_load_ushort v223, v246, s[40:43], s28 offen offset:928\n"
"s_waitcnt vmcnt(6)\n"
"v_lshl_or_b32 v224, v217, 16, v216\n"
"s_waitcnt vmcnt(4)\n"
"v_lshl_or_b32 v225, v219, 16, v218\n"
"s_waitcnt vmcnt(2)\n"
"v_lshl_or_b32 v226, v221, 16, v220\n"
"s_waitcnt vmcnt(0)\n"
"v_lshl_or_b32 v227, v223, 16, v222\n"
"s_nop 1\n"
"v_mfma_f32_16x16x32_bf16 v[132:135], v[224:227], v[240:243], v[132:135]\n"
"buffer_load_ushort v216, v245, s[40:43], s28 offen offset:192\n"
"buffer_load_ushort v217, v245, s[40:43], s28 offen offset:448\n"
"buffer_load_ushort v218, v245, s[40:43], s28 offen offset:704\n"
"buffer_load_ushort v219, v245, s[40:43], s28 offen offset:960\n"
"buffer_load_ushort v220, v246, s[40:43], s28 offen offset:192\n"
"buffer_load_ushort v221, v246, s[40:43], s28 offen offset:448\n"
"buffer_load_ushort v222, v246, s[40:43], s28 offen offset:704\n"
"buffer_load_ushort v223, v246, s[40:43], s28 offen offset:960\n"
"s_waitcnt vmcnt(6)\n"
"v_lshl_or_b32 v224, v217, 16, v216\n"
"s_waitcnt vmcnt(4)\n"
"v_lshl_or_b32 v225, v219, 16, v218\n"
"s_waitcnt vmcnt(2)\n"
"v_lshl_or_b32 v226, v221, 16, v220\n"
"s_waitcnt vmcnt(0)\n"
"v_lshl_or_b32 v227, v223, 16, v222\n"
"s_nop 1\n"
"v_mfma_f32_16x16x32_bf16 v[136:139], v[224:227], v[240:243], v[136:139]\n"
"buffer_load_ushort v216, v245, s[40:43], s28 offen offset:224\n"
"buffer_load_ushort v217, v245, s[40:43], s28 offen offset:480\n"
"buffer_load_ushort v218, v245, s[40:43], s28 offen offset:736\n"
"buffer_load_ushort v219, v245, s[40:43], s28 offen offset:992\n"
"buffer_load_ushort v220, v246, s[40:43], s28 offen offset:224\n"
"buffer_load_ushort v221, v246, s[40:43], s28 offen offset:480\n"
"buffer_load_ushort v222, v246, s[40:43], s28 offen offset:736\n"
"buffer_load_ushort v223, v246, s[40:43], s28 offen offset:992\n"
"s_waitcnt vmcnt(6)\n"
"v_lshl_or_b32 v224, v217, 16, v216\n"
"s_waitcnt vmcnt(4)\n"
"v_lshl_or_b32 v225, v219, 16, v218\n"
"s_waitcnt vmcnt(2)\n"
"v_lshl_or_b32 v226, v221, 16, v220\n"
"s_waitcnt vmcnt(0)\n"
"v_lshl_or_b32 v227, v223, 16, v222\n"
"s_nop 1\n"
"v_mfma_f32_16x16x32_bf16 v[140:143], v[224:227], v[240:243], v[140:143]\n"
"s_add_u32 s28, s28, 8192\n"
"s_cmp_lg_u32 s28, 262144\n"
"s_nop 7\n"
"s_cbranch_scc1 L_slow12b_%=\n"
"s_branch L_slow12r_%=\n"
"L_spin1_%=:\n"
"s_mov_b32 s28, 0x40000\n"
"L_spinl1_%=:\n"
"s_sleep 1\n"
"buffer_load_dwordx2 v[34:35], v38, s[56:59], 0 offen offset:2048 sc1\n"
"s_waitcnt vmcnt(0)\n"
"v_cmp_ne_u64 vcc, v[34:35], v[46:47]\n"
"s_nop 4\n"
"s_cbranch_vccz L_spinr1_%=\n"
"s_sub_u32 s28, s28, 1\n"
"s_cbranch_scc0 L_spinl1_%=\n"
"s_branch L_spinr1_%=\n"
"L_spin2_%=:\n"
"s_mov_b32 s28, 0x40000\n"
"L_spinl2_%=:\n"
"s_sleep 1\n"
"buffer_load_dwordx2 v[34:35], v38, s[56:59], 0 offen offset:1024 sc1\n"
"s_waitcnt vmcnt(0)\n"
"v_cmp_ne_u64 vcc, v[34:35], v[46:47]\n"
"s_nop 4\n"
"s_cbranch_vccz L_spinr2_%=\n"
"s_sub_u32 s28, s28, 1\n"
"s_cbranch_scc0 L_spinl2_%=\n"
"s_branch L_spinr2_%=\n"
"L_spin3_%=:\n"
"s_mov_b32 s28, 0x40000\n"
"L_spinl3_%=:\n"
"s_sleep 1\n"
"buffer_load_dwordx2 v[34:35], off, s[56:59], s29 offset:3072 sc1\n"
"s_waitcnt vmcnt(0)\n"
"v_cmp_ne_u64 vcc, v[34:35], v[46:47]\n"
"s_nop 4\n"
"s_cbranch_vccz L_spinr3_%=\n"
"s_sub_u32 s28, s28, 1\n"
"s_cbranch_scc0 L_spinl3_%=\n"
"s_branch L_spinr3_%=\n"
"L_end_%=:\n"
  :: "s"(__builtin_amdgcn_kernarg_segment_ptr()), "s"(blockIdx.x), "{v0}"(threadIdx.x) : CLOB);
}

extern "C" void kernel_launch(void* const* d_in, const int* in_sizes, int n_in, void* d_out, int out_size, void* d_ws, size_t ws_size, hipStream_t stream) {
  (void)hipFuncSetAttribute((const void*)mha_fused, hipFuncAttributeMaxDynamicSharedMemorySize, 163840);
  mha_fused<<<256, 256, 163840, stream>>>((const char*)d_in[0], (const char*)d_in[1], (const char*)d_in[2], (const char*)d_in[3], (const char*)d_in[4], (char*)d_out, (char*)d_ws, (char*)d_ws);
}
```
